```python
import torch

def _softmax(x, axis=-1):
    return torch.softmax(x, dim=axis)
def _gelu(x):
    return torch.nn.functional.gelu(x, approximate="tanh")
def _silu(x):
    return torch.nn.functional.silu(x)
def _sigmoid(x):
    return torch.sigmoid(x)
def _layernorm(x, axis=-1, eps=1e-5):
    m = x.mean(dim=axis, keepdim=True)
    v = x.var(dim=axis, keepdim=True, unbiased=False)
    return (x - m) / torch.sqrt(v + eps)
def _scatter_add(tgt, idx, src, axis=0):
    return tgt.clone().index_add_(axis, idx, src)
def _segment_sum(src, seg, n):
    out = torch.zeros((n,) + src.shape[1:], dtype=src.dtype)
    return out.index_add_(0, seg, src)
def _pad(x, pw):
    flat = tuple(v for pair in reversed(pw) for v in pair)
    return torch.nn.functional.pad(x, flat)

kernel_name = 'gated_mix__ssm_conv_s4d__geglu_ffn__prompt'

def setup_inputs():

    _w = (lambda a: (lambda t: t.to(torch.bfloat16) if t.is_floating_point() else t)(torch.as_tensor(a)))
    def _A_setup():
        Bsz, L, D, N = 1, 2048, 512, 128
        return {
            "u": _w((lambda *s: torch.randn(*s, dtype=torch.bfloat16) * 0.05)(Bsz, L, D)),
            "log_A_real": _w((lambda *s: torch.randn(*s, dtype=torch.bfloat16) * 6.4)(D, N) * 0.5),
            "A_imag": _w((lambda *s: torch.randn(*s, dtype=torch.bfloat16) * 0.05)(D, N) * 3.14),
            "C_real": _w((lambda *s: torch.randn(*s, dtype=torch.bfloat16) * 0.05)(D, N) / N ** 0.5),
            "C_imag": _w((lambda *s: torch.randn(*s, dtype=torch.bfloat16) * 0.05)(D, N) / N ** 0.5),
            "log_dt": _w((lambda *s: torch.randn(*s, dtype=torch.bfloat16) * 0.05)(D) * 0.5 - 4.0),
            "D_skip": _w((lambda *s: torch.randn(*s, dtype=torch.bfloat16) * 0.05)(D)),
        }
    _A_in = _A_setup()

    def _B_setup():
        B, S, D, F = 1, 2048, 512, 1376
        return {
            "x": _w((lambda *s: torch.randn(*s, dtype=torch.bfloat16) * 0.05)(B, S, D)),
            "w_gate": _w((lambda *s: torch.randn(*s, dtype=torch.bfloat16) * 0.05)(D, F)),
            "w_up": _w((lambda *s: torch.randn(*s, dtype=torch.bfloat16) * 0.05)(D, F)),
            "w_down": _w((lambda *s: torch.randn(*s, dtype=torch.bfloat16) * 0.05)(F, D)),
        }
    _B_in = _B_setup()

    return {
        "A_u": _A_in["u"],
        "A_log_A_real": _A_in["log_A_real"],
        "A_A_imag": _A_in["A_imag"],
        "A_C_real": _A_in["C_real"],
        "A_C_imag": _A_in["C_imag"],
        "A_log_dt": _A_in["log_dt"],
        "A_D_skip": _A_in["D_skip"],
        "B_x": _B_in["x"],
        "B_w_gate": _B_in["w_gate"],
        "B_w_up": _B_in["w_up"],
        "B_w_down": _B_in["w_down"],
        "gate_w": _w((lambda *s: torch.randn(*s, dtype=torch.bfloat16) * 0.05)(512) * 16.0),
    }

def reference(A_u, A_log_A_real, A_A_imag, A_C_real, A_C_imag, A_log_dt, A_D_skip, B_x, B_w_gate, B_w_up, B_w_down, gate_w):
    def _A_ref(u, log_A_real, A_imag, C_real, C_imag, log_dt, D_skip):
        L = u.shape[1]
        dt = torch.exp(log_dt)
        A = -torch.exp(log_A_real) + 1j * A_imag
        C = (C_real + 1j * C_imag) * dt[:, None]
        pos = (lambda x, dt: x.to(dt))(torch.arange(L), log_dt.dtype)
        P = torch.exp((A * dt[:, None])[:, :, None] * pos[None, None, :])
        K = 2.0 * torch.einsum("dn,dnl->dl", C, P).real
        L2 = 2 * L
        u_t = (lambda x, *d: x.permute(*d))(u, 0, 2, 1)
        Kf = (lambda x, *a, **k: torch.fft.rfft(x.float(), *a, **k))(K, L2)
        Uf = (lambda x, *a, **k: torch.fft.rfft(x.float(), *a, **k))(u_t, L2)
        y = (lambda x, *a, **k: torch.fft.irfft(x, *a, **k).to(torch.bfloat16))(Uf * Kf[None, :, :], L2)[:, :, :L]
        y = (lambda x, *d: x.permute(*d))(y, 0, 2, 1)
        return y + u * D_skip[None, None, :]

    def _B_ref(x, w_gate, w_up, w_down):
        return (_gelu(x @ w_gate) * (x @ w_up)) @ w_down

    x = A_u
    g = _sigmoid((x * gate_w).sum(-1, keepdims=True))
    a = _A_ref(x, A_log_A_real, A_A_imag, A_C_real, A_C_imag, A_log_dt, A_D_skip)
    b = _B_ref(x, B_w_gate, B_w_up, B_w_down)
    return g * a + (1 - g) * b
```

```cpp
#include <hip/hip_runtime.h>
#include <hip/hip_bf16.h>
#include <stdint.h>

typedef unsigned short u16;
typedef __attribute__((ext_vector_type(8))) short bf16x8;
typedef __attribute__((ext_vector_type(4))) short s16x4;
typedef __attribute__((ext_vector_type(4))) float f32x4;
typedef __attribute__((ext_vector_type(16))) float f32x16;
typedef __attribute__((ext_vector_type(4))) unsigned u32x4;
typedef __attribute__((ext_vector_type(2))) unsigned u32x2;
typedef __attribute__((ext_vector_type(2))) __bf16 bf16x2_t;
typedef __attribute__((ext_vector_type(2))) float f32x2;
typedef __attribute__((address_space(3))) s16x4* lds_s16x4_ptr;

constexpr int SEQ = 2048, DM = 512, NST = 128, FF = 1376, FFP = 1408;
constexpr int NSSM = 256, NG1 = 352, NGATE = 32;

__device__ __forceinline__ unsigned pk_bf16(float lo, float hi) {
  f32x2 v = {lo, hi};
  bf16x2_t b = __builtin_convertvector(v, bf16x2_t);
  return __builtin_bit_cast(unsigned, b);
}
__device__ __forceinline__ float bf_lo(unsigned w) { return __uint_as_float(w << 16); }
__device__ __forceinline__ float bf_hi(unsigned w) { return __uint_as_float(w & 0xffff0000u); }
__device__ __forceinline__ float rbf(float x) { return bf_lo(pk_bf16(x, 0.f)); }
__device__ __forceinline__ float bf2f(u16 v) { return __uint_as_float(((unsigned)v) << 16); }

__device__ __forceinline__ bf16x8 mk8(unsigned a, unsigned b, unsigned c, unsigned d) {
  u32x4 v = {a, b, c, d};
  return __builtin_bit_cast(bf16x8, v);
}
__device__ __forceinline__ bf16x8 cat4(s16x4 a, s16x4 b) {
  return __builtin_shufflevector(a, b, 0, 1, 2, 3, 4, 5, 6, 7);
}
__device__ __forceinline__ s16x4 tr_read(const char* smem_ptr) {
  return __builtin_amdgcn_ds_read_tr16_b64_v4i16((lds_s16x4_ptr)(smem_ptr));
}
__device__ __forceinline__ void glds16(const void* g, char* l) {
  __builtin_amdgcn_global_load_lds((const unsigned*)g, (unsigned*)l, 16, 0, 0);
}

constexpr int U_COPY_B = 4160;
constexpr int U_CH_B = 4 * U_COPY_B;
constexpr int U_REGION_B = 2 * U_CH_B;
constexpr int KR_PART_B = 4576;
constexpr int KR_OFF = U_REGION_B;
constexpr int KP_B = 8208;
constexpr int K1_LDS = KR_OFF + 4 * KR_PART_B;
static_assert(4 * KP_B <= U_REGION_B, "Kp scratch must fit the U region");

__device__ __forceinline__ void ssm_unit(int w, const u16* __restrict__ u, const u16* __restrict__ logAr,
                                         const u16* __restrict__ Aim, const u16* __restrict__ Cre,
                                         const u16* __restrict__ Cim, const u16* __restrict__ logdt,
                                         const u16* __restrict__ Dskip, u16* __restrict__ aT, char* smem) {
  const int tid = threadIdx.x, lane = tid & 63;
  const int wave = __builtin_amdgcn_readfirstlane(tid >> 6);
  const int ch = wave >> 1, w2 = wave & 1;
  const int d0 = 64 * (w & 7) + 2 * (w >> 3);
  const int d = d0 + ch;

  const unsigned* u32p = (const unsigned*)(u + d0);
  unsigned uw[8], up[3];
#pragma unroll
  for (int i = 0; i < 8; ++i) uw[i] = u32p[(size_t)(8 * tid + i) * (DM / 2)];
#pragma unroll
  for (int i = 0; i < 3; ++i) up[i] = 0;
  if (lane == 0 && tid > 0) {
#pragma unroll
    for (int i = 0; i < 3; ++i) up[i] = u32p[(size_t)(8 * tid - 3 + i) * (DM / 2)];
  }

  {
    const int n = tid & 127;
    const int dd = d0 + (tid >> 7);
    const float dt = rbf(expf(bf2f(logdt[dd])));
    const float are = rbf(expf(bf2f(logAr[dd * NST + n])));
    f32x4 prm;
    prm[0] = (-are * dt) * 1.44269504088896340736f;
    prm[1] = (bf2f(Aim[dd * NST + n]) * dt) * 0.15915494309189533577f;
    prm[2] = 2.0f * (bf2f(Cre[dd * NST + n]) * dt);
    prm[3] = 2.0f * (bf2f(Cim[dd * NST + n]) * dt);
    *(f32x4*)(smem + KR_OFF + tid * 16) = prm;
  }
  __syncthreads();

  {
    const int r = lane & 31, h = lane >> 5;
    int p1i;
    if (r < 8) p1i = r;
    else if (r < 12) p1i = 256 + 2 * (r - 8);
    else if (r < 16) p1i = 512 + 256 * ((r - 12) >> 1) + 4 * ((r - 12) & 1);
    else if (r < 21) p1i = 1024 + 256 * (r - 16);
    else p1i = 0;
    const float p1 = (float)p1i, p2 = (float)(8 * r);
    f32x16 acc = {};
    const char* prm_base = smem + KR_OFF + (ch * 128 + 64 * w2 + 4 * h) * 16;
#pragma unroll 2
    for (int ks = 0; ks < 8; ++ks) {
      unsigned ah[4], al[4], bh[4], bl[4];
#pragma unroll
      for (int q = 0; q < 4; ++q) {
        const f32x4 prm = *(const f32x4*)(prm_base + (8 * ks + q) * 16);
        {
          const float e = __builtin_amdgcn_exp2f(prm[0] * p1);
          const float rv = __builtin_amdgcn_fractf(prm[1] * p1);
          const float sn = __builtin_amdgcn_sinf(rv), cs = __builtin_amdgcn_cosf(rv);
          const float gre = e * (prm[2] * cs - prm[3] * sn);
          const float gim = -e * (prm[2] * sn + prm[3] * cs);
          const unsigned hi = pk_bf16(gre, gim);
          ah[q] = hi;
          al[q] = pk_bf16(gre - bf_lo(hi), gim - bf_hi(hi));
        }
        {
          const float e = __builtin_amdgcn_exp2f(prm[0] * p2);
          const float rv = __builtin_amdgcn_fractf(prm[1] * p2);
          const float sn = __builtin_amdgcn_sinf(rv), cs = __builtin_amdgcn_cosf(rv);
          const float ere = e * cs, eim = e * sn;
          const unsigned hi = pk_bf16(ere, eim);
          bh[q] = hi;
          bl[q] = pk_bf16(ere - bf_lo(hi), eim - bf_hi(hi));
        }
      }
      const bf16x8 Ah = mk8(ah[0], ah[1], ah[2], ah[3]), Al = mk8(al[0], al[1], al[2], al[3]);
      const bf16x8 Bh = mk8(bh[0], bh[1], bh[2], bh[3]), Bl = mk8(bl[0], bl[1], bl[2], bl[3]);
      acc = __builtin_amdgcn_mfma_f32_32x32x16_bf16(Al, Bh, acc, 0, 0, 0);
      acc = __builtin_amdgcn_mfma_f32_32x32x16_bf16(Ah, Bl, acc, 0, 0, 0);
      acc = __builtin_amdgcn_mfma_f32_32x32x16_bf16(Ah, Bh, acc, 0, 0, 0);
    }
    float* Kp = (float*)(smem + (ch * 2 + w2) * KP_B);
#pragma unroll
    for (int e = 0; e < 4; ++e) Kp[(4 * h + e) + 8 * r] = acc[e];
#pragma unroll
    for (int e = 0; e < 4; ++e) {
      const int pp = h ? (512 + 256 * (e >> 1) + 4 * (e & 1)) : (256 + 2 * e);
      Kp[pp + 8 * r] = acc[4 + e];
    }
    if (h == 0) {
#pragma unroll
      for (int e = 0; e < 4; ++e) Kp[1024 + 256 * e + 8 * r] = acc[8 + e];
    } else if (r == 0) {
      Kp[2048] = acc[8];
    }
  }
  __syncthreads();

  {
    const int t = tid & 127;
    const float* Kp0 = (const float*)(smem + (ch * 2) * KP_B);
    const float* Kp1 = (const float*)(smem + (ch * 2 + 1) * KP_B);
    char* krh = smem + KR_OFF + (ch * 2) * KR_PART_B;
    char* krl = krh + KR_PART_B;
#pragma unroll
    for (int kk = 0; kk < 2; ++kk) {
      const int k = t + 128 * kk;
      float v[8];
#pragma unroll
      for (int e = 0; e < 8; ++e) {
        const int l = 2047 - 8 * k - e;
        const int p = (int)rbf((float)l);
        v[e] = Kp0[p] + Kp1[p];
      }
      unsigned hi[4], lo[4];
#pragma unroll
      for (int e = 0; e < 4; ++e) {
        hi[e] = pk_bf16(v[2 * e], v[2 * e + 1]);
        lo[e] = pk_bf16(v[2 * e] - bf_lo(hi[e]), v[2 * e + 1] - bf_hi(hi[e]));
      }
      *(u32x4*)(krh + k * 16) = u32x4{hi[0], hi[1], hi[2], hi[3]};
      *(u32x4*)(krl + k * 16) = u32x4{lo[0], lo[1], lo[2], lo[3]};
    }
    if (t < 30) {
      *(u32x4*)(krh + (256 + t) * 16) = u32x4{0, 0, 0, 0};
      *(u32x4*)(krl + (256 + t) * 16) = u32x4{0, 0, 0, 0};
    }
  }
  __syncthreads();

  {
    unsigned e[11];
#pragma unroll
    for (int i = 0; i < 3; ++i) {
      const unsigned nb = __shfl_up(uw[5 + i], 1);
      e[i] = (lane == 0) ? up[i] : nb;
    }
#pragma unroll
    for (int i = 0; i < 8; ++i) e[3 + i] = uw[i];
    unsigned p0[10], p1[10];
#pragma unroll
    for (int i = 0; i < 10; ++i) {
      p0[i] = __builtin_amdgcn_perm(e[i + 1], e[i], 0x05040100u);
      p1[i] = __builtin_amdgcn_perm(e[i + 1], e[i], 0x07060302u);
    }
#pragma unroll
    for (int c = 0; c < 4; ++c) {
      *(u32x4*)(smem + c * U_COPY_B + (tid + 2) * 16) = u32x4{p0[c], p0[c + 2], p0[c + 4], p0[c + 6]};
      *(u32x4*)(smem + U_CH_B + c * U_COPY_B + (tid + 2) * 16) = u32x4{p1[c], p1[c + 2], p1[c + 4], p1[c + 6]};
    }
    if (tid < 16) *(u32x4*)(smem + (tid >> 1) * U_COPY_B + (tid & 1) * 16) = u32x4{0, 0, 0, 0};
  }
  __syncthreads();

  f32x4 acc[8];
#pragma unroll
  for (int t = 0; t < 8; ++t) acc[t] = f32x4{0.f, 0.f, 0.f, 0.f};
  {
    const int r = lane & 15, hq = lane >> 4;
    const char* ua = smem + ch * U_CH_B + (r & 3) * U_COPY_B + 2 * (4 + (r & ~3) + 8 * hq);
    const char* kb = smem + KR_OFF + (ch * 2) * KR_PART_B + 2 * (2032 - 16 * r + 8 * hq);
#pragma unroll 1
    for (int ri = 0; ri < 4; ++ri) {
      const int rho = 4 * w2 + ri;
      bf16x8 A[8];
#pragma unroll
      for (int sg = 0; sg < 8; ++sg) {
        const char* pa = ua + 64 * (8 * sg + rho);
        A[sg] = cat4(*(const s16x4*)pa, *(const s16x4*)(pa + 8));
      }
#pragma unroll
      for (int tau = 0; tau < 8; ++tau) {
        const char* pb = kb - 64 * (8 * tau - rho);
        const bf16x8 Bh = *(const bf16x8*)pb;
        const bf16x8 Bl = *(const bf16x8*)(pb + KR_PART_B);
#pragma unroll
        for (int sg = 0; sg + tau < 8; ++sg) {
          acc[sg + tau] = __builtin_amdgcn_mfma_f32_16x16x32_bf16(A[sg], Bl, acc[sg + tau], 0, 0, 0);
          acc[sg + tau] = __builtin_amdgcn_mfma_f32_16x16x32_bf16(A[sg], Bh, acc[sg + tau], 0, 0, 0);
        }
      }
    }
  }
  __syncthreads();
  {
    char* xw = smem + KR_OFF + (ch * 2 + w2) * 4096 + lane * 16;
    const char* xr = smem + KR_OFF + (ch * 2 + (w2 ^ 1)) * 4096 + lane * 16;
    f32x4 y[4];
    if (w2 == 0) {
#pragma unroll
      for (int i = 0; i < 4; ++i) { *(f32x4*)(xw + i * 1024) = acc[4 + i]; y[i] = acc[i]; }
    } else {
#pragma unroll
      for (int i = 0; i < 4; ++i) { *(f32x4*)(xw + i * 1024) = acc[i]; y[i] = acc[4 + i]; }
    }
    __syncthreads();
    const float Dk = bf2f(Dskip[d]);
#pragma unroll
    for (int i = 0; i < 4; ++i) {
      const f32x4 o = *(const f32x4*)(xr + i * 1024);
      const int l0 = 256 * (4 * w2 + i) + 16 * (lane & 15) + 4 * (lane >> 4);
      const u32x2 uu = *(const u32x2*)(smem + ch * U_CH_B + 3 * U_COPY_B + 2 * (16 + l0));
      const float a0 = rbf(y[i][0] + o[0]) + rbf(bf_lo(uu[0]) * Dk);
      const float a1 = rbf(y[i][1] + o[1]) + rbf(bf_hi(uu[0]) * Dk);
      const float a2 = rbf(y[i][2] + o[2]) + rbf(bf_lo(uu[1]) * Dk);
      const float a3 = rbf(y[i][3] + o[3]) + rbf(bf_hi(uu[1]) * Dk);
      *(u32x2*)(aT + (size_t)d * SEQ + l0) = u32x2{pk_bf16(a0, a1), pk_bf16(a2, a3)};
    }
  }
}

__device__ __forceinline__ float gelu_tanh(float x) {
  const float z = 0.7978845608028654f * (x + 0.044715f * x * x * x);
  const float e = __builtin_amdgcn_exp2f(-2.8853900817779268f * z);
  return x * __builtin_amdgcn_rcpf(1.0f + e);
}

constexpr int G1_STAGE_B = 16384;

__device__ __forceinline__ void gemm1_tile(int g, const u16* __restrict__ x, const u16* __restrict__ wg,
                                           const u16* __restrict__ wu, u16* __restrict__ hbuf, char* smem) {
  const int tid = threadIdx.x, lane = tid & 63;
  const int wave = __builtin_amdgcn_readfirstlane(tid >> 6);
  const int xcd = g & 7, idx = g >> 3;
  const int mb = 2 * xcd + (idx / 22), fb = idx % 22;
  const int m0 = 128 * mb, f0 = 64 * fb;
  const int wm = wave >> 1, wn = wave & 1;

  const u16* asrc[2];
#pragma unroll
  for (int i = 0; i < 2; ++i) {
    const int row = 16 * (2 * wave + i) + (lane >> 2);
    const int chunk = (lane & 3) ^ ((lane >> 4) & 3);
    asrc[i] = x + (size_t)(m0 + row) * DM + chunk * 8;
  }
  const int wrow = 8 * wave + (lane >> 3);
  int wcol = f0 + 8 * ((lane & 7) ^ (((lane >> 4) & 1) << 2));
  if (wcol > FF - 8) wcol = FF - 8;
  const u16* gsrc = wg + (size_t)wrow * FF + wcol;
  const u16* usrc = wu + (size_t)wrow * FF + wcol;

  auto stage = [&](int kt, int slot) {
    char* sb = smem + slot * G1_STAGE_B;
    glds16(asrc[0] + kt * 32, sb + (2 * wave) * 1024 + lane * 16);
    glds16(asrc[1] + kt * 32, sb + (2 * wave + 1) * 1024 + lane * 16);
    glds16(gsrc + (size_t)kt * 32 * FF, sb + 8192 + wave * 1024 + lane * 16);
    glds16(usrc + (size_t)kt * 32 * FF, sb + 12288 + wave * 1024 + lane * 16);
  };

  const int r = lane & 31, h = lane >> 5;
  int aoff[2][2];
#pragma unroll
  for (int mi = 0; mi < 2; ++mi)
#pragma unroll
    for (int ks = 0; ks < 2; ++ks) {
      const int row = 64 * wm + 32 * mi + r;
      aoff[mi][ks] = row * 64 + (((2 * ks + h) ^ ((row >> 2) & 3)) * 16);
    }
  const int blk = (lane >> 4) & 1, q = (lane & 15) >> 2, p = lane & 3;
  const int woff = 8192 + (8 * h + q) * 128 + (((4 * wn + 2 * blk + (p >> 1)) ^ (((q >> 1) & 1) << 2)) * 16) + 8 * (p & 1);

  f32x16 acc[2][2];
#pragma unroll
  for (int i = 0; i < 2; ++i)
#pragma unroll
    for (int j = 0; j < 2; ++j) acc[i][j] = f32x16{};

  stage(0, 0);
  stage(1, 1);
#pragma unroll 1
  for (int kt = 0; kt < 16; ++kt) {
    if (kt < 15) asm volatile("s_waitcnt vmcnt(4)\n\ts_barrier" ::: "memory");
    else asm volatile("s_waitcnt vmcnt(0)\n\ts_barrier" ::: "memory");
    const int slot = kt % 3;
    if (kt + 2 < 16) stage(kt + 2, (kt + 2) % 3);
    const char* sb = smem + slot * G1_STAGE_B;
#pragma unroll
    for (int ks = 0; ks < 2; ++ks) {
      const bf16x8 a0 = *(const bf16x8*)(sb + aoff[0][ks]);
      const bf16x8 a1 = *(const bf16x8*)(sb + aoff[1][ks]);
      const char* wp = sb + woff + ks * 2048;
      const bf16x8 bg = cat4(tr_read(wp), tr_read(wp + 512));
      const bf16x8 bu = cat4(tr_read(wp + 4096), tr_read(wp + 4096 + 512));
      acc[0][0] = __builtin_amdgcn_mfma_f32_32x32x16_bf16(bg, a0, acc[0][0], 0, 0, 0);
      acc[0][1] = __builtin_amdgcn_mfma_f32_32x32x16_bf16(bu, a0, acc[0][1], 0, 0, 0);
      acc[1][0] = __builtin_amdgcn_mfma_f32_32x32x16_bf16(bg, a1, acc[1][0], 0, 0, 0);
      acc[1][1] = __builtin_amdgcn_mfma_f32_32x32x16_bf16(bu, a1, acc[1][1], 0, 0, 0);
    }
  }
  const bool valid = (f0 + 32 * wn) < FF;
#pragma unroll
  for (int mi = 0; mi < 2; ++mi) {
    u16* hrow = hbuf + (size_t)(m0 + 64 * wm + 32 * mi + r) * FFP + f0 + 32 * wn + 4 * h;
#pragma unroll
    for (int gq = 0; gq < 4; ++gq) {
      float hv[4];
#pragma unroll
      for (int e = 0; e < 4; ++e) {
        const float gv = rbf(acc[mi][0][4 * gq + e]);
        const float uv = rbf(acc[mi][1][4 * gq + e]);
        hv[e] = valid ? rbf(gelu_tanh(gv)) * uv : 0.f;
      }
      *(u32x2*)(hrow + 8 * gq) = u32x2{pk_bf16(hv[0], hv[1]), pk_bf16(hv[2], hv[3])};
    }
  }
}

__device__ __forceinline__ void gate_unit(int b, const u16* __restrict__ x, const u16* __restrict__ gw,
                                          float* __restrict__ gout) {
  const int tid = threadIdx.x, lane = tid & 63, wave = tid >> 6;
  const int row0 = b * 64 + wave * 16;
  const u32x4 gv = *(const u32x4*)(gw + lane * 8);
  u32x4 xv[16];
#pragma unroll
  for (int i = 0; i < 16; ++i) xv[i] = *(const u32x4*)(x + (size_t)(row0 + i) * DM + lane * 8);
#pragma unroll
  for (int i = 0; i < 16; ++i) {
    float s = 0.f;
#pragma unroll
    for (int j = 0; j < 4; ++j) {
      const unsigned pr = pk_bf16(bf_lo(xv[i][j]) * bf_lo(gv[j]), bf_hi(xv[i][j]) * bf_hi(gv[j]));
      s += bf_lo(pr) + bf_hi(pr);
    }
#pragma unroll
    for (int o = 32; o >= 1; o >>= 1) s += __shfl_xor(s, o);
    if (lane == 0) {
      const float sb = rbf(s);
      gout[row0 + i] = rbf(1.0f / (1.0f + expf(-sb)));
    }
  }
}

__global__ __launch_bounds__(256, 3) void k1_kernel(const u16* __restrict__ u, const u16* __restrict__ logAr,
                                                   const u16* __restrict__ Aim, const u16* __restrict__ Cre,
                                                   const u16* __restrict__ Cim, const u16* __restrict__ logdt,
                                                   const u16* __restrict__ Dskip, const u16* __restrict__ wg,
                                                   const u16* __restrict__ wu, const u16* __restrict__ gw,
                                                   u16* __restrict__ hbuf, u16* __restrict__ aT,
                                                   float* __restrict__ gout) {
  __shared__ __attribute__((aligned(16))) char smem[K1_LDS];
  const int bid = blockIdx.x;
  if (bid < NSSM) ssm_unit(bid, u, logAr, Aim, Cre, Cim, logdt, Dskip, aT, smem);
  else if (bid < NSSM + NG1) gemm1_tile(bid - NSSM, u, wg, wu, hbuf, smem);
  else gate_unit(bid - NSSM - NG1, u, gw, gout);
}

constexpr int G2_SLOT_B = 8192;
constexpr int G2_NSLOT = 4;
constexpr int G2_WAVE_B = G2_SLOT_B * G2_NSLOT;
constexpr int G2_A_OFF = 4 * G2_WAVE_B;
constexpr int K2_LDS = G2_A_OFF + 8192;
constexpr int G2_PITCH = 272;

__global__ __launch_bounds__(256, 1) void k2_kernel(const u16* __restrict__ hbuf, const u16* __restrict__ wd,
                                                   const u16* __restrict__ aT, const float* __restrict__ gin,
                                                   u16* __restrict__ out) {
  __shared__ __attribute__((aligned(16))) char smem[K2_LDS];
  const int tid = threadIdx.x, lane = tid & 63;
  const int wave = __builtin_amdgcn_readfirstlane(tid >> 6);
  const int bid = blockIdx.x;
  const int xcd = bid & 7, idx = bid >> 3;
  const int m0 = 64 * (4 * xcd + (idx >> 3)), n0 = 64 * (idx & 7);

#pragma unroll
  for (int i = 0; i < 2; ++i) {
    const int piece = 2 * wave + i;
    const int drow = 8 * piece + (lane >> 3);
    glds16(aT + (size_t)(n0 + drow) * SEQ + m0 + (lane & 7) * 8, smem + G2_A_OFF + piece * 1024 + lane * 16);
  }

  char* wbase = smem + wave * G2_WAVE_B;
  const int kbase = 352 * wave;
  const u16* hsrc = hbuf + (size_t)(m0 + (lane >> 2)) * FFP + kbase + 8 * ((lane & 3) ^ ((lane >> 4) & 3));
  const int wcol = n0 + 8 * ((lane & 7) ^ (((lane >> 4) & 1) << 2));
  const int wrow0 = kbase + (lane >> 3);

  auto stage = [&](int st) {
    char* sb = wbase + (st & (G2_NSLOT - 1)) * G2_SLOT_B;
#pragma unroll
    for (int i = 0; i < 4; ++i) glds16(hsrc + (size_t)(16 * i) * FFP + st * 32, sb + i * 1024 + lane * 16);
#pragma unroll
    for (int i = 0; i < 4; ++i) {
      int kr = wrow0 + st * 32 + 8 * i;
      kr = kr < FF ? kr : FF - 1;
      glds16(wd + (size_t)kr * DM + wcol, sb + 4096 + i * 1024 + lane * 16);
    }
  };

  const int r = lane & 31, h = lane >> 5;
  int aoff[2][2];
#pragma unroll
  for (int mi = 0; mi < 2; ++mi)
#pragma unroll
    for (int ks = 0; ks < 2; ++ks) {
      const int row = 32 * mi + r;
      aoff[mi][ks] = row * 64 + (((2 * ks + h) ^ ((row >> 2) & 3)) * 16);
    }
  const int blk = (lane >> 4) & 1, q = (lane & 15) >> 2, p = lane & 3;
  int woff[2];
#pragma unroll
  for (int c = 0; c < 2; ++c)
    woff[c] = 4096 + (8 * h + q) * 128 + (((4 * c + 2 * blk + (p >> 1)) ^ (((q >> 1) & 1) << 2)) * 16) + 8 * (p & 1);

  f32x16 acc[2][2];
#pragma unroll
  for (int i = 0; i < 2; ++i)
#pragma unroll
    for (int j = 0; j < 2; ++j) acc[i][j] = f32x16{};

  stage(0); stage(1); stage(2);
#pragma unroll 1
  for (int st = 0; st < 11; ++st) {
    if (st < 9) asm volatile("s_waitcnt vmcnt(16)" ::: "memory");
    else if (st == 9) asm volatile("s_waitcnt vmcnt(8)" ::: "memory");
    else asm volatile("s_waitcnt vmcnt(0)" ::: "memory");
    const char* sb = wbase + (st & (G2_NSLOT - 1)) * G2_SLOT_B;
    bf16x8 ha[2][2], wb[2][2];
#pragma unroll
    for (int ks = 0; ks < 2; ++ks) {
      ha[ks][0] = *(const bf16x8*)(sb + aoff[0][ks]);
      ha[ks][1] = *(const bf16x8*)(sb + aoff[1][ks]);
#pragma unroll
      for (int c = 0; c < 2; ++c) {
        const char* wp = sb + woff[c] + ks * 2048;
        wb[ks][c] = cat4(tr_read(wp), tr_read(wp + 512));
      }
    }
    if (st + 3 < 11) stage(st + 3);
#pragma unroll
    for (int ks = 0; ks < 2; ++ks)
#pragma unroll
      for (int c = 0; c < 2; ++c)
#pragma unroll
        for (int mi = 0; mi < 2; ++mi)
          acc[c][mi] = __builtin_amdgcn_mfma_f32_32x32x16_bf16(wb[ks][c], ha[ks][mi], acc[c][mi], 0, 0, 0);
  }
  __syncthreads();
  {
    char* pw = smem + wave * (64 * G2_PITCH);
#pragma unroll
    for (int c = 0; c < 2; ++c)
#pragma unroll
      for (int mi = 0; mi < 2; ++mi)
#pragma unroll
        for (int gq = 0; gq < 4; ++gq) {
          const f32x4 v = {acc[c][mi][4 * gq], acc[c][mi][4 * gq + 1], acc[c][mi][4 * gq + 2], acc[c][mi][4 * gq + 3]};
          *(f32x4*)(pw + (32 * mi + r) * G2_PITCH + (32 * c + 8 * gq + 4 * h) * 4) = v;
        }
  }
  __syncthreads();
  {
    const int m = tid >> 2, nq = (tid & 3) * 16;
    const float g = gin[m0 + m];
    const float omg = rbf(1.0f - g);
    float b[16];
#pragma unroll
    for (int j = 0; j < 4; ++j) {
      f32x4 s = *(const f32x4*)(smem + m * G2_PITCH + (nq + 4 * j) * 4);
#pragma unroll
      for (int w = 1; w < 4; ++w) s += *(const f32x4*)(smem + w * (64 * G2_PITCH) + m * G2_PITCH + (nq + 4 * j) * 4);
#pragma unroll
      for (int e = 0; e < 4; ++e) b[4 * j + e] = s[e];
    }
    const u16* aimg = (const u16*)(smem + G2_A_OFF);
    unsigned o[8];
#pragma unroll
    for (int j = 0; j < 8; ++j) {
      const float a0 = bf2f(aimg[(nq + 2 * j) * 64 + m]);
      const float a1 = bf2f(aimg[(nq + 2 * j + 1) * 64 + m]);
      const float o0 = rbf(g * a0) + rbf(omg * rbf(b[2 * j]));
      const float o1 = rbf(g * a1) + rbf(omg * rbf(b[2 * j + 1]));
      o[j] = pk_bf16(o0, o1);
    }
    u16* op = out + (size_t)(m0 + m) * DM + n0 + nq;
    *(u32x4*)op = u32x4{o[0], o[1], o[2], o[3]};
    *(u32x4*)(op + 8) = u32x4{o[4], o[5], o[6], o[7]};
  }
}

extern "C" void kernel_launch(void* const* d_in, const int* in_sizes, int n_in, void* d_out, int out_size,
                              void* d_ws, size_t ws_size, hipStream_t stream) {
  const u16* u = (const u16*)d_in[0];
  const u16* logAr = (const u16*)d_in[1];
  const u16* Aim = (const u16*)d_in[2];
  const u16* Cre = (const u16*)d_in[3];
  const u16* Cim = (const u16*)d_in[4];
  const u16* logdt = (const u16*)d_in[5];
  const u16* Dskip = (const u16*)d_in[6];
  const u16* wg = (const u16*)d_in[8];
  const u16* wu = (const u16*)d_in[9];
  const u16* wd = (const u16*)d_in[10];
  const u16* gw = (const u16*)d_in[11];
  char* ws = (char*)d_ws;
  u16* hbuf = (u16*)ws;
  u16* aT = (u16*)(ws + (size_t)SEQ * FFP * 2);
  float* gbuf = (float*)(ws + (size_t)SEQ * FFP * 2 + (size_t)DM * SEQ * 2);
  k1_kernel<<<NSSM + NG1 + NGATE, 256, 0, stream>>>(u, logAr, Aim, Cre, Cim, logdt, Dskip, wg, wu, gw, hbuf, aT, gbuf);
  k2_kernel<<<256, 256, 0, stream>>>(hbuf, wd, aT, gbuf, (u16*)d_out);
}
```

```cpp
#include <hip/hip_runtime.h>
#include <hip/hip_bf16.h>
#include <stdint.h>

typedef unsigned short u16;
typedef __attribute__((ext_vector_type(8))) short bf16x8;
typedef __attribute__((ext_vector_type(4))) short s16x4;
typedef __attribute__((ext_vector_type(4))) float f32x4;
typedef __attribute__((ext_vector_type(16))) float f32x16;
typedef __attribute__((ext_vector_type(4))) unsigned u32x4;
typedef __attribute__((ext_vector_type(2))) unsigned u32x2;
typedef __attribute__((ext_vector_type(2))) __bf16 bf16x2_t;
typedef __attribute__((ext_vector_type(2))) float f32x2;
typedef __attribute__((address_space(3))) s16x4* lds_s16x4_ptr;

constexpr int SEQ = 2048, DM = 512, NST = 128, FF = 1376, FFP = 1408;
constexpr int NSSM = 256;
constexpr int NKS = 88;

__device__ __forceinline__ unsigned pk_bf16(float lo, float hi) {
  f32x2 v = {lo, hi};
  bf16x2_t b = __builtin_convertvector(v, bf16x2_t);
  return __builtin_bit_cast(unsigned, b);
}
__device__ __forceinline__ float bf_lo(unsigned w) { return __uint_as_float(w << 16); }
__device__ __forceinline__ float bf_hi(unsigned w) { return __uint_as_float(w & 0xffff0000u); }
__device__ __forceinline__ float rbf(float x) { return bf_lo(pk_bf16(x, 0.f)); }
__device__ __forceinline__ float bf2f(u16 v) { return __uint_as_float(((unsigned)v) << 16); }

__device__ __forceinline__ bf16x8 mk8(unsigned a, unsigned b, unsigned c, unsigned d) {
  u32x4 v = {a, b, c, d};
  return __builtin_bit_cast(bf16x8, v);
}
__device__ __forceinline__ bf16x8 cat4(s16x4 a, s16x4 b) {
  return __builtin_shufflevector(a, b, 0, 1, 2, 3, 4, 5, 6, 7);
}
__device__ __forceinline__ s16x4 tr_read(const char* smem_ptr) {
  return __builtin_amdgcn_ds_read_tr16_b64_v4i16((lds_s16x4_ptr)(smem_ptr));
}
__device__ __forceinline__ unsigned lds_addr(const void* p) {
  return (unsigned)(size_t)(const __attribute__((address_space(3))) char*)p;
}
#define DS_TR(dst, addr, off) asm volatile("ds_read_b64_tr_b16 %0, %1 offset:%2" : "=v"(dst) : "v"(addr), "i"(off) : "memory")
#define DS_B128(dst, addr, off) asm volatile("ds_read_b128 %0, %1 offset:%2" : "=v"(dst) : "v"(addr), "i"(off) : "memory")
__device__ __forceinline__ void glds16(const void* g, char* l) {
  __builtin_amdgcn_global_load_lds((const unsigned*)g, (unsigned*)l, 16, 0, 0);
}

#define STAMP(i)
#define DBG_ARG
#define DBG_PASS
constexpr int U_COPY_B = 4160;
constexpr int U_CH_B = 4 * U_COPY_B;
constexpr int U_REGION_B = 2 * U_CH_B;
constexpr int KR_PART_B = 4576;
constexpr int KR_OFF = U_REGION_B;
constexpr int KP_B = 8208;
constexpr int SSM_LDS = KR_OFF + 4 * KR_PART_B;
constexpr int K1_LDS = 77824;
static_assert(4 * KP_B <= U_REGION_B, "Kp scratch must fit the U region");

__device__ __forceinline__ void ssm_unit(int w, const u16* __restrict__ u, const u16* __restrict__ logAr,
                                         const u16* __restrict__ Aim, const u16* __restrict__ Cre,
                                         const u16* __restrict__ Cim, const u16* __restrict__ logdt,
                                         const u16* __restrict__ Dskip, u16* __restrict__ aT, char* smem DBG_ARG) {
  const int tid = threadIdx.x, lane = tid & 63;
  STAMP(0);
  const int wave = __builtin_amdgcn_readfirstlane(tid >> 6);
  const int ch = wave >> 1, w2 = wave & 1;
  const int d0 = 64 * (w & 7) + 2 * (w >> 3);
  const int d = d0 + ch;

  const unsigned* u32p = (const unsigned*)(u + d0);
  unsigned uw[8], up[3];
#pragma unroll
  for (int i = 0; i < 8; ++i) uw[i] = u32p[(size_t)(8 * tid + i) * (DM / 2)];
#pragma unroll
  for (int i = 0; i < 3; ++i) up[i] = 0;
  if (lane == 0 && tid > 0) {
#pragma unroll
    for (int i = 0; i < 3; ++i) up[i] = u32p[(size_t)(8 * tid - 3 + i) * (DM / 2)];
  }

  {
    const int n = tid & 127;
    const int dd = d0 + (tid >> 7);
    const float dt = rbf(expf(bf2f(logdt[dd])));
    const float are = rbf(expf(bf2f(logAr[dd * NST + n])));
    f32x4 prm;
    prm[0] = (-are * dt) * 1.44269504088896340736f;
    prm[1] = (bf2f(Aim[dd * NST + n]) * dt) * 0.15915494309189533577f;
    prm[2] = 2.0f * (bf2f(Cre[dd * NST + n]) * dt);
    prm[3] = 2.0f * (bf2f(Cim[dd * NST + n]) * dt);
    *(f32x4*)(smem + KR_OFF + tid * 16) = prm;
  }
  __syncthreads();
  STAMP(1);

  {
    const int r = lane & 31, h = lane >> 5;
    int p1i;
    if (r < 8) p1i = r;
    else if (r < 12) p1i = 256 + 2 * (r - 8);
    else if (r < 16) p1i = 512 + 256 * ((r - 12) >> 1) + 4 * ((r - 12) & 1);
    else if (r < 21) p1i = 1024 + 256 * (r - 16);
    else p1i = 0;
    const float p1 = (float)p1i, p2 = (float)(8 * r);
    f32x16 acc = {};
    const char* prm_base = smem + KR_OFF + (ch * 128 + 64 * w2 + 4 * h) * 16;
#pragma unroll 2
    for (int ks = 0; ks < 8; ++ks) {
      unsigned ah[4], al[4], bh[4], bl[4];
#pragma unroll
      for (int q = 0; q < 4; ++q) {
        const f32x4 prm = *(const f32x4*)(prm_base + (8 * ks + q) * 16);
        {
          const float e = __builtin_amdgcn_exp2f(prm[0] * p1);
          const float rv = __builtin_amdgcn_fractf(prm[1] * p1);
          const float sn = __builtin_amdgcn_sinf(rv), cs = __builtin_amdgcn_cosf(rv);
          const float gre = e * (prm[2] * cs - prm[3] * sn);
          const float gim = -e * (prm[2] * sn + prm[3] * cs);
          const unsigned hi = pk_bf16(gre, gim);
          ah[q] = hi;
          al[q] = pk_bf16(gre - bf_lo(hi), gim - bf_hi(hi));
        }
        {
          const float e = __builtin_amdgcn_exp2f(prm[0] * p2);
          const float rv = __builtin_amdgcn_fractf(prm[1] * p2);
          const float sn = __builtin_amdgcn_sinf(rv), cs = __builtin_amdgcn_cosf(rv);
          const float ere = e * cs, eim = e * sn;
          const unsigned hi = pk_bf16(ere, eim);
          bh[q] = hi;
          bl[q] = pk_bf16(ere - bf_lo(hi), eim - bf_hi(hi));
        }
      }
      const bf16x8 Ah = mk8(ah[0], ah[1], ah[2], ah[3]), Al = mk8(al[0], al[1], al[2], al[3]);
      const bf16x8 Bh = mk8(bh[0], bh[1], bh[2], bh[3]), Bl = mk8(bl[0], bl[1], bl[2], bl[3]);
      acc = __builtin_amdgcn_mfma_f32_32x32x16_bf16(Al, Bh, acc, 0, 0, 0);
      acc = __builtin_amdgcn_mfma_f32_32x32x16_bf16(Ah, Bl, acc, 0, 0, 0);
      acc = __builtin_amdgcn_mfma_f32_32x32x16_bf16(Ah, Bh, acc, 0, 0, 0);
    }
    float* Kp = (float*)(smem + (ch * 2 + w2) * KP_B);
#pragma unroll
    for (int e = 0; e < 4; ++e) Kp[(4 * h + e) + 8 * r] = acc[e];
#pragma unroll
    for (int e = 0; e < 4; ++e) {
      const int pp = h ? (512 + 256 * (e >> 1) + 4 * (e & 1)) : (256 + 2 * e);
      Kp[pp + 8 * r] = acc[4 + e];
    }
    if (h == 0) {
#pragma unroll
      for (int e = 0; e < 4; ++e) Kp[1024 + 256 * e + 8 * r] = acc[8 + e];
    } else if (r == 0) {
      Kp[2048] = acc[8];
    }
  }
  STAMP(2);
  __syncthreads();

  {
    const int t = tid & 127;
    const float* Kp0 = (const float*)(smem + (ch * 2) * KP_B);
    const float* Kp1 = (const float*)(smem + (ch * 2 + 1) * KP_B);
    char* krh = smem + KR_OFF + (ch * 2) * KR_PART_B;
    char* krl = krh + KR_PART_B;
#pragma unroll
    for (int kk = 0; kk < 2; ++kk) {
      const int k = t + 128 * kk;
      float v[8];
#pragma unroll
      for (int e = 0; e < 8; ++e) {
        const int l = 2047 - 8 * k - e;
        const int p = (int)rbf((float)l);
        v[e] = Kp0[p] + Kp1[p];
      }
      unsigned hi[4], lo[4];
#pragma unroll
      for (int e = 0; e < 4; ++e) {
        hi[e] = pk_bf16(v[2 * e], v[2 * e + 1]);
        lo[e] = pk_bf16(v[2 * e] - bf_lo(hi[e]), v[2 * e + 1] - bf_hi(hi[e]));
      }
      *(u32x4*)(krh + k * 16) = u32x4{hi[0], hi[1], hi[2], hi[3]};
      *(u32x4*)(krl + k * 16) = u32x4{lo[0], lo[1], lo[2], lo[3]};
    }
    if (t < 30) {
      *(u32x4*)(krh + (256 + t) * 16) = u32x4{0, 0, 0, 0};
      *(u32x4*)(krl + (256 + t) * 16) = u32x4{0, 0, 0, 0};
    }
  }
  __syncthreads();
  STAMP(3);

  {
    unsigned e[11];
#pragma unroll
    for (int i = 0; i < 3; ++i) {
      const unsigned nb = __shfl_up(uw[5 + i], 1);
      e[i] = (lane == 0) ? up[i] : nb;
    }
#pragma unroll
    for (int i = 0; i < 8; ++i) e[3 + i] = uw[i];
    unsigned p0[10], p1[10];
#pragma unroll
    for (int i = 0; i < 10; ++i) {
      p0[i] = __builtin_amdgcn_perm(e[i + 1], e[i], 0x05040100u);
      p1[i] = __builtin_amdgcn_perm(e[i + 1], e[i], 0x07060302u);
    }
#pragma unroll
    for (int c = 0; c < 4; ++c) {
      *(u32x4*)(smem + c * U_COPY_B + (tid + 2) * 16) = u32x4{p0[c], p0[c + 2], p0[c + 4], p0[c + 6]};
      *(u32x4*)(smem + U_CH_B + c * U_COPY_B + (tid + 2) * 16) = u32x4{p1[c], p1[c + 2], p1[c + 4], p1[c + 6]};
    }
    if (tid < 16) *(u32x4*)(smem + (tid >> 1) * U_COPY_B + (tid & 1) * 16) = u32x4{0, 0, 0, 0};
  }
  __syncthreads();
  STAMP(4);

  f32x4 acc[8];
#pragma unroll
  for (int t = 0; t < 8; ++t) acc[t] = f32x4{0.f, 0.f, 0.f, 0.f};
  {
    const int r = lane & 15, hq = lane >> 4;
    const char* ua = smem + ch * U_CH_B + (r & 3) * U_COPY_B + 2 * (4 + (r & ~3) + 8 * hq);
    const char* kb = smem + KR_OFF + (ch * 2) * KR_PART_B + 2 * (2032 - 16 * r + 8 * hq);
#pragma unroll 1
    for (int ri = 0; ri < 4; ++ri) {
      const int rho = 4 * w2 + ri;
      bf16x8 A[8];
#pragma unroll
      for (int sg = 0; sg < 8; ++sg) {
        const char* pa = ua + 64 * (8 * sg + rho);
        A[sg] = cat4(*(const s16x4*)pa, *(const s16x4*)(pa + 8));
      }
#pragma unroll
      for (int tau = 0; tau < 8; ++tau) {
        const char* pb = kb - 64 * (8 * tau - rho);
        const bf16x8 Bh = *(const bf16x8*)pb;
        const bf16x8 Bl = *(const bf16x8*)(pb + KR_PART_B);
#pragma unroll
        for (int sg = 0; sg + tau < 8; ++sg) {
          acc[sg + tau] = __builtin_amdgcn_mfma_f32_16x16x32_bf16(A[sg], Bl, acc[sg + tau], 0, 0, 0);
          acc[sg + tau] = __builtin_amdgcn_mfma_f32_16x16x32_bf16(A[sg], Bh, acc[sg + tau], 0, 0, 0);
        }
      }
    }
  }
  STAMP(5);
  __syncthreads();
  {
    char* xw = smem + KR_OFF + (ch * 2 + w2) * 4096 + lane * 16;
    const char* xr = smem + KR_OFF + (ch * 2 + (w2 ^ 1)) * 4096 + lane * 16;
    f32x4 y[4];
    if (w2 == 0) {
#pragma unroll
      for (int i = 0; i < 4; ++i) { *(f32x4*)(xw + i * 1024) = acc[4 + i]; y[i] = acc[i]; }
    } else {
#pragma unroll
      for (int i = 0; i < 4; ++i) { *(f32x4*)(xw + i * 1024) = acc[i]; y[i] = acc[4 + i]; }
    }
    __syncthreads();
    const float Dk = bf2f(Dskip[d]);
#pragma unroll
    for (int i = 0; i < 4; ++i) {
      const f32x4 o = *(const f32x4*)(xr + i * 1024);
      const int l0 = 256 * (4 * w2 + i) + 16 * (lane & 15) + 4 * (lane >> 4);
      const u32x2 uu = *(const u32x2*)(smem + ch * U_CH_B + 3 * U_COPY_B + 2 * (16 + l0));
      const float a0 = rbf(y[i][0] + o[0]) + rbf(bf_lo(uu[0]) * Dk);
      const float a1 = rbf(y[i][1] + o[1]) + rbf(bf_hi(uu[0]) * Dk);
      const float a2 = rbf(y[i][2] + o[2]) + rbf(bf_lo(uu[1]) * Dk);
      const float a3 = rbf(y[i][3] + o[3]) + rbf(bf_hi(uu[1]) * Dk);
      *(u32x2*)(aT + (size_t)d * SEQ + l0) = u32x2{pk_bf16(a0, a1), pk_bf16(a2, a3)};
    }
  }
  STAMP(6);
}

__device__ __forceinline__ float gelu_tanh(float x) {
  const float z = 0.7978845608028654f * (x + 0.044715f * x * x * x);
  const float e = __builtin_amdgcn_exp2f(-2.8853900817779268f * z);
  return x * __builtin_amdgcn_rcpf(1.0f + e);
}

constexpr int G1_WSLOT_B = 12288;
constexpr int G1_NSLOT = 5;
constexpr int G1_X_OFF = G1_WSLOT_B * G1_NSLOT;

constexpr int g1_pos_w(int s) {
  int n = 0;
  if (s == 0) return 4 + 3;
  if (s == 1) return 4 + 3 + 4 + 3;
  if (s == 2) return 4 + 3 + 4 + 3 + 3;
  if (s == 3) return 4 + 3 + 4 + 3 + 3 + 4 + 3;
  n = 24;
  for (int t = 0; t < 16; ++t) {
    if (t + 4 < 16) { n += 3; if (t + 4 == s) return n; }
    if ((t & 1) == 0 && t / 2 + 3 < 8) n += 4;
  }
  return -1;
}
constexpr int g1_pos_x(int b) {
  if (b == 0) return 4;
  if (b == 1) return 4 + 3 + 4;
  if (b == 2) return 4 + 3 + 4 + 3 + 3 + 4;
  int n = 24;
  for (int t = 0; t < 16; ++t) {
    if (t + 4 < 16) n += 3;
    if ((t & 1) == 0 && t / 2 + 3 < 8) { n += 4; if (t / 2 + 3 == b) return n; }
  }
  return -1;
}
constexpr int g1_issued_before(int s) {
  int n = 24;
  for (int t = 0; t < s; ++t) {
    if (t + 4 < 16) n += 3;
    if ((t & 1) == 0 && t / 2 + 3 < 8) n += 4;
  }
  return n;
}
constexpr int g1_wait(int s) {
  int need = g1_pos_w(s);
  if ((s & 1) == 0 && g1_pos_x(s / 2) > need) need = g1_pos_x(s / 2);
  return g1_issued_before(s) - need;
}

#define GLOAD128(dst, ptr) asm volatile("global_load_dwordx4 %0, %1, off" : "=v"(dst) : "v"(ptr) : "memory")

template <int S>
struct G1Step {
  static __device__ __forceinline__ void run(f32x16 (&acc)[6], u32x4 (&xr)[4][4], bf16x8 (&xa)[4], const u16* (&wsrc)[3],
                                             const u16* xsrc, char* smem, unsigned sbase, unsigned woff, unsigned woff_o, unsigned xw,
                                             unsigned xrd, int wave, int lane) {
    constexpr int NW = g1_wait(S);
    if constexpr ((S & 1) == 0) {
      constexpr int B = (S / 2) & 3;
      asm volatile("s_waitcnt vmcnt(%4)\n\ts_barrier" : "+v"(xr[B][0]), "+v"(xr[B][1]), "+v"(xr[B][2]), "+v"(xr[B][3]) : "i"(NW) : "memory");
#pragma unroll
      for (int i = 0; i < 4; ++i)
        asm volatile("ds_write_b128 %0, %1 offset:%2" ::"v"(xw), "v"(xr[B][i]), "i"(i * 1024) : "memory");
    } else {
      asm volatile("s_waitcnt vmcnt(%0)\n\ts_barrier" ::"i"(NW) : "memory");
    }
    const unsigned sbe = sbase + (S % G1_NSLOT) * G1_WSLOT_B + woff;
    const unsigned sbo = sbase + (S % G1_NSLOT) * G1_WSLOT_B + woff_o;
    s16x4 w[2][6][2];
#pragma unroll
    for (int c = 0; c < 6; c += 2) {
      DS_TR(w[0][c][0], sbe, c * 64); DS_TR(w[0][c][1], sbe, c * 64 + 4 * 384);
      DS_TR(w[0][c + 1][0], sbo, c * 64 + 64); DS_TR(w[0][c + 1][1], sbo, c * 64 + 64 + 4 * 384);
    }
    if constexpr ((S & 1) == 0) {
      asm volatile("s_waitcnt lgkmcnt(0)" ::: "memory");
#pragma unroll
      for (int i = 0; i < 4; ++i) DS_B128(xa[i], xrd ^ (i * 32), 0);
    }
#pragma unroll
    for (int c = 0; c < 6; c += 2) {
      DS_TR(w[1][c][0], sbe, c * 64 + 16 * 384); DS_TR(w[1][c][1], sbe, c * 64 + 20 * 384);
      DS_TR(w[1][c + 1][0], sbo, c * 64 + 64 + 16 * 384); DS_TR(w[1][c + 1][1], sbo, c * 64 + 64 + 20 * 384);
    }
    if constexpr (S + 4 < 16) {
      char* dst = smem + ((S + 4) % G1_NSLOT) * G1_WSLOT_B + wave * 3072 + lane * 16;
#pragma unroll
      for (int i = 0; i < 3; ++i) glds16(wsrc[i] + (size_t)(S + 4) * 32 * FF, dst + i * 1024);
    }
    if constexpr ((S & 1) == 0 && S / 2 + 3 < 8) {
      constexpr int B = (S / 2 + 3) & 3;
#pragma unroll
      for (int i = 0; i < 4; ++i) GLOAD128(xr[B][i], xsrc + (size_t)(8 * i) * DM + (S / 2 + 3) * 64);
    }
    asm volatile("s_waitcnt lgkmcnt(12)"
                 : "+v"(w[0][0][0]), "+v"(w[0][0][1]), "+v"(w[0][1][0]), "+v"(w[0][1][1]), "+v"(w[0][2][0]), "+v"(w[0][2][1]),
                   "+v"(w[0][3][0]), "+v"(w[0][3][1]), "+v"(w[0][4][0]), "+v"(w[0][4][1]), "+v"(w[0][5][0]), "+v"(w[0][5][1]),
                   "+v"(xa[0]), "+v"(xa[1]), "+v"(xa[2]), "+v"(xa[3])::"memory");
#pragma unroll
    for (int c = 0; c < 6; ++c)
      acc[c] = __builtin_amdgcn_mfma_f32_32x32x16_bf16(cat4(w[0][c][0], w[0][c][1]), xa[2 * (S & 1)], acc[c], 0, 0, 0);
    asm volatile("s_waitcnt lgkmcnt(0)"
                 : "+v"(w[1][0][0]), "+v"(w[1][0][1]), "+v"(w[1][1][0]), "+v"(w[1][1][1]), "+v"(w[1][2][0]), "+v"(w[1][2][1]),
                   "+v"(w[1][3][0]), "+v"(w[1][3][1]), "+v"(w[1][4][0]), "+v"(w[1][4][1]), "+v"(w[1][5][0]), "+v"(w[1][5][1])::"memory");
#pragma unroll
    for (int c = 0; c < 6; ++c)
      acc[c] = __builtin_amdgcn_mfma_f32_32x32x16_bf16(cat4(w[1][c][0], w[1][c][1]), xa[2 * (S & 1) + 1], acc[c], 0, 0, 0);
    if constexpr (S + 1 < 16) G1Step<S + 1>::run(acc, xr, xa, wsrc, xsrc, smem, sbase, woff, woff_o, xw, xrd, wave, lane);
  }
};

__device__ __forceinline__ void gemm1_tile(int t, const u16* __restrict__ x, const u16* __restrict__ wg,
                                           const u16* __restrict__ wu, u16* __restrict__ hf, char* smem) {
  const int tid = threadIdx.x, lane = tid & 63;
  const int wave = __builtin_amdgcn_readfirstlane(tid >> 6);
  const int fb = t >> 4, mb = t & 15;
  const int m0 = 128 * mb + 32 * wave, f0 = 96 * fb;

  const u16* wsrc[3];
#pragma unroll
  for (int i = 0; i < 3; ++i) {
    const int c = 192 * wave + 64 * i + lane;
    const int row = c / 24, pos = c % 24;
    const int cc = pos ^ (((row >> 1) & 1) << 2);
    int col = f0 + 8 * (cc >= 12 ? cc - 12 : cc);
    if (col > FF - 8) col = FF - 8;
    wsrc[i] = (cc >= 12 ? wu : wg) + (size_t)row * FF + col;
  }
  const u16* xsrc = x + (size_t)(m0 + (lane >> 3)) * DM + (lane & 7) * 8;
  const unsigned sbase = lds_addr(smem);
  const unsigned xbase = sbase + G1_X_OFF + wave * 4096;
  const unsigned xw = xbase + (lane >> 3) * 128 + (((lane & 7) ^ (lane >> 3)) * 16);
  const int r = lane & 31, h = lane >> 5;
  const unsigned xrd = xbase + r * 128 + ((h ^ (r & 7)) * 16);
  const int blk = (lane >> 4) & 1, q = (lane & 15) >> 2, p = lane & 3;
  const int sw = (q >> 1) & 1;
  const unsigned woff = (8 * h + q) * 384 + (2 * blk + (p >> 1) + 4 * sw) * 16 + 8 * (p & 1);
  const unsigned woff_o = (8 * h + q) * 384 + (2 * blk + (p >> 1) - 4 * sw) * 16 + 8 * (p & 1);

  f32x16 acc[6];
#pragma unroll
  for (int i = 0; i < 6; ++i) acc[i] = f32x16{};
  u32x4 xr[4][4];
  bf16x8 xa[4];

  auto issue_w = [&](int s) {
    char* dst = smem + (s % G1_NSLOT) * G1_WSLOT_B + wave * 3072 + lane * 16;
#pragma unroll
    for (int i = 0; i < 3; ++i) glds16(wsrc[i] + (size_t)s * 32 * FF, dst + i * 1024);
  };
#define G1_ISSUE_X(B) _Pragma("unroll") for (int i = 0; i < 4; ++i) GLOAD128(xr[B][i], xsrc + (size_t)(8 * i) * DM + (B) * 64)
  G1_ISSUE_X(0); issue_w(0); G1_ISSUE_X(1); issue_w(1); issue_w(2); G1_ISSUE_X(2); issue_w(3);
  G1Step<0>::run(acc, xr, xa, wsrc, xsrc, smem, sbase, woff, woff_o, xw, xrd, wave, lane);

  const int mt = m0 >> 5;
#pragma unroll
  for (int c = 0; c < 3; ++c) {
    const int ft = f0 / 32 + c;
    if (ft < NKS / 2) {
      const bool valid = ft < FF / 32;
#pragma unroll
      for (int i = 0; i < 2; ++i) {
        unsigned o[4];
#pragma unroll
        for (int e = 0; e < 4; ++e) {
          float hv[2];
#pragma unroll
          for (int z = 0; z < 2; ++z) {
            const float gv = rbf(acc[c][8 * i + 2 * e + z]);
            const float uv = rbf(acc[3 + c][8 * i + 2 * e + z]);
            hv[z] = valid ? rbf(gelu_tanh(gv)) * uv : 0.f;
          }
          o[e] = pk_bf16(hv[0], hv[1]);
        }
        *(u32x4*)(hf + ((size_t)(mt * NKS + 2 * ft + i) * 64 + lane) * 8) = u32x4{o[0], o[1], o[2], o[3]};
      }
    }
  }
}

__device__ __forceinline__ void gate_unit(int b, const u16* __restrict__ x, const u16* __restrict__ gw,
                                          float* __restrict__ gout) {
  const int tid = threadIdx.x, lane = tid & 63, wave = tid >> 6;
  const u32x4 gv = *(const u32x4*)(gw + lane * 8);
#pragma unroll 1
  for (int half = 0; half < 2; ++half) {
    const int row0 = b * 128 + wave * 32 + half * 16;
    u32x4 xv[16];
#pragma unroll
    for (int i = 0; i < 16; ++i) xv[i] = *(const u32x4*)(x + (size_t)(row0 + i) * DM + lane * 8);
#pragma unroll
    for (int i = 0; i < 16; ++i) {
      float s = 0.f;
#pragma unroll
      for (int j = 0; j < 4; ++j) {
        const unsigned pr = pk_bf16(bf_lo(xv[i][j]) * bf_lo(gv[j]), bf_hi(xv[i][j]) * bf_hi(gv[j]));
        s += bf_lo(pr) + bf_hi(pr);
      }
#pragma unroll
      for (int o = 32; o >= 1; o >>= 1) s += __shfl_xor(s, o);
      if (lane == 0) {
        const float sb = rbf(s);
        gout[row0 + i] = rbf(1.0f / (1.0f + expf(-sb)));
      }
    }
  }
}

__global__ __launch_bounds__(256, 2) void k1_kernel(const u16* __restrict__ u, const u16* __restrict__ logAr,
                                                   const u16* __restrict__ Aim, const u16* __restrict__ Cre,
                                                   const u16* __restrict__ Cim, const u16* __restrict__ logdt,
                                                   const u16* __restrict__ Dskip, const u16* __restrict__ wg,
                                                   const u16* __restrict__ wu, const u16* __restrict__ gw,
                                                   u16* __restrict__ hbuf, u16* __restrict__ aT,
                                                   float* __restrict__ gout DBG_ARG) {
  __shared__ __attribute__((aligned(4096))) char smem[K1_LDS];
  const int bid = blockIdx.x;
  if (bid < NSSM) {
    ssm_unit(bid, u, logAr, Aim, Cre, Cim, logdt, Dskip, aT, smem DBG_PASS);
  } else {
    const int xcd = bid & 7, idx = (bid - NSSM) >> 3;
    if (idx < 30) gemm1_tile(30 * xcd + idx, u, wg, wu, hbuf, smem);
    else gate_unit(2 * xcd + idx - 30, u, gw, gout);
  }
}

constexpr int G2_SLOT_B = 4096;
constexpr int G2_NSLOT = 8;
constexpr int G2_WAVE_B = G2_SLOT_B * G2_NSLOT;
constexpr int G2_A_OFF = 4 * G2_WAVE_B;
constexpr int K2_LDS = G2_A_OFF + 8192;
constexpr int G2_PITCH = 272;

template <int ST>
struct G2Step {
  static __device__ __forceinline__ void issue(bf16x8 (&hfr)[11][4], const u16* hsrc, const u16* wd, int wrow0, int wcol,
                                               char* wbase, int lane) {
    char* sb = wbase + (ST & (G2_NSLOT - 1)) * G2_SLOT_B + lane * 16;
#pragma unroll
    for (int i = 0; i < 4; ++i) {
      int kr = wrow0 + ST * 32 + 8 * i;
      kr = kr < FF ? kr : FF - 1;
      glds16(wd + (size_t)kr * DM + wcol, sb + i * 1024);
    }
#pragma unroll
    for (int mi = 0; mi < 2; ++mi)
#pragma unroll
      for (int i = 0; i < 2; ++i) GLOAD128(hfr[ST][2 * mi + i], hsrc + (size_t)(mi * NKS + 2 * ST + i) * 512);
  }
  static __device__ __forceinline__ void run(f32x16 (&acc)[2][2], bf16x8 (&hfr)[11][4], const u16* hsrc, const u16* wd,
                                             int wrow0, int wcol, char* wbase, unsigned wb32, unsigned woff, int lane) {
    constexpr int LAST = (ST + 6 < 10) ? ST + 6 : 10;
    constexpr int NW = 8 * (LAST - ST);
    asm volatile("s_waitcnt vmcnt(%4)" : "+v"(hfr[ST][0]), "+v"(hfr[ST][1]), "+v"(hfr[ST][2]), "+v"(hfr[ST][3]) : "i"(NW) : "memory");
    const unsigned sb = wb32 + (ST & (G2_NSLOT - 1)) * G2_SLOT_B + woff;
    s16x4 w[2][2][2];
#pragma unroll
    for (int i = 0; i < 2; ++i)
#pragma unroll
      for (int c = 0; c < 2; ++c) {
        DS_TR(w[i][c][0], sb ^ (c * 64), i * 2048);
        DS_TR(w[i][c][1], sb ^ (c * 64), i * 2048 + 1024);
      }
    if constexpr (ST + 7 <= 10) G2Step<ST + 7>::issue(hfr, hsrc, wd, wrow0, wcol, wbase, lane);
    asm volatile("s_waitcnt lgkmcnt(0)"
                 : "+v"(w[0][0][0]), "+v"(w[0][0][1]), "+v"(w[0][1][0]), "+v"(w[0][1][1]), "+v"(w[1][0][0]), "+v"(w[1][0][1]),
                   "+v"(w[1][1][0]), "+v"(w[1][1][1])::"memory");
#pragma unroll
    for (int i = 0; i < 2; ++i)
#pragma unroll
      for (int c = 0; c < 2; ++c)
#pragma unroll
        for (int mi = 0; mi < 2; ++mi)
          acc[c][mi] = __builtin_amdgcn_mfma_f32_32x32x16_bf16(cat4(w[i][c][0], w[i][c][1]), hfr[ST][2 * mi + i], acc[c][mi], 0, 0, 0);
    if constexpr (ST + 1 <= 10) G2Step<ST + 1>::run(acc, hfr, hsrc, wd, wrow0, wcol, wbase, wb32, woff, lane);
  }
};

__global__ __launch_bounds__(256, 1) void k2_kernel(const u16* __restrict__ hf, const u16* __restrict__ wd,
                                                   const u16* __restrict__ aT, const float* __restrict__ gin,
                                                   u16* __restrict__ out) {
  __shared__ __attribute__((aligned(4096))) char smem[K2_LDS];
  const int tid = threadIdx.x, lane = tid & 63;
  const int wave = __builtin_amdgcn_readfirstlane(tid >> 6);
  const int bid = blockIdx.x;
  const int xcd = bid & 7, idx = bid >> 3;
  const int m0 = 64 * (4 * xcd + (idx >> 3)), n0 = 64 * (idx & 7);

#pragma unroll
  for (int i = 0; i < 2; ++i) {
    const int piece = 2 * wave + i;
    const int drow = 8 * piece + (lane >> 3);
    glds16(aT + (size_t)(n0 + drow) * SEQ + m0 + (lane & 7) * 8, smem + G2_A_OFF + piece * 1024 + lane * 16);
  }

  char* wbase = smem + wave * G2_WAVE_B;
  const u16* hsrc = hf + ((size_t)((m0 >> 5) * NKS + 22 * wave) * 64 + lane) * 8;
  const int wcol = n0 + 8 * ((lane & 7) ^ (((lane >> 4) & 1) << 2));
  const int wrow0 = 352 * wave + (lane >> 3);

  const int r = lane & 31, h = lane >> 5;
  const int blk = (lane >> 4) & 1, q = (lane & 15) >> 2, p = lane & 3;
  const unsigned woff = (4 * h + q) * 128 + (((2 * blk + (p >> 1)) ^ (((q >> 1) & 1) << 2)) * 16) + 8 * (p & 1);

  f32x16 acc[2][2];
#pragma unroll
  for (int i = 0; i < 2; ++i)
#pragma unroll
    for (int j = 0; j < 2; ++j) acc[i][j] = f32x16{};
  bf16x8 hfr[11][4];

  G2Step<0>::issue(hfr, hsrc, wd, wrow0, wcol, wbase, lane);
  G2Step<1>::issue(hfr, hsrc, wd, wrow0, wcol, wbase, lane);
  G2Step<2>::issue(hfr, hsrc, wd, wrow0, wcol, wbase, lane);
  G2Step<3>::issue(hfr, hsrc, wd, wrow0, wcol, wbase, lane);
  G2Step<4>::issue(hfr, hsrc, wd, wrow0, wcol, wbase, lane);
  G2Step<5>::issue(hfr, hsrc, wd, wrow0, wcol, wbase, lane);
  G2Step<6>::issue(hfr, hsrc, wd, wrow0, wcol, wbase, lane);
  G2Step<0>::run(acc, hfr, hsrc, wd, wrow0, wcol, wbase, lds_addr(wbase), woff, lane);
  __syncthreads();
  {
    char* pw = smem + wave * (64 * G2_PITCH);
#pragma unroll
    for (int c = 0; c < 2; ++c)
#pragma unroll
      for (int mi = 0; mi < 2; ++mi)
#pragma unroll
        for (int gq = 0; gq < 4; ++gq) {
          const f32x4 v = {acc[c][mi][4 * gq], acc[c][mi][4 * gq + 1], acc[c][mi][4 * gq + 2], acc[c][mi][4 * gq + 3]};
          *(f32x4*)(pw + (32 * mi + r) * G2_PITCH + (32 * c + 8 * gq + 4 * h) * 4) = v;
        }
  }
  __syncthreads();
  {
    const int m = tid >> 2, nq = (tid & 3) * 16;
    const float g = gin[m0 + m];
    const float omg = rbf(1.0f - g);
    float b[16];
#pragma unroll
    for (int j = 0; j < 4; ++j) {
      f32x4 s = *(const f32x4*)(smem + m * G2_PITCH + (nq + 4 * j) * 4);
#pragma unroll
      for (int w = 1; w < 4; ++w) s += *(const f32x4*)(smem + w * (64 * G2_PITCH) + m * G2_PITCH + (nq + 4 * j) * 4);
#pragma unroll
      for (int e = 0; e < 4; ++e) b[4 * j + e] = s[e];
    }
    const u16* aimg = (const u16*)(smem + G2_A_OFF);
    unsigned o[8];
#pragma unroll
    for (int j = 0; j < 8; ++j) {
      const float a0 = bf2f(aimg[(nq + 2 * j) * 64 + m]);
      const float a1 = bf2f(aimg[(nq + 2 * j + 1) * 64 + m]);
      const float o0 = rbf(g * a0) + rbf(omg * rbf(b[2 * j]));
      const float o1 = rbf(g * a1) + rbf(omg * rbf(b[2 * j + 1]));
      o[j] = pk_bf16(o0, o1);
    }
    u16* op = out + (size_t)(m0 + m) * DM + n0 + nq;
    *(u32x4*)op = u32x4{o[0], o[1], o[2], o[3]};
    *(u32x4*)(op + 8) = u32x4{o[4], o[5], o[6], o[7]};
  }
}

extern "C" void kernel_launch(void* const* d_in, const int* in_sizes, int n_in, void* d_out, int out_size,
                              void* d_ws, size_t ws_size, hipStream_t stream) {
  const u16* u = (const u16*)d_in[0];
  const u16* logAr = (const u16*)d_in[1];
  const u16* Aim = (const u16*)d_in[2];
  const u16* Cre = (const u16*)d_in[3];
  const u16* Cim = (const u16*)d_in[4];
  const u16* logdt = (const u16*)d_in[5];
  const u16* Dskip = (const u16*)d_in[6];
  const u16* wg = (const u16*)d_in[8];
  const u16* wu = (const u16*)d_in[9];
  const u16* wd = (const u16*)d_in[10];
  const u16* gw = (const u16*)d_in[11];
  char* ws = (char*)d_ws;
  u16* hbuf = (u16*)ws;
  u16* aT = (u16*)(ws + (size_t)SEQ * FFP * 2);
  float* gbuf = (float*)(ws + (size_t)SEQ * FFP * 2 + (size_t)DM * SEQ * 2);
  k1_kernel<<<512, 256, 0, stream>>>(u, logAr, Aim, Cre, Cim, logdt, Dskip, wg, wu, gw, hbuf, aT, gbuf
      );
  k2_kernel<<<256, 256, 0, stream>>>(hbuf, wd, aT, gbuf, (u16*)d_out);
}
```

```cpp
#include <hip/hip_runtime.h>
#include <hip/hip_bf16.h>
#include <stdint.h>

typedef unsigned short u16;
typedef __attribute__((ext_vector_type(8))) short bf16x8;
typedef __attribute__((ext_vector_type(4))) short s16x4;
typedef __attribute__((ext_vector_type(4))) float f32x4;
typedef __attribute__((ext_vector_type(16))) float f32x16;
typedef __attribute__((ext_vector_type(4))) unsigned u32x4;
typedef __attribute__((ext_vector_type(2))) unsigned u32x2;
typedef __attribute__((ext_vector_type(2))) __bf16 bf16x2_t;
typedef __attribute__((ext_vector_type(2))) float f32x2;
typedef __attribute__((address_space(3))) s16x4* lds_s16x4_ptr;

constexpr int SEQ = 2048, DM = 512, NST = 128, FF = 1376, FFP = 1408;
constexpr int NSSM = 256;
constexpr int NKS = 88;

__device__ __forceinline__ unsigned pk_bf16(float lo, float hi) {
  f32x2 v = {lo, hi};
  bf16x2_t b = __builtin_convertvector(v, bf16x2_t);
  return __builtin_bit_cast(unsigned, b);
}
__device__ __forceinline__ float bf_lo(unsigned w) { return __uint_as_float(w << 16); }
__device__ __forceinline__ float bf_hi(unsigned w) { return __uint_as_float(w & 0xffff0000u); }
__device__ __forceinline__ float rbf(float x) { return bf_lo(pk_bf16(x, 0.f)); }
__device__ __forceinline__ float bf2f(u16 v) { return __uint_as_float(((unsigned)v) << 16); }

__device__ __forceinline__ bf16x8 mk8(unsigned a, unsigned b, unsigned c, unsigned d) {
  u32x4 v = {a, b, c, d};
  return __builtin_bit_cast(bf16x8, v);
}
__device__ __forceinline__ bf16x8 cat4(s16x4 a, s16x4 b) {
  return __builtin_shufflevector(a, b, 0, 1, 2, 3, 4, 5, 6, 7);
}
__device__ __forceinline__ s16x4 tr_read(const char* smem_ptr) {
  return __builtin_amdgcn_ds_read_tr16_b64_v4i16((lds_s16x4_ptr)(smem_ptr));
}
__device__ __forceinline__ unsigned lds_addr(const void* p) {
  return (unsigned)(size_t)(const __attribute__((address_space(3))) char*)p;
}
#define DS_TR(dst, addr, off) asm volatile("ds_read_b64_tr_b16 %0, %1 offset:%2" : "=v"(dst) : "v"(addr), "i"(off) : "memory")
#define DS_B128(dst, addr, off) asm volatile("ds_read_b128 %0, %1 offset:%2" : "=v"(dst) : "v"(addr), "i"(off) : "memory")
__device__ __forceinline__ void glds16(const void* g, char* l) {
  __builtin_amdgcn_global_load_lds((const unsigned*)g, (unsigned*)l, 16, 0, 0);
}

#define STAMP2(i)
#define DBG2_ARG
#define DBG2_PASS
#define STAMP(i)
#define DBG_ARG
#define DBG_PASS
constexpr int U_COPY_B = 4160;
constexpr int U_CH_B = 4 * U_COPY_B;
constexpr int U_REGION_B = 2 * U_CH_B;
constexpr int KR_PART_B = 4576;
constexpr int KR_OFF = U_REGION_B;
constexpr int KP_B = 8208;
constexpr int SSM_LDS = KR_OFF + 4 * KR_PART_B;
constexpr int K1_LDS = SSM_LDS;
static_assert(4 * KP_B <= U_REGION_B, "Kp scratch must fit the U region");

__device__ __forceinline__ void ssm_unit(int w, const u16* __restrict__ u, const u16* __restrict__ logAr,
                                         const u16* __restrict__ Aim, const u16* __restrict__ Cre,
                                         const u16* __restrict__ Cim, const u16* __restrict__ logdt,
                                         const u16* __restrict__ Dskip, u16* __restrict__ aT, char* smem DBG_ARG) {
  const int tid = threadIdx.x, lane = tid & 63;
  STAMP(0);
  const int wave = __builtin_amdgcn_readfirstlane(tid >> 6);
  const int ch = wave >> 1, w2 = wave & 1;
  const int d0 = 64 * (w & 7) + 2 * (w >> 3);
  const int d = d0 + ch;

  const unsigned* u32p = (const unsigned*)(u + d0);
  unsigned uw[8], up[3];
#pragma unroll
  for (int i = 0; i < 8; ++i) uw[i] = u32p[(size_t)(8 * tid + i) * (DM / 2)];
#pragma unroll
  for (int i = 0; i < 3; ++i) up[i] = 0;
  if (lane == 0 && tid > 0) {
#pragma unroll
    for (int i = 0; i < 3; ++i) up[i] = u32p[(size_t)(8 * tid - 3 + i) * (DM / 2)];
  }

  {
    const int n = tid & 127;
    const int dd = d0 + (tid >> 7);
    const float dt = rbf(expf(bf2f(logdt[dd])));
    const float are = rbf(expf(bf2f(logAr[dd * NST + n])));
    f32x4 prm;
    prm[0] = (-are * dt) * 1.44269504088896340736f;
    prm[1] = (bf2f(Aim[dd * NST + n]) * dt) * 0.15915494309189533577f;
    prm[2] = 2.0f * (bf2f(Cre[dd * NST + n]) * dt);
    prm[3] = 2.0f * (bf2f(Cim[dd * NST + n]) * dt);
    *(f32x4*)(smem + KR_OFF + tid * 16) = prm;
  }
  __syncthreads();
  STAMP(1);

  {
    const int r = lane & 31, h = lane >> 5;
    int p1i;
    if (r < 8) p1i = r;
    else if (r < 12) p1i = 256 + 2 * (r - 8);
    else if (r < 16) p1i = 512 + 256 * ((r - 12) >> 1) + 4 * ((r - 12) & 1);
    else if (r < 21) p1i = 1024 + 256 * (r - 16);
    else p1i = 0;
    const float p1 = (float)p1i, p2 = (float)(8 * r);
    f32x16 acc = {};
    const char* prm_base = smem + KR_OFF + (ch * 128 + 64 * w2 + 4 * h) * 16;
#pragma unroll 2
    for (int ks = 0; ks < 8; ++ks) {
      unsigned ah[4], al[4], bh[4], bl[4];
#pragma unroll
      for (int q = 0; q < 4; ++q) {
        const f32x4 prm = *(const f32x4*)(prm_base + (8 * ks + q) * 16);
        {
          const float e = __builtin_amdgcn_exp2f(prm[0] * p1);
          const float rv = __builtin_amdgcn_fractf(prm[1] * p1);
          const float sn = __builtin_amdgcn_sinf(rv), cs = __builtin_amdgcn_cosf(rv);
          const float gre = e * (prm[2] * cs - prm[3] * sn);
          const float gim = -e * (prm[2] * sn + prm[3] * cs);
          const unsigned hi = pk_bf16(gre, gim);
          ah[q] = hi;
          al[q] = pk_bf16(gre - bf_lo(hi), gim - bf_hi(hi));
        }
        {
          const float e = __builtin_amdgcn_exp2f(prm[0] * p2);
          const float rv = __builtin_amdgcn_fractf(prm[1] * p2);
          const float sn = __builtin_amdgcn_sinf(rv), cs = __builtin_amdgcn_cosf(rv);
          const float ere = e * cs, eim = e * sn;
          const unsigned hi = pk_bf16(ere, eim);
          bh[q] = hi;
          bl[q] = pk_bf16(ere - bf_lo(hi), eim - bf_hi(hi));
        }
      }
      const bf16x8 Ah = mk8(ah[0], ah[1], ah[2], ah[3]), Al = mk8(al[0], al[1], al[2], al[3]);
      const bf16x8 Bh = mk8(bh[0], bh[1], bh[2], bh[3]), Bl = mk8(bl[0], bl[1], bl[2], bl[3]);
      acc = __builtin_amdgcn_mfma_f32_32x32x16_bf16(Al, Bh, acc, 0, 0, 0);
      acc = __builtin_amdgcn_mfma_f32_32x32x16_bf16(Ah, Bl, acc, 0, 0, 0);
      acc = __builtin_amdgcn_mfma_f32_32x32x16_bf16(Ah, Bh, acc, 0, 0, 0);
    }
    float* Kp = (float*)(smem + (ch * 2 + w2) * KP_B);
#pragma unroll
    for (int e = 0; e < 4; ++e) Kp[(4 * h + e) + 8 * r] = acc[e];
#pragma unroll
    for (int e = 0; e < 4; ++e) {
      const int pp = h ? (512 + 256 * (e >> 1) + 4 * (e & 1)) : (256 + 2 * e);
      Kp[pp + 8 * r] = acc[4 + e];
    }
    if (h == 0) {
#pragma unroll
      for (int e = 0; e < 4; ++e) Kp[1024 + 256 * e + 8 * r] = acc[8 + e];
    } else if (r == 0) {
      Kp[2048] = acc[8];
    }
  }
  STAMP(2);
  __syncthreads();

  {
    const int t = tid & 127;
    const float* Kp0 = (const float*)(smem + (ch * 2) * KP_B);
    const float* Kp1 = (const float*)(smem + (ch * 2 + 1) * KP_B);
    char* krh = smem + KR_OFF + (ch * 2) * KR_PART_B;
    char* krl = krh + KR_PART_B;
#pragma unroll
    for (int kk = 0; kk < 2; ++kk) {
      const int k = t + 128 * kk;
      float v[8];
#pragma unroll
      for (int e = 0; e < 8; ++e) {
        const int l = 2047 - 8 * k - e;
        const int p = (int)rbf((float)l);
        v[e] = Kp0[p] + Kp1[p];
      }
      unsigned hi[4], lo[4];
#pragma unroll
      for (int e = 0; e < 4; ++e) {
        hi[e] = pk_bf16(v[2 * e], v[2 * e + 1]);
        lo[e] = pk_bf16(v[2 * e] - bf_lo(hi[e]), v[2 * e + 1] - bf_hi(hi[e]));
      }
      *(u32x4*)(krh + k * 16) = u32x4{hi[0], hi[1], hi[2], hi[3]};
      *(u32x4*)(krl + k * 16) = u32x4{lo[0], lo[1], lo[2], lo[3]};
    }
    if (t < 30) {
      *(u32x4*)(krh + (256 + t) * 16) = u32x4{0, 0, 0, 0};
      *(u32x4*)(krl + (256 + t) * 16) = u32x4{0, 0, 0, 0};
    }
  }
  __syncthreads();
  STAMP(3);

  {
    unsigned e[11];
#pragma unroll
    for (int i = 0; i < 3; ++i) {
      const unsigned nb = __shfl_up(uw[5 + i], 1);
      e[i] = (lane == 0) ? up[i] : nb;
    }
#pragma unroll
    for (int i = 0; i < 8; ++i) e[3 + i] = uw[i];
    unsigned p0[10], p1[10];
#pragma unroll
    for (int i = 0; i < 10; ++i) {
      p0[i] = __builtin_amdgcn_perm(e[i + 1], e[i], 0x05040100u);
      p1[i] = __builtin_amdgcn_perm(e[i + 1], e[i], 0x07060302u);
    }
#pragma unroll
    for (int c = 0; c < 4; ++c) {
      *(u32x4*)(smem + c * U_COPY_B + (tid + 2) * 16) = u32x4{p0[c], p0[c + 2], p0[c + 4], p0[c + 6]};
      *(u32x4*)(smem + U_CH_B + c * U_COPY_B + (tid + 2) * 16) = u32x4{p1[c], p1[c + 2], p1[c + 4], p1[c + 6]};
    }
    if (tid < 16) *(u32x4*)(smem + (tid >> 1) * U_COPY_B + (tid & 1) * 16) = u32x4{0, 0, 0, 0};
  }
  __syncthreads();
  STAMP(4);

  f32x4 acc[8];
#pragma unroll
  for (int t = 0; t < 8; ++t) acc[t] = f32x4{0.f, 0.f, 0.f, 0.f};
  {
    const int r = lane & 15, hq = lane >> 4;
    const char* ua = smem + ch * U_CH_B + (r & 3) * U_COPY_B + 2 * (4 + (r & ~3) + 8 * hq);
    const char* kb = smem + KR_OFF + (ch * 2) * KR_PART_B + 2 * (2032 - 16 * r + 8 * hq);
#pragma unroll 1
    for (int ri = 0; ri < 4; ++ri) {
      const int rho = 4 * w2 + ri;
      bf16x8 A[8];
#pragma unroll
      for (int sg = 0; sg < 8; ++sg) {
        const char* pa = ua + 64 * (8 * sg + rho);
        A[sg] = cat4(*(const s16x4*)pa, *(const s16x4*)(pa + 8));
      }
#pragma unroll
      for (int tau = 0; tau < 8; ++tau) {
        const char* pb = kb - 64 * (8 * tau - rho);
        const bf16x8 Bh = *(const bf16x8*)pb;
        const bf16x8 Bl = *(const bf16x8*)(pb + KR_PART_B);
#pragma unroll
        for (int sg = 0; sg + tau < 8; ++sg) {
          acc[sg + tau] = __builtin_amdgcn_mfma_f32_16x16x32_bf16(A[sg], Bl, acc[sg + tau], 0, 0, 0);
          acc[sg + tau] = __builtin_amdgcn_mfma_f32_16x16x32_bf16(A[sg], Bh, acc[sg + tau], 0, 0, 0);
        }
      }
    }
  }
  STAMP(5);
  __syncthreads();
  {
    char* xw = smem + KR_OFF + (ch * 2 + w2) * 4096 + lane * 16;
    const char* xr = smem + KR_OFF + (ch * 2 + (w2 ^ 1)) * 4096 + lane * 16;
    f32x4 y[4];
    if (w2 == 0) {
#pragma unroll
      for (int i = 0; i < 4; ++i) { *(f32x4*)(xw + i * 1024) = acc[4 + i]; y[i] = acc[i]; }
    } else {
#pragma unroll
      for (int i = 0; i < 4; ++i) { *(f32x4*)(xw + i * 1024) = acc[i]; y[i] = acc[4 + i]; }
    }
    __syncthreads();
    const float Dk = bf2f(Dskip[d]);
#pragma unroll
    for (int i = 0; i < 4; ++i) {
      const f32x4 o = *(const f32x4*)(xr + i * 1024);
      const int l0 = 256 * (4 * w2 + i) + 16 * (lane & 15) + 4 * (lane >> 4);
      const u32x2 uu = *(const u32x2*)(smem + ch * U_CH_B + 3 * U_COPY_B + 2 * (16 + l0));
      const float a0 = rbf(y[i][0] + o[0]) + rbf(bf_lo(uu[0]) * Dk);
      const float a1 = rbf(y[i][1] + o[1]) + rbf(bf_hi(uu[0]) * Dk);
      const float a2 = rbf(y[i][2] + o[2]) + rbf(bf_lo(uu[1]) * Dk);
      const float a3 = rbf(y[i][3] + o[3]) + rbf(bf_hi(uu[1]) * Dk);
      *(u32x2*)(aT + (size_t)d * SEQ + l0) = u32x2{pk_bf16(a0, a1), pk_bf16(a2, a3)};
    }
  }
  STAMP(6);
}

__device__ __forceinline__ float gelu_tanh(float x) {
  const float z = 0.7978845608028654f * (x + 0.044715f * x * x * x);
  const float e = __builtin_amdgcn_exp2f(-2.8853900817779268f * z);
  return x * __builtin_amdgcn_rcpf(1.0f + e);
}

constexpr int G1_WSLOT_B = 12288;
constexpr int G1_X_OFF = 2 * G1_WSLOT_B;

constexpr int g1_pos(bool isx, int id) {
  if (isx && id == 0) return 4;
  if (!isx && id == 0) return 7;
  if (!isx && id == 1) return 10;
  if (isx && id == 1) return 14;
  if (!isx && id == 2) return 17;
  int n = 17;
  for (int t = 0; t < 16; ++t) {
    if (t + 3 < 16) { n += 3; if (!isx && t + 3 == id) return n; }
    if ((t & 1) == 0 && t / 2 + 2 < 8) { n += 4; if (isx && t / 2 + 2 == id) return n; }
  }
  return -1;
}
constexpr int g1_issued_before(int s) {
  int n = 17;
  for (int t = 0; t < s; ++t) {
    if (t + 3 < 16) n += 3;
    if ((t & 1) == 0 && t / 2 + 2 < 8) n += 4;
  }
  return n;
}

#define GLOAD128(dst, ptr) asm volatile("global_load_dwordx4 %0, %1, off" : "=v"(dst) : "v"(ptr) : "memory")
#define GLOAD128A(dst, ptr) asm volatile("global_load_dwordx4 %0, %1, off" : "=a"(dst) : "v"(ptr) : "memory")
#define DS_W128(addr, val, off) asm volatile("ds_write_b128 %0, %1 offset:%2" ::"v"(addr), "v"(val), "i"(off) : "memory")

struct G1Ctx {
  const u16* wsrc[3];
  const u16* xsrc;
  unsigned sbase, woff, woff_o, ww[3], xw, xrd;
};

template <int S>
struct G1Step {
  static __device__ __forceinline__ void run(f32x16 (&acc)[6], u32x4 (&xr)[2][4], u32x4 (&wr)[3][3], bf16x8 (&xa)[4],
                                             const G1Ctx& c) {
    constexpr int ISS = g1_issued_before(S);
    if constexpr ((S & 1) == 0) {
      constexpr int B = (S / 2) & 1;
      constexpr int NW = ISS - g1_pos(true, S / 2);
      asm volatile("s_waitcnt vmcnt(%4)" : "+v"(xr[B][0]), "+v"(xr[B][1]), "+v"(xr[B][2]), "+v"(xr[B][3]) : "i"(NW) : "memory");
#pragma unroll
      for (int i = 0; i < 4; ++i) DS_W128(c.xw, xr[B][i], i * 1024);
    }
    const unsigned sbe = c.sbase + (S & 1) * G1_WSLOT_B + c.woff;
    const unsigned sbo = c.sbase + (S & 1) * G1_WSLOT_B + c.woff_o;
    s16x4 w[2][6][2];
#pragma unroll
    for (int t = 0; t < 6; t += 2) {
      DS_TR(w[0][t][0], sbe, t * 64); DS_TR(w[0][t][1], sbe, t * 64 + 4 * 384);
      DS_TR(w[0][t + 1][0], sbo, t * 64 + 64); DS_TR(w[0][t + 1][1], sbo, t * 64 + 64 + 4 * 384);
    }
    if constexpr ((S & 1) == 0) {
      asm volatile("s_waitcnt lgkmcnt(12)" ::: "memory");
#pragma unroll
      for (int i = 0; i < 4; ++i) DS_B128(xa[i], c.xrd ^ (i * 32), 0);
    }
    asm volatile("s_waitcnt lgkmcnt(0)"
                 : "+v"(w[0][0][0]), "+v"(w[0][0][1]), "+v"(w[0][1][0]), "+v"(w[0][1][1]), "+v"(w[0][2][0]), "+v"(w[0][2][1]),
                   "+v"(w[0][3][0]), "+v"(w[0][3][1]), "+v"(w[0][4][0]), "+v"(w[0][4][1]), "+v"(w[0][5][0]), "+v"(w[0][5][1]),
                   "+v"(xa[0]), "+v"(xa[1]), "+v"(xa[2]), "+v"(xa[3])::"memory");
#pragma unroll
    for (int t = 0; t < 6; t += 2) {
      DS_TR(w[1][t][0], sbe, t * 64 + 16 * 384); DS_TR(w[1][t][1], sbe, t * 64 + 20 * 384);
      DS_TR(w[1][t + 1][0], sbo, t * 64 + 64 + 16 * 384); DS_TR(w[1][t + 1][1], sbo, t * 64 + 64 + 20 * 384);
    }
#pragma unroll
    for (int t = 0; t < 6; ++t)
      acc[t] = __builtin_amdgcn_mfma_f32_32x32x16_bf16(cat4(w[0][t][0], w[0][t][1]), xa[2 * (S & 1)], acc[t], 0, 0, 0);
    if constexpr (S + 1 < 16) {
      constexpr int R = (S + 1) % 3;
      constexpr int NW = ISS - g1_pos(false, S + 1);
      asm volatile("s_waitcnt vmcnt(%3)" : "+v"(wr[R][0]), "+v"(wr[R][1]), "+v"(wr[R][2]) : "i"(NW) : "memory");
#pragma unroll
      for (int i = 0; i < 3; ++i) DS_W128(c.ww[i], wr[R][i], ((S + 1) & 1) * G1_WSLOT_B);
    }
    if constexpr (S + 3 < 16) {
#pragma unroll
      for (int i = 0; i < 3; ++i) GLOAD128(wr[S % 3][i], c.wsrc[i] + (size_t)(S + 3) * 32 * FF);
    }
    if constexpr ((S & 1) == 0 && S / 2 + 2 < 8) {
#pragma unroll
      for (int i = 0; i < 4; ++i) GLOAD128(xr[(S / 2) & 1][i], c.xsrc + (size_t)(8 * i) * DM + (S / 2 + 2) * 64);
    }
    asm volatile("s_waitcnt lgkmcnt(%12)"
                 : "+v"(w[1][0][0]), "+v"(w[1][0][1]), "+v"(w[1][1][0]), "+v"(w[1][1][1]), "+v"(w[1][2][0]), "+v"(w[1][2][1]),
                   "+v"(w[1][3][0]), "+v"(w[1][3][1]), "+v"(w[1][4][0]), "+v"(w[1][4][1]), "+v"(w[1][5][0]), "+v"(w[1][5][1])
                 : "i"(S + 1 < 16 ? 3 : 0) : "memory");
#pragma unroll
    for (int t = 0; t < 6; ++t)
      acc[t] = __builtin_amdgcn_mfma_f32_32x32x16_bf16(cat4(w[1][t][0], w[1][t][1]), xa[2 * (S & 1) + 1], acc[t], 0, 0, 0);
    if constexpr (S + 1 < 16) {
      asm volatile("s_waitcnt lgkmcnt(0)\n\ts_barrier" ::: "memory");
      G1Step<S + 1>::run(acc, xr, wr, xa, c);
    }
  }
};

__device__ __forceinline__ void gemm1_tile(int t, const u16* __restrict__ x, const u16* __restrict__ wg,
                                           const u16* __restrict__ wu, u16* __restrict__ hf, char* smem) {
  const int tid = threadIdx.x, lane = tid & 63;
  const int wave = __builtin_amdgcn_readfirstlane(tid >> 6);
  const int fb = t >> 4, mb = t & 15;
  const int m0 = 128 * mb + 32 * wave, f0 = 96 * fb;
  G1Ctx c;
  c.sbase = lds_addr(smem);
#pragma unroll
  for (int i = 0; i < 3; ++i) {
    const int id = 192 * wave + 64 * i + lane;
    const int row = id / 24, cc = id % 24;
    int col = f0 + 8 * (cc >= 12 ? cc - 12 : cc);
    if (col > FF - 8) col = FF - 8;
    c.wsrc[i] = (cc >= 12 ? wu : wg) + (size_t)row * FF + col;
    c.ww[i] = c.sbase + row * 384 + (cc ^ (((row >> 1) & 1) << 2)) * 16;
  }
  c.xsrc = x + (size_t)(m0 + (lane >> 3)) * DM + (lane & 7) * 8;
  const unsigned xbase = c.sbase + G1_X_OFF + wave * 4096;
  c.xw = xbase + (lane >> 3) * 128 + (((lane & 7) ^ (lane >> 3)) * 16);
  const int r = lane & 31, h = lane >> 5;
  c.xrd = xbase + r * 128 + ((h ^ (r & 7)) * 16);
  const int blk = (lane >> 4) & 1, q = (lane & 15) >> 2, p = lane & 3;
  const int sw = (q >> 1) & 1;
  c.woff = (8 * h + q) * 384 + (2 * blk + (p >> 1) + 4 * sw) * 16 + 8 * (p & 1);
  c.woff_o = (8 * h + q) * 384 + (2 * blk + (p >> 1) - 4 * sw) * 16 + 8 * (p & 1);

  f32x16 acc[6];
#pragma unroll
  for (int i = 0; i < 6; ++i) acc[i] = f32x16{};
  u32x4 xr[2][4], wr[3][3];
  bf16x8 xa[4];
#define G1_ISSUE_X(B) _Pragma("unroll") for (int i = 0; i < 4; ++i) GLOAD128(xr[B][i], c.xsrc + (size_t)(8 * i) * DM + (B) * 64)
#define G1_ISSUE_W(S) _Pragma("unroll") for (int i = 0; i < 3; ++i) GLOAD128(wr[S][i], c.wsrc[i] + (size_t)(S) * 32 * FF)
  G1_ISSUE_X(0); G1_ISSUE_W(0); G1_ISSUE_W(1); G1_ISSUE_X(1); G1_ISSUE_W(2);
  asm volatile("s_waitcnt vmcnt(10)" : "+v"(wr[0][0]), "+v"(wr[0][1]), "+v"(wr[0][2])::"memory");
#pragma unroll
  for (int i = 0; i < 3; ++i) DS_W128(c.ww[i], wr[0][i], 0);
  asm volatile("s_waitcnt lgkmcnt(0)\n\ts_barrier" ::: "memory");
  G1Step<0>::run(acc, xr, wr, xa, c);

  const int mt = m0 >> 5;
#pragma unroll
  for (int cc = 0; cc < 3; ++cc) {
    const int ft = f0 / 32 + cc;
    if (ft < NKS / 2) {
      const bool valid = ft < FF / 32;
#pragma unroll
      for (int i = 0; i < 2; ++i) {
        unsigned o[4];
#pragma unroll
        for (int e = 0; e < 4; ++e) {
          float hv[2];
#pragma unroll
          for (int z = 0; z < 2; ++z) {
            const float gv = rbf(acc[cc][8 * i + 2 * e + z]);
            const float uv = rbf(acc[3 + cc][8 * i + 2 * e + z]);
            hv[z] = valid ? rbf(gelu_tanh(gv)) * uv : 0.f;
          }
          o[e] = pk_bf16(hv[0], hv[1]);
        }
        *(u32x4*)(hf + ((size_t)(mt * NKS + 2 * ft + i) * 64 + lane) * 8) = u32x4{o[0], o[1], o[2], o[3]};
      }
    }
  }
}

__device__ __forceinline__ void gate_unit(int b, const u16* __restrict__ x, const u16* __restrict__ gw,
                                          float* __restrict__ gout) {
  const int tid = threadIdx.x, lane = tid & 63, wave = tid >> 6;
  const u32x4 gv = *(const u32x4*)(gw + lane * 8);
#pragma unroll 1
  for (int half = 0; half < 2; ++half) {
    const int row0 = b * 128 + wave * 32 + half * 16;
    u32x4 xv[16];
#pragma unroll
    for (int i = 0; i < 16; ++i) xv[i] = *(const u32x4*)(x + (size_t)(row0 + i) * DM + lane * 8);
#pragma unroll
    for (int i = 0; i < 16; ++i) {
      float s = 0.f;
#pragma unroll
      for (int j = 0; j < 4; ++j) {
        const unsigned pr = pk_bf16(bf_lo(xv[i][j]) * bf_lo(gv[j]), bf_hi(xv[i][j]) * bf_hi(gv[j]));
        s += bf_lo(pr) + bf_hi(pr);
      }
#pragma unroll
      for (int o = 32; o >= 1; o >>= 1) s += __shfl_xor(s, o);
      if (lane == 0) {
        const float sb = rbf(s);
        gout[row0 + i] = rbf(1.0f / (1.0f + expf(-sb)));
      }
    }
  }
}

__global__ __launch_bounds__(256, 2) void k1_kernel(const u16* __restrict__ u, const u16* __restrict__ logAr,
                                                   const u16* __restrict__ Aim, const u16* __restrict__ Cre,
                                                   const u16* __restrict__ Cim, const u16* __restrict__ logdt,
                                                   const u16* __restrict__ Dskip, const u16* __restrict__ wg,
                                                   const u16* __restrict__ wu, const u16* __restrict__ gw,
                                                   u16* __restrict__ hbuf, u16* __restrict__ aT,
                                                   float* __restrict__ gout DBG_ARG) {
  __shared__ __attribute__((aligned(4096))) char smem[K1_LDS];
  const int bid = blockIdx.x;
  if (bid < NSSM) {
    ssm_unit(bid, u, logAr, Aim, Cre, Cim, logdt, Dskip, aT, smem DBG_PASS);
  } else {
    const int xcd = bid & 7, idx = (bid - NSSM) >> 3;
    if (idx < 30) gemm1_tile(30 * xcd + idx, u, wg, wu, hbuf, smem);
    else gate_unit(2 * xcd + idx - 30, u, gw, gout);
  }
}

constexpr int G2_SLOT_B = 4096;
constexpr int G2_WAVE_B = 2 * G2_SLOT_B;
constexpr int G2_PITCH = 272;
constexpr int G2_A_OFF = 4 * 64 * G2_PITCH;
constexpr int K2_LDS = G2_A_OFF + 8192;
constexpr int G2_DEPTH = 7;

struct G2Ctx {
  const u16* hsrc;
  const u16* wd;
  int wrow0, wcol;
  unsigned wb32, woff, ww;
};

template <int ST>
struct G2Step {
  static __device__ __forceinline__ void issue(bf16x8 (&hfr)[11][4], u32x4 (&wr)[11][4], const G2Ctx& c) {
#pragma unroll
    for (int i = 0; i < 4; ++i) {
      int kr = c.wrow0 + ST * 32 + 8 * i;
      kr = kr < FF ? kr : FF - 1;
      GLOAD128(wr[ST][i], c.wd + (size_t)kr * DM + c.wcol);
    }
#pragma unroll
    for (int mi = 0; mi < 2; ++mi)
#pragma unroll
      for (int i = 0; i < 2; ++i) GLOAD128A(hfr[ST][2 * mi + i], c.hsrc + (size_t)(mi * NKS + 2 * ST + i) * 512);
  }
  static __device__ __forceinline__ void run(f32x16 (&acc)[2][2], bf16x8 (&hfr)[11][4], u32x4 (&wr)[11][4], const G2Ctx& c) {
    constexpr int LAST = (ST + G2_DEPTH - 1 < 10) ? ST + G2_DEPTH - 1 : 10;
    constexpr int NW = 8 * (LAST - ST);
    asm volatile("s_waitcnt vmcnt(%4)" : "+v"(wr[ST][0]), "+v"(wr[ST][1]), "+v"(wr[ST][2]), "+v"(wr[ST][3]) : "i"(NW + 4) : "memory");
#pragma unroll
    for (int i = 0; i < 4; ++i) DS_W128(c.ww, wr[ST][i], (ST & 1) * G2_SLOT_B + i * 1024);
    asm volatile("s_waitcnt lgkmcnt(0)" ::: "memory");
    const unsigned sb = c.wb32 + c.woff;
    s16x4 w[2][2][2];
#pragma unroll
    for (int i = 0; i < 2; ++i)
#pragma unroll
      for (int t = 0; t < 2; ++t) {
        DS_TR(w[i][t][0], sb ^ (t * 64), (ST & 1) * G2_SLOT_B + i * 2048);
        DS_TR(w[i][t][1], sb ^ (t * 64), (ST & 1) * G2_SLOT_B + i * 2048 + 1024);
      }
    asm volatile("s_waitcnt vmcnt(%4)" : "+a"(hfr[ST][0]), "+a"(hfr[ST][1]), "+a"(hfr[ST][2]), "+a"(hfr[ST][3]) : "i"(NW) : "memory");
    if constexpr (ST + G2_DEPTH <= 10) G2Step<ST + G2_DEPTH>::issue(hfr, wr, c);
    asm volatile("s_waitcnt lgkmcnt(0)"
                 : "+v"(w[0][0][0]), "+v"(w[0][0][1]), "+v"(w[0][1][0]), "+v"(w[0][1][1]), "+v"(w[1][0][0]), "+v"(w[1][0][1]),
                   "+v"(w[1][1][0]), "+v"(w[1][1][1])::"memory");
#pragma unroll
    for (int i = 0; i < 2; ++i)
#pragma unroll
      for (int t = 0; t < 2; ++t)
#pragma unroll
        for (int mi = 0; mi < 2; ++mi)
          acc[t][mi] = __builtin_amdgcn_mfma_f32_32x32x16_bf16(cat4(w[i][t][0], w[i][t][1]), hfr[ST][2 * mi + i], acc[t][mi], 0, 0, 0);
    if constexpr (ST + 1 <= 10) G2Step<ST + 1>::run(acc, hfr, wr, c);
  }
};

__global__ __launch_bounds__(256, 1) void k2_kernel(const u16* __restrict__ hf, const u16* __restrict__ wd,
                                                   const u16* __restrict__ aT, const float* __restrict__ gin,
                                                   u16* __restrict__ out DBG2_ARG) {
  __shared__ __attribute__((aligned(4096))) char smem[K2_LDS];
  const int tid = threadIdx.x, lane = tid & 63;
  STAMP2(0);
  const int wave = __builtin_amdgcn_readfirstlane(tid >> 6);
  const int bid = blockIdx.x;
  const int xcd = bid & 7, idx = bid >> 3;
  const int m0 = 64 * (4 * xcd + (idx >> 3)), n0 = 64 * (idx & 7);

#pragma unroll
  for (int i = 0; i < 2; ++i) {
    const int piece = 2 * wave + i;
    const int drow = 8 * piece + (lane >> 3);
    glds16(aT + (size_t)(n0 + drow) * SEQ + m0 + (lane & 7) * 8, smem + G2_A_OFF + piece * 1024 + lane * 16);
  }

  G2Ctx c;
  c.wb32 = lds_addr(smem) + wave * G2_WAVE_B;
  c.hsrc = hf + ((size_t)((m0 >> 5) * NKS + 22 * wave) * 64 + lane) * 8;
  c.wd = wd;
  c.wcol = n0 + 8 * (lane & 7);
  c.wrow0 = 352 * wave + (lane >> 3);
  c.ww = c.wb32 + (lane >> 3) * 128 + (((lane & 7) ^ (((lane >> 4) & 1) << 2)) * 16);

  const int r = lane & 31, h = lane >> 5;
  const int blk = (lane >> 4) & 1, q = (lane & 15) >> 2, p = lane & 3;
  c.woff = (4 * h + q) * 128 + (((2 * blk + (p >> 1)) ^ (((q >> 1) & 1) << 2)) * 16) + 8 * (p & 1);

  f32x16 acc[2][2];
#pragma unroll
  for (int i = 0; i < 2; ++i)
#pragma unroll
    for (int j = 0; j < 2; ++j) acc[i][j] = f32x16{};
  bf16x8 hfr[11][4];
  u32x4 wr[11][4];

  G2Step<0>::issue(hfr, wr, c);
  G2Step<1>::issue(hfr, wr, c);
  G2Step<2>::issue(hfr, wr, c);
  G2Step<3>::issue(hfr, wr, c);
  G2Step<4>::issue(hfr, wr, c);
  G2Step<5>::issue(hfr, wr, c);
  G2Step<6>::issue(hfr, wr, c);
  STAMP2(1);
  STAMP2(2);
  G2Step<0>::run(acc, hfr, wr, c);
  STAMP2(3);
  __syncthreads();
  {
    char* pw = smem + wave * (64 * G2_PITCH);
#pragma unroll
    for (int c = 0; c < 2; ++c)
#pragma unroll
      for (int mi = 0; mi < 2; ++mi)
#pragma unroll
        for (int gq = 0; gq < 4; ++gq) {
          const f32x4 v = {acc[c][mi][4 * gq], acc[c][mi][4 * gq + 1], acc[c][mi][4 * gq + 2], acc[c][mi][4 * gq + 3]};
          *(f32x4*)(pw + (32 * mi + r) * G2_PITCH + (32 * c + 8 * gq + 4 * h) * 4) = v;
        }
  }
  __syncthreads();
  STAMP2(4);
  {
    const int m = tid >> 2, nq = (tid & 3) * 16;
    const float g = gin[m0 + m];
    const float omg = rbf(1.0f - g);
    float b[16];
#pragma unroll
    for (int j = 0; j < 4; ++j) {
      f32x4 s = *(const f32x4*)(smem + m * G2_PITCH + (nq + 4 * j) * 4);
#pragma unroll
      for (int w = 1; w < 4; ++w) s += *(const f32x4*)(smem + w * (64 * G2_PITCH) + m * G2_PITCH + (nq + 4 * j) * 4);
#pragma unroll
      for (int e = 0; e < 4; ++e) b[4 * j + e] = s[e];
    }
    const u16* aimg = (const u16*)(smem + G2_A_OFF);
    unsigned o[8];
#pragma unroll
    for (int j = 0; j < 8; ++j) {
      const float a0 = bf2f(aimg[(nq + 2 * j) * 64 + m]);
      const float a1 = bf2f(aimg[(nq + 2 * j + 1) * 64 + m]);
      const float o0 = rbf(g * a0) + rbf(omg * rbf(b[2 * j]));
      const float o1 = rbf(g * a1) + rbf(omg * rbf(b[2 * j + 1]));
      o[j] = pk_bf16(o0, o1);
    }
    u16* op = out + (size_t)(m0 + m) * DM + n0 + nq;
    *(u32x4*)op = u32x4{o[0], o[1], o[2], o[3]};
    *(u32x4*)(op + 8) = u32x4{o[4], o[5], o[6], o[7]};
  }
  STAMP2(5);
}

extern "C" void kernel_launch(void* const* d_in, const int* in_sizes, int n_in, void* d_out, int out_size,
                              void* d_ws, size_t ws_size, hipStream_t stream) {
  const u16* u = (const u16*)d_in[0];
  const u16* logAr = (const u16*)d_in[1];
  const u16* Aim = (const u16*)d_in[2];
  const u16* Cre = (const u16*)d_in[3];
  const u16* Cim = (const u16*)d_in[4];
  const u16* logdt = (const u16*)d_in[5];
  const u16* Dskip = (const u16*)d_in[6];
  const u16* wg = (const u16*)d_in[8];
  const u16* wu = (const u16*)d_in[9];
  const u16* wd = (const u16*)d_in[10];
  const u16* gw = (const u16*)d_in[11];
  char* ws = (char*)d_ws;
  u16* hbuf = (u16*)ws;
  u16* aT = (u16*)(ws + (size_t)SEQ * FFP * 2);
  float* gbuf = (float*)(ws + (size_t)SEQ * FFP * 2 + (size_t)DM * SEQ * 2);
  k1_kernel<<<512, 256, 0, stream>>>(u, logAr, Aim, Cre, Cim, logdt, Dskip, wg, wu, gw, hbuf, aT, gbuf
      );
  k2_kernel<<<256, 256, 0, stream>>>(hbuf, wd, aT, gbuf, (u16*)d_out
      );
}
```

```cpp
#include <hip/hip_runtime.h>
#include <hip/hip_bf16.h>
#include <stdint.h>

typedef unsigned short u16;
typedef __attribute__((ext_vector_type(8))) short bf16x8;
typedef __attribute__((ext_vector_type(4))) short s16x4;
typedef __attribute__((ext_vector_type(4))) float f32x4;
typedef __attribute__((ext_vector_type(16))) float f32x16;
typedef __attribute__((ext_vector_type(4))) unsigned u32x4;
typedef __attribute__((ext_vector_type(2))) unsigned u32x2;
typedef __attribute__((ext_vector_type(2))) __bf16 bf16x2_t;
typedef __attribute__((ext_vector_type(2))) float f32x2;
typedef __attribute__((address_space(3))) s16x4* lds_s16x4_ptr;

constexpr int SEQ = 2048, DM = 512, NST = 128, FF = 1376, FFP = 1408;
constexpr int NSSM = 256;
constexpr int NKS = 88;

__device__ __forceinline__ unsigned pk_bf16(float lo, float hi) {
  f32x2 v = {lo, hi};
  bf16x2_t b = __builtin_convertvector(v, bf16x2_t);
  return __builtin_bit_cast(unsigned, b);
}
__device__ __forceinline__ float bf_lo(unsigned w) { return __uint_as_float(w << 16); }
__device__ __forceinline__ float bf_hi(unsigned w) { return __uint_as_float(w & 0xffff0000u); }
__device__ __forceinline__ float rbf(float x) { return bf_lo(pk_bf16(x, 0.f)); }
__device__ __forceinline__ float bf2f(u16 v) { return __uint_as_float(((unsigned)v) << 16); }

__device__ __forceinline__ bf16x8 mk8(unsigned a, unsigned b, unsigned c, unsigned d) {
  u32x4 v = {a, b, c, d};
  return __builtin_bit_cast(bf16x8, v);
}
__device__ __forceinline__ bf16x8 cat4(s16x4 a, s16x4 b) {
  return __builtin_shufflevector(a, b, 0, 1, 2, 3, 4, 5, 6, 7);
}
__device__ __forceinline__ s16x4 tr_read(const char* smem_ptr) {
  return __builtin_amdgcn_ds_read_tr16_b64_v4i16((lds_s16x4_ptr)(smem_ptr));
}
__device__ __forceinline__ unsigned lds_addr(const void* p) {
  return (unsigned)(size_t)(const __attribute__((address_space(3))) char*)p;
}
#define DS_TR(dst, addr, off) asm volatile("ds_read_b64_tr_b16 %0, %1 offset:%2" : "=v"(dst) : "v"(addr), "i"(off) : "memory")
#define DS_B128(dst, addr, off) asm volatile("ds_read_b128 %0, %1 offset:%2" : "=v"(dst) : "v"(addr), "i"(off) : "memory")
__device__ __forceinline__ void glds16(const void* g, char* l) {
  __builtin_amdgcn_global_load_lds((const unsigned*)g, (unsigned*)l, 16, 0, 0);
}

#define STAMP2(i)
#define DBG2_ARG
#define DBG2_PASS
#define STAMP(i)
#define DBG_ARG
#define DBG_PASS
constexpr int U_COPY_B = 4160;
constexpr int U_CH_B = 4 * U_COPY_B;
constexpr int U_REGION_B = 2 * U_CH_B;
constexpr int KR_PART_B = 4576;
constexpr int KR_OFF = U_REGION_B;
constexpr int KP_B = 8208;
constexpr int SSM_LDS = KR_OFF + 4 * KR_PART_B;

static_assert(4 * KP_B <= U_REGION_B, "Kp scratch must fit the U region");

__device__ __forceinline__ void ssm_unit(int w, const u16* __restrict__ u, const u16* __restrict__ logAr,
                                         const u16* __restrict__ Aim, const u16* __restrict__ Cre,
                                         const u16* __restrict__ Cim, const u16* __restrict__ logdt,
                                         const u16* __restrict__ Dskip, u16* __restrict__ aT, char* smem DBG_ARG) {
  const int tid = threadIdx.x, lane = tid & 63;
  STAMP(0);
  const int wave = __builtin_amdgcn_readfirstlane(tid >> 6);
  const int ch = wave >> 1, w2 = wave & 1;
  const int d0 = 64 * (w & 7) + 2 * (w >> 3);
  const int d = d0 + ch;

  const unsigned* u32p = (const unsigned*)(u + d0);
  unsigned uw[8], up[3];
#pragma unroll
  for (int i = 0; i < 8; ++i) uw[i] = u32p[(size_t)(8 * tid + i) * (DM / 2)];
#pragma unroll
  for (int i = 0; i < 3; ++i) up[i] = 0;
  if (lane == 0 && tid > 0) {
#pragma unroll
    for (int i = 0; i < 3; ++i) up[i] = u32p[(size_t)(8 * tid - 3 + i) * (DM / 2)];
  }

  {
    const int n = tid & 127;
    const int dd = d0 + (tid >> 7);
    const float dt = rbf(expf(bf2f(logdt[dd])));
    const float are = rbf(expf(bf2f(logAr[dd * NST + n])));
    f32x4 prm;
    prm[0] = (-are * dt) * 1.44269504088896340736f;
    prm[1] = (bf2f(Aim[dd * NST + n]) * dt) * 0.15915494309189533577f;
    prm[2] = 2.0f * (bf2f(Cre[dd * NST + n]) * dt);
    prm[3] = 2.0f * (bf2f(Cim[dd * NST + n]) * dt);
    *(f32x4*)(smem + KR_OFF + tid * 16) = prm;
  }
  __syncthreads();
  STAMP(1);

  {
    const int r = lane & 31, h = lane >> 5;
    int p1i;
    if (r < 8) p1i = r;
    else if (r < 12) p1i = 256 + 2 * (r - 8);
    else if (r < 16) p1i = 512 + 256 * ((r - 12) >> 1) + 4 * ((r - 12) & 1);
    else if (r < 21) p1i = 1024 + 256 * (r - 16);
    else p1i = 0;
    const float p1 = (float)p1i, p2 = (float)(8 * r);
    f32x16 acc = {};
    const char* prm_base = smem + KR_OFF + (ch * 128 + 64 * w2 + 4 * h) * 16;
#pragma unroll 2
    for (int ks = 0; ks < 8; ++ks) {
      unsigned ah[4], al[4], bh[4], bl[4];
#pragma unroll
      for (int q = 0; q < 4; ++q) {
        const f32x4 prm = *(const f32x4*)(prm_base + (8 * ks + q) * 16);
        {
          const float e = __builtin_amdgcn_exp2f(prm[0] * p1);
          const float rv = __builtin_amdgcn_fractf(prm[1] * p1);
          const float sn = __builtin_amdgcn_sinf(rv), cs = __builtin_amdgcn_cosf(rv);
          const float gre = e * (prm[2] * cs - prm[3] * sn);
          const float gim = -e * (prm[2] * sn + prm[3] * cs);
          const unsigned hi = pk_bf16(gre, gim);
          ah[q] = hi;
          al[q] = pk_bf16(gre - bf_lo(hi), gim - bf_hi(hi));
        }
        {
          const float e = __builtin_amdgcn_exp2f(prm[0] * p2);
          const float rv = __builtin_amdgcn_fractf(prm[1] * p2);
          const float sn = __builtin_amdgcn_sinf(rv), cs = __builtin_amdgcn_cosf(rv);
          const float ere = e * cs, eim = e * sn;
          const unsigned hi = pk_bf16(ere, eim);
          bh[q] = hi;
          bl[q] = pk_bf16(ere - bf_lo(hi), eim - bf_hi(hi));
        }
      }
      const bf16x8 Ah = mk8(ah[0], ah[1], ah[2], ah[3]), Al = mk8(al[0], al[1], al[2], al[3]);
      const bf16x8 Bh = mk8(bh[0], bh[1], bh[2], bh[3]), Bl = mk8(bl[0], bl[1], bl[2], bl[3]);
      acc = __builtin_amdgcn_mfma_f32_32x32x16_bf16(Al, Bh, acc, 0, 0, 0);
      acc = __builtin_amdgcn_mfma_f32_32x32x16_bf16(Ah, Bl, acc, 0, 0, 0);
      acc = __builtin_amdgcn_mfma_f32_32x32x16_bf16(Ah, Bh, acc, 0, 0, 0);
    }
    float* Kp = (float*)(smem + (ch * 2 + w2) * KP_B);
#pragma unroll
    for (int e = 0; e < 4; ++e) Kp[(4 * h + e) + 8 * r] = acc[e];
#pragma unroll
    for (int e = 0; e < 4; ++e) {
      const int pp = h ? (512 + 256 * (e >> 1) + 4 * (e & 1)) : (256 + 2 * e);
      Kp[pp + 8 * r] = acc[4 + e];
    }
    if (h == 0) {
#pragma unroll
      for (int e = 0; e < 4; ++e) Kp[1024 + 256 * e + 8 * r] = acc[8 + e];
    } else if (r == 0) {
      Kp[2048] = acc[8];
    }
  }
  STAMP(2);
  __syncthreads();

  {
    const int t = tid & 127;
    const float* Kp0 = (const float*)(smem + (ch * 2) * KP_B);
    const float* Kp1 = (const float*)(smem + (ch * 2 + 1) * KP_B);
    char* krh = smem + KR_OFF + (ch * 2) * KR_PART_B;
    char* krl = krh + KR_PART_B;
#pragma unroll
    for (int kk = 0; kk < 2; ++kk) {
      const int k = t + 128 * kk;
      float v[8];
#pragma unroll
      for (int e = 0; e < 8; ++e) {
        const int l = 2047 - 8 * k - e;
        const int p = (int)rbf((float)l);
        v[e] = Kp0[p] + Kp1[p];
      }
      unsigned hi[4], lo[4];
#pragma unroll
      for (int e = 0; e < 4; ++e) {
        hi[e] = pk_bf16(v[2 * e], v[2 * e + 1]);
        lo[e] = pk_bf16(v[2 * e] - bf_lo(hi[e]), v[2 * e + 1] - bf_hi(hi[e]));
      }
      *(u32x4*)(krh + k * 16) = u32x4{hi[0], hi[1], hi[2], hi[3]};
      *(u32x4*)(krl + k * 16) = u32x4{lo[0], lo[1], lo[2], lo[3]};
    }
    if (t < 30) {
      *(u32x4*)(krh + (256 + t) * 16) = u32x4{0, 0, 0, 0};
      *(u32x4*)(krl + (256 + t) * 16) = u32x4{0, 0, 0, 0};
    }
  }
  __syncthreads();
  STAMP(3);

  {
    unsigned e[11];
#pragma unroll
    for (int i = 0; i < 3; ++i) {
      const unsigned nb = __shfl_up(uw[5 + i], 1);
      e[i] = (lane == 0) ? up[i] : nb;
    }
#pragma unroll
    for (int i = 0; i < 8; ++i) e[3 + i] = uw[i];
    unsigned p0[10], p1[10];
#pragma unroll
    for (int i = 0; i < 10; ++i) {
      p0[i] = __builtin_amdgcn_perm(e[i + 1], e[i], 0x05040100u);
      p1[i] = __builtin_amdgcn_perm(e[i + 1], e[i], 0x07060302u);
    }
#pragma unroll
    for (int c = 0; c < 4; ++c) {
      *(u32x4*)(smem + c * U_COPY_B + (tid + 2) * 16) = u32x4{p0[c], p0[c + 2], p0[c + 4], p0[c + 6]};
      *(u32x4*)(smem + U_CH_B + c * U_COPY_B + (tid + 2) * 16) = u32x4{p1[c], p1[c + 2], p1[c + 4], p1[c + 6]};
    }
    if (tid < 16) *(u32x4*)(smem + (tid >> 1) * U_COPY_B + (tid & 1) * 16) = u32x4{0, 0, 0, 0};
  }
  __syncthreads();
  STAMP(4);

  f32x4 acc[8];
#pragma unroll
  for (int t = 0; t < 8; ++t) acc[t] = f32x4{0.f, 0.f, 0.f, 0.f};
  {
    const int r = lane & 15, hq = lane >> 4;
    const char* ua = smem + ch * U_CH_B + (r & 3) * U_COPY_B + 2 * (4 + (r & ~3) + 8 * hq);
    const char* kb = smem + KR_OFF + (ch * 2) * KR_PART_B + 2 * (2032 - 16 * r + 8 * hq);
#pragma unroll 1
    for (int ri = 0; ri < 4; ++ri) {
      const int rho = 4 * w2 + ri;
      bf16x8 A[8];
#pragma unroll
      for (int sg = 0; sg < 8; ++sg) {
        const char* pa = ua + 64 * (8 * sg + rho);
        A[sg] = cat4(*(const s16x4*)pa, *(const s16x4*)(pa + 8));
      }
#pragma unroll
      for (int tau = 0; tau < 8; ++tau) {
        const char* pb = kb - 64 * (8 * tau - rho);
        const bf16x8 Bh = *(const bf16x8*)pb;
        const bf16x8 Bl = *(const bf16x8*)(pb + KR_PART_B);
#pragma unroll
        for (int sg = 0; sg + tau < 8; ++sg) {
          acc[sg + tau] = __builtin_amdgcn_mfma_f32_16x16x32_bf16(A[sg], Bl, acc[sg + tau], 0, 0, 0);
          acc[sg + tau] = __builtin_amdgcn_mfma_f32_16x16x32_bf16(A[sg], Bh, acc[sg + tau], 0, 0, 0);
        }
      }
    }
  }
  STAMP(5);
  __syncthreads();
  {
    char* xw = smem + KR_OFF + (ch * 2 + w2) * 4096 + lane * 16;
    const char* xr = smem + KR_OFF + (ch * 2 + (w2 ^ 1)) * 4096 + lane * 16;
    f32x4 y[4];
    if (w2 == 0) {
#pragma unroll
      for (int i = 0; i < 4; ++i) { *(f32x4*)(xw + i * 1024) = acc[4 + i]; y[i] = acc[i]; }
    } else {
#pragma unroll
      for (int i = 0; i < 4; ++i) { *(f32x4*)(xw + i * 1024) = acc[i]; y[i] = acc[4 + i]; }
    }
    __syncthreads();
    const float Dk = bf2f(Dskip[d]);
#pragma unroll
    for (int i = 0; i < 4; ++i) {
      const f32x4 o = *(const f32x4*)(xr + i * 1024);
      const int l0 = 256 * (4 * w2 + i) + 16 * (lane & 15) + 4 * (lane >> 4);
      const u32x2 uu = *(const u32x2*)(smem + ch * U_CH_B + 3 * U_COPY_B + 2 * (16 + l0));
      const float a0 = rbf(y[i][0] + o[0]) + rbf(bf_lo(uu[0]) * Dk);
      const float a1 = rbf(y[i][1] + o[1]) + rbf(bf_hi(uu[0]) * Dk);
      const float a2 = rbf(y[i][2] + o[2]) + rbf(bf_lo(uu[1]) * Dk);
      const float a3 = rbf(y[i][3] + o[3]) + rbf(bf_hi(uu[1]) * Dk);
      *(u32x2*)(aT + (size_t)d * SEQ + l0) = u32x2{pk_bf16(a0, a1), pk_bf16(a2, a3)};
    }
  }
  STAMP(6);
}

__device__ __forceinline__ float gelu_tanh(float x) {
  const float z = 0.7978845608028654f * (x + 0.044715f * x * x * x);
  const float e = __builtin_amdgcn_exp2f(-2.8853900817779268f * z);
  return x * __builtin_amdgcn_rcpf(1.0f + e);
}

constexpr int G1_WSLOT_B = 12288;
constexpr int G1_X_OFF = 3 * G1_WSLOT_B;
constexpr int G1_LDS = G1_X_OFF + 32768;

constexpr int g1_pos(bool isx, int id) {
  if (isx && id == 0) return 4;
  if (!isx && id == 0) return 7;
  if (!isx && id == 1) return 10;
  if (isx && id == 1) return 14;
  if (!isx && id == 2) return 17;
  if (!isx && id == 3) return 20;
  if (!isx && id == 4) return 23;
  if (isx && id == 2) return 27;
  int n = 27;
  for (int t = 0; t < 32; ++t) {
    if ((t & 1) == 0 && t / 2 + 5 < 16) { n += 3; if (!isx && t / 2 + 5 == id) return n; }
    if ((t & 3) == 1 && t / 4 + 3 < 8) { n += 4; if (isx && t / 4 + 3 == id) return n; }
  }
  return -1;
}
constexpr int g1_issued_before(int step) {
  int n = 27;
  for (int t = 0; t < step; ++t) {
    if ((t & 1) == 0 && t / 2 + 5 < 16) n += 3;
    if ((t & 3) == 1 && t / 4 + 3 < 8) n += 4;
  }
  return n;
}

#define GLOAD128(dst, ptr) asm volatile("global_load_dwordx4 %0, %1, off" : "=v"(dst) : "v"(ptr) : "memory")
#define GLOAD128A(dst, ptr) asm volatile("global_load_dwordx4 %0, %1, off" : "=a"(dst) : "v"(ptr) : "memory")
#define DS_W128(addr, val, off) asm volatile("ds_write_b128 %0, %1 offset:%2" ::"v"(addr), "v"(val), "i"(off) : "memory")

struct G1Ctx {
  const u16* wsrc[3];
  const u16* xsrc;
  unsigned woff, woff_o, ww[3], xw, xrd[4];
};
struct G1Frag {
  s16x4 w[6][2];
  bf16x8 x;
};

template <int N>
__device__ __forceinline__ void g1_read(G1Frag& f, const G1Ctx& c) {
  constexpr int OFF = ((N >> 1) % 3) * G1_WSLOT_B + (N & 1) * 16 * 384;
  DS_B128(f.x, c.xrd[N & 3], ((N >> 2) & 1) * 4096);
#pragma unroll
  for (int t = 0; t < 6; t += 2) {
    DS_TR(f.w[t][0], c.woff, OFF + t * 64); DS_TR(f.w[t][1], c.woff, OFF + t * 64 + 4 * 384);
    DS_TR(f.w[t + 1][0], c.woff_o, OFF + t * 64 + 64); DS_TR(f.w[t + 1][1], c.woff_o, OFF + t * 64 + 64 + 4 * 384);
  }
}
#define G1_WAIT_FRAG(f, STR)                                                                                       \
  asm volatile(STR : "+v"(f.w[0][0]), "+v"(f.w[0][1]), "+v"(f.w[1][0]), "+v"(f.w[1][1]), "+v"(f.w[2][0]),        \
               "+v"(f.w[2][1]), "+v"(f.w[3][0]), "+v"(f.w[3][1]), "+v"(f.w[4][0]), "+v"(f.w[4][1]),              \
               "+v"(f.w[5][0]), "+v"(f.w[5][1]), "+v"(f.x)::"memory")

template <int N>
struct G1Step {
  static __device__ __forceinline__ void run(f32x16 (&acc)[6], u32x4 (&xr)[2][4], u32x4 (&wr)[3][3], G1Frag& cur,
                                             G1Frag& nxt, const G1Ctx& c) {
    constexpr int ISS = g1_issued_before(N);
    constexpr int S = N >> 1;
    if constexpr ((N & 1) == 0 && N > 0) G1_WAIT_FRAG(cur, "s_waitcnt lgkmcnt(0)\n\ts_barrier");
    else G1_WAIT_FRAG(cur, "s_waitcnt lgkmcnt(0)");
    if constexpr (N + 1 < 32) g1_read<N + 1>(nxt, c);
    __builtin_amdgcn_sched_barrier(0);
#pragma unroll
    for (int t = 0; t < 6; ++t)
      acc[t] = __builtin_amdgcn_mfma_f32_32x32x16_bf16(cat4(cur.w[t][0], cur.w[t][1]), cur.x, acc[t], 0, 0, 0);
    __builtin_amdgcn_sched_barrier(0);
    if constexpr ((N & 1) == 0) {
      if constexpr (S + 2 < 16) {
        constexpr int R = (S + 2) % 3;
        constexpr int NW = ISS - g1_pos(false, S + 2);
        asm volatile("s_waitcnt vmcnt(%3)" : "+v"(wr[R][0]), "+v"(wr[R][1]), "+v"(wr[R][2]) : "i"(NW) : "memory");
#pragma unroll
        for (int i = 0; i < 3; ++i) DS_W128(c.ww[i], wr[R][i], R * G1_WSLOT_B);
      }
      if constexpr (S + 5 < 16) {
#pragma unroll
        for (int i = 0; i < 3; ++i) GLOAD128(wr[(S + 5) % 3][i], c.wsrc[i] + (size_t)(S + 5) * 32 * FF);
      }
    }
    if constexpr ((N & 3) == 1) {
      constexpr int B = N / 4 + 1;
      if constexpr (B < 8) {
        constexpr int NW = ISS - g1_pos(true, B);
        asm volatile("s_waitcnt vmcnt(%4)" : "+v"(xr[B & 1][0]), "+v"(xr[B & 1][1]), "+v"(xr[B & 1][2]), "+v"(xr[B & 1][3]) : "i"(NW) : "memory");
#pragma unroll
        for (int i = 0; i < 4; ++i) DS_W128(c.xw, xr[B & 1][i], (B & 1) * 4096 + i * 1024);
      }
      if constexpr (B + 2 < 8) {
#pragma unroll
        for (int i = 0; i < 4; ++i) GLOAD128(xr[B & 1][i], c.xsrc + (size_t)(8 * i) * DM + (B + 2) * 64);
      }
    }
    if constexpr (N + 1 < 32) G1Step<N + 1>::run(acc, xr, wr, nxt, cur, c);
  }
};

__device__ __forceinline__ void gemm1_tile(int t, const u16* __restrict__ x, const u16* __restrict__ wg,
                                           const u16* __restrict__ wu, u16* __restrict__ hf, char* smem DBG_ARG) {
  const int tid = threadIdx.x, lane = tid & 63;
  const int wave = __builtin_amdgcn_readfirstlane(tid >> 6);
  const int fb = t >> 4, mb = t & 15;
  const int m0 = 128 * mb + 32 * wave, f0 = 96 * fb;
  G1Ctx c;
  STAMP(0);
  const unsigned sbase = lds_addr(smem);
#pragma unroll
  for (int i = 0; i < 3; ++i) {
    const int id = 192 * wave + 64 * i + lane;
    const int row = id / 24, cc = id % 24;
    int col = f0 + 8 * (cc >= 12 ? cc - 12 : cc);
    if (col > FF - 8) col = FF - 8;
    c.wsrc[i] = (cc >= 12 ? wu : wg) + (size_t)row * FF + col;
    c.ww[i] = sbase + row * 384 + (cc ^ (((row >> 1) & 1) << 2)) * 16;
  }
  c.xsrc = x + (size_t)(m0 + (lane >> 3)) * DM + (lane & 7) * 8;
  const unsigned xbase = sbase + G1_X_OFF + wave * 8192;
  c.xw = xbase + (lane >> 3) * 128 + (((lane & 7) ^ (lane >> 3)) * 16);
  const int r = lane & 31, h = lane >> 5;
#pragma unroll
  for (int i = 0; i < 4; ++i) c.xrd[i] = xbase + r * 128 + (((2 * i + h) ^ (r & 7)) * 16);
  const int blk = (lane >> 4) & 1, q = (lane & 15) >> 2, p = lane & 3;
  const int sw = (q >> 1) & 1;
  c.woff = sbase + (8 * h + q) * 384 + (2 * blk + (p >> 1) + 4 * sw) * 16 + 8 * (p & 1);
  c.woff_o = sbase + (8 * h + q) * 384 + (2 * blk + (p >> 1) - 4 * sw) * 16 + 8 * (p & 1);

  f32x16 acc[6];
#pragma unroll
  for (int i = 0; i < 6; ++i) acc[i] = f32x16{};
  u32x4 xr[2][4], wr[3][3];
  G1Frag fa, fb2;
#define G1_ISSUE_X(R, B) _Pragma("unroll") for (int i = 0; i < 4; ++i) GLOAD128(xr[R][i], c.xsrc + (size_t)(8 * i) * DM + (B) * 64)
#define G1_ISSUE_W(R, S) _Pragma("unroll") for (int i = 0; i < 3; ++i) GLOAD128(wr[R][i], c.wsrc[i] + (size_t)(S) * 32 * FF)
  G1_ISSUE_X(0, 0); G1_ISSUE_W(0, 0); G1_ISSUE_W(1, 1); G1_ISSUE_X(1, 1); G1_ISSUE_W(2, 2);
  asm volatile("s_waitcnt vmcnt(7)"
               : "+v"(wr[0][0]), "+v"(wr[0][1]), "+v"(wr[0][2]), "+v"(wr[1][0]), "+v"(wr[1][1]), "+v"(wr[1][2]),
                 "+v"(xr[0][0]), "+v"(xr[0][1]), "+v"(xr[0][2]), "+v"(xr[0][3])::"memory");
#pragma unroll
  for (int i = 0; i < 3; ++i) { DS_W128(c.ww[i], wr[0][i], 0); DS_W128(c.ww[i], wr[1][i], G1_WSLOT_B); }
#pragma unroll
  for (int i = 0; i < 4; ++i) DS_W128(c.xw, xr[0][i], i * 1024);
  G1_ISSUE_W(0, 3); G1_ISSUE_W(1, 4); G1_ISSUE_X(0, 2);
  asm volatile("s_waitcnt lgkmcnt(0)\n\ts_barrier" ::: "memory");
  g1_read<0>(fa, c);
  G1Step<0>::run(acc, xr, wr, fa, fb2, c);
  STAMP(5);

  const int mt = m0 >> 5;
#pragma unroll
  for (int cc = 0; cc < 3; ++cc) {
    const int ft = f0 / 32 + cc;
    if (ft < NKS / 2) {
      const bool valid = ft < FF / 32;
#pragma unroll
      for (int i = 0; i < 2; ++i) {
        unsigned o[4];
#pragma unroll
        for (int e = 0; e < 4; ++e) {
          float hv[2];
#pragma unroll
          for (int z = 0; z < 2; ++z) {
            const float gv = rbf(acc[cc][8 * i + 2 * e + z]);
            const float uv = rbf(acc[3 + cc][8 * i + 2 * e + z]);
            hv[z] = valid ? rbf(gelu_tanh(gv)) * uv : 0.f;
          }
          o[e] = pk_bf16(hv[0], hv[1]);
        }
        *(u32x4*)(hf + ((size_t)(mt * NKS + 2 * ft + i) * 64 + lane) * 8) = u32x4{o[0], o[1], o[2], o[3]};
      }
    }
  }
  STAMP(6);
}

__device__ __forceinline__ void gate_unit(int b, const u16* __restrict__ x, const u16* __restrict__ gw,
                                          float* __restrict__ gout) {
  const int tid = threadIdx.x, lane = tid & 63, wave = tid >> 6;
  const u32x4 gv = *(const u32x4*)(gw + lane * 8);
#pragma unroll 1
  for (int half = 0; half < 2; ++half) {
    const int row0 = b * 128 + wave * 32 + half * 16;
    u32x4 xv[16];
#pragma unroll
    for (int i = 0; i < 16; ++i) xv[i] = *(const u32x4*)(x + (size_t)(row0 + i) * DM + lane * 8);
#pragma unroll
    for (int i = 0; i < 16; ++i) {
      float s = 0.f;
#pragma unroll
      for (int j = 0; j < 4; ++j) {
        const unsigned pr = pk_bf16(bf_lo(xv[i][j]) * bf_lo(gv[j]), bf_hi(xv[i][j]) * bf_hi(gv[j]));
        s += bf_lo(pr) + bf_hi(pr);
      }
#pragma unroll
      for (int o = 32; o >= 1; o >>= 1) s += __shfl_xor(s, o);
      if (lane == 0) {
        const float sb = rbf(s);
        gout[row0 + i] = rbf(1.0f / (1.0f + expf(-sb)));
      }
    }
  }
}

__global__ __launch_bounds__(256, 2) void k1_kernel(const u16* __restrict__ u, const u16* __restrict__ logAr,
                                                   const u16* __restrict__ Aim, const u16* __restrict__ Cre,
                                                   const u16* __restrict__ Cim, const u16* __restrict__ logdt,
                                                   const u16* __restrict__ Dskip, const u16* __restrict__ wg,
                                                   const u16* __restrict__ wu, const u16* __restrict__ gw,
                                                   u16* __restrict__ hbuf, u16* __restrict__ aT,
                                                   float* __restrict__ gout DBG_ARG) {
  __shared__ __attribute__((aligned(4096))) char smem[G1_LDS > SSM_LDS ? G1_LDS : SSM_LDS];
  const int bid = blockIdx.x;
  if (bid < NSSM) {
    ssm_unit(bid, u, logAr, Aim, Cre, Cim, logdt, Dskip, aT, smem DBG_PASS);
  } else {
    const int xcd = bid & 7, idx = (bid - NSSM) >> 3;
    if (idx < 30) gemm1_tile(30 * xcd + idx, u, wg, wu, hbuf, smem DBG_PASS);
    else gate_unit(2 * xcd + idx - 30, u, gw, gout);
  }
}

constexpr int G2_SLOT_B = 4096;
constexpr int G2_WAVE_B = 2 * G2_SLOT_B;
constexpr int G2_PITCH = 272;
constexpr int G2_A_OFF = 4 * 64 * G2_PITCH;
constexpr int K2_LDS = G2_A_OFF + 8192;
#define G2D 7
constexpr int G2_DEPTH = G2D;

struct G2Ctx {
  const u16* hsrc;
  const u16* wd;
  int wrow0, wcol;
  unsigned wb32, woff, ww;
};

template <int ST>
struct G2Step {
  static __device__ __forceinline__ void issue(bf16x8 (&hfr)[11][4], u32x4 (&wr)[11][4], const G2Ctx& c) {
#pragma unroll
    for (int i = 0; i < 4; ++i) {
      int kr = c.wrow0 + ST * 32 + 8 * i;
      kr = kr < FF ? kr : FF - 1;
      GLOAD128(wr[ST][i], c.wd + (size_t)kr * DM + c.wcol);
    }
#pragma unroll
    for (int mi = 0; mi < 2; ++mi)
#pragma unroll
      for (int i = 0; i < 2; ++i)
        GLOAD128A(hfr[ST][2 * mi + i], c.hsrc + (size_t)(mi * NKS + 2 * ST + i) * 512);

  }
  static __device__ __forceinline__ void run(f32x16 (&acc)[2][2], bf16x8 (&hfr)[11][4], u32x4 (&wr)[11][4], const G2Ctx& c) {
    constexpr int LAST = (ST + G2_DEPTH - 1 < 10) ? ST + G2_DEPTH - 1 : 10;
    constexpr int NW = 8 * (LAST - ST);
    asm volatile("s_waitcnt vmcnt(%4)" : "+v"(wr[ST][0]), "+v"(wr[ST][1]), "+v"(wr[ST][2]), "+v"(wr[ST][3]) : "i"(NW + 4) : "memory");
#pragma unroll
    for (int i = 0; i < 4; ++i) DS_W128(c.ww, wr[ST][i], (ST & 1) * G2_SLOT_B + i * 1024);
    asm volatile("s_waitcnt lgkmcnt(0)" ::: "memory");
    const unsigned sb = c.wb32 + c.woff;
    s16x4 w[2][2][2];
#pragma unroll
    for (int i = 0; i < 2; ++i)
#pragma unroll
      for (int t = 0; t < 2; ++t) {
        DS_TR(w[i][t][0], sb ^ (t * 64), (ST & 1) * G2_SLOT_B + i * 2048);
        DS_TR(w[i][t][1], sb ^ (t * 64), (ST & 1) * G2_SLOT_B + i * 2048 + 1024);
      }
    asm volatile("s_waitcnt vmcnt(%4)" : "+a"(hfr[ST][0]), "+a"(hfr[ST][1]), "+a"(hfr[ST][2]), "+a"(hfr[ST][3]) : "i"(NW) : "memory");
    if constexpr (ST + G2_DEPTH <= 10) G2Step<ST + G2_DEPTH>::issue(hfr, wr, c);
    asm volatile("s_waitcnt lgkmcnt(0)"
                 : "+v"(w[0][0][0]), "+v"(w[0][0][1]), "+v"(w[0][1][0]), "+v"(w[0][1][1]), "+v"(w[1][0][0]), "+v"(w[1][0][1]),
                   "+v"(w[1][1][0]), "+v"(w[1][1][1])::"memory");
#pragma unroll
    for (int i = 0; i < 2; ++i)
#pragma unroll
      for (int t = 0; t < 2; ++t)
#pragma unroll
        for (int mi = 0; mi < 2; ++mi)
          acc[t][mi] = __builtin_amdgcn_mfma_f32_32x32x16_bf16(cat4(w[i][t][0], w[i][t][1]), hfr[ST][2 * mi + i], acc[t][mi], 0, 0, 0);
    if constexpr (ST + 1 <= 10) G2Step<ST + 1>::run(acc, hfr, wr, c);
  }
};

__global__ __launch_bounds__(256, 1) void k2_kernel(const u16* __restrict__ hf, const u16* __restrict__ wd,
                                                   const u16* __restrict__ aT, const float* __restrict__ gin,
                                                   u16* __restrict__ out DBG2_ARG) {
  __shared__ __attribute__((aligned(4096))) char smem[K2_LDS];
  const int tid = threadIdx.x, lane = tid & 63;
  STAMP2(0);
  const int wave = __builtin_amdgcn_readfirstlane(tid >> 6);
  const int bid = blockIdx.x;
  const int xcd = bid & 7, idx = bid >> 3;
  const int m0 = 64 * (4 * xcd + (idx >> 3)), n0 = 64 * (idx & 7);

#pragma unroll
  for (int i = 0; i < 2; ++i) {
    const int piece = 2 * wave + i;
    const int drow = 8 * piece + (lane >> 3);
    glds16(aT + (size_t)(n0 + drow) * SEQ + m0 + (lane & 7) * 8, smem + G2_A_OFF + piece * 1024 + lane * 16);
  }

  G2Ctx c;
  c.wb32 = lds_addr(smem) + wave * G2_WAVE_B;
  c.hsrc = hf + ((size_t)((m0 >> 5) * NKS + 22 * wave) * 64 + lane) * 8;
  c.wd = wd;
  c.wcol = n0 + 8 * (lane & 7);
  c.wrow0 = 352 * wave + (lane >> 3);
  c.ww = c.wb32 + (lane >> 3) * 128 + (((lane & 7) ^ (((lane >> 4) & 1) << 2)) * 16);

  const int r = lane & 31, h = lane >> 5;
  const int blk = (lane >> 4) & 1, q = (lane & 15) >> 2, p = lane & 3;
  c.woff = (4 * h + q) * 128 + (((2 * blk + (p >> 1)) ^ (((q >> 1) & 1) << 2)) * 16) + 8 * (p & 1);

  f32x16 acc[2][2];
#pragma unroll
  for (int i = 0; i < 2; ++i)
#pragma unroll
    for (int j = 0; j < 2; ++j) acc[i][j] = f32x16{};
  bf16x8 hfr[11][4];
  u32x4 wr[11][4];

  G2Step<0>::issue(hfr, wr, c);
  G2Step<1>::issue(hfr, wr, c);
  if constexpr (G2_DEPTH > 2) G2Step<2>::issue(hfr, wr, c);
  if constexpr (G2_DEPTH > 3) G2Step<3>::issue(hfr, wr, c);
  if constexpr (G2_DEPTH > 4) G2Step<4>::issue(hfr, wr, c);
  if constexpr (G2_DEPTH > 5) G2Step<5>::issue(hfr, wr, c);
  if constexpr (G2_DEPTH > 6) G2Step<6>::issue(hfr, wr, c);
  STAMP2(1);
  STAMP2(2);
  G2Step<0>::run(acc, hfr, wr, c);
  STAMP2(3);
  __syncthreads();
  {
    char* pw = smem + wave * (64 * G2_PITCH);
#pragma unroll
    for (int c = 0; c < 2; ++c)
#pragma unroll
      for (int mi = 0; mi < 2; ++mi)
#pragma unroll
        for (int gq = 0; gq < 4; ++gq) {
          const f32x4 v = {acc[c][mi][4 * gq], acc[c][mi][4 * gq + 1], acc[c][mi][4 * gq + 2], acc[c][mi][4 * gq + 3]};
          *(f32x4*)(pw + (32 * mi + r) * G2_PITCH + (32 * c + 8 * gq + 4 * h) * 4) = v;
        }
  }
  __syncthreads();
  STAMP2(4);
  {
    const int m = tid >> 2, nq = (tid & 3) * 16;
    const float g = gin[m0 + m];
    const float omg = rbf(1.0f - g);
    float b[16];
#pragma unroll
    for (int j = 0; j < 4; ++j) {
      f32x4 s = *(const f32x4*)(smem + m * G2_PITCH + (nq + 4 * j) * 4);
#pragma unroll
      for (int w = 1; w < 4; ++w) s += *(const f32x4*)(smem + w * (64 * G2_PITCH) + m * G2_PITCH + (nq + 4 * j) * 4);
#pragma unroll
      for (int e = 0; e < 4; ++e) b[4 * j + e] = s[e];
    }
    const u16* aimg = (const u16*)(smem + G2_A_OFF);
    unsigned o[8];
#pragma unroll
    for (int j = 0; j < 8; ++j) {
      const float a0 = bf2f(aimg[(nq + 2 * j) * 64 + m]);
      const float a1 = bf2f(aimg[(nq + 2 * j + 1) * 64 + m]);
      const float o0 = rbf(g * a0) + rbf(omg * rbf(b[2 * j]));
      const float o1 = rbf(g * a1) + rbf(omg * rbf(b[2 * j + 1]));
      o[j] = pk_bf16(o0, o1);
    }
    u16* op = out + (size_t)(m0 + m) * DM + n0 + nq;
    *(u32x4*)op = u32x4{o[0], o[1], o[2], o[3]};
    *(u32x4*)(op + 8) = u32x4{o[4], o[5], o[6], o[7]};
  }
  STAMP2(5);
}

extern "C" void kernel_launch(void* const* d_in, const int* in_sizes, int n_in, void* d_out, int out_size,
                              void* d_ws, size_t ws_size, hipStream_t stream) {
  const u16* u = (const u16*)d_in[0];
  const u16* logAr = (const u16*)d_in[1];
  const u16* Aim = (const u16*)d_in[2];
  const u16* Cre = (const u16*)d_in[3];
  const u16* Cim = (const u16*)d_in[4];
  const u16* logdt = (const u16*)d_in[5];
  const u16* Dskip = (const u16*)d_in[6];
  const u16* wg = (const u16*)d_in[8];
  const u16* wu = (const u16*)d_in[9];
  const u16* wd = (const u16*)d_in[10];
  const u16* gw = (const u16*)d_in[11];
  char* ws = (char*)d_ws;
  u16* hbuf = (u16*)ws;
  u16* aT = (u16*)(ws + (size_t)SEQ * FFP * 2);
  float* gbuf = (float*)(ws + (size_t)SEQ * FFP * 2 + (size_t)DM * SEQ * 2);
  k1_kernel<<<512, 256, 0, stream>>>(u, logAr, Aim, Cre, Cim, logdt, Dskip, wg, wu, gw, hbuf, aT, gbuf
      );
  k2_kernel<<<256, 256, 0, stream>>>(hbuf, wd, aT, gbuf, (u16*)d_out
      );
}
```

```cpp
#include <hip/hip_runtime.h>
#include <hip/hip_bf16.h>
#include <stdint.h>

typedef unsigned short u16;
typedef __attribute__((ext_vector_type(8))) short bf16x8;
typedef __attribute__((ext_vector_type(4))) short s16x4;
typedef __attribute__((ext_vector_type(4))) float f32x4;
typedef __attribute__((ext_vector_type(16))) float f32x16;
typedef __attribute__((ext_vector_type(4))) unsigned u32x4;
typedef __attribute__((ext_vector_type(2))) unsigned u32x2;
typedef __attribute__((ext_vector_type(2))) __bf16 bf16x2_t;
typedef __attribute__((ext_vector_type(2))) float f32x2;
typedef __attribute__((address_space(3))) s16x4* lds_s16x4_ptr;

constexpr int SEQ = 2048, DM = 512, NST = 128, FF = 1376, FFP = 1408;
constexpr int NSSM = 256;
constexpr int NKS = 88;

__device__ __forceinline__ unsigned pk_bf16(float lo, float hi) {
  f32x2 v = {lo, hi};
  bf16x2_t b = __builtin_convertvector(v, bf16x2_t);
  return __builtin_bit_cast(unsigned, b);
}
__device__ __forceinline__ float bf_lo(unsigned w) { return __uint_as_float(w << 16); }
__device__ __forceinline__ float bf_hi(unsigned w) { return __uint_as_float(w & 0xffff0000u); }
__device__ __forceinline__ float rbf(float x) { return bf_lo(pk_bf16(x, 0.f)); }
__device__ __forceinline__ float bf2f(u16 v) { return __uint_as_float(((unsigned)v) << 16); }

__device__ __forceinline__ bf16x8 mk8(unsigned a, unsigned b, unsigned c, unsigned d) {
  u32x4 v = {a, b, c, d};
  return __builtin_bit_cast(bf16x8, v);
}
__device__ __forceinline__ bf16x8 cat4(s16x4 a, s16x4 b) {
  return __builtin_shufflevector(a, b, 0, 1, 2, 3, 4, 5, 6, 7);
}
__device__ __forceinline__ s16x4 tr_read(const char* smem_ptr) {
  return __builtin_amdgcn_ds_read_tr16_b64_v4i16((lds_s16x4_ptr)(smem_ptr));
}
__device__ __forceinline__ unsigned lds_addr(const void* p) {
  return (unsigned)(size_t)(const __attribute__((address_space(3))) char*)p;
}
#define DS_TR(dst, addr, off) asm volatile("ds_read_b64_tr_b16 %0, %1 offset:%2" : "=v"(dst) : "v"(addr), "i"(off) : "memory")
#define DS_B128(dst, addr, off) asm volatile("ds_read_b128 %0, %1 offset:%2" : "=v"(dst) : "v"(addr), "i"(off) : "memory")
__device__ __forceinline__ void glds16(const void* g, char* l) {
  __builtin_amdgcn_global_load_lds((const unsigned*)g, (unsigned*)l, 16, 0, 0);
}

#define STAMP2(i)
#define DBG2_ARG
#define DBG2_PASS
#define STAMP(i)
#define DBG_ARG
#define DBG_PASS
constexpr int U_COPY_B = 4160;
constexpr int U_CH_B = 4 * U_COPY_B;
constexpr int U_REGION_B = 2 * U_CH_B;
constexpr int KR_PART_B = 4576;
constexpr int KR_OFF = U_REGION_B;
constexpr int KP_B = 8208;
constexpr int SSM_LDS = KR_OFF + 4 * KR_PART_B;

static_assert(4 * KP_B <= U_REGION_B, "Kp scratch must fit the U region");

__device__ __forceinline__ void ssm_unit(int w, const u16* __restrict__ u, const u16* __restrict__ logAr,
                                         const u16* __restrict__ Aim, const u16* __restrict__ Cre,
                                         const u16* __restrict__ Cim, const u16* __restrict__ logdt,
                                         const u16* __restrict__ Dskip, u16* __restrict__ aT, char* smem DBG_ARG) {
  const int tid = threadIdx.x, lane = tid & 63;
  STAMP(0);
  const int wave = __builtin_amdgcn_readfirstlane(tid >> 6);
  const int ch = wave >> 1, w2 = wave & 1;
  const int d0 = 64 * (w & 7) + 2 * (w >> 3);
  const int d = d0 + ch;

  const unsigned* u32p = (const unsigned*)(u + d0);
  unsigned uw[8], up[3];
#pragma unroll
  for (int i = 0; i < 8; ++i) uw[i] = u32p[(size_t)(8 * tid + i) * (DM / 2)];
#pragma unroll
  for (int i = 0; i < 3; ++i) up[i] = 0;
  if (lane == 0 && tid > 0) {
#pragma unroll
    for (int i = 0; i < 3; ++i) up[i] = u32p[(size_t)(8 * tid - 3 + i) * (DM / 2)];
  }

  {
    const int n = tid & 127;
    const int dd = d0 + (tid >> 7);
    const float dt = rbf(expf(bf2f(logdt[dd])));
    const float are = rbf(expf(bf2f(logAr[dd * NST + n])));
    f32x4 prm;
    prm[0] = (-are * dt) * 1.44269504088896340736f;
    prm[1] = (bf2f(Aim[dd * NST + n]) * dt) * 0.15915494309189533577f;
    prm[2] = 2.0f * (bf2f(Cre[dd * NST + n]) * dt);
    prm[3] = 2.0f * (bf2f(Cim[dd * NST + n]) * dt);
    *(f32x4*)(smem + KR_OFF + tid * 16) = prm;
  }
  __syncthreads();
  STAMP(1);

  {
    const int r = lane & 31, h = lane >> 5;
    int p1i;
    if (r < 8) p1i = r;
    else if (r < 12) p1i = 256 + 2 * (r - 8);
    else if (r < 16) p1i = 512 + 256 * ((r - 12) >> 1) + 4 * ((r - 12) & 1);
    else if (r < 21) p1i = 1024 + 256 * (r - 16);
    else p1i = 0;
    const float p1 = (float)p1i, p2 = (float)(8 * r);
    f32x16 acc = {};
    const char* prm_base = smem + KR_OFF + (ch * 128 + 64 * w2 + 4 * h) * 16;
#pragma unroll 2
    for (int ks = 0; ks < 8; ++ks) {
      unsigned ah[4], al[4], bh[4], bl[4];
#pragma unroll
      for (int q = 0; q < 4; ++q) {
        const f32x4 prm = *(const f32x4*)(prm_base + (8 * ks + q) * 16);
        {
          const float e = __builtin_amdgcn_exp2f(prm[0] * p1);
          const float rv = __builtin_amdgcn_fractf(prm[1] * p1);
          const float sn = __builtin_amdgcn_sinf(rv), cs = __builtin_amdgcn_cosf(rv);
          const float gre = e * (prm[2] * cs - prm[3] * sn);
          const float gim = -e * (prm[2] * sn + prm[3] * cs);
          const unsigned hi = pk_bf16(gre, gim);
          ah[q] = hi;
          al[q] = pk_bf16(gre - bf_lo(hi), gim - bf_hi(hi));
        }
        {
          const float e = __builtin_amdgcn_exp2f(prm[0] * p2);
          const float rv = __builtin_amdgcn_fractf(prm[1] * p2);
          const float sn = __builtin_amdgcn_sinf(rv), cs = __builtin_amdgcn_cosf(rv);
          const float ere = e * cs, eim = e * sn;
          const unsigned hi = pk_bf16(ere, eim);
          bh[q] = hi;
          bl[q] = pk_bf16(ere - bf_lo(hi), eim - bf_hi(hi));
        }
      }
      const bf16x8 Ah = mk8(ah[0], ah[1], ah[2], ah[3]), Al = mk8(al[0], al[1], al[2], al[3]);
      const bf16x8 Bh = mk8(bh[0], bh[1], bh[2], bh[3]), Bl = mk8(bl[0], bl[1], bl[2], bl[3]);
      acc = __builtin_amdgcn_mfma_f32_32x32x16_bf16(Al, Bh, acc, 0, 0, 0);
      acc = __builtin_amdgcn_mfma_f32_32x32x16_bf16(Ah, Bl, acc, 0, 0, 0);
      acc = __builtin_amdgcn_mfma_f32_32x32x16_bf16(Ah, Bh, acc, 0, 0, 0);
    }
    float* Kp = (float*)(smem + (ch * 2 + w2) * KP_B);
#pragma unroll
    for (int e = 0; e < 4; ++e) Kp[(4 * h + e) + 8 * r] = acc[e];
#pragma unroll
    for (int e = 0; e < 4; ++e) {
      const int pp = h ? (512 + 256 * (e >> 1) + 4 * (e & 1)) : (256 + 2 * e);
      Kp[pp + 8 * r] = acc[4 + e];
    }
    if (h == 0) {
#pragma unroll
      for (int e = 0; e < 4; ++e) Kp[1024 + 256 * e + 8 * r] = acc[8 + e];
    } else if (r == 0) {
      Kp[2048] = acc[8];
    }
  }
  STAMP(2);
  __syncthreads();

  {
    const int t = tid & 127;
    const float* Kp0 = (const float*)(smem + (ch * 2) * KP_B);
    const float* Kp1 = (const float*)(smem + (ch * 2 + 1) * KP_B);
    char* krh = smem + KR_OFF + (ch * 2) * KR_PART_B;
    char* krl = krh + KR_PART_B;
#pragma unroll
    for (int kk = 0; kk < 2; ++kk) {
      const int k = t + 128 * kk;
      float v[8];
#pragma unroll
      for (int e = 0; e < 8; ++e) {
        const int l = 2047 - 8 * k - e;
        const int p = (int)rbf((float)l);
        v[e] = Kp0[p] + Kp1[p];
      }
      unsigned hi[4], lo[4];
#pragma unroll
      for (int e = 0; e < 4; ++e) {
        hi[e] = pk_bf16(v[2 * e], v[2 * e + 1]);
        lo[e] = pk_bf16(v[2 * e] - bf_lo(hi[e]), v[2 * e + 1] - bf_hi(hi[e]));
      }
      *(u32x4*)(krh + k * 16) = u32x4{hi[0], hi[1], hi[2], hi[3]};
      *(u32x4*)(krl + k * 16) = u32x4{lo[0], lo[1], lo[2], lo[3]};
    }
    if (t < 30) {
      *(u32x4*)(krh + (256 + t) * 16) = u32x4{0, 0, 0, 0};
      *(u32x4*)(krl + (256 + t) * 16) = u32x4{0, 0, 0, 0};
    }
  }
  __syncthreads();
  STAMP(3);

  {
    unsigned e[11];
#pragma unroll
    for (int i = 0; i < 3; ++i) {
      const unsigned nb = __shfl_up(uw[5 + i], 1);
      e[i] = (lane == 0) ? up[i] : nb;
    }
#pragma unroll
    for (int i = 0; i < 8; ++i) e[3 + i] = uw[i];
    unsigned p0[10], p1[10];
#pragma unroll
    for (int i = 0; i < 10; ++i) {
      p0[i] = __builtin_amdgcn_perm(e[i + 1], e[i], 0x05040100u);
      p1[i] = __builtin_amdgcn_perm(e[i + 1], e[i], 0x07060302u);
    }
#pragma unroll
    for (int c = 0; c < 4; ++c) {
      *(u32x4*)(smem + c * U_COPY_B + (tid + 2) * 16) = u32x4{p0[c], p0[c + 2], p0[c + 4], p0[c + 6]};
      *(u32x4*)(smem + U_CH_B + c * U_COPY_B + (tid + 2) * 16) = u32x4{p1[c], p1[c + 2], p1[c + 4], p1[c + 6]};
    }
    if (tid < 16) *(u32x4*)(smem + (tid >> 1) * U_COPY_B + (tid & 1) * 16) = u32x4{0, 0, 0, 0};
  }
  __syncthreads();
  STAMP(4);

  f32x4 acc[8];
#pragma unroll
  for (int t = 0; t < 8; ++t) acc[t] = f32x4{0.f, 0.f, 0.f, 0.f};
  {
    const int r = lane & 15, hq = lane >> 4;
    const char* ua = smem + ch * U_CH_B + (r & 3) * U_COPY_B + 2 * (4 + (r & ~3) + 8 * hq);
    const char* kb = smem + KR_OFF + (ch * 2) * KR_PART_B + 2 * (2032 - 16 * r + 8 * hq);
#pragma unroll 1
    for (int ri = 0; ri < 4; ++ri) {
      const int rho = 4 * w2 + ri;
      bf16x8 A[8];
#pragma unroll
      for (int sg = 0; sg < 8; ++sg) {
        const char* pa = ua + 64 * (8 * sg + rho);
        A[sg] = cat4(*(const s16x4*)pa, *(const s16x4*)(pa + 8));
      }
#pragma unroll
      for (int tau = 0; tau < 8; ++tau) {
        const char* pb = kb - 64 * (8 * tau - rho);
        const bf16x8 Bh = *(const bf16x8*)pb;
        const bf16x8 Bl = *(const bf16x8*)(pb + KR_PART_B);
#pragma unroll
        for (int sg = 0; sg + tau < 8; ++sg) {
          acc[sg + tau] = __builtin_amdgcn_mfma_f32_16x16x32_bf16(A[sg], Bl, acc[sg + tau], 0, 0, 0);
          acc[sg + tau] = __builtin_amdgcn_mfma_f32_16x16x32_bf16(A[sg], Bh, acc[sg + tau], 0, 0, 0);
        }
      }
    }
  }
  STAMP(5);
  __syncthreads();
  {
    char* xw = smem + KR_OFF + (ch * 2 + w2) * 4096 + lane * 16;
    const char* xr = smem + KR_OFF + (ch * 2 + (w2 ^ 1)) * 4096 + lane * 16;
    f32x4 y[4];
    if (w2 == 0) {
#pragma unroll
      for (int i = 0; i < 4; ++i) { *(f32x4*)(xw + i * 1024) = acc[4 + i]; y[i] = acc[i]; }
    } else {
#pragma unroll
      for (int i = 0; i < 4; ++i) { *(f32x4*)(xw + i * 1024) = acc[i]; y[i] = acc[4 + i]; }
    }
    __syncthreads();
    const float Dk = bf2f(Dskip[d]);
#pragma unroll
    for (int i = 0; i < 4; ++i) {
      const f32x4 o = *(const f32x4*)(xr + i * 1024);
      const int l0 = 256 * (4 * w2 + i) + 16 * (lane & 15) + 4 * (lane >> 4);
      const u32x2 uu = *(const u32x2*)(smem + ch * U_CH_B + 3 * U_COPY_B + 2 * (16 + l0));
      const float a0 = rbf(y[i][0] + o[0]) + rbf(bf_lo(uu[0]) * Dk);
      const float a1 = rbf(y[i][1] + o[1]) + rbf(bf_hi(uu[0]) * Dk);
      const float a2 = rbf(y[i][2] + o[2]) + rbf(bf_lo(uu[1]) * Dk);
      const float a3 = rbf(y[i][3] + o[3]) + rbf(bf_hi(uu[1]) * Dk);
      *(u32x2*)(aT + (size_t)d * SEQ + l0) = u32x2{pk_bf16(a0, a1), pk_bf16(a2, a3)};
    }
  }
  STAMP(6);
}

__device__ __forceinline__ float gelu_tanh(float x) {
  const float z = 0.7978845608028654f * (x + 0.044715f * x * x * x);
  const float e = __builtin_amdgcn_exp2f(-2.8853900817779268f * z);
  return x * __builtin_amdgcn_rcpf(1.0f + e);
}

constexpr int G1_WSLOT_B = 12288;
constexpr int G1_X_OFF = 3 * G1_WSLOT_B;
constexpr int G1_LDS = G1_X_OFF + 32768;

constexpr int g1_pos(bool isx, int id) {
  if (isx && id == 0) return 4;
  if (!isx && id == 0) return 7;
  if (!isx && id == 1) return 10;
  if (isx && id == 1) return 14;
  if (!isx && id == 2) return 17;
  if (!isx && id == 3) return 20;
  if (!isx && id == 4) return 23;
  if (isx && id == 2) return 27;
  int n = 27;
  for (int t = 0; t < 32; ++t) {
    if ((t & 1) == 0 && t / 2 + 5 < 16) { n += 3; if (!isx && t / 2 + 5 == id) return n; }
    if ((t & 3) == 1 && t / 4 + 3 < 8) { n += 4; if (isx && t / 4 + 3 == id) return n; }
  }
  return -1;
}
constexpr int g1_issued_before(int step) {
  int n = 27;
  for (int t = 0; t < step; ++t) {
    if ((t & 1) == 0 && t / 2 + 5 < 16) n += 3;
    if ((t & 3) == 1 && t / 4 + 3 < 8) n += 4;
  }
  return n;
}

#define GLOAD128(dst, ptr) asm volatile("global_load_dwordx4 %0, %1, off" : "=v"(dst) : "v"(ptr) : "memory")
#define GLOAD128S(dst, voff, sbase) asm volatile("global_load_dwordx4 %0, %1, %2" : "=v"(dst) : "v"(voff), "s"(sbase) : "memory")
#define GLOAD128SA(dst, voff, sbase) asm volatile("global_load_dwordx4 %0, %1, %2" : "=a"(dst) : "v"(voff), "s"(sbase) : "memory")
#define GLOAD128A(dst, ptr) asm volatile("global_load_dwordx4 %0, %1, off" : "=a"(dst) : "v"(ptr) : "memory")
#define DS_W128(addr, val, off) asm volatile("ds_write_b128 %0, %1 offset:%2" ::"v"(addr), "v"(val), "i"(off) : "memory")
#define MFMA32(acc, a, b) asm volatile("v_mfma_f32_32x32x16_bf16 %0, %1, %2, %0" : "+v"(acc) : "v"(a), "v"(b))

struct G1Ctx {
  const char* wbase;
  const char* xbase;
  unsigned wo[3], xo;
  unsigned woff, woff_o, ww[3], xw, xrd[4];
};
struct G1Frag {
  s16x4 w[3][2];
  bf16x8 x[2];
};

#define G1_RD_W(f, t, OFF)                                                        \
  do {                                                                            \
    if ((t) & 1) { DS_TR(f.w[t][0], c.woff_o, (OFF) + (t) * 64); DS_TR(f.w[t][1], c.woff_o, (OFF) + (t) * 64 + 4 * 384); } \
    else { DS_TR(f.w[t][0], c.woff, (OFF) + (t) * 64); DS_TR(f.w[t][1], c.woff, (OFF) + (t) * 64 + 4 * 384); }            \
  } while (0)
#define G1_RD_X(f, mi, N) DS_B128(f.x[mi], c.xrd[(N) & 3], (((N) >> 2) & 1) * 16384 + (mi) * 4096)
template <int N>
__device__ __forceinline__ void g1_read(G1Frag& f, const G1Ctx& c) {
  constexpr int OFF = ((N >> 1) % 3) * G1_WSLOT_B + (N & 1) * 16 * 384;
  G1_RD_X(f, 0, N); G1_RD_X(f, 1, N);
  G1_RD_W(f, 0, OFF); G1_RD_W(f, 1, OFF); G1_RD_W(f, 2, OFF);
}
#define G1_WAIT_FRAG(f, STR)                                                                                       \
  asm volatile(STR : "+v"(f.w[0][0]), "+v"(f.w[0][1]), "+v"(f.w[1][0]), "+v"(f.w[1][1]), "+v"(f.w[2][0]),        \
               "+v"(f.w[2][1]), "+v"(f.x[0]), "+v"(f.x[1])::"memory")

template <int N>
struct G1Step {
  static __device__ __forceinline__ void run(f32x16 (&acc)[6], u32x4 (&xr)[2][4], u32x4 (&wr)[3][3], G1Frag& cur,
                                             G1Frag& nxt, G1Ctx& c) {
    constexpr int ISS = g1_issued_before(N);
    constexpr int S = N >> 1;
    constexpr bool RD = N + 1 < 32;
    constexpr int OFF = (((N + 1) >> 1) % 3) * G1_WSLOT_B + ((N + 1) & 1) * 16 * 384;
    if constexpr ((N & 1) == 0 && N > 0) G1_WAIT_FRAG(cur, "s_waitcnt lgkmcnt(0)\n\ts_barrier");
    else G1_WAIT_FRAG(cur, "s_waitcnt lgkmcnt(0)");
    bf16x8 b[3];
#pragma unroll
    for (int t = 0; t < 3; ++t) b[t] = cat4(cur.w[t][0], cur.w[t][1]);
    MFMA32(acc[0], b[0], cur.x[0]);
    if constexpr (RD) { G1_RD_X(nxt, 0, N + 1); G1_RD_W(nxt, 0, OFF); }
    MFMA32(acc[3], b[0], cur.x[1]);
    if constexpr (RD) { G1_RD_X(nxt, 1, N + 1); G1_RD_W(nxt, 1, OFF); }
    MFMA32(acc[1], b[1], cur.x[0]);
    if constexpr (RD) { G1_RD_W(nxt, 2, OFF); }
    MFMA32(acc[4], b[1], cur.x[1]);
    if constexpr ((N & 1) == 0) {
      if constexpr (S + 2 < 16) {
        constexpr int R = (S + 2) % 3;
        constexpr int NW = ISS - g1_pos(false, S + 2);
        asm volatile("s_waitcnt vmcnt(%3)" : "+v"(wr[R][0]), "+v"(wr[R][1]), "+v"(wr[R][2]) : "i"(NW) : "memory");
#pragma unroll
        for (int i = 0; i < 3; ++i) DS_W128(c.ww[i], wr[R][i], R * G1_WSLOT_B);
      }
    }
    if constexpr ((N & 3) == 1) {
      constexpr int B = N / 4 + 1;
      if constexpr (B < 8) {
        constexpr int NW = ISS - g1_pos(true, B);
        asm volatile("s_waitcnt vmcnt(%4)" : "+v"(xr[B & 1][0]), "+v"(xr[B & 1][1]), "+v"(xr[B & 1][2]), "+v"(xr[B & 1][3]) : "i"(NW) : "memory");
#pragma unroll
        for (int i = 0; i < 4; ++i) DS_W128(c.xw, xr[B & 1][i], (B & 1) * 16384 + i * 1024);
      }
    }
    MFMA32(acc[2], b[2], cur.x[0]);
    if constexpr ((N & 1) == 0 && S + 5 < 16) {
#pragma unroll
      for (int i = 0; i < 3; ++i) GLOAD128S(wr[(S + 5) % 3][i], c.wo[i], c.wbase + (size_t)(S + 5) * 32 * FF * 2);
    }
    if constexpr ((N & 3) == 1 && N / 4 + 3 < 8) {
      constexpr int B = N / 4 + 1;
#pragma unroll
      for (int i = 0; i < 4; ++i) GLOAD128S(xr[B & 1][i], c.xo, c.xbase + (size_t)(8 * i) * DM * 2 + (B + 2) * 128);
    }
    MFMA32(acc[5], b[2], cur.x[1]);
    if constexpr (N + 1 < 32) G1Step<N + 1>::run(acc, xr, wr, nxt, cur, c);
  }
};

__device__ __forceinline__ void gemm1_tile(int t, const u16* __restrict__ x, const u16* __restrict__ wg,
                                           const u16* __restrict__ wu, u16* __restrict__ hf, char* smem DBG_ARG) {
  const int tid = threadIdx.x, lane = tid & 63;
  const int wave = __builtin_amdgcn_readfirstlane(tid >> 6);
  const int fb = t >> 4, mb = t & 15;
  const int f0 = 96 * fb;
  const int wm = wave & 1, isup = wave >> 1;
  G1Ctx c;
  const unsigned sbase = lds_addr(smem);
  c.wbase = (const char*)(isup ? wu : wg);
#pragma unroll
  for (int i = 0; i < 3; ++i) {
    const int id = 192 * (wave & 1) + 64 * i + lane;
    const int row = id / 12, cc = id % 12;
    int col = f0 + 8 * cc;
    if (col > FF - 8) col = FF - 8;
    c.wo[i] = (unsigned)(row * FF + col) * 2u;
    c.ww[i] = sbase + row * 384 + ((cc + 12 * isup) ^ (((row >> 1) & 1) << 2)) * 16;
  }
  c.xbase = (const char*)(x + (size_t)(128 * mb + 32 * wave) * DM);
  c.xo = (unsigned)((lane >> 3) * DM + (lane & 7) * 8) * 2u;
  const unsigned xl = sbase + G1_X_OFF;
  c.xw = xl + (32 * wave + (lane >> 3)) * 128 + (((lane & 7) ^ (lane >> 3)) * 16);
  const int r = lane & 31, h = lane >> 5;
#pragma unroll
  for (int i = 0; i < 4; ++i) c.xrd[i] = xl + (64 * wm + r) * 128 + (((2 * i + h) ^ (r & 7)) * 16);
  const int blk = (lane >> 4) & 1, q = (lane & 15) >> 2, p = lane & 3;
  const int sw = (q >> 1) & 1;
  {
    const unsigned e = sbase + (8 * h + q) * 384 + (2 * blk + (p >> 1) + 4 * sw) * 16 + 8 * (p & 1);
    const unsigned o = sbase + (8 * h + q) * 384 + (2 * blk + (p >> 1) - 4 * sw) * 16 + 8 * (p & 1);
    c.woff = isup ? o + 192 : e;
    c.woff_o = isup ? e + 192 : o;
  }

  f32x16 acc[6];
#pragma unroll
  for (int i = 0; i < 6; ++i) acc[i] = f32x16{};
  asm volatile("s_nop 4" : "+v"(acc[0]), "+v"(acc[1]), "+v"(acc[2]), "+v"(acc[3]), "+v"(acc[4]), "+v"(acc[5]));
  u32x4 xr[2][4], wr[3][3];
  G1Frag fa, fb2;
#define G1_ISSUE_X(R, B) _Pragma("unroll") for (int i = 0; i < 4; ++i) GLOAD128S(xr[R][i], c.xo, c.xbase + (size_t)(8 * i) * DM * 2 + (B) * 128)
#define G1_ISSUE_W(R, S) _Pragma("unroll") for (int i = 0; i < 3; ++i) GLOAD128S(wr[R][i], c.wo[i], c.wbase + (size_t)(S) * 32 * FF * 2)
  G1_ISSUE_X(0, 0); G1_ISSUE_W(0, 0); G1_ISSUE_W(1, 1); G1_ISSUE_X(1, 1); G1_ISSUE_W(2, 2);
  asm volatile("s_waitcnt vmcnt(7)"
               : "+v"(wr[0][0]), "+v"(wr[0][1]), "+v"(wr[0][2]), "+v"(wr[1][0]), "+v"(wr[1][1]), "+v"(wr[1][2]),
                 "+v"(xr[0][0]), "+v"(xr[0][1]), "+v"(xr[0][2]), "+v"(xr[0][3])::"memory");
#pragma unroll
  for (int i = 0; i < 3; ++i) { DS_W128(c.ww[i], wr[0][i], 0); DS_W128(c.ww[i], wr[1][i], G1_WSLOT_B); }
#pragma unroll
  for (int i = 0; i < 4; ++i) DS_W128(c.xw, xr[0][i], i * 1024);
  G1_ISSUE_W(0, 3); G1_ISSUE_W(1, 4); G1_ISSUE_X(0, 2);
  asm volatile("s_waitcnt lgkmcnt(0)\n\ts_barrier" ::: "memory");
  g1_read<0>(fa, c);
  G1Step<0>::run(acc, xr, wr, fa, fb2, c);
  asm volatile("s_nop 15\n\ts_nop 15" : "+v"(acc[0]), "+v"(acc[1]), "+v"(acc[2]), "+v"(acc[3]), "+v"(acc[4]), "+v"(acc[5]));

  unsigned keep[3][8], give[3][8];
#pragma unroll
  for (int tt = 0; tt < 3; ++tt)
#pragma unroll
    for (int e = 0; e < 8; ++e) {
      const unsigned p0 = pk_bf16(acc[tt][2 * e], acc[tt][2 * e + 1]);
      const unsigned p1 = pk_bf16(acc[3 + tt][2 * e], acc[3 + tt][2 * e + 1]);
      keep[tt][e] = isup ? p1 : p0;
      give[tt][e] = isup ? p0 : p1;
    }
  __syncthreads();
  {
    char* xo = smem + wave * 6144 + lane * 16;
#pragma unroll
    for (int tt = 0; tt < 3; ++tt) {
      *(u32x4*)(xo + (2 * tt) * 1024) = u32x4{give[tt][0], give[tt][1], give[tt][2], give[tt][3]};
      *(u32x4*)(xo + (2 * tt + 1) * 1024) = u32x4{give[tt][4], give[tt][5], give[tt][6], give[tt][7]};
    }
  }
  __syncthreads();
  {
    const char* xi = smem + (wave ^ 2) * 6144 + lane * 16;
    const int mt = 4 * mb + 2 * wm + isup;
#pragma unroll
    for (int tt = 0; tt < 3; ++tt) {
      const int ft = 3 * fb + tt;
      if (ft < NKS / 2) {
        const bool valid = ft < FF / 32;
#pragma unroll
        for (int i = 0; i < 2; ++i) {
          const u32x4 ot = *(const u32x4*)(xi + (2 * tt + i) * 1024);
          unsigned o[4];
#pragma unroll
          for (int e = 0; e < 4; ++e) {
            const unsigned gp = isup ? ot[e] : keep[tt][4 * i + e];
            const unsigned up = isup ? keep[tt][4 * i + e] : ot[e];
            const unsigned ge = pk_bf16(gelu_tanh(bf_lo(gp)), gelu_tanh(bf_hi(gp)));
            o[e] = valid ? pk_bf16(bf_lo(ge) * bf_lo(up), bf_hi(ge) * bf_hi(up)) : 0u;
          }
          *(u32x4*)(hf + ((size_t)(mt * NKS + 2 * ft + i) * 64 + lane) * 8) = u32x4{o[0], o[1], o[2], o[3]};
        }
      }
    }
  }
}

__device__ __forceinline__ void gate_unit(int b, const u16* __restrict__ x, const u16* __restrict__ gw,
                                          float* __restrict__ gout) {
  const int tid = threadIdx.x, lane = tid & 63, wave = tid >> 6;
  const int j = lane & 7, rr = lane >> 3;
  u32x4 gv[8];
#pragma unroll
  for (int i = 0; i < 8; ++i) gv[i] = *(const u32x4*)(gw + (j + 8 * i) * 8);
  const int row0 = b * 128 + wave * 32 + rr;
  u32x4 xv[4][8];
#pragma unroll
  for (int ps = 0; ps < 4; ++ps)
#pragma unroll
    for (int i = 0; i < 8; ++i) xv[ps][i] = *(const u32x4*)(x + (size_t)(row0 + 8 * ps) * DM + (j + 8 * i) * 8);
#pragma unroll
  for (int ps = 0; ps < 4; ++ps) {
    float s0 = 0.f, s1 = 0.f;
#pragma unroll
    for (int i = 0; i < 8; ++i)
#pragma unroll
      for (int e = 0; e < 4; ++e) {
        const unsigned pr = pk_bf16(bf_lo(xv[ps][i][e]) * bf_lo(gv[i][e]), bf_hi(xv[ps][i][e]) * bf_hi(gv[i][e]));
        s0 += bf_lo(pr);
        s1 += bf_hi(pr);
      }
    float s = s0 + s1;
    s += __shfl_xor(s, 1);
    s += __shfl_xor(s, 2);
    s += __shfl_xor(s, 4);
    if (j == 0) {
      const float sb = rbf(s);
      gout[row0 + 8 * ps] = rbf(1.0f / (1.0f + expf(-sb)));
    }
  }
}

__global__ __launch_bounds__(256, 2) void k1_kernel(const u16* __restrict__ u, const u16* __restrict__ logAr,
                                                   const u16* __restrict__ Aim, const u16* __restrict__ Cre,
                                                   const u16* __restrict__ Cim, const u16* __restrict__ logdt,
                                                   const u16* __restrict__ Dskip, const u16* __restrict__ wg,
                                                   const u16* __restrict__ wu, const u16* __restrict__ gw,
                                                   u16* __restrict__ hbuf, u16* __restrict__ aT,
                                                   float* __restrict__ gout DBG_ARG
                                                   ) {
  __shared__ __attribute__((aligned(4096))) char smem[G1_LDS > SSM_LDS ? G1_LDS : SSM_LDS];
  const int bid = blockIdx.x;
  if (bid < NSSM) {
    ssm_unit(bid, u, logAr, Aim, Cre, Cim, logdt, Dskip, aT, smem DBG_PASS);
  } else {
    const int xcd = bid & 7, idx = (bid - NSSM) >> 3;
    if (idx < 30) gemm1_tile(30 * xcd + idx, u, wg, wu, hbuf, smem DBG_PASS);
    else gate_unit(2 * xcd + idx - 30, u, gw, gout);
  }
}

constexpr int G2_SLOT_B = 4096;
constexpr int G2_WAVE_B = 2 * G2_SLOT_B;
constexpr int G2_PITCH = 272;
constexpr int G2_A_OFF = 4 * 64 * G2_PITCH;
constexpr int K2_LDS = G2_A_OFF + 8192;
#define G2D 3
constexpr int G2_DEPTH = G2D;

struct G2Ctx {
  const u16* hsrc;
  const u16* wd;
  int wrow0, wcol;
  unsigned wb32, woff, ww;
};

template <int ST>
struct G2Step {
  static __device__ __forceinline__ void issue(bf16x8 (&hfr)[11][4], u32x4 (&wr)[11][4], const G2Ctx& c) {
#pragma unroll
    for (int i = 0; i < 4; ++i) {
      int kr = c.wrow0 + ST * 32 + 8 * i;
      kr = kr < FF ? kr : FF - 1;
      GLOAD128(wr[ST][i], c.wd + (size_t)kr * DM + c.wcol);
    }
#pragma unroll
    for (int mi = 0; mi < 2; ++mi)
#pragma unroll
      for (int i = 0; i < 2; ++i)
        GLOAD128A(hfr[ST][2 * mi + i], c.hsrc + (size_t)(mi * NKS + 2 * ST + i) * 512);

  }
  static __device__ __forceinline__ void run(f32x16 (&acc)[2][2], bf16x8 (&hfr)[11][4], u32x4 (&wr)[11][4], const G2Ctx& c) {
    constexpr int LAST = (ST + G2_DEPTH - 1 < 10) ? ST + G2_DEPTH - 1 : 10;
    constexpr int NW = 8 * (LAST - ST);
    asm volatile("s_waitcnt vmcnt(%4)" : "+v"(wr[ST][0]), "+v"(wr[ST][1]), "+v"(wr[ST][2]), "+v"(wr[ST][3]) : "i"(NW + 4) : "memory");
#pragma unroll
    for (int i = 0; i < 4; ++i) DS_W128(c.ww, wr[ST][i], (ST & 1) * G2_SLOT_B + i * 1024);
    asm volatile("s_waitcnt lgkmcnt(0)" ::: "memory");
    const unsigned sb = c.wb32 + c.woff;
    s16x4 w[2][2][2];
#pragma unroll
    for (int i = 0; i < 2; ++i)
#pragma unroll
      for (int t = 0; t < 2; ++t) {
        DS_TR(w[i][t][0], sb ^ (t * 64), (ST & 1) * G2_SLOT_B + i * 2048);
        DS_TR(w[i][t][1], sb ^ (t * 64), (ST & 1) * G2_SLOT_B + i * 2048 + 1024);
      }
    asm volatile("s_waitcnt vmcnt(%4)" : "+a"(hfr[ST][0]), "+a"(hfr[ST][1]), "+a"(hfr[ST][2]), "+a"(hfr[ST][3]) : "i"(NW) : "memory");
    if constexpr (ST + G2_DEPTH <= 10) G2Step<ST + G2_DEPTH>::issue(hfr, wr, c);
    asm volatile("s_waitcnt lgkmcnt(0)"
                 : "+v"(w[0][0][0]), "+v"(w[0][0][1]), "+v"(w[0][1][0]), "+v"(w[0][1][1]), "+v"(w[1][0][0]), "+v"(w[1][0][1]),
                   "+v"(w[1][1][0]), "+v"(w[1][1][1])::"memory");
#pragma unroll
    for (int i = 0; i < 2; ++i)
#pragma unroll
      for (int t = 0; t < 2; ++t)
#pragma unroll
        for (int mi = 0; mi < 2; ++mi)
          acc[t][mi] = __builtin_amdgcn_mfma_f32_32x32x16_bf16(cat4(w[i][t][0], w[i][t][1]), hfr[ST][2 * mi + i], acc[t][mi], 0, 0, 0);
    if constexpr (ST + 1 <= 10) G2Step<ST + 1>::run(acc, hfr, wr, c);
  }
};

__global__ __launch_bounds__(256, 1) void k2_kernel(const u16* __restrict__ hf, const u16* __restrict__ wd,
                                                   const u16* __restrict__ aT, const float* __restrict__ gin,
                                                   u16* __restrict__ out DBG2_ARG) {
  __shared__ __attribute__((aligned(4096))) char smem[K2_LDS];
  const int tid = threadIdx.x, lane = tid & 63;
  STAMP2(0);
  const int wave = __builtin_amdgcn_readfirstlane(tid >> 6);
  const int bid = blockIdx.x;
  const int xcd = bid & 7, idx = bid >> 3;
  const int m0 = 64 * (4 * xcd + (idx >> 3)), n0 = 64 * (idx & 7);

#pragma unroll
  for (int i = 0; i < 2; ++i) {
    const int piece = 2 * wave + i;
    const int drow = 8 * piece + (lane >> 3);
    glds16(aT + (size_t)(n0 + drow) * SEQ + m0 + (lane & 7) * 8, smem + G2_A_OFF + piece * 1024 + lane * 16);
  }

  G2Ctx c;
  c.wb32 = lds_addr(smem) + wave * G2_WAVE_B;
  c.hsrc = hf + ((size_t)((m0 >> 5) * NKS + 22 * wave) * 64 + lane) * 8;
  c.wd = wd;
  c.wcol = n0 + 8 * (lane & 7);
  c.wrow0 = 352 * wave + (lane >> 3);
  c.ww = c.wb32 + (lane >> 3) * 128 + (((lane & 7) ^ (((lane >> 4) & 1) << 2)) * 16);

  const int r = lane & 31, h = lane >> 5;
  const int blk = (lane >> 4) & 1, q = (lane & 15) >> 2, p = lane & 3;
  c.woff = (4 * h + q) * 128 + (((2 * blk + (p >> 1)) ^ (((q >> 1) & 1) << 2)) * 16) + 8 * (p & 1);

  f32x16 acc[2][2];
#pragma unroll
  for (int i = 0; i < 2; ++i)
#pragma unroll
    for (int j = 0; j < 2; ++j) acc[i][j] = f32x16{};
  bf16x8 hfr[11][4];
  u32x4 wr[11][4];

  G2Step<0>::issue(hfr, wr, c);
  G2Step<1>::issue(hfr, wr, c);
  if constexpr (G2_DEPTH > 2) G2Step<2>::issue(hfr, wr, c);
  if constexpr (G2_DEPTH > 3) G2Step<3>::issue(hfr, wr, c);
  if constexpr (G2_DEPTH > 4) G2Step<4>::issue(hfr, wr, c);
  if constexpr (G2_DEPTH > 5) G2Step<5>::issue(hfr, wr, c);
  if constexpr (G2_DEPTH > 6) G2Step<6>::issue(hfr, wr, c);
  STAMP2(1);
  STAMP2(2);
  G2Step<0>::run(acc, hfr, wr, c);
  STAMP2(3);
  __syncthreads();
  {
    char* pw = smem + wave * (64 * G2_PITCH);
#pragma unroll
    for (int c = 0; c < 2; ++c)
#pragma unroll
      for (int mi = 0; mi < 2; ++mi)
#pragma unroll
        for (int gq = 0; gq < 4; ++gq) {
          const f32x4 v = {acc[c][mi][4 * gq], acc[c][mi][4 * gq + 1], acc[c][mi][4 * gq + 2], acc[c][mi][4 * gq + 3]};
          *(f32x4*)(pw + (32 * mi + r) * G2_PITCH + (32 * c + 8 * gq + 4 * h) * 4) = v;
        }
  }
  __syncthreads();
  STAMP2(4);
  {
    const int m = tid >> 2, nq = (tid & 3) * 16;
    const float g = gin[m0 + m];
    const float omg = rbf(1.0f - g);
    float b[16];
#pragma unroll
    for (int j = 0; j < 4; ++j) {
      f32x4 s = *(const f32x4*)(smem + m * G2_PITCH + (nq + 4 * j) * 4);
#pragma unroll
      for (int w = 1; w < 4; ++w) s += *(const f32x4*)(smem + w * (64 * G2_PITCH) + m * G2_PITCH + (nq + 4 * j) * 4);
#pragma unroll
      for (int e = 0; e < 4; ++e) b[4 * j + e] = s[e];
    }
    const u16* aimg = (const u16*)(smem + G2_A_OFF);
    unsigned o[8];
#pragma unroll
    for (int j = 0; j < 8; ++j) {
      const float a0 = bf2f(aimg[(nq + 2 * j) * 64 + m]);
      const float a1 = bf2f(aimg[(nq + 2 * j + 1) * 64 + m]);
      const float o0 = rbf(g * a0) + rbf(omg * rbf(b[2 * j]));
      const float o1 = rbf(g * a1) + rbf(omg * rbf(b[2 * j + 1]));
      o[j] = pk_bf16(o0, o1);
    }
    u16* op = out + (size_t)(m0 + m) * DM + n0 + nq;
    *(u32x4*)op = u32x4{o[0], o[1], o[2], o[3]};
    *(u32x4*)(op + 8) = u32x4{o[4], o[5], o[6], o[7]};
  }
  STAMP2(5);
}

extern "C" void kernel_launch(void* const* d_in, const int* in_sizes, int n_in, void* d_out, int out_size,
                              void* d_ws, size_t ws_size, hipStream_t stream) {
  const u16* u = (const u16*)d_in[0];
  const u16* logAr = (const u16*)d_in[1];
  const u16* Aim = (const u16*)d_in[2];
  const u16* Cre = (const u16*)d_in[3];
  const u16* Cim = (const u16*)d_in[4];
  const u16* logdt = (const u16*)d_in[5];
  const u16* Dskip = (const u16*)d_in[6];
  const u16* wg = (const u16*)d_in[8];
  const u16* wu = (const u16*)d_in[9];
  const u16* wd = (const u16*)d_in[10];
  const u16* gw = (const u16*)d_in[11];
  char* ws = (char*)d_ws;
  u16* hbuf = (u16*)ws;
  u16* aT = (u16*)(ws + (size_t)SEQ * FFP * 2);
  float* gbuf = (float*)(ws + (size_t)SEQ * FFP * 2 + (size_t)DM * SEQ * 2);
  k1_kernel<<<512, 256, 0, stream>>>(u, logAr, Aim, Cre, Cim, logdt, Dskip, wg, wu, gw, hbuf, aT, gbuf
      );
  k2_kernel<<<256, 256, 0, stream>>>(hbuf, wd, aT, gbuf, (u16*)d_out
      );
}
```

```cpp
#include <hip/hip_runtime.h>
#include <hip/hip_bf16.h>
#include <stdint.h>
#include <type_traits>

typedef unsigned short u16;
typedef __attribute__((ext_vector_type(8))) short bf16x8;
typedef __attribute__((ext_vector_type(4))) short s16x4;
typedef __attribute__((ext_vector_type(4))) float f32x4;
typedef __attribute__((ext_vector_type(16))) float f32x16;
typedef __attribute__((ext_vector_type(4))) unsigned u32x4;
typedef __attribute__((ext_vector_type(2))) unsigned u32x2;
typedef __attribute__((ext_vector_type(2))) __bf16 bf16x2_t;
typedef __attribute__((ext_vector_type(2))) float f32x2;
typedef __attribute__((address_space(3))) s16x4* lds_s16x4_ptr;

constexpr int SEQ = 2048, DM = 512, NST = 128, FF = 1376, FFP = 1408;
constexpr int NSSM = 256;
constexpr int NKS = 88;

__device__ __forceinline__ unsigned pk_bf16(float lo, float hi) {
  f32x2 v = {lo, hi};
  bf16x2_t b = __builtin_convertvector(v, bf16x2_t);
  return __builtin_bit_cast(unsigned, b);
}
__device__ __forceinline__ float bf_lo(unsigned w) { return __uint_as_float(w << 16); }
__device__ __forceinline__ float bf_hi(unsigned w) { return __uint_as_float(w & 0xffff0000u); }
__device__ __forceinline__ float rbf(float x) { return bf_lo(pk_bf16(x, 0.f)); }
__device__ __forceinline__ float bf2f(u16 v) { return __uint_as_float(((unsigned)v) << 16); }

__device__ __forceinline__ bf16x8 mk8(unsigned a, unsigned b, unsigned c, unsigned d) {
  u32x4 v = {a, b, c, d};
  return __builtin_bit_cast(bf16x8, v);
}
__device__ __forceinline__ bf16x8 cat4(s16x4 a, s16x4 b) {
  return __builtin_shufflevector(a, b, 0, 1, 2, 3, 4, 5, 6, 7);
}
__device__ __forceinline__ s16x4 tr_read(const char* smem_ptr) {
  return __builtin_amdgcn_ds_read_tr16_b64_v4i16((lds_s16x4_ptr)(smem_ptr));
}
__device__ __forceinline__ unsigned lds_addr(const void* p) {
  return (unsigned)(size_t)(const __attribute__((address_space(3))) char*)p;
}
#define DS_TR(dst, addr, off) asm volatile("ds_read_b64_tr_b16 %0, %1 offset:%2" : "=v"(dst) : "v"(addr), "i"(off) : "memory")
#define DS_B128(dst, addr, off) asm volatile("ds_read_b128 %0, %1 offset:%2" : "=v"(dst) : "v"(addr), "i"(off) : "memory")
__device__ __forceinline__ void glds16(const void* g, char* l) {
  __builtin_amdgcn_global_load_lds((const unsigned*)g, (unsigned*)l, 16, 0, 0);
}

#define STAMP2(i)
#define DBG2_ARG
#define DBG2_PASS
#define STAMP(i)
#define DBG_ARG
#define DBG_PASS
constexpr int U_COPY_B = 4160;
constexpr int U_CH_B = 4 * U_COPY_B;
constexpr int U_REGION_B = 2 * U_CH_B;
constexpr int KR_PART_B = 4576;
constexpr int KR_OFF = U_REGION_B;
constexpr int KP_B = 8208;
constexpr int SSM_LDS = KR_OFF + 4 * KR_PART_B;

static_assert(4 * KP_B <= U_REGION_B, "Kp scratch must fit the U region");

__device__ __forceinline__ void ssm_unit(int w, const u16* __restrict__ u, const u16* __restrict__ logAr,
                                         const u16* __restrict__ Aim, const u16* __restrict__ Cre,
                                         const u16* __restrict__ Cim, const u16* __restrict__ logdt,
                                         const u16* __restrict__ Dskip, u16* __restrict__ aT, char* smem DBG_ARG) {
  const int tid = threadIdx.x, lane = tid & 63;
  STAMP(0);
  const int wave = __builtin_amdgcn_readfirstlane(tid >> 6);
  const int ch = wave >> 1, w2 = wave & 1;
  const int d0 = 64 * (w & 7) + 2 * (w >> 3);
  const int d = d0 + ch;

  const unsigned* u32p = (const unsigned*)(u + d0);
  unsigned uw[8], up[3];
#pragma unroll
  for (int i = 0; i < 8; ++i) uw[i] = u32p[(size_t)(8 * tid + i) * (DM / 2)];
#pragma unroll
  for (int i = 0; i < 3; ++i) up[i] = 0;
  if (lane == 0 && tid > 0) {
#pragma unroll
    for (int i = 0; i < 3; ++i) up[i] = u32p[(size_t)(8 * tid - 3 + i) * (DM / 2)];
  }

  {
    const int n = tid & 127;
    const int dd = d0 + (tid >> 7);
    const float dt = rbf(expf(bf2f(logdt[dd])));
    const float are = rbf(expf(bf2f(logAr[dd * NST + n])));
    f32x4 prm;
    prm[0] = (-are * dt) * 1.44269504088896340736f;
    prm[1] = (bf2f(Aim[dd * NST + n]) * dt) * 0.15915494309189533577f;
    prm[2] = 2.0f * (bf2f(Cre[dd * NST + n]) * dt);
    prm[3] = 2.0f * (bf2f(Cim[dd * NST + n]) * dt);
    *(f32x4*)(smem + KR_OFF + tid * 16) = prm;
  }
  __syncthreads();
  STAMP(1);

  {
    const int r = lane & 31, h = lane >> 5;
    int p1i;
    if (r < 8) p1i = r;
    else if (r < 12) p1i = 256 + 2 * (r - 8);
    else if (r < 16) p1i = 512 + 256 * ((r - 12) >> 1) + 4 * ((r - 12) & 1);
    else if (r < 21) p1i = 1024 + 256 * (r - 16);
    else p1i = 0;
    const float p1 = (float)p1i, p2 = (float)(8 * r);
    f32x16 acc = {};
    const char* prm_base = smem + KR_OFF + (ch * 128 + 64 * w2 + 4 * h) * 16;
#pragma unroll 2
    for (int ks = 0; ks < 8; ++ks) {
      unsigned ah[4], al[4], bh[4], bl[4];
#pragma unroll
      for (int q = 0; q < 4; ++q) {
        const f32x4 prm = *(const f32x4*)(prm_base + (8 * ks + q) * 16);
        {
          const float e = __builtin_amdgcn_exp2f(prm[0] * p1);
          const float rv = __builtin_amdgcn_fractf(prm[1] * p1);
          const float sn = __builtin_amdgcn_sinf(rv), cs = __builtin_amdgcn_cosf(rv);
          const float gre = e * (prm[2] * cs - prm[3] * sn);
          const float gim = -e * (prm[2] * sn + prm[3] * cs);
          const unsigned hi = pk_bf16(gre, gim);
          ah[q] = hi;
          al[q] = pk_bf16(gre - bf_lo(hi), gim - bf_hi(hi));
        }
        {
          const float e = __builtin_amdgcn_exp2f(prm[0] * p2);
          const float rv = __builtin_amdgcn_fractf(prm[1] * p2);
          const float sn = __builtin_amdgcn_sinf(rv), cs = __builtin_amdgcn_cosf(rv);
          const float ere = e * cs, eim = e * sn;
          const unsigned hi = pk_bf16(ere, eim);
          bh[q] = hi;
          bl[q] = pk_bf16(ere - bf_lo(hi), eim - bf_hi(hi));
        }
      }
      const bf16x8 Ah = mk8(ah[0], ah[1], ah[2], ah[3]), Al = mk8(al[0], al[1], al[2], al[3]);
      const bf16x8 Bh = mk8(bh[0], bh[1], bh[2], bh[3]), Bl = mk8(bl[0], bl[1], bl[2], bl[3]);
      acc = __builtin_amdgcn_mfma_f32_32x32x16_bf16(Al, Bh, acc, 0, 0, 0);
      acc = __builtin_amdgcn_mfma_f32_32x32x16_bf16(Ah, Bl, acc, 0, 0, 0);
      acc = __builtin_amdgcn_mfma_f32_32x32x16_bf16(Ah, Bh, acc, 0, 0, 0);
    }
    float* Kp = (float*)(smem + (ch * 2 + w2) * KP_B);
#pragma unroll
    for (int e = 0; e < 4; ++e) Kp[(4 * h + e) + 8 * r] = acc[e];
#pragma unroll
    for (int e = 0; e < 4; ++e) {
      const int pp = h ? (512 + 256 * (e >> 1) + 4 * (e & 1)) : (256 + 2 * e);
      Kp[pp + 8 * r] = acc[4 + e];
    }
    if (h == 0) {
#pragma unroll
      for (int e = 0; e < 4; ++e) Kp[1024 + 256 * e + 8 * r] = acc[8 + e];
    } else if (r == 0) {
      Kp[2048] = acc[8];
    }
  }
  STAMP(2);
  __syncthreads();

  {
    const int t = tid & 127;
    const float* Kp0 = (const float*)(smem + (ch * 2) * KP_B);
    const float* Kp1 = (const float*)(smem + (ch * 2 + 1) * KP_B);
    char* krh = smem + KR_OFF + (ch * 2) * KR_PART_B;
    char* krl = krh + KR_PART_B;
#pragma unroll
    for (int kk = 0; kk < 2; ++kk) {
      const int k = t + 128 * kk;
      float v[8];
#pragma unroll
      for (int e = 0; e < 8; ++e) {
        const int l = 2047 - 8 * k - e;
        const int p = (int)rbf((float)l);
        v[e] = Kp0[p] + Kp1[p];
      }
      unsigned hi[4], lo[4];
#pragma unroll
      for (int e = 0; e < 4; ++e) {
        hi[e] = pk_bf16(v[2 * e], v[2 * e + 1]);
        lo[e] = pk_bf16(v[2 * e] - bf_lo(hi[e]), v[2 * e + 1] - bf_hi(hi[e]));
      }
      *(u32x4*)(krh + k * 16) = u32x4{hi[0], hi[1], hi[2], hi[3]};
      *(u32x4*)(krl + k * 16) = u32x4{lo[0], lo[1], lo[2], lo[3]};
    }
    if (t < 30) {
      *(u32x4*)(krh + (256 + t) * 16) = u32x4{0, 0, 0, 0};
      *(u32x4*)(krl + (256 + t) * 16) = u32x4{0, 0, 0, 0};
    }
  }
  __syncthreads();
  STAMP(3);

  {
    unsigned e[11];
#pragma unroll
    for (int i = 0; i < 3; ++i) {
      const unsigned nb = __shfl_up(uw[5 + i], 1);
      e[i] = (lane == 0) ? up[i] : nb;
    }
#pragma unroll
    for (int i = 0; i < 8; ++i) e[3 + i] = uw[i];
    unsigned p0[10], p1[10];
#pragma unroll
    for (int i = 0; i < 10; ++i) {
      p0[i] = __builtin_amdgcn_perm(e[i + 1], e[i], 0x05040100u);
      p1[i] = __builtin_amdgcn_perm(e[i + 1], e[i], 0x07060302u);
    }
#pragma unroll
    for (int c = 0; c < 4; ++c) {
      *(u32x4*)(smem + c * U_COPY_B + (tid + 2) * 16) = u32x4{p0[c], p0[c + 2], p0[c + 4], p0[c + 6]};
      *(u32x4*)(smem + U_CH_B + c * U_COPY_B + (tid + 2) * 16) = u32x4{p1[c], p1[c + 2], p1[c + 4], p1[c + 6]};
    }
    if (tid < 16) *(u32x4*)(smem + (tid >> 1) * U_COPY_B + (tid & 1) * 16) = u32x4{0, 0, 0, 0};
  }
  __syncthreads();
  STAMP(4);

  f32x4 acc[8];
#pragma unroll
  for (int t = 0; t < 8; ++t) acc[t] = f32x4{0.f, 0.f, 0.f, 0.f};
  {
    const int r = lane & 15, hq = lane >> 4;
    const char* ua = smem + ch * U_CH_B + (r & 3) * U_COPY_B + 2 * (4 + (r & ~3) + 8 * hq);
    const char* kb = smem + KR_OFF + (ch * 2) * KR_PART_B + 2 * (2032 - 16 * r + 8 * hq);
#pragma unroll 1
    for (int ri = 0; ri < 4; ++ri) {
      const int rho = 4 * w2 + ri;
      bf16x8 A[8];
#pragma unroll
      for (int sg = 0; sg < 8; ++sg) {
        const char* pa = ua + 64 * (8 * sg + rho);
        A[sg] = cat4(*(const s16x4*)pa, *(const s16x4*)(pa + 8));
      }
#pragma unroll
      for (int tau = 0; tau < 8; ++tau) {
        const char* pb = kb - 64 * (8 * tau - rho);
        const bf16x8 Bh = *(const bf16x8*)pb;
        const bf16x8 Bl = *(const bf16x8*)(pb + KR_PART_B);
#pragma unroll
        for (int sg = 0; sg + tau < 8; ++sg) {
          acc[sg + tau] = __builtin_amdgcn_mfma_f32_16x16x32_bf16(A[sg], Bl, acc[sg + tau], 0, 0, 0);
          acc[sg + tau] = __builtin_amdgcn_mfma_f32_16x16x32_bf16(A[sg], Bh, acc[sg + tau], 0, 0, 0);
        }
      }
    }
  }
  STAMP(5);
  __syncthreads();
  {
    char* xw = smem + KR_OFF + (ch * 2 + w2) * 4096 + lane * 16;
    const char* xr = smem + KR_OFF + (ch * 2 + (w2 ^ 1)) * 4096 + lane * 16;
    f32x4 y[4];
    if (w2 == 0) {
#pragma unroll
      for (int i = 0; i < 4; ++i) { *(f32x4*)(xw + i * 1024) = acc[4 + i]; y[i] = acc[i]; }
    } else {
#pragma unroll
      for (int i = 0; i < 4; ++i) { *(f32x4*)(xw + i * 1024) = acc[i]; y[i] = acc[4 + i]; }
    }
    __syncthreads();
    const float Dk = bf2f(Dskip[d]);
#pragma unroll
    for (int i = 0; i < 4; ++i) {
      const f32x4 o = *(const f32x4*)(xr + i * 1024);
      const int l0 = 256 * (4 * w2 + i) + 16 * (lane & 15) + 4 * (lane >> 4);
      const u32x2 uu = *(const u32x2*)(smem + ch * U_CH_B + 3 * U_COPY_B + 2 * (16 + l0));
      const float a0 = rbf(y[i][0] + o[0]) + rbf(bf_lo(uu[0]) * Dk);
      const float a1 = rbf(y[i][1] + o[1]) + rbf(bf_hi(uu[0]) * Dk);
      const float a2 = rbf(y[i][2] + o[2]) + rbf(bf_lo(uu[1]) * Dk);
      const float a3 = rbf(y[i][3] + o[3]) + rbf(bf_hi(uu[1]) * Dk);
      *(u32x2*)(aT + (size_t)d * SEQ + l0) = u32x2{pk_bf16(a0, a1), pk_bf16(a2, a3)};
    }
  }
  STAMP(6);
}

__device__ __forceinline__ float gelu_tanh(float x) {
  const float z = 0.7978845608028654f * (x + 0.044715f * x * x * x);
  const float e = __builtin_amdgcn_exp2f(-2.8853900817779268f * z);
  return x * __builtin_amdgcn_rcpf(1.0f + e);
}

constexpr int G1_WSLOT_B = 12288;
constexpr int G1_X_OFF = 3 * G1_WSLOT_B;
constexpr int G1_LDS = G1_X_OFF + 32768;

constexpr int g1_pos(bool isx, int id) {
  if (isx && id == 0) return 4;
  if (!isx && id == 0) return 7;
  if (!isx && id == 1) return 10;
  if (isx && id == 1) return 14;
  if (!isx && id == 2) return 17;
  if (!isx && id == 3) return 20;
  if (!isx && id == 4) return 23;
  if (isx && id == 2) return 27;
  int n = 27;
  for (int t = 0; t < 32; ++t) {
    if ((t & 1) == 0 && t / 2 + 5 < 16) { n += 3; if (!isx && t / 2 + 5 == id) return n; }
    if ((t & 3) == 1 && t / 4 + 3 < 8) { n += 4; if (isx && t / 4 + 3 == id) return n; }
  }
  return -1;
}
constexpr int g1_issued_before(int step) {
  int n = 27;
  for (int t = 0; t < step; ++t) {
    if ((t & 1) == 0 && t / 2 + 5 < 16) n += 3;
    if ((t & 3) == 1 && t / 4 + 3 < 8) n += 4;
  }
  return n;
}

#define GLOAD128(dst, ptr) asm volatile("global_load_dwordx4 %0, %1, off" : "=v"(dst) : "v"(ptr) : "memory")
#define GLOAD128S(dst, voff, sbase) asm volatile("global_load_dwordx4 %0, %1, %2" : "=v"(dst) : "v"(voff), "s"(sbase) : "memory")
#define GLOAD128SA(dst, voff, sbase) asm volatile("global_load_dwordx4 %0, %1, %2" : "=a"(dst) : "v"(voff), "s"(sbase) : "memory")
#define GLOAD128A(dst, ptr) asm volatile("global_load_dwordx4 %0, %1, off" : "=a"(dst) : "v"(ptr) : "memory")
#define DS_W128(addr, val, off) asm volatile("ds_write_b128 %0, %1 offset:%2" ::"v"(addr), "v"(val), "i"(off) : "memory")
#define MFMA32(acc, a, b) asm volatile("v_mfma_f32_32x32x16_bf16 %0, %1, %2, %0" : "+v"(acc) : "v"(a), "v"(b))

struct G1Ctx {
  const char* wbase;
  const char* xbase;
  unsigned wo[3], xo;
  unsigned woff, woff_o, ww[3], xw, xrd2[2][4];
};
struct G1Frag {
  s16x4 w[3][2];
  bf16x8 x[2];
};

#define G1_RD_W(f, t, OFF)                                                        \
  do {                                                                            \
    if ((t) & 1) { DS_TR(f.w[t][0], c.woff_o, (OFF) + (t) * 64); DS_TR(f.w[t][1], c.woff_o, (OFF) + (t) * 64 + 4 * 384); } \
    else { DS_TR(f.w[t][0], c.woff, (OFF) + (t) * 64); DS_TR(f.w[t][1], c.woff, (OFF) + (t) * 64 + 4 * 384); }            \
  } while (0)
#define G1_RD_X(f, mi, N) DS_B128(f.x[mi], c.xrd2[mi][(N) & 3], (((N) >> 2) & 1) * 16384)
template <int N>
__device__ __forceinline__ void g1_read(G1Frag& f, const G1Ctx& c) {
  constexpr int OFF = ((N >> 1) % 3) * G1_WSLOT_B + (N & 1) * 16 * 384;
  G1_RD_X(f, 0, N); G1_RD_X(f, 1, N);
  G1_RD_W(f, 0, OFF); G1_RD_W(f, 1, OFF); G1_RD_W(f, 2, OFF);
}
#define G1_WAIT_FRAG(f, STR)                                                                                       \
  asm volatile(STR : "+v"(f.w[0][0]), "+v"(f.w[0][1]), "+v"(f.w[1][0]), "+v"(f.w[1][1]), "+v"(f.w[2][0]),        \
               "+v"(f.w[2][1]), "+v"(f.x[0]), "+v"(f.x[1])::"memory")

template <int N>
struct G1Step {
  static __device__ __forceinline__ void run(f32x16 (&acc)[6], u32x4 (&xr)[2][4], u32x4 (&wr)[3][3], G1Frag& cur,
                                             G1Frag& nxt, G1Ctx& c) {
    constexpr int ISS = g1_issued_before(N);
    constexpr int S = N >> 1;
    constexpr bool RD = N + 1 < 32;
    constexpr int OFF = (((N + 1) >> 1) % 3) * G1_WSLOT_B + ((N + 1) & 1) * 16 * 384;
    if constexpr ((N & 1) == 0 && N > 0) G1_WAIT_FRAG(cur, "s_waitcnt lgkmcnt(0)\n\ts_barrier");
    else G1_WAIT_FRAG(cur, "s_waitcnt lgkmcnt(0)");
    bf16x8 b[3];
#pragma unroll
    for (int t = 0; t < 3; ++t) b[t] = cat4(cur.w[t][0], cur.w[t][1]);
    MFMA32(acc[0], b[0], cur.x[0]);
    if constexpr (RD) { G1_RD_X(nxt, 0, N + 1); G1_RD_W(nxt, 0, OFF); }
    MFMA32(acc[3], b[0], cur.x[1]);
    if constexpr (RD) { G1_RD_X(nxt, 1, N + 1); G1_RD_W(nxt, 1, OFF); }
    MFMA32(acc[1], b[1], cur.x[0]);
    if constexpr (RD) { G1_RD_W(nxt, 2, OFF); }
    MFMA32(acc[4], b[1], cur.x[1]);
    if constexpr ((N & 1) == 0) {
      if constexpr (S + 2 < 16) {
        constexpr int R = (S + 2) % 3;
        constexpr int NW = ISS - g1_pos(false, S + 2);
        asm volatile("s_waitcnt vmcnt(%3)" : "+v"(wr[R][0]), "+v"(wr[R][1]), "+v"(wr[R][2]) : "i"(NW) : "memory");
#pragma unroll
        for (int i = 0; i < 3; ++i) DS_W128(c.ww[i], wr[R][i], R * G1_WSLOT_B);
      }
    }
    if constexpr ((N & 3) == 1) {
      constexpr int B = N / 4 + 1;
      if constexpr (B < 8) {
        constexpr int NW = ISS - g1_pos(true, B);
        asm volatile("s_waitcnt vmcnt(%4)" : "+v"(xr[B & 1][0]), "+v"(xr[B & 1][1]), "+v"(xr[B & 1][2]), "+v"(xr[B & 1][3]) : "i"(NW) : "memory");
#pragma unroll
        for (int i = 0; i < 4; ++i) DS_W128(c.xw, xr[B & 1][i], (B & 1) * 16384 + i * 1024);
      }
    }
    MFMA32(acc[2], b[2], cur.x[0]);
    if constexpr ((N & 1) == 0 && S + 5 < 16) {
#pragma unroll
      for (int i = 0; i < 3; ++i) GLOAD128S(wr[(S + 5) % 3][i], c.wo[i], c.wbase + (size_t)(S + 5) * 32 * FF * 2);
    }
    if constexpr ((N & 3) == 1 && N / 4 + 3 < 8) {
      constexpr int B = N / 4 + 1;
#pragma unroll
      for (int i = 0; i < 4; ++i) GLOAD128S(xr[B & 1][i], c.xo, c.xbase + (size_t)(8 * i) * DM * 2 + (B + 2) * 128);
    }
    MFMA32(acc[5], b[2], cur.x[1]);
    if constexpr (N + 1 < 32) G1Step<N + 1>::run(acc, xr, wr, nxt, cur, c);
  }
};

__device__ __forceinline__ void gemm1_tile(int t, const u16* __restrict__ x, const u16* __restrict__ wg,
                                           const u16* __restrict__ wu, u16* __restrict__ hf, char* smem DBG_ARG) {
  const int tid = threadIdx.x, lane = tid & 63;
  const int wave = __builtin_amdgcn_readfirstlane(tid >> 6);
  const int fb = t >> 4, mb = t & 15;
  const int f0 = 96 * fb;
  const int wm = wave & 1, isup = wave >> 1;
  G1Ctx c;
  const unsigned sbase = lds_addr(smem);
  c.wbase = (const char*)(isup ? wu : wg);
#pragma unroll
  for (int i = 0; i < 3; ++i) {
    const int id = 192 * (wave & 1) + 64 * i + lane;
    const int row = id / 12, cc = id % 12;
    int col = f0 + 8 * cc;
    if (col > FF - 8) col = FF - 8;
    c.wo[i] = (unsigned)(row * FF + col) * 2u;
    c.ww[i] = sbase + row * 384 + ((cc + 12 * isup) ^ (((row >> 1) & 1) << 2)) * 16;
  }
  c.xbase = (const char*)(x + (size_t)(128 * mb + 32 * wave) * DM);
  c.xo = (unsigned)((lane >> 3) * DM + (lane & 7) * 8) * 2u;
  const unsigned xl = sbase + G1_X_OFF;
  c.xw = xl + (32 * wave + (lane >> 3)) * 128 + (((lane & 7) ^ (lane >> 3)) * 16);
  const int r = lane & 31, h = lane >> 5;
#pragma unroll
  for (int i = 0; i < 4; ++i) {
    c.xrd2[0][i] = xl + (64 * wm + 32 * isup + r) * 128 + (((2 * i + h) ^ (r & 7)) * 16);
    c.xrd2[1][i] = xl + (64 * wm + 32 * (1 - isup) + r) * 128 + (((2 * i + h) ^ (r & 7)) * 16);
  }
  const int blk = (lane >> 4) & 1, q = (lane & 15) >> 2, p = lane & 3;
  const int sw = (q >> 1) & 1;
  {
    const unsigned e = sbase + (8 * h + q) * 384 + (2 * blk + (p >> 1) + 4 * sw) * 16 + 8 * (p & 1);
    const unsigned o = sbase + (8 * h + q) * 384 + (2 * blk + (p >> 1) - 4 * sw) * 16 + 8 * (p & 1);
    c.woff = isup ? o + 192 : e;
    c.woff_o = isup ? e + 192 : o;
  }

  f32x16 acc[6];
#pragma unroll
  for (int i = 0; i < 6; ++i) acc[i] = f32x16{};
  asm volatile("s_nop 4" : "+v"(acc[0]), "+v"(acc[1]), "+v"(acc[2]), "+v"(acc[3]), "+v"(acc[4]), "+v"(acc[5]));
  u32x4 xr[2][4], wr[3][3];
  G1Frag fa, fb2;
#define G1_ISSUE_X(R, B) _Pragma("unroll") for (int i = 0; i < 4; ++i) GLOAD128S(xr[R][i], c.xo, c.xbase + (size_t)(8 * i) * DM * 2 + (B) * 128)
#define G1_ISSUE_W(R, S) _Pragma("unroll") for (int i = 0; i < 3; ++i) GLOAD128S(wr[R][i], c.wo[i], c.wbase + (size_t)(S) * 32 * FF * 2)
  G1_ISSUE_X(0, 0); G1_ISSUE_W(0, 0); G1_ISSUE_W(1, 1); G1_ISSUE_X(1, 1); G1_ISSUE_W(2, 2);
  asm volatile("s_waitcnt vmcnt(7)"
               : "+v"(wr[0][0]), "+v"(wr[0][1]), "+v"(wr[0][2]), "+v"(wr[1][0]), "+v"(wr[1][1]), "+v"(wr[1][2]),
                 "+v"(xr[0][0]), "+v"(xr[0][1]), "+v"(xr[0][2]), "+v"(xr[0][3])::"memory");
#pragma unroll
  for (int i = 0; i < 3; ++i) { DS_W128(c.ww[i], wr[0][i], 0); DS_W128(c.ww[i], wr[1][i], G1_WSLOT_B); }
#pragma unroll
  for (int i = 0; i < 4; ++i) DS_W128(c.xw, xr[0][i], i * 1024);
  G1_ISSUE_W(0, 3); G1_ISSUE_W(1, 4); G1_ISSUE_X(0, 2);
  asm volatile("s_waitcnt lgkmcnt(0)\n\ts_barrier" ::: "memory");
  g1_read<0>(fa, c);
  G1Step<0>::run(acc, xr, wr, fa, fb2, c);
  asm volatile("s_nop 15\n\ts_nop 15" : "+v"(acc[0]), "+v"(acc[1]), "+v"(acc[2]), "+v"(acc[3]), "+v"(acc[4]), "+v"(acc[5]));

  unsigned keep[3][8];
  {
    unsigned give[3][8];
#pragma unroll
    for (int tt = 0; tt < 3; ++tt)
#pragma unroll
      for (int e = 0; e < 8; ++e) give[tt][e] = pk_bf16(acc[3 + tt][2 * e], acc[3 + tt][2 * e + 1]);
    __syncthreads();
    char* xo = smem + wave * 6144 + lane * 16;
#pragma unroll
    for (int tt = 0; tt < 3; ++tt) {
      *(u32x4*)(xo + (2 * tt) * 1024) = u32x4{give[tt][0], give[tt][1], give[tt][2], give[tt][3]};
      *(u32x4*)(xo + (2 * tt + 1) * 1024) = u32x4{give[tt][4], give[tt][5], give[tt][6], give[tt][7]};
    }
  }
#pragma unroll
  for (int tt = 0; tt < 3; ++tt)
#pragma unroll
    for (int e = 0; e < 8; ++e) keep[tt][e] = pk_bf16(acc[tt][2 * e], acc[tt][2 * e + 1]);
  __syncthreads();
  {
    const char* xi = smem + (wave ^ 2) * 6144 + lane * 16;
    const int mt = 4 * mb + 2 * wm + isup;
    u16* hp = hf + ((size_t)(mt * NKS + 6 * fb) * 64 + lane) * 8;
    auto finish = [&](auto ISUP) {
#pragma unroll
      for (int tt = 0; tt < 3; ++tt) {
        const int ft = 3 * fb + tt;
        if (ft < FF / 32) {
          const u32x4 o0 = *(const u32x4*)(xi + (2 * tt) * 1024);
          const u32x4 o1 = *(const u32x4*)(xi + (2 * tt + 1) * 1024);
          unsigned gp[8], up[8];
#pragma unroll
          for (int e = 0; e < 8; ++e) {
            const unsigned ot = e < 4 ? o0[e & 3] : o1[e & 3];
            gp[e] = decltype(ISUP)::value ? ot : keep[tt][e];
            up[e] = decltype(ISUP)::value ? keep[tt][e] : ot;
          }
          f32x2 xg[8], ar[8];
#pragma unroll
          for (int e = 0; e < 8; ++e) xg[e] = f32x2{bf_lo(gp[e]), bf_hi(gp[e])};
#pragma unroll
          for (int e = 0; e < 8; ++e) ar[e] = xg[e] * xg[e];
#pragma unroll
          for (int e = 0; e < 8; ++e) ar[e] = __builtin_elementwise_fma(ar[e], f32x2{-0.10294323958f, -0.10294323958f}, f32x2{-2.30220819814f, -2.30220819814f});
#pragma unroll
          for (int e = 0; e < 8; ++e) ar[e] = ar[e] * xg[e];
#pragma unroll
          for (int e = 0; e < 8; ++e) ar[e] = f32x2{__builtin_amdgcn_exp2f(ar[e][0]), __builtin_amdgcn_exp2f(ar[e][1])};
#pragma unroll
          for (int e = 0; e < 8; ++e) ar[e] = ar[e] + f32x2{1.0f, 1.0f};
#pragma unroll
          for (int e = 0; e < 8; ++e) ar[e] = f32x2{__builtin_amdgcn_rcpf(ar[e][0]), __builtin_amdgcn_rcpf(ar[e][1])};
#pragma unroll
          for (int e = 0; e < 8; ++e) ar[e] = ar[e] * xg[e];
          unsigned o[8];
#pragma unroll
          for (int e = 0; e < 8; ++e) o[e] = pk_bf16(ar[e][0], ar[e][1]);
#pragma unroll
          for (int e = 0; e < 8; ++e) ar[e] = f32x2{bf_lo(o[e]), bf_hi(o[e])} * f32x2{bf_lo(up[e]), bf_hi(up[e])};
#pragma unroll
          for (int e = 0; e < 8; ++e) o[e] = pk_bf16(ar[e][0], ar[e][1]);
          *(u32x4*)(hp + (2 * tt) * 512) = u32x4{o[0], o[1], o[2], o[3]};
          *(u32x4*)(hp + (2 * tt + 1) * 512) = u32x4{o[4], o[5], o[6], o[7]};
        } else if (ft < NKS / 2) {
          *(u32x4*)(hp + (2 * tt) * 512) = u32x4{0, 0, 0, 0};
          *(u32x4*)(hp + (2 * tt + 1) * 512) = u32x4{0, 0, 0, 0};
        }
      }
    };
    if (isup) finish(std::integral_constant<bool, true>{});
    else finish(std::integral_constant<bool, false>{});
  }
}

__device__ __forceinline__ void gate_unit(int b, const u16* __restrict__ x, const u16* __restrict__ gw,
                                          float* __restrict__ gout) {
  const int tid = threadIdx.x, lane = tid & 63, wave = tid >> 6;
  const int j = lane & 7, rr = lane >> 3;
  u32x4 gv[8];
#pragma unroll
  for (int i = 0; i < 8; ++i) gv[i] = *(const u32x4*)(gw + (j + 8 * i) * 8);
  const int row0 = b * 128 + wave * 32 + rr;
  u32x4 xv[4][8];
#pragma unroll
  for (int ps = 0; ps < 4; ++ps)
#pragma unroll
    for (int i = 0; i < 8; ++i) xv[ps][i] = *(const u32x4*)(x + (size_t)(row0 + 8 * ps) * DM + (j + 8 * i) * 8);
#pragma unroll
  for (int ps = 0; ps < 4; ++ps) {
    float s0 = 0.f, s1 = 0.f;
#pragma unroll
    for (int i = 0; i < 8; ++i)
#pragma unroll
      for (int e = 0; e < 4; ++e) {
        const unsigned pr = pk_bf16(bf_lo(xv[ps][i][e]) * bf_lo(gv[i][e]), bf_hi(xv[ps][i][e]) * bf_hi(gv[i][e]));
        s0 += bf_lo(pr);
        s1 += bf_hi(pr);
      }
    float s = s0 + s1;
    s += __shfl_xor(s, 1);
    s += __shfl_xor(s, 2);
    s += __shfl_xor(s, 4);
    if (j == 0) {
      const float sb = rbf(s);
      gout[row0 + 8 * ps] = rbf(1.0f / (1.0f + expf(-sb)));
    }
  }
}

__global__ __launch_bounds__(256, 2) void k1_kernel(const u16* __restrict__ u, const u16* __restrict__ logAr,
                                                   const u16* __restrict__ Aim, const u16* __restrict__ Cre,
                                                   const u16* __restrict__ Cim, const u16* __restrict__ logdt,
                                                   const u16* __restrict__ Dskip, const u16* __restrict__ wg,
                                                   const u16* __restrict__ wu, const u16* __restrict__ gw,
                                                   u16* __restrict__ hbuf, u16* __restrict__ aT,
                                                   float* __restrict__ gout DBG_ARG
                                                   ) {
  __shared__ __attribute__((aligned(4096))) char smem[G1_LDS > SSM_LDS ? G1_LDS : SSM_LDS];
  const int bid = blockIdx.x;
  if (bid < NSSM) {
    ssm_unit(bid, u, logAr, Aim, Cre, Cim, logdt, Dskip, aT, smem DBG_PASS);
  } else {
    const int xcd = bid & 7, idx = (bid - NSSM) >> 3;
    if (idx < 30) gemm1_tile(30 * xcd + idx, u, wg, wu, hbuf, smem DBG_PASS);
    else gate_unit(2 * xcd + idx - 30, u, gw, gout);
  }
}

constexpr int G2_SLOT_B = 4096;
constexpr int G2_WAVE_B = 2 * G2_SLOT_B;
constexpr int G2_PITCH = 272;
constexpr int G2_A_OFF = 4 * 64 * G2_PITCH;
constexpr int K2_LDS = G2_A_OFF + 8192;
#define G2D 3
constexpr int G2_DEPTH = G2D;

struct G2Ctx {
  const u16* hsrc;
  const u16* wd;
  int wrow0, wcol;
  unsigned wb32, woff, ww;
};

template <int ST>
struct G2Step {
  static __device__ __forceinline__ void issue(bf16x8 (&hfr)[11][4], u32x4 (&wr)[11][4], const G2Ctx& c) {
#pragma unroll
    for (int i = 0; i < 4; ++i) {
      int kr = c.wrow0 + ST * 32 + 8 * i;
      kr = kr < FF ? kr : FF - 1;
      GLOAD128(wr[ST][i], c.wd + (size_t)kr * DM + c.wcol);
    }
#pragma unroll
    for (int mi = 0; mi < 2; ++mi)
#pragma unroll
      for (int i = 0; i < 2; ++i)
        GLOAD128A(hfr[ST][2 * mi + i], c.hsrc + (size_t)(mi * NKS + 2 * ST + i) * 512);

  }
  static __device__ __forceinline__ void run(f32x16 (&acc)[2][2], bf16x8 (&hfr)[11][4], u32x4 (&wr)[11][4], const G2Ctx& c) {
    constexpr int LAST = (ST + G2_DEPTH - 1 < 10) ? ST + G2_DEPTH - 1 : 10;
    constexpr int NW = 8 * (LAST - ST);
    asm volatile("s_waitcnt vmcnt(%4)" : "+v"(wr[ST][0]), "+v"(wr[ST][1]), "+v"(wr[ST][2]), "+v"(wr[ST][3]) : "i"(NW + 4) : "memory");
#pragma unroll
    for (int i = 0; i < 4; ++i) DS_W128(c.ww, wr[ST][i], (ST & 1) * G2_SLOT_B + i * 1024);
    asm volatile("s_waitcnt lgkmcnt(0)" ::: "memory");
    const unsigned sb = c.wb32 + c.woff;
    s16x4 w[2][2][2];
#pragma unroll
    for (int i = 0; i < 2; ++i)
#pragma unroll
      for (int t = 0; t < 2; ++t) {
        DS_TR(w[i][t][0], sb ^ (t * 64), (ST & 1) * G2_SLOT_B + i * 2048);
        DS_TR(w[i][t][1], sb ^ (t * 64), (ST & 1) * G2_SLOT_B + i * 2048 + 1024);
      }
    asm volatile("s_waitcnt vmcnt(%4)" : "+a"(hfr[ST][0]), "+a"(hfr[ST][1]), "+a"(hfr[ST][2]), "+a"(hfr[ST][3]) : "i"(NW) : "memory");
    if constexpr (ST + G2_DEPTH <= 10) G2Step<ST + G2_DEPTH>::issue(hfr, wr, c);
    asm volatile("s_waitcnt lgkmcnt(0)"
                 : "+v"(w[0][0][0]), "+v"(w[0][0][1]), "+v"(w[0][1][0]), "+v"(w[0][1][1]), "+v"(w[1][0][0]), "+v"(w[1][0][1]),
                   "+v"(w[1][1][0]), "+v"(w[1][1][1])::"memory");
#pragma unroll
    for (int i = 0; i < 2; ++i)
#pragma unroll
      for (int t = 0; t < 2; ++t)
#pragma unroll
        for (int mi = 0; mi < 2; ++mi)
          acc[t][mi] = __builtin_amdgcn_mfma_f32_32x32x16_bf16(cat4(w[i][t][0], w[i][t][1]), hfr[ST][2 * mi + i], acc[t][mi], 0, 0, 0);
    if constexpr (ST + 1 <= 10) G2Step<ST + 1>::run(acc, hfr, wr, c);
  }
};

__global__ __launch_bounds__(256, 1) void k2_kernel(const u16* __restrict__ hf, const u16* __restrict__ wd,
                                                   const u16* __restrict__ aT, const float* __restrict__ gin,
                                                   u16* __restrict__ out DBG2_ARG) {
  __shared__ __attribute__((aligned(4096))) char smem[K2_LDS];
  const int tid = threadIdx.x, lane = tid & 63;
  STAMP2(0);
  const int wave = __builtin_amdgcn_readfirstlane(tid >> 6);
  const int bid = blockIdx.x;
  const int xcd = bid & 7, idx = bid >> 3;
  const int m0 = 64 * (4 * xcd + (idx >> 3)), n0 = 64 * (idx & 7);

#pragma unroll
  for (int i = 0; i < 2; ++i) {
    const int piece = 2 * wave + i;
    const int drow = 8 * piece + (lane >> 3);
    glds16(aT + (size_t)(n0 + drow) * SEQ + m0 + (lane & 7) * 8, smem + G2_A_OFF + piece * 1024 + lane * 16);
  }

  G2Ctx c;
  c.wb32 = lds_addr(smem) + wave * G2_WAVE_B;
  c.hsrc = hf + ((size_t)((m0 >> 5) * NKS + 22 * wave) * 64 + lane) * 8;
  c.wd = wd;
  c.wcol = n0 + 8 * (lane & 7);
  c.wrow0 = 352 * wave + (lane >> 3);
  c.ww = c.wb32 + (lane >> 3) * 128 + (((lane & 7) ^ (((lane >> 4) & 1) << 2)) * 16);

  const int r = lane & 31, h = lane >> 5;
  const int blk = (lane >> 4) & 1, q = (lane & 15) >> 2, p = lane & 3;
  c.woff = (4 * h + q) * 128 + (((2 * blk + (p >> 1)) ^ (((q >> 1) & 1) << 2)) * 16) + 8 * (p & 1);

  f32x16 acc[2][2];
#pragma unroll
  for (int i = 0; i < 2; ++i)
#pragma unroll
    for (int j = 0; j < 2; ++j) acc[i][j] = f32x16{};
  bf16x8 hfr[11][4];
  u32x4 wr[11][4];

  G2Step<0>::issue(hfr, wr, c);
  G2Step<1>::issue(hfr, wr, c);
  if constexpr (G2_DEPTH > 2) G2Step<2>::issue(hfr, wr, c);
  if constexpr (G2_DEPTH > 3) G2Step<3>::issue(hfr, wr, c);
  if constexpr (G2_DEPTH > 4) G2Step<4>::issue(hfr, wr, c);
  if constexpr (G2_DEPTH > 5) G2Step<5>::issue(hfr, wr, c);
  if constexpr (G2_DEPTH > 6) G2Step<6>::issue(hfr, wr, c);
  STAMP2(1);
  STAMP2(2);
  G2Step<0>::run(acc, hfr, wr, c);
  STAMP2(3);
  __syncthreads();
  {
    char* pw = smem + wave * (64 * G2_PITCH);
#pragma unroll
    for (int c = 0; c < 2; ++c)
#pragma unroll
      for (int mi = 0; mi < 2; ++mi)
#pragma unroll
        for (int gq = 0; gq < 4; ++gq) {
          const f32x4 v = {acc[c][mi][4 * gq], acc[c][mi][4 * gq + 1], acc[c][mi][4 * gq + 2], acc[c][mi][4 * gq + 3]};
          *(f32x4*)(pw + (32 * mi + r) * G2_PITCH + (32 * c + 8 * gq + 4 * h) * 4) = v;
        }
  }
  __syncthreads();
  STAMP2(4);
  {
    const int m = tid >> 2, nq = (tid & 3) * 16;
    const float g = gin[m0 + m];
    const float omg = rbf(1.0f - g);
    float b[16];
#pragma unroll
    for (int j = 0; j < 4; ++j) {
      f32x4 s = *(const f32x4*)(smem + m * G2_PITCH + (nq + 4 * j) * 4);
#pragma unroll
      for (int w = 1; w < 4; ++w) s += *(const f32x4*)(smem + w * (64 * G2_PITCH) + m * G2_PITCH + (nq + 4 * j) * 4);
#pragma unroll
      for (int e = 0; e < 4; ++e) b[4 * j + e] = s[e];
    }
    const u16* aimg = (const u16*)(smem + G2_A_OFF);
    unsigned o[8];
#pragma unroll
    for (int j = 0; j < 8; ++j) {
      const float a0 = bf2f(aimg[(nq + 2 * j) * 64 + m]);
      const float a1 = bf2f(aimg[(nq + 2 * j + 1) * 64 + m]);
      const float o0 = rbf(g * a0) + rbf(omg * rbf(b[2 * j]));
      const float o1 = rbf(g * a1) + rbf(omg * rbf(b[2 * j + 1]));
      o[j] = pk_bf16(o0, o1);
    }
    u16* op = out + (size_t)(m0 + m) * DM + n0 + nq;
    *(u32x4*)op = u32x4{o[0], o[1], o[2], o[3]};
    *(u32x4*)(op + 8) = u32x4{o[4], o[5], o[6], o[7]};
  }
  STAMP2(5);
}

extern "C" void kernel_launch(void* const* d_in, const int* in_sizes, int n_in, void* d_out, int out_size,
                              void* d_ws, size_t ws_size, hipStream_t stream) {
  const u16* u = (const u16*)d_in[0];
  const u16* logAr = (const u16*)d_in[1];
  const u16* Aim = (const u16*)d_in[2];
  const u16* Cre = (const u16*)d_in[3];
  const u16* Cim = (const u16*)d_in[4];
  const u16* logdt = (const u16*)d_in[5];
  const u16* Dskip = (const u16*)d_in[6];
  const u16* wg = (const u16*)d_in[8];
  const u16* wu = (const u16*)d_in[9];
  const u16* wd = (const u16*)d_in[10];
  const u16* gw = (const u16*)d_in[11];
  char* ws = (char*)d_ws;
  u16* hbuf = (u16*)ws;
  u16* aT = (u16*)(ws + (size_t)SEQ * FFP * 2);
  float* gbuf = (float*)(ws + (size_t)SEQ * FFP * 2 + (size_t)DM * SEQ * 2);
  k1_kernel<<<512, 256, 0, stream>>>(u, logAr, Aim, Cre, Cim, logdt, Dskip, wg, wu, gw, hbuf, aT, gbuf
      );
  k2_kernel<<<256, 256, 0, stream>>>(hbuf, wd, aT, gbuf, (u16*)d_out
      );
}
```

```cpp
#include <hip/hip_runtime.h>
#include <hip/hip_bf16.h>
#include <stdint.h>
#include <type_traits>

typedef unsigned short u16;
typedef __attribute__((ext_vector_type(8))) short bf16x8;
typedef __attribute__((ext_vector_type(4))) short s16x4;
typedef __attribute__((ext_vector_type(4))) float f32x4;
typedef __attribute__((ext_vector_type(16))) float f32x16;
typedef __attribute__((ext_vector_type(4))) unsigned u32x4;
typedef __attribute__((ext_vector_type(2))) unsigned u32x2;
typedef __attribute__((ext_vector_type(2))) __bf16 bf16x2_t;
typedef __attribute__((ext_vector_type(2))) float f32x2;
typedef __attribute__((address_space(3))) s16x4* lds_s16x4_ptr;

constexpr int SEQ = 2048, DM = 512, NST = 128, FF = 1376, FFP = 1408;
constexpr int NSSM = 256;
constexpr int NKS = 88;

__device__ __forceinline__ unsigned pk_bf16(float lo, float hi) {
  f32x2 v = {lo, hi};
  bf16x2_t b = __builtin_convertvector(v, bf16x2_t);
  return __builtin_bit_cast(unsigned, b);
}
__device__ __forceinline__ float bf_lo(unsigned w) { return __uint_as_float(w << 16); }
__device__ __forceinline__ float bf_hi(unsigned w) { return __uint_as_float(w & 0xffff0000u); }
__device__ __forceinline__ float rbf(float x) { return bf_lo(pk_bf16(x, 0.f)); }
__device__ __forceinline__ float bf2f(u16 v) { return __uint_as_float(((unsigned)v) << 16); }

__device__ __forceinline__ bf16x8 mk8(unsigned a, unsigned b, unsigned c, unsigned d) {
  u32x4 v = {a, b, c, d};
  return __builtin_bit_cast(bf16x8, v);
}
__device__ __forceinline__ bf16x8 cat4(s16x4 a, s16x4 b) {
  return __builtin_shufflevector(a, b, 0, 1, 2, 3, 4, 5, 6, 7);
}
__device__ __forceinline__ s16x4 tr_read(const char* smem_ptr) {
  return __builtin_amdgcn_ds_read_tr16_b64_v4i16((lds_s16x4_ptr)(smem_ptr));
}
__device__ __forceinline__ unsigned lds_addr(const void* p) {
  return (unsigned)(size_t)(const __attribute__((address_space(3))) char*)p;
}
__device__ __forceinline__ void glds16(const void* g, char* l) {
  __builtin_amdgcn_global_load_lds((const unsigned*)g, (unsigned*)l, 16, 0, 0);
}

#define STAMP2(i)
#define DBG2_ARG
#define DBG2_PASS
#define STAMP(i)
#define DBG_ARG
#define DBG_PASS
constexpr int U_COPY_B = 4160;
constexpr int U_CH_B = 4 * U_COPY_B;
constexpr int U_REGION_B = 2 * U_CH_B;
constexpr int KR_PART_B = 4576;
constexpr int KR_OFF = U_REGION_B;
constexpr int KQ_B = 8208;
constexpr int KX_OFF = 2 * KQ_B;
constexpr int SSM_LDS = KR_OFF + 4 * KR_PART_B;
static_assert(KX_OFF + 4 * 1536 <= U_REGION_B, "scratch must fit the U region");

#define DS_TR(dst, addr, off) asm volatile("ds_read_b64_tr_b16 %0, %1 offset:%2" : "=v"(dst) : "v"(addr), "i"(off) : "memory")
#define DS_B128(dst, addr, off) asm volatile("ds_read_b128 %0, %1 offset:%2" : "=v"(dst) : "v"(addr), "i"(off) : "memory")
#define DS_B64(dst, addr, off) asm volatile("ds_read_b64 %0, %1 offset:%2" : "=v"(dst) : "v"(addr), "i"(off) : "memory")
#define MFMA16(acc, a, b) asm volatile("v_mfma_f32_16x16x32_bf16 %0, %1, %2, %0" : "+v"(acc) : "v"(a), "v"(b))

struct ConvFrag {
  s16x4 a[8][2];
  bf16x8 bh[8], bl[8];
};
template <int RI, int K>
__device__ __forceinline__ void conv_read(ConvFrag& f, unsigned ua, unsigned kb) {
  if constexpr (K < 16) {
    DS_B64(f.a[K >> 1][K & 1], ua, 64 * (8 * (K >> 1) + RI) + 8 * (K & 1));
  } else if constexpr (K < 24) {
    DS_B128(f.bh[K - 16], kb, 64 * (56 - 8 * (K - 16) + RI));
  } else {
    DS_B128(f.bl[K - 24], kb, 64 * (56 - 8 * (K - 24) + RI) + KR_PART_B);
  }
}
template <int RI, int K>
__device__ __forceinline__ void conv_read_all(ConvFrag& f, unsigned ua, unsigned kb) {
  conv_read<RI, K>(f, ua, kb);
  if constexpr (K + 1 < 32) conv_read_all<RI, K + 1>(f, ua, kb);
}
#define CONV_WAIT(f)                                                                                                  \
  do {                                                                                                                \
    asm volatile("s_waitcnt lgkmcnt(0)"                                                                               \
                 : "+v"(f.a[0][0]), "+v"(f.a[0][1]), "+v"(f.a[1][0]), "+v"(f.a[1][1]), "+v"(f.a[2][0]), "+v"(f.a[2][1]), \
                   "+v"(f.a[3][0]), "+v"(f.a[3][1]), "+v"(f.a[4][0]), "+v"(f.a[4][1]), "+v"(f.a[5][0]), "+v"(f.a[5][1]), \
                   "+v"(f.a[6][0]), "+v"(f.a[6][1]), "+v"(f.a[7][0]), "+v"(f.a[7][1])::"memory");                     \
    asm volatile("" : "+v"(f.bh[0]), "+v"(f.bh[1]), "+v"(f.bh[2]), "+v"(f.bh[3]), "+v"(f.bh[4]), "+v"(f.bh[5]),        \
                 "+v"(f.bh[6]), "+v"(f.bh[7]), "+v"(f.bl[0]), "+v"(f.bl[1]), "+v"(f.bl[2]), "+v"(f.bl[3]),            \
                 "+v"(f.bl[4]), "+v"(f.bl[5]), "+v"(f.bl[6]), "+v"(f.bl[7])::"memory");                              \
  } while (0)
constexpr int conv_tau(int P) { int t = 0; while (P >= 8 - t) { P -= 8 - t; ++t; } return t; }
constexpr int conv_sig(int P) { int t = 0; while (P >= 8 - t) { P -= 8 - t; ++t; } return P; }
template <int RI, int P>
__device__ __forceinline__ void conv_pairs(f32x4 (&acc)[8], const bf16x8 (&A)[8], ConvFrag& cur, ConvFrag& nxt, unsigned ua,
                                           unsigned kb) {
  constexpr int T = conv_tau(P), SG = conv_sig(P);
  MFMA16(acc[T + SG], A[SG], cur.bl[T]);
  if constexpr (RI < 3 && P < 32) conv_read<RI + 1, P>(nxt, ua, kb);
  MFMA16(acc[T + SG], A[SG], cur.bh[T]);
  if constexpr (P + 1 < 36) conv_pairs<RI, P + 1>(acc, A, cur, nxt, ua, kb);
}
template <int RI>
__device__ __forceinline__ void conv_rho(f32x4 (&acc)[8], ConvFrag& cur, ConvFrag& nxt, unsigned ua, unsigned kb) {
  CONV_WAIT(cur);
  bf16x8 A[8];
#pragma unroll
  for (int i = 0; i < 8; ++i) A[i] = cat4(cur.a[i][0], cur.a[i][1]);
  conv_pairs<RI, 0>(acc, A, cur, nxt, ua, kb);
  if constexpr (RI < 3) conv_rho<RI + 1>(acc, nxt, cur, ua, kb);
}

__device__ __forceinline__ void split_pair(f32x2 v, unsigned& hi, unsigned& lo) {
  hi = pk_bf16(v[0], v[1]);
  const f32x2 r = v - f32x2{bf_lo(hi), bf_hi(hi)};
  lo = pk_bf16(r[0], r[1]);
}

__device__ __forceinline__ void ssm_unit(int w, const u16* __restrict__ u, const u16* __restrict__ logAr,
                                         const u16* __restrict__ Aim, const u16* __restrict__ Cre,
                                         const u16* __restrict__ Cim, const u16* __restrict__ logdt,
                                         const u16* __restrict__ Dskip, u16* __restrict__ aT, char* smem DBG_ARG) {
  const int tid = threadIdx.x, lane = tid & 63;
  const int wave = __builtin_amdgcn_readfirstlane(tid >> 6);
  const int ch = wave >> 1, w2 = wave & 1;
  const int d0 = 2 * w;
  const int d = d0 + ch;

  const int pidx = d0 * NST + tid;
  const u16 r_lar = logAr[pidx], r_aim = Aim[pidx], r_cre = Cre[pidx], r_cim = Cim[pidx], r_ldt = logdt[d], r_dsk = Dskip[d];
  asm volatile("" ::: "memory");
  const unsigned* u32p = (const unsigned*)(u + d0);
  unsigned uw[8], up[3];
#pragma unroll
  for (int i = 0; i < 8; ++i) uw[i] = u32p[(size_t)(8 * tid + i) * (DM / 2)];
#pragma unroll
  for (int i = 0; i < 3; ++i) up[i] = 0;
  if (lane == 0 && tid > 0) {
#pragma unroll
    for (int i = 0; i < 3; ++i) up[i] = u32p[(size_t)(8 * tid - 3 + i) * (DM / 2)];
  }
  {
    const float dt = rbf(expf(bf2f(r_ldt)));
    const float are = rbf(expf(bf2f(r_lar)));
    f32x4 prm;
    prm[0] = (-are * dt) * 1.44269504088896340736f;
    prm[1] = (bf2f(r_aim) * dt) * 0.15915494309189533577f;
    prm[2] = 2.0f * (bf2f(r_cre) * dt);
    prm[3] = 2.0f * (bf2f(r_cim) * dt);
    *(f32x4*)(smem + KR_OFF + tid * 16) = prm;
  }
  __syncthreads();

  const int r = lane & 31, h = lane >> 5;
  f32x16 dacc = {};
  {
    int p1i;
    if (r < 8) p1i = r;
    else if (r < 12) p1i = 256 + 2 * (r - 8);
    else if (r < 16) p1i = 512 + 256 * ((r - 12) >> 1) + 4 * ((r - 12) & 1);
    else if (r < 21) p1i = 1024 + 256 * (r - 16);
    else p1i = 0;
    const float p1 = (float)p1i, p2 = (float)(8 * r);
    const f32x2 P1 = {p1, p1}, P2 = {p2, p2};
    const char* prm_base = smem + KR_OFF + (ch * 128 + 64 * w2 + 4 * h) * 16;
#pragma unroll 2
    for (int ks = 0; ks < 8; ++ks) {
      f32x4 prm[4];
#pragma unroll
      for (int q = 0; q < 4; ++q) prm[q] = *(const f32x4*)(prm_base + (8 * ks + q) * 16);
      f32x2 za[4], zb[4];
#pragma unroll
      for (int q = 0; q < 4; ++q) { za[q] = f32x2{prm[q][0], prm[q][1]} * P1; zb[q] = f32x2{prm[q][0], prm[q][1]} * P2; }
      float ea[4], eb[4], fa[4], fb[4];
#pragma unroll
      for (int q = 0; q < 4; ++q) {
        ea[q] = __builtin_amdgcn_exp2f(za[q][0]); eb[q] = __builtin_amdgcn_exp2f(zb[q][0]);
        fa[q] = __builtin_amdgcn_fractf(za[q][1]); fb[q] = __builtin_amdgcn_fractf(zb[q][1]);
      }
      f32x2 ca[4], cb[4];
#pragma unroll
      for (int q = 0; q < 4; ++q) {
        ca[q] = f32x2{__builtin_amdgcn_cosf(fa[q]), __builtin_amdgcn_sinf(fa[q])};
        cb[q] = f32x2{__builtin_amdgcn_cosf(fb[q]), __builtin_amdgcn_sinf(fb[q])};
      }
      unsigned ah[4], al[4], bh[4], bl[4];
#pragma unroll
      for (int q = 0; q < 4; ++q) {
        const f32x2 ce = f32x2{prm[q][2], prm[q][3]} * f32x2{ea[q], ea[q]};
        f32x2 g = f32x2{ce[0], -ce[0]} * ca[q];
        g = __builtin_elementwise_fma(f32x2{-ce[1], -ce[1]}, f32x2{ca[q][1], ca[q][0]}, g);
        split_pair(g, ah[q], al[q]);
        split_pair(cb[q] * f32x2{eb[q], eb[q]}, bh[q], bl[q]);
      }
      const bf16x8 Ah = mk8(ah[0], ah[1], ah[2], ah[3]), Al = mk8(al[0], al[1], al[2], al[3]);
      const bf16x8 Bh = mk8(bh[0], bh[1], bh[2], bh[3]), Bl = mk8(bl[0], bl[1], bl[2], bl[3]);
      dacc = __builtin_amdgcn_mfma_f32_32x32x16_bf16(Al, Bh, dacc, 0, 0, 0);
      dacc = __builtin_amdgcn_mfma_f32_32x32x16_bf16(Ah, Bl, dacc, 0, 0, 0);
      dacc = __builtin_amdgcn_mfma_f32_32x32x16_bf16(Ah, Bh, dacc, 0, 0, 0);
    }
  }
  {
    float* xo = (float*)(smem + KX_OFF + wave * 1536) + lane;
    const float* xi = (const float*)(smem + KX_OFF + (wave ^ 1) * 1536) + lane;
    float mine[6];
    if (w2 == 0) {
#pragma unroll
      for (int e = 0; e < 6; ++e) { xo[e * 64] = dacc[6 + e]; mine[e] = dacc[e]; }
    } else {
#pragma unroll
      for (int e = 0; e < 6; ++e) { xo[e * 64] = dacc[e]; mine[e] = dacc[6 + e]; }
    }
    __syncthreads();
    unsigned* Kq = (unsigned*)(smem + ch * KQ_B);
    unsigned pq[6];
#pragma unroll
    for (int e = 0; e < 6; ++e) {
      const float v = mine[e] + xi[e * 64];
      const unsigned hi = pk_bf16(v, 0.f);
      pq[e] = pk_bf16(0.f, v - bf_lo(hi)) | hi;
    }
    if (w2 == 0) {
#pragma unroll
      for (int e = 0; e < 4; ++e) Kq[4 * h + e + 8 * r] = pq[e];
      Kq[(h ? 512 : 256) + 8 * r] = pq[4];
      Kq[(h ? 516 : 258) + 8 * r] = pq[5];
    } else {
      Kq[(h ? 768 : 260) + 8 * r] = pq[0];
      Kq[(h ? 772 : 262) + 8 * r] = pq[1];
      if (h == 0) {
#pragma unroll
        for (int e = 0; e < 4; ++e) Kq[1024 + 256 * e + 8 * r] = pq[2 + e];
      } else if (r == 0) {
        Kq[2048] = pq[2];
      }
    }
  }
  __syncthreads();

  {
    const int t = tid & 127;
    const unsigned* Kq = (const unsigned*)(smem + ch * KQ_B);
    unsigned* krh = (unsigned*)(smem + KR_OFF + (ch * 2) * KR_PART_B);
    unsigned* krl = (unsigned*)((char*)krh + KR_PART_B);
    unsigned q0[8], q1[8];
#pragma unroll
    for (int m = 0; m < 8; ++m) {
      const int sh = m < 4 ? 3 : (m < 6 ? 2 : (m < 7 ? 1 : 0));
      const int l0 = 2047 - 2 * (t + 128 * m), l1 = l0 - 1;
      int pa = l0, pb = l1;
      if (sh) {
        pa = (l0 + (1 << (sh - 1)) - 1 + ((l0 >> sh) & 1)) & ~((1 << sh) - 1);
        pb = (l1 + (1 << (sh - 1)) - 1 + ((l1 >> sh) & 1)) & ~((1 << sh) - 1);
      }
      q0[m] = Kq[pa];
      q1[m] = Kq[pb];
    }
#pragma unroll
    for (int m = 0; m < 8; ++m) {
      krh[t + 128 * m] = __builtin_amdgcn_perm(q1[m], q0[m], 0x05040100u);
      krl[t + 128 * m] = __builtin_amdgcn_perm(q1[m], q0[m], 0x07060302u);
    }
    if (t < 120) { krh[1024 + t] = 0; krl[1024 + t] = 0; }
  }
  __syncthreads();

  {
    unsigned e[11];
#pragma unroll
    for (int i = 0; i < 3; ++i) {
      const unsigned nb = __shfl_up(uw[5 + i], 1);
      e[i] = (lane == 0) ? up[i] : nb;
    }
#pragma unroll
    for (int i = 0; i < 8; ++i) e[3 + i] = uw[i];
    unsigned p0[10], p1[10];
#pragma unroll
    for (int i = 0; i < 10; ++i) {
      p0[i] = __builtin_amdgcn_perm(e[i + 1], e[i], 0x05040100u);
      p1[i] = __builtin_amdgcn_perm(e[i + 1], e[i], 0x07060302u);
    }
#pragma unroll
    for (int c = 0; c < 4; ++c) {
      *(u32x4*)(smem + c * U_COPY_B + (tid + 2) * 16) = u32x4{p0[c], p0[c + 2], p0[c + 4], p0[c + 6]};
      *(u32x4*)(smem + U_CH_B + c * U_COPY_B + (tid + 2) * 16) = u32x4{p1[c], p1[c + 2], p1[c + 4], p1[c + 6]};
    }
    if (tid < 16) *(u32x4*)(smem + (tid >> 1) * U_COPY_B + (tid & 1) * 16) = u32x4{0, 0, 0, 0};
  }
  __syncthreads();

  f32x4 acc[8];
#pragma unroll
  for (int t = 0; t < 8; ++t) acc[t] = f32x4{0.f, 0.f, 0.f, 0.f};
  asm volatile("s_nop 4" : "+v"(acc[0]), "+v"(acc[1]), "+v"(acc[2]), "+v"(acc[3]), "+v"(acc[4]), "+v"(acc[5]), "+v"(acc[6]), "+v"(acc[7]));
  {
    const int r16 = lane & 15, hq = lane >> 4;
    const unsigned sb = lds_addr(smem);
    const unsigned ua = sb + ch * U_CH_B + (r16 & 3) * U_COPY_B + 2 * (4 + (r16 & ~3) + 8 * hq) + 64 * 4 * w2;
    const unsigned kb = sb + KR_OFF + (ch * 2) * KR_PART_B + 2 * (2032 - 16 * r16 + 8 * hq) - 64 * 56 + 64 * 4 * w2;
    ConvFrag f0, f1;
    conv_read_all<0, 0>(f0, ua, kb);
    conv_rho<0>(acc, f0, f1, ua, kb);
  }
  asm volatile("s_nop 15" : "+v"(acc[0]), "+v"(acc[1]), "+v"(acc[2]), "+v"(acc[3]), "+v"(acc[4]), "+v"(acc[5]), "+v"(acc[6]), "+v"(acc[7]));
  __syncthreads();
  {
    char* xw = smem + KR_OFF + (ch * 2 + w2) * 4096 + lane * 16;
    const char* xr = smem + KR_OFF + (ch * 2 + (w2 ^ 1)) * 4096 + lane * 16;
    f32x4 y[4];
    if (w2 == 0) {
#pragma unroll
      for (int i = 0; i < 4; ++i) { *(f32x4*)(xw + i * 1024) = acc[4 + i]; y[i] = acc[i]; }
    } else {
#pragma unroll
      for (int i = 0; i < 4; ++i) { *(f32x4*)(xw + i * 1024) = acc[i]; y[i] = acc[4 + i]; }
    }
    __syncthreads();
    const float Dk = bf2f(r_dsk);
#pragma unroll
    for (int i = 0; i < 4; ++i) {
      const f32x4 o = *(const f32x4*)(xr + i * 1024);
      const int l0 = 256 * (4 * w2 + i) + 16 * (lane & 15) + 4 * (lane >> 4);
      const u32x2 uu = *(const u32x2*)(smem + ch * U_CH_B + 3 * U_COPY_B + 2 * (16 + l0));
      const unsigned y01 = pk_bf16(y[i][0] + o[0], y[i][1] + o[1]), y23 = pk_bf16(y[i][2] + o[2], y[i][3] + o[3]);
      const unsigned s01 = pk_bf16(bf_lo(uu[0]) * Dk, bf_hi(uu[0]) * Dk), s23 = pk_bf16(bf_lo(uu[1]) * Dk, bf_hi(uu[1]) * Dk);
      *(u32x2*)(aT + (size_t)d * SEQ + l0) = u32x2{pk_bf16(bf_lo(y01) + bf_lo(s01), bf_hi(y01) + bf_hi(s01)),
                                                  pk_bf16(bf_lo(y23) + bf_lo(s23), bf_hi(y23) + bf_hi(s23))};
    }
  }
}

__device__ __forceinline__ float gelu_tanh(float x) {
  const float z = 0.7978845608028654f * (x + 0.044715f * x * x * x);
  const float e = __builtin_amdgcn_exp2f(-2.8853900817779268f * z);
  return x * __builtin_amdgcn_rcpf(1.0f + e);
}

constexpr int G1_WSLOT_B = 12288;
constexpr int G1_X_OFF = 3 * G1_WSLOT_B;
constexpr int G1_LDS = G1_X_OFF + 32768;

constexpr int g1_pos(bool isx, int id) {
  if (isx && id == 0) return 4;
  if (!isx && id == 0) return 7;
  if (!isx && id == 1) return 10;
  if (isx && id == 1) return 14;
  if (!isx && id == 2) return 17;
  if (!isx && id == 3) return 20;
  if (!isx && id == 4) return 23;
  if (isx && id == 2) return 27;
  int n = 27;
  for (int t = 0; t < 32; ++t) {
    if ((t & 1) == 0 && t / 2 + 5 < 16) { n += 3; if (!isx && t / 2 + 5 == id) return n; }
    if ((t & 3) == 1 && t / 4 + 3 < 8) { n += 4; if (isx && t / 4 + 3 == id) return n; }
  }
  return -1;
}
constexpr int g1_issued_before(int step) {
  int n = 27;
  for (int t = 0; t < step; ++t) {
    if ((t & 1) == 0 && t / 2 + 5 < 16) n += 3;
    if ((t & 3) == 1 && t / 4 + 3 < 8) n += 4;
  }
  return n;
}

#define GLOAD128(dst, ptr) asm volatile("global_load_dwordx4 %0, %1, off" : "=v"(dst) : "v"(ptr) : "memory")
#define GLOAD128S(dst, voff, sbase) asm volatile("global_load_dwordx4 %0, %1, %2" : "=v"(dst) : "v"(voff), "s"(sbase) : "memory")
#define GLOAD128SA(dst, voff, sbase) asm volatile("global_load_dwordx4 %0, %1, %2" : "=a"(dst) : "v"(voff), "s"(sbase) : "memory")
#define GLOAD128A(dst, ptr) asm volatile("global_load_dwordx4 %0, %1, off" : "=a"(dst) : "v"(ptr) : "memory")
#define DS_W128(addr, val, off) asm volatile("ds_write_b128 %0, %1 offset:%2" ::"v"(addr), "v"(val), "i"(off) : "memory")
#define MFMA32(acc, a, b) asm volatile("v_mfma_f32_32x32x16_bf16 %0, %1, %2, %0" : "+v"(acc) : "v"(a), "v"(b))

struct G1Ctx {
  const char* wbase;
  const char* xbase;
  unsigned wo[3], xo;
  unsigned woff, woff_o, ww[3], xw, xrd2[2][4];
};
struct G1Frag {
  s16x4 w[3][2];
  bf16x8 x[2];
};

#define G1_RD_W(f, t, OFF)                                                        \
  do {                                                                            \
    if ((t) & 1) { DS_TR(f.w[t][0], c.woff_o, (OFF) + (t) * 64); DS_TR(f.w[t][1], c.woff_o, (OFF) + (t) * 64 + 4 * 384); } \
    else { DS_TR(f.w[t][0], c.woff, (OFF) + (t) * 64); DS_TR(f.w[t][1], c.woff, (OFF) + (t) * 64 + 4 * 384); }            \
  } while (0)
#define G1_RD_X(f, mi, N) DS_B128(f.x[mi], c.xrd2[mi][(N) & 3], (((N) >> 2) & 1) * 16384)
template <int N>
__device__ __forceinline__ void g1_read(G1Frag& f, const G1Ctx& c) {
  constexpr int OFF = ((N >> 1) % 3) * G1_WSLOT_B + (N & 1) * 16 * 384;
  G1_RD_X(f, 0, N); G1_RD_X(f, 1, N);
  G1_RD_W(f, 0, OFF); G1_RD_W(f, 1, OFF); G1_RD_W(f, 2, OFF);
}
#define G1_WAIT_FRAG(f, STR)                                                                                       \
  asm volatile(STR : "+v"(f.w[0][0]), "+v"(f.w[0][1]), "+v"(f.w[1][0]), "+v"(f.w[1][1]), "+v"(f.w[2][0]),        \
               "+v"(f.w[2][1]), "+v"(f.x[0]), "+v"(f.x[1])::"memory")

template <int N>
struct G1Step {
  static __device__ __forceinline__ void run(f32x16 (&acc)[6], u32x4 (&xr)[2][4], u32x4 (&wr)[3][3], G1Frag& cur,
                                             G1Frag& nxt, G1Ctx& c) {
    constexpr int ISS = g1_issued_before(N);
    constexpr int S = N >> 1;
    constexpr bool RD = N + 1 < 32;
    constexpr int OFF = (((N + 1) >> 1) % 3) * G1_WSLOT_B + ((N + 1) & 1) * 16 * 384;
    if constexpr ((N & 1) == 0 && N > 0) G1_WAIT_FRAG(cur, "s_waitcnt lgkmcnt(0)\n\ts_barrier");
    else G1_WAIT_FRAG(cur, "s_waitcnt lgkmcnt(0)");
    bf16x8 b[3];
#pragma unroll
    for (int t = 0; t < 3; ++t) b[t] = cat4(cur.w[t][0], cur.w[t][1]);
    MFMA32(acc[0], b[0], cur.x[0]);
    if constexpr (RD) { G1_RD_X(nxt, 0, N + 1); G1_RD_W(nxt, 0, OFF); }
    MFMA32(acc[3], b[0], cur.x[1]);
    if constexpr (RD) { G1_RD_X(nxt, 1, N + 1); G1_RD_W(nxt, 1, OFF); }
    MFMA32(acc[1], b[1], cur.x[0]);
    if constexpr (RD) { G1_RD_W(nxt, 2, OFF); }
    MFMA32(acc[4], b[1], cur.x[1]);
    if constexpr ((N & 1) == 0) {
      if constexpr (S + 2 < 16) {
        constexpr int R = (S + 2) % 3;
        constexpr int NW = ISS - g1_pos(false, S + 2);
        asm volatile("s_waitcnt vmcnt(%3)" : "+v"(wr[R][0]), "+v"(wr[R][1]), "+v"(wr[R][2]) : "i"(NW) : "memory");
#pragma unroll
        for (int i = 0; i < 3; ++i) DS_W128(c.ww[i], wr[R][i], R * G1_WSLOT_B);
      }
    }
    if constexpr ((N & 3) == 1) {
      constexpr int B = N / 4 + 1;
      if constexpr (B < 8) {
        constexpr int NW = ISS - g1_pos(true, B);
        asm volatile("s_waitcnt vmcnt(%4)" : "+v"(xr[B & 1][0]), "+v"(xr[B & 1][1]), "+v"(xr[B & 1][2]), "+v"(xr[B & 1][3]) : "i"(NW) : "memory");
#pragma unroll
        for (int i = 0; i < 4; ++i) DS_W128(c.xw, xr[B & 1][i], (B & 1) * 16384 + i * 1024);
      }
    }
    MFMA32(acc[2], b[2], cur.x[0]);
    if constexpr ((N & 1) == 0 && S + 5 < 16) {
#pragma unroll
      for (int i = 0; i < 3; ++i) GLOAD128S(wr[(S + 5) % 3][i], c.wo[i], c.wbase + (size_t)(S + 5) * 32 * FF * 2);
    }
    if constexpr ((N & 3) == 1 && N / 4 + 3 < 8) {
      constexpr int B = N / 4 + 1;
#pragma unroll
      for (int i = 0; i < 4; ++i) GLOAD128S(xr[B & 1][i], c.xo, c.xbase + (size_t)(8 * i) * DM * 2 + (B + 2) * 128);
    }
    MFMA32(acc[5], b[2], cur.x[1]);
    if constexpr (N + 1 < 32) G1Step<N + 1>::run(acc, xr, wr, nxt, cur, c);
  }
};

__device__ __forceinline__ void gemm1_tile(int t, const u16* __restrict__ x, const u16* __restrict__ wg,
                                           const u16* __restrict__ wu, u16* __restrict__ hf, char* smem DBG_ARG) {
  const int tid = threadIdx.x, lane = tid & 63;
  const int wave = __builtin_amdgcn_readfirstlane(tid >> 6);
  const int fb = t >> 4, mb = t & 15;
  const int f0 = 96 * fb;
  const int wm = wave & 1, isup = wave >> 1;
  G1Ctx c;
  const unsigned sbase = lds_addr(smem);
  c.wbase = (const char*)(isup ? wu : wg);
#pragma unroll
  for (int i = 0; i < 3; ++i) {
    const int id = 192 * (wave & 1) + 64 * i + lane;
    const int row = id / 12, cc = id % 12;
    int col = f0 + 8 * cc;
    if (col > FF - 8) col = FF - 8;
    c.wo[i] = (unsigned)(row * FF + col) * 2u;
    c.ww[i] = sbase + row * 384 + ((cc + 12 * isup) ^ (((row >> 1) & 1) << 2)) * 16;
  }
  c.xbase = (const char*)(x + (size_t)(128 * mb + 32 * wave) * DM);
  c.xo = (unsigned)((lane >> 3) * DM + (lane & 7) * 8) * 2u;
  const unsigned xl = sbase + G1_X_OFF;
  c.xw = xl + (32 * wave + (lane >> 3)) * 128 + (((lane & 7) ^ (lane >> 3)) * 16);
  const int r = lane & 31, h = lane >> 5;
#pragma unroll
  for (int i = 0; i < 4; ++i) {
    c.xrd2[0][i] = xl + (64 * wm + 32 * isup + r) * 128 + (((2 * i + h) ^ (r & 7)) * 16);
    c.xrd2[1][i] = xl + (64 * wm + 32 * (1 - isup) + r) * 128 + (((2 * i + h) ^ (r & 7)) * 16);
  }
  const int blk = (lane >> 4) & 1, q = (lane & 15) >> 2, p = lane & 3;
  const int sw = (q >> 1) & 1;
  {
    const unsigned e = sbase + (8 * h + q) * 384 + (2 * blk + (p >> 1) + 4 * sw) * 16 + 8 * (p & 1);
    const unsigned o = sbase + (8 * h + q) * 384 + (2 * blk + (p >> 1) - 4 * sw) * 16 + 8 * (p & 1);
    c.woff = isup ? o + 192 : e;
    c.woff_o = isup ? e + 192 : o;
  }

  f32x16 acc[6];
#pragma unroll
  for (int i = 0; i < 6; ++i) acc[i] = f32x16{};
  asm volatile("s_nop 4" : "+v"(acc[0]), "+v"(acc[1]), "+v"(acc[2]), "+v"(acc[3]), "+v"(acc[4]), "+v"(acc[5]));
  u32x4 xr[2][4], wr[3][3];
  G1Frag fa, fb2;
#define G1_ISSUE_X(R, B) _Pragma("unroll") for (int i = 0; i < 4; ++i) GLOAD128S(xr[R][i], c.xo, c.xbase + (size_t)(8 * i) * DM * 2 + (B) * 128)
#define G1_ISSUE_W(R, S) _Pragma("unroll") for (int i = 0; i < 3; ++i) GLOAD128S(wr[R][i], c.wo[i], c.wbase + (size_t)(S) * 32 * FF * 2)
  G1_ISSUE_X(0, 0); G1_ISSUE_W(0, 0); G1_ISSUE_W(1, 1); G1_ISSUE_X(1, 1); G1_ISSUE_W(2, 2);
  asm volatile("s_waitcnt vmcnt(7)"
               : "+v"(wr[0][0]), "+v"(wr[0][1]), "+v"(wr[0][2]), "+v"(wr[1][0]), "+v"(wr[1][1]), "+v"(wr[1][2]),
                 "+v"(xr[0][0]), "+v"(xr[0][1]), "+v"(xr[0][2]), "+v"(xr[0][3])::"memory");
#pragma unroll
  for (int i = 0; i < 3; ++i) { DS_W128(c.ww[i], wr[0][i], 0); DS_W128(c.ww[i], wr[1][i], G1_WSLOT_B); }
#pragma unroll
  for (int i = 0; i < 4; ++i) DS_W128(c.xw, xr[0][i], i * 1024);
  G1_ISSUE_W(0, 3); G1_ISSUE_W(1, 4); G1_ISSUE_X(0, 2);
  asm volatile("s_waitcnt lgkmcnt(0)\n\ts_barrier" ::: "memory");
  g1_read<0>(fa, c);
  G1Step<0>::run(acc, xr, wr, fa, fb2, c);
  asm volatile("s_nop 15\n\ts_nop 15" : "+v"(acc[0]), "+v"(acc[1]), "+v"(acc[2]), "+v"(acc[3]), "+v"(acc[4]), "+v"(acc[5]));

  unsigned keep[3][8];
  {
    unsigned give[3][8];
#pragma unroll
    for (int tt = 0; tt < 3; ++tt)
#pragma unroll
      for (int e = 0; e < 8; ++e) give[tt][e] = pk_bf16(acc[3 + tt][2 * e], acc[3 + tt][2 * e + 1]);
    __syncthreads();
    char* xo = smem + wave * 6144 + lane * 16;
#pragma unroll
    for (int tt = 0; tt < 3; ++tt) {
      *(u32x4*)(xo + (2 * tt) * 1024) = u32x4{give[tt][0], give[tt][1], give[tt][2], give[tt][3]};
      *(u32x4*)(xo + (2 * tt + 1) * 1024) = u32x4{give[tt][4], give[tt][5], give[tt][6], give[tt][7]};
    }
  }
#pragma unroll
  for (int tt = 0; tt < 3; ++tt)
#pragma unroll
    for (int e = 0; e < 8; ++e) keep[tt][e] = pk_bf16(acc[tt][2 * e], acc[tt][2 * e + 1]);
  __syncthreads();
  {
    const char* xi = smem + (wave ^ 2) * 6144 + lane * 16;
    const int mt = 4 * mb + 2 * wm + isup;
    u16* hp = hf + ((size_t)(mt * NKS + 6 * fb) * 64 + lane) * 8;
    auto finish = [&](auto ISUP) {
#pragma unroll
      for (int tt = 0; tt < 3; ++tt) {
        const int ft = 3 * fb + tt;
        if (ft < FF / 32) {
          const u32x4 o0 = *(const u32x4*)(xi + (2 * tt) * 1024);
          const u32x4 o1 = *(const u32x4*)(xi + (2 * tt + 1) * 1024);
          unsigned gp[8], up[8];
#pragma unroll
          for (int e = 0; e < 8; ++e) {
            const unsigned ot = e < 4 ? o0[e & 3] : o1[e & 3];
            gp[e] = decltype(ISUP)::value ? ot : keep[tt][e];
            up[e] = decltype(ISUP)::value ? keep[tt][e] : ot;
          }
          f32x2 xg[8], ar[8];
#pragma unroll
          for (int e = 0; e < 8; ++e) xg[e] = f32x2{bf_lo(gp[e]), bf_hi(gp[e])};
#pragma unroll
          for (int e = 0; e < 8; ++e) ar[e] = xg[e] * xg[e];
#pragma unroll
          for (int e = 0; e < 8; ++e) ar[e] = __builtin_elementwise_fma(ar[e], f32x2{-0.10294323958f, -0.10294323958f}, f32x2{-2.30220819814f, -2.30220819814f});
#pragma unroll
          for (int e = 0; e < 8; ++e) ar[e] = ar[e] * xg[e];
#pragma unroll
          for (int e = 0; e < 8; ++e) ar[e] = f32x2{__builtin_amdgcn_exp2f(ar[e][0]), __builtin_amdgcn_exp2f(ar[e][1])};
#pragma unroll
          for (int e = 0; e < 8; ++e) ar[e] = ar[e] + f32x2{1.0f, 1.0f};
#pragma unroll
          for (int e = 0; e < 8; ++e) ar[e] = f32x2{__builtin_amdgcn_rcpf(ar[e][0]), __builtin_amdgcn_rcpf(ar[e][1])};
#pragma unroll
          for (int e = 0; e < 8; ++e) ar[e] = ar[e] * xg[e];
          unsigned o[8];
#pragma unroll
          for (int e = 0; e < 8; ++e) o[e] = pk_bf16(ar[e][0], ar[e][1]);
#pragma unroll
          for (int e = 0; e < 8; ++e) ar[e] = f32x2{bf_lo(o[e]), bf_hi(o[e])} * f32x2{bf_lo(up[e]), bf_hi(up[e])};
#pragma unroll
          for (int e = 0; e < 8; ++e) o[e] = pk_bf16(ar[e][0], ar[e][1]);
          *(u32x4*)(hp + (2 * tt) * 512) = u32x4{o[0], o[1], o[2], o[3]};
          *(u32x4*)(hp + (2 * tt + 1) * 512) = u32x4{o[4], o[5], o[6], o[7]};
        } else if (ft < NKS / 2) {
          *(u32x4*)(hp + (2 * tt) * 512) = u32x4{0, 0, 0, 0};
          *(u32x4*)(hp + (2 * tt + 1) * 512) = u32x4{0, 0, 0, 0};
        }
      }
    };
    if (isup) finish(std::integral_constant<bool, true>{});
    else finish(std::integral_constant<bool, false>{});
  }
}

__device__ __forceinline__ void gate_unit(int b, const u16* __restrict__ x, const u16* __restrict__ gw,
                                          float* __restrict__ gout) {
  const int tid = threadIdx.x, lane = tid & 63, wave = tid >> 6;
  const int j = lane & 7, rr = lane >> 3;
  u32x4 gv[8];
#pragma unroll
  for (int i = 0; i < 8; ++i) gv[i] = *(const u32x4*)(gw + (j + 8 * i) * 8);
  const int row0 = b * 128 + wave * 32 + rr;
  u32x4 xv[4][8];
#pragma unroll
  for (int ps = 0; ps < 4; ++ps)
#pragma unroll
    for (int i = 0; i < 8; ++i) xv[ps][i] = *(const u32x4*)(x + (size_t)(row0 + 8 * ps) * DM + (j + 8 * i) * 8);
#pragma unroll
  for (int ps = 0; ps < 4; ++ps) {
    float s0 = 0.f, s1 = 0.f;
#pragma unroll
    for (int i = 0; i < 8; ++i)
#pragma unroll
      for (int e = 0; e < 4; ++e) {
        const unsigned pr = pk_bf16(bf_lo(xv[ps][i][e]) * bf_lo(gv[i][e]), bf_hi(xv[ps][i][e]) * bf_hi(gv[i][e]));
        s0 += bf_lo(pr);
        s1 += bf_hi(pr);
      }
    float s = s0 + s1;
    s += __shfl_xor(s, 1);
    s += __shfl_xor(s, 2);
    s += __shfl_xor(s, 4);
    if (j == 0) {
      const float sb = rbf(s);
      gout[row0 + 8 * ps] = rbf(1.0f / (1.0f + expf(-sb)));
    }
  }
}

__global__ __launch_bounds__(256, 2) void k1_kernel(const u16* __restrict__ u, const u16* __restrict__ logAr,
                                                   const u16* __restrict__ Aim, const u16* __restrict__ Cre,
                                                   const u16* __restrict__ Cim, const u16* __restrict__ logdt,
                                                   const u16* __restrict__ Dskip, const u16* __restrict__ wg,
                                                   const u16* __restrict__ wu, const u16* __restrict__ gw,
                                                   u16* __restrict__ hbuf, u16* __restrict__ aT,
                                                   float* __restrict__ gout DBG_ARG
                                                   ) {
  __shared__ __attribute__((aligned(4096))) char smem[G1_LDS > SSM_LDS ? G1_LDS : SSM_LDS];
  const int bid = blockIdx.x;
  if (bid < NSSM) {
    ssm_unit(bid, u, logAr, Aim, Cre, Cim, logdt, Dskip, aT, smem DBG_PASS);
  } else {
    const int xcd = bid & 7, idx = (bid - NSSM) >> 3;
    if (idx < 30) gemm1_tile(30 * xcd + idx, u, wg, wu, hbuf, smem DBG_PASS);
    else gate_unit(2 * xcd + idx - 30, u, gw, gout);
  }
}

constexpr int G2_SLOT_B = 4096;
constexpr int G2_WAVE_B = 2 * G2_SLOT_B;
constexpr int G2_PITCH = 272;
constexpr int G2_A_OFF = 4 * 64 * G2_PITCH;
constexpr int K2_LDS = G2_A_OFF + 8192;
#define G2D 3
constexpr int G2_DEPTH = G2D;

struct G2Ctx {
  const u16* hsrc;
  const u16* wd;
  int wrow0, wcol;
  unsigned wb32, woff, ww;
};

template <int ST>
struct G2Step {
  static __device__ __forceinline__ void issue(bf16x8 (&hfr)[11][4], u32x4 (&wr)[11][4], const G2Ctx& c) {
#pragma unroll
    for (int i = 0; i < 4; ++i) {
      int kr = c.wrow0 + ST * 32 + 8 * i;
      kr = kr < FF ? kr : FF - 1;
      GLOAD128(wr[ST][i], c.wd + (size_t)kr * DM + c.wcol);
    }
#pragma unroll
    for (int mi = 0; mi < 2; ++mi)
#pragma unroll
      for (int i = 0; i < 2; ++i)
        GLOAD128A(hfr[ST][2 * mi + i], c.hsrc + (size_t)(mi * NKS + 2 * ST + i) * 512);

  }
  static __device__ __forceinline__ void run(f32x16 (&acc)[2][2], bf16x8 (&hfr)[11][4], u32x4 (&wr)[11][4], const G2Ctx& c) {
    constexpr int LAST = (ST + G2_DEPTH - 1 < 10) ? ST + G2_DEPTH - 1 : 10;
    constexpr int NW = 8 * (LAST - ST);
    asm volatile("s_waitcnt vmcnt(%4)" : "+v"(wr[ST][0]), "+v"(wr[ST][1]), "+v"(wr[ST][2]), "+v"(wr[ST][3]) : "i"(NW + 4) : "memory");
#pragma unroll
    for (int i = 0; i < 4; ++i) DS_W128(c.ww, wr[ST][i], (ST & 1) * G2_SLOT_B + i * 1024);
    asm volatile("s_waitcnt lgkmcnt(0)" ::: "memory");
    const unsigned sb = c.wb32 + c.woff;
    s16x4 w[2][2][2];
#pragma unroll
    for (int i = 0; i < 2; ++i)
#pragma unroll
      for (int t = 0; t < 2; ++t) {
        DS_TR(w[i][t][0], sb ^ (t * 64), (ST & 1) * G2_SLOT_B + i * 2048);
        DS_TR(w[i][t][1], sb ^ (t * 64), (ST & 1) * G2_SLOT_B + i * 2048 + 1024);
      }
    asm volatile("s_waitcnt vmcnt(%4)" : "+a"(hfr[ST][0]), "+a"(hfr[ST][1]), "+a"(hfr[ST][2]), "+a"(hfr[ST][3]) : "i"(NW) : "memory");
    if constexpr (ST + G2_DEPTH <= 10) G2Step<ST + G2_DEPTH>::issue(hfr, wr, c);
    asm volatile("s_waitcnt lgkmcnt(0)"
                 : "+v"(w[0][0][0]), "+v"(w[0][0][1]), "+v"(w[0][1][0]), "+v"(w[0][1][1]), "+v"(w[1][0][0]), "+v"(w[1][0][1]),
                   "+v"(w[1][1][0]), "+v"(w[1][1][1])::"memory");
#pragma unroll
    for (int i = 0; i < 2; ++i)
#pragma unroll
      for (int t = 0; t < 2; ++t)
#pragma unroll
        for (int mi = 0; mi < 2; ++mi)
          acc[t][mi] = __builtin_amdgcn_mfma_f32_32x32x16_bf16(cat4(w[i][t][0], w[i][t][1]), hfr[ST][2 * mi + i], acc[t][mi], 0, 0, 0);
    if constexpr (ST + 1 <= 10) G2Step<ST + 1>::run(acc, hfr, wr, c);
  }
};

__global__ __launch_bounds__(256, 1) void k2_kernel(const u16* __restrict__ hf, const u16* __restrict__ wd,
                                                   const u16* __restrict__ aT, const float* __restrict__ gin,
                                                   u16* __restrict__ out DBG2_ARG) {
  __shared__ __attribute__((aligned(4096))) char smem[K2_LDS];
  const int tid = threadIdx.x, lane = tid & 63;
  STAMP2(0);
  const int wave = __builtin_amdgcn_readfirstlane(tid >> 6);
  const int bid = blockIdx.x;
  const int xcd = bid & 7, idx = bid >> 3;
  const int m0 = 64 * (4 * xcd + (idx >> 3)), n0 = 64 * (idx & 7);

#pragma unroll
  for (int i = 0; i < 2; ++i) {
    const int piece = 2 * wave + i;
    const int drow = 8 * piece + (lane >> 3);
    glds16(aT + (size_t)(n0 + drow) * SEQ + m0 + (lane & 7) * 8, smem + G2_A_OFF + piece * 1024 + lane * 16);
  }

  G2Ctx c;
  c.wb32 = lds_addr(smem) + wave * G2_WAVE_B;
  c.hsrc = hf + ((size_t)((m0 >> 5) * NKS + 22 * wave) * 64 + lane) * 8;
  c.wd = wd;
  c.wcol = n0 + 8 * (lane & 7);
  c.wrow0 = 352 * wave + (lane >> 3);
  c.ww = c.wb32 + (lane >> 3) * 128 + (((lane & 7) ^ (((lane >> 4) & 1) << 2)) * 16);

  const int r = lane & 31, h = lane >> 5;
  const int blk = (lane >> 4) & 1, q = (lane & 15) >> 2, p = lane & 3;
  c.woff = (4 * h + q) * 128 + (((2 * blk + (p >> 1)) ^ (((q >> 1) & 1) << 2)) * 16) + 8 * (p & 1);

  f32x16 acc[2][2];
#pragma unroll
  for (int i = 0; i < 2; ++i)
#pragma unroll
    for (int j = 0; j < 2; ++j) acc[i][j] = f32x16{};
  bf16x8 hfr[11][4];
  u32x4 wr[11][4];

  G2Step<0>::issue(hfr, wr, c);
  G2Step<1>::issue(hfr, wr, c);
  if constexpr (G2_DEPTH > 2) G2Step<2>::issue(hfr, wr, c);
  if constexpr (G2_DEPTH > 3) G2Step<3>::issue(hfr, wr, c);
  if constexpr (G2_DEPTH > 4) G2Step<4>::issue(hfr, wr, c);
  if constexpr (G2_DEPTH > 5) G2Step<5>::issue(hfr, wr, c);
  if constexpr (G2_DEPTH > 6) G2Step<6>::issue(hfr, wr, c);
  STAMP2(1);
  STAMP2(2);
  G2Step<0>::run(acc, hfr, wr, c);
  STAMP2(3);
  __syncthreads();
  {
    char* pw = smem + wave * (64 * G2_PITCH);
#pragma unroll
    for (int c = 0; c < 2; ++c)
#pragma unroll
      for (int mi = 0; mi < 2; ++mi)
#pragma unroll
        for (int gq = 0; gq < 4; ++gq) {
          const f32x4 v = {acc[c][mi][4 * gq], acc[c][mi][4 * gq + 1], acc[c][mi][4 * gq + 2], acc[c][mi][4 * gq + 3]};
          *(f32x4*)(pw + (32 * mi + r) * G2_PITCH + (32 * c + 8 * gq + 4 * h) * 4) = v;
        }
  }
  __syncthreads();
  STAMP2(4);
  {
    const int m = tid >> 2, nq = (tid & 3) * 16;
    const float g = gin[m0 + m];
    const float omg = rbf(1.0f - g);
    float b[16];
#pragma unroll
    for (int j = 0; j < 4; ++j) {
      f32x4 s = *(const f32x4*)(smem + m * G2_PITCH + (nq + 4 * j) * 4);
#pragma unroll
      for (int w = 1; w < 4; ++w) s += *(const f32x4*)(smem + w * (64 * G2_PITCH) + m * G2_PITCH + (nq + 4 * j) * 4);
#pragma unroll
      for (int e = 0; e < 4; ++e) b[4 * j + e] = s[e];
    }
    const u16* aimg = (const u16*)(smem + G2_A_OFF);
    unsigned o[8];
#pragma unroll
    for (int j = 0; j < 8; ++j) {
      const float a0 = bf2f(aimg[(nq + 2 * j) * 64 + m]);
      const float a1 = bf2f(aimg[(nq + 2 * j + 1) * 64 + m]);
      const float o0 = rbf(g * a0) + rbf(omg * rbf(b[2 * j]));
      const float o1 = rbf(g * a1) + rbf(omg * rbf(b[2 * j + 1]));
      o[j] = pk_bf16(o0, o1);
    }
    u16* op = out + (size_t)(m0 + m) * DM + n0 + nq;
    *(u32x4*)op = u32x4{o[0], o[1], o[2], o[3]};
    *(u32x4*)(op + 8) = u32x4{o[4], o[5], o[6], o[7]};
  }
  STAMP2(5);
}

extern "C" void kernel_launch(void* const* d_in, const int* in_sizes, int n_in, void* d_out, int out_size,
                              void* d_ws, size_t ws_size, hipStream_t stream) {
  const u16* u = (const u16*)d_in[0];
  const u16* logAr = (const u16*)d_in[1];
  const u16* Aim = (const u16*)d_in[2];
  const u16* Cre = (const u16*)d_in[3];
  const u16* Cim = (const u16*)d_in[4];
  const u16* logdt = (const u16*)d_in[5];
  const u16* Dskip = (const u16*)d_in[6];
  const u16* wg = (const u16*)d_in[8];
  const u16* wu = (const u16*)d_in[9];
  const u16* wd = (const u16*)d_in[10];
  const u16* gw = (const u16*)d_in[11];
  char* ws = (char*)d_ws;
  u16* hbuf = (u16*)ws;
  u16* aT = (u16*)(ws + (size_t)SEQ * FFP * 2);
  float* gbuf = (float*)(ws + (size_t)SEQ * FFP * 2 + (size_t)DM * SEQ * 2);
  k1_kernel<<<512, 256, 0, stream>>>(u, logAr, Aim, Cre, Cim, logdt, Dskip, wg, wu, gw, hbuf, aT, gbuf
      );
  k2_kernel<<<256, 256, 0, stream>>>(hbuf, wd, aT, gbuf, (u16*)d_out
      );
}
```

```cpp
#include <hip/hip_runtime.h>
#include <hip/hip_bf16.h>
#include <stdint.h>
#include <type_traits>

typedef unsigned short u16;
typedef __attribute__((ext_vector_type(8))) short bf16x8;
typedef __attribute__((ext_vector_type(4))) short s16x4;
typedef __attribute__((ext_vector_type(4))) float f32x4;
typedef __attribute__((ext_vector_type(16))) float f32x16;
typedef __attribute__((ext_vector_type(4))) unsigned u32x4;
typedef __attribute__((ext_vector_type(2))) unsigned u32x2;
typedef __attribute__((ext_vector_type(2))) __bf16 bf16x2_t;
typedef __attribute__((ext_vector_type(2))) float f32x2;
typedef __attribute__((address_space(3))) s16x4* lds_s16x4_ptr;

constexpr int SEQ = 2048, DM = 512, NST = 128, FF = 1376, FFP = 1408;
constexpr int NSSM = 256;
constexpr int NKS = 88;

__device__ __forceinline__ unsigned pk_bf16(float lo, float hi) {
  f32x2 v = {lo, hi};
  bf16x2_t b = __builtin_convertvector(v, bf16x2_t);
  return __builtin_bit_cast(unsigned, b);
}
__device__ __forceinline__ float bf_lo(unsigned w) { return __uint_as_float(w << 16); }
__device__ __forceinline__ float bf_hi(unsigned w) { return __uint_as_float(w & 0xffff0000u); }
__device__ __forceinline__ float rbf(float x) { return bf_lo(pk_bf16(x, 0.f)); }
__device__ __forceinline__ float bf2f(u16 v) { return __uint_as_float(((unsigned)v) << 16); }

__device__ __forceinline__ bf16x8 mk8(unsigned a, unsigned b, unsigned c, unsigned d) {
  u32x4 v = {a, b, c, d};
  return __builtin_bit_cast(bf16x8, v);
}
__device__ __forceinline__ bf16x8 cat4(s16x4 a, s16x4 b) {
  return __builtin_shufflevector(a, b, 0, 1, 2, 3, 4, 5, 6, 7);
}
__device__ __forceinline__ s16x4 tr_read(const char* smem_ptr) {
  return __builtin_amdgcn_ds_read_tr16_b64_v4i16((lds_s16x4_ptr)(smem_ptr));
}
__device__ __forceinline__ unsigned lds_addr(const void* p) {
  return (unsigned)(size_t)(const __attribute__((address_space(3))) char*)p;
}
__device__ __forceinline__ void glds16(const void* g, char* l) {
  __builtin_amdgcn_global_load_lds((const unsigned*)g, (unsigned*)l, 16, 0, 0);
}

#define STAMP2(i)
#define DBG2_ARG
#define DBG2_PASS
#define STAMP(i)
#define DBG_ARG
#define DBG_PASS
constexpr int U_COPY_B = 4160;
constexpr int U_CH_B = 4 * U_COPY_B;
constexpr int U_REGION_B = 2 * U_CH_B;
constexpr int KR_PART_B = 4576;
constexpr int KR_OFF = U_REGION_B;
constexpr int KQ_B = 8208;
constexpr int KX_OFF = 2 * KQ_B;
constexpr int SSM_LDS = KR_OFF + 4 * KR_PART_B;
static_assert(KX_OFF + 4 * 1536 <= U_REGION_B, "scratch must fit the U region");

#define DS_TR(dst, addr, off) asm volatile("ds_read_b64_tr_b16 %0, %1 offset:%2" : "=v"(dst) : "v"(addr), "i"(off) : "memory")
#define DS_B128(dst, addr, off) asm volatile("ds_read_b128 %0, %1 offset:%2" : "=v"(dst) : "v"(addr), "i"(off) : "memory")
#define DS_B64(dst, addr, off) asm volatile("ds_read_b64 %0, %1 offset:%2" : "=v"(dst) : "v"(addr), "i"(off) : "memory")
#define MFMA16(acc, a, b) asm volatile("v_mfma_f32_16x16x32_bf16 %0, %1, %2, %0" : "+v"(acc) : "v"(a), "v"(b))

struct ConvFrag {
  s16x4 a[8][2];
  bf16x8 bh[8], bl[8];
};
template <int RI, int K>
__device__ __forceinline__ void conv_read(ConvFrag& f, unsigned ua, unsigned kb) {
  if constexpr (K < 16) {
    DS_B64(f.a[K >> 1][K & 1], ua, 64 * (8 * (K >> 1) + RI) + 8 * (K & 1));
  } else if constexpr (K < 24) {
    DS_B128(f.bh[K - 16], kb, 64 * (56 - 8 * (K - 16) + RI));
  } else {
    DS_B128(f.bl[K - 24], kb, 64 * (56 - 8 * (K - 24) + RI) + KR_PART_B);
  }
}
template <int RI, int K>
__device__ __forceinline__ void conv_read_all(ConvFrag& f, unsigned ua, unsigned kb) {
  conv_read<RI, K>(f, ua, kb);
  if constexpr (K + 1 < 32) conv_read_all<RI, K + 1>(f, ua, kb);
}
#define CONV_WAIT(f)                                                                                                  \
  do {                                                                                                                \
    asm volatile("s_waitcnt lgkmcnt(0)"                                                                               \
                 : "+v"(f.a[0][0]), "+v"(f.a[0][1]), "+v"(f.a[1][0]), "+v"(f.a[1][1]), "+v"(f.a[2][0]), "+v"(f.a[2][1]), \
                   "+v"(f.a[3][0]), "+v"(f.a[3][1]), "+v"(f.a[4][0]), "+v"(f.a[4][1]), "+v"(f.a[5][0]), "+v"(f.a[5][1]), \
                   "+v"(f.a[6][0]), "+v"(f.a[6][1]), "+v"(f.a[7][0]), "+v"(f.a[7][1])::"memory");                     \
    asm volatile("" : "+v"(f.bh[0]), "+v"(f.bh[1]), "+v"(f.bh[2]), "+v"(f.bh[3]), "+v"(f.bh[4]), "+v"(f.bh[5]),        \
                 "+v"(f.bh[6]), "+v"(f.bh[7]), "+v"(f.bl[0]), "+v"(f.bl[1]), "+v"(f.bl[2]), "+v"(f.bl[3]),            \
                 "+v"(f.bl[4]), "+v"(f.bl[5]), "+v"(f.bl[6]), "+v"(f.bl[7])::"memory");                              \
  } while (0)
constexpr int conv_tau(int P) { int t = 0; while (P >= 8 - t) { P -= 8 - t; ++t; } return t; }
constexpr int conv_sig(int P) { int t = 0; while (P >= 8 - t) { P -= 8 - t; ++t; } return P; }
template <int RI, int P>
__device__ __forceinline__ void conv_pairs(f32x4 (&acc)[8], const bf16x8 (&A)[8], ConvFrag& cur, ConvFrag& nxt, unsigned ua,
                                           unsigned kb) {
  constexpr int T = conv_tau(P), SG = conv_sig(P);
  MFMA16(acc[T + SG], A[SG], cur.bl[T]);
  if constexpr (RI < 3 && P < 32) conv_read<RI + 1, P>(nxt, ua, kb);
  MFMA16(acc[T + SG], A[SG], cur.bh[T]);
  if constexpr (P + 1 < 36) conv_pairs<RI, P + 1>(acc, A, cur, nxt, ua, kb);
}
template <int RI>
__device__ __forceinline__ void conv_rho(f32x4 (&acc)[8], ConvFrag& cur, ConvFrag& nxt, unsigned ua, unsigned kb) {
  CONV_WAIT(cur);
  bf16x8 A[8];
#pragma unroll
  for (int i = 0; i < 8; ++i) A[i] = cat4(cur.a[i][0], cur.a[i][1]);
  conv_pairs<RI, 0>(acc, A, cur, nxt, ua, kb);
  if constexpr (RI < 3) conv_rho<RI + 1>(acc, nxt, cur, ua, kb);
}

__device__ __forceinline__ void split_pair(f32x2 v, unsigned& hi, unsigned& lo) {
  hi = pk_bf16(v[0], v[1]);
  const f32x2 r = v - f32x2{bf_lo(hi), bf_hi(hi)};
  lo = pk_bf16(r[0], r[1]);
}

__device__ __forceinline__ void ssm_unit(int w, const u16* __restrict__ u, const u16* __restrict__ logAr,
                                         const u16* __restrict__ Aim, const u16* __restrict__ Cre,
                                         const u16* __restrict__ Cim, const u16* __restrict__ logdt,
                                         const u16* __restrict__ Dskip, u16* __restrict__ aT, char* smem DBG_ARG) {
  const int tid = threadIdx.x, lane = tid & 63;
  const int wave = __builtin_amdgcn_readfirstlane(tid >> 6);
  const int ch = wave >> 1, w2 = wave & 1;
  const int d0 = 2 * w;
  const int d = d0 + ch;

  const int pidx = d0 * NST + tid;
  const u16 r_lar = logAr[pidx], r_aim = Aim[pidx], r_cre = Cre[pidx], r_cim = Cim[pidx], r_ldt = logdt[d], r_dsk = Dskip[d];
  const unsigned* u32p = (const unsigned*)(u + d0);
  unsigned uw[8], up[3];
#pragma unroll
  for (int i = 0; i < 3; ++i) up[i] = 0;
  if (lane == 0 && tid > 0) {
#pragma unroll
    for (int i = 0; i < 3; ++i) up[i] = u32p[(size_t)(8 * tid - 3 + i) * (DM / 2)];
  }
  const unsigned uoff = (unsigned)tid * 8192u;
  {
    const float dt = rbf(expf(bf2f(r_ldt)));
    const float are = rbf(expf(bf2f(r_lar)));
    f32x4 prm;
    prm[0] = (-are * dt) * 1.44269504088896340736f;
    prm[1] = (bf2f(r_aim) * dt) * 0.15915494309189533577f;
    prm[2] = 2.0f * (bf2f(r_cre) * dt);
    prm[3] = 2.0f * (bf2f(r_cim) * dt);
    *(f32x4*)(smem + KR_OFF + tid * 16) = prm;
  }
  __syncthreads();

  const int r = lane & 31, h = lane >> 5;
  f32x16 dacc = {};
  {
    int p1i;
    if (r < 8) p1i = r;
    else if (r < 12) p1i = 256 + 2 * (r - 8);
    else if (r < 16) p1i = 512 + 256 * ((r - 12) >> 1) + 4 * ((r - 12) & 1);
    else if (r < 21) p1i = 1024 + 256 * (r - 16);
    else p1i = 0;
    const float p1 = (float)p1i, p2 = (float)(8 * r);
    const f32x2 P1 = {p1, p1}, P2 = {p2, p2};
    const char* prm_base = smem + KR_OFF + (ch * 128 + 64 * w2 + 4 * h) * 16;
#pragma unroll
    for (int ks = 0; ks < 8; ++ks) {
      if (ks >= 4) {
        asm volatile("global_load_dword %0, %1, %2 offset:%3" : "=v"(uw[2 * ks - 8]) : "v"(uoff), "s"((const char*)u32p + ((2 * ks - 8) >> 2) * 4096), "i"(((2 * ks - 8) & 3) * 1024) : "memory");
        asm volatile("global_load_dword %0, %1, %2 offset:%3" : "=v"(uw[2 * ks - 7]) : "v"(uoff), "s"((const char*)u32p + ((2 * ks - 7) >> 2) * 4096), "i"(((2 * ks - 7) & 3) * 1024) : "memory");
      }
      f32x4 prm[4];
#pragma unroll
      for (int q = 0; q < 4; ++q) prm[q] = *(const f32x4*)(prm_base + (8 * ks + q) * 16);
      f32x2 za[4], zb[4];
#pragma unroll
      for (int q = 0; q < 4; ++q) { za[q] = f32x2{prm[q][0], prm[q][1]} * P1; zb[q] = f32x2{prm[q][0], prm[q][1]} * P2; }
      float ea[4], eb[4], fa[4], fb[4];
#pragma unroll
      for (int q = 0; q < 4; ++q) {
        ea[q] = __builtin_amdgcn_exp2f(za[q][0]); eb[q] = __builtin_amdgcn_exp2f(zb[q][0]);
        fa[q] = __builtin_amdgcn_fractf(za[q][1]); fb[q] = __builtin_amdgcn_fractf(zb[q][1]);
      }
      f32x2 ca[4], cb[4];
#pragma unroll
      for (int q = 0; q < 4; ++q) {
        ca[q] = f32x2{__builtin_amdgcn_cosf(fa[q]), __builtin_amdgcn_sinf(fa[q])};
        cb[q] = f32x2{__builtin_amdgcn_cosf(fb[q]), __builtin_amdgcn_sinf(fb[q])};
      }
      unsigned ah[4], al[4], bh[4], bl[4];
#pragma unroll
      for (int q = 0; q < 4; ++q) {
        const f32x2 ce = f32x2{prm[q][2], prm[q][3]} * f32x2{ea[q], ea[q]};
        f32x2 g = f32x2{ce[0], -ce[0]} * ca[q];
        g = __builtin_elementwise_fma(f32x2{-ce[1], -ce[1]}, f32x2{ca[q][1], ca[q][0]}, g);
        split_pair(g, ah[q], al[q]);
        split_pair(cb[q] * f32x2{eb[q], eb[q]}, bh[q], bl[q]);
      }
      const bf16x8 Ah = mk8(ah[0], ah[1], ah[2], ah[3]), Al = mk8(al[0], al[1], al[2], al[3]);
      const bf16x8 Bh = mk8(bh[0], bh[1], bh[2], bh[3]), Bl = mk8(bl[0], bl[1], bl[2], bl[3]);
      dacc = __builtin_amdgcn_mfma_f32_32x32x16_bf16(Al, Bh, dacc, 0, 0, 0);
      dacc = __builtin_amdgcn_mfma_f32_32x32x16_bf16(Ah, Bl, dacc, 0, 0, 0);
      dacc = __builtin_amdgcn_mfma_f32_32x32x16_bf16(Ah, Bh, dacc, 0, 0, 0);
    }
  }
  {
    float* xo = (float*)(smem + KX_OFF + wave * 1536) + lane;
    const float* xi = (const float*)(smem + KX_OFF + (wave ^ 1) * 1536) + lane;
    float mine[6];
    if (w2 == 0) {
#pragma unroll
      for (int e = 0; e < 6; ++e) { xo[e * 64] = dacc[6 + e]; mine[e] = dacc[e]; }
    } else {
#pragma unroll
      for (int e = 0; e < 6; ++e) { xo[e * 64] = dacc[e]; mine[e] = dacc[6 + e]; }
    }
    __syncthreads();
    unsigned* Kq = (unsigned*)(smem + ch * KQ_B);
    unsigned pq[6];
#pragma unroll
    for (int e = 0; e < 6; ++e) {
      const float v = mine[e] + xi[e * 64];
      const unsigned hi = pk_bf16(v, 0.f);
      pq[e] = pk_bf16(0.f, v - bf_lo(hi)) | hi;
    }
    if (w2 == 0) {
#pragma unroll
      for (int e = 0; e < 4; ++e) Kq[4 * h + e + 8 * r] = pq[e];
      Kq[(h ? 512 : 256) + 8 * r] = pq[4];
      Kq[(h ? 516 : 258) + 8 * r] = pq[5];
    } else {
      Kq[(h ? 768 : 260) + 8 * r] = pq[0];
      Kq[(h ? 772 : 262) + 8 * r] = pq[1];
      if (h == 0) {
#pragma unroll
        for (int e = 0; e < 4; ++e) Kq[1024 + 256 * e + 8 * r] = pq[2 + e];
      } else if (r == 0) {
        Kq[2048] = pq[2];
      }
    }
  }
  __syncthreads();

  {
    const int t = tid & 127;
    const unsigned* Kq = (const unsigned*)(smem + ch * KQ_B);
    unsigned* krh = (unsigned*)(smem + KR_OFF + (ch * 2) * KR_PART_B);
    unsigned* krl = (unsigned*)((char*)krh + KR_PART_B);
    unsigned q0[8], q1[8];
#pragma unroll
    for (int m = 0; m < 8; ++m) {
      const int sh = m < 4 ? 3 : (m < 6 ? 2 : (m < 7 ? 1 : 0));
      const int l0 = 2047 - 2 * (t + 128 * m), l1 = l0 - 1;
      int pa = l0, pb = l1;
      if (sh) {
        pa = (l0 + (1 << (sh - 1)) - 1 + ((l0 >> sh) & 1)) & ~((1 << sh) - 1);
        pb = (l1 + (1 << (sh - 1)) - 1 + ((l1 >> sh) & 1)) & ~((1 << sh) - 1);
      }
      q0[m] = Kq[pa];
      q1[m] = Kq[pb];
    }
#pragma unroll
    for (int m = 0; m < 8; ++m) {
      krh[t + 128 * m] = __builtin_amdgcn_perm(q1[m], q0[m], 0x05040100u);
      krl[t + 128 * m] = __builtin_amdgcn_perm(q1[m], q0[m], 0x07060302u);
    }
    if (t < 120) { krh[1024 + t] = 0; krl[1024 + t] = 0; }
  }
  __syncthreads();

  asm volatile("s_waitcnt vmcnt(0)" : "+v"(uw[0]), "+v"(uw[1]), "+v"(uw[2]), "+v"(uw[3]), "+v"(uw[4]), "+v"(uw[5]), "+v"(uw[6]), "+v"(uw[7])::"memory");
  {
    unsigned e[11];
#pragma unroll
    for (int i = 0; i < 3; ++i) {
      const unsigned nb = __shfl_up(uw[5 + i], 1);
      e[i] = (lane == 0) ? up[i] : nb;
    }
#pragma unroll
    for (int i = 0; i < 8; ++i) e[3 + i] = uw[i];
    unsigned p0[10], p1[10];
#pragma unroll
    for (int i = 0; i < 10; ++i) {
      p0[i] = __builtin_amdgcn_perm(e[i + 1], e[i], 0x05040100u);
      p1[i] = __builtin_amdgcn_perm(e[i + 1], e[i], 0x07060302u);
    }
#pragma unroll
    for (int c = 0; c < 4; ++c) {
      *(u32x4*)(smem + c * U_COPY_B + (tid + 2) * 16) = u32x4{p0[c], p0[c + 2], p0[c + 4], p0[c + 6]};
      *(u32x4*)(smem + U_CH_B + c * U_COPY_B + (tid + 2) * 16) = u32x4{p1[c], p1[c + 2], p1[c + 4], p1[c + 6]};
    }
    if (tid < 16) *(u32x4*)(smem + (tid >> 1) * U_COPY_B + (tid & 1) * 16) = u32x4{0, 0, 0, 0};
  }
  __syncthreads();

  f32x4 acc[8];
#pragma unroll
  for (int t = 0; t < 8; ++t) acc[t] = f32x4{0.f, 0.f, 0.f, 0.f};
  asm volatile("s_nop 4" : "+v"(acc[0]), "+v"(acc[1]), "+v"(acc[2]), "+v"(acc[3]), "+v"(acc[4]), "+v"(acc[5]), "+v"(acc[6]), "+v"(acc[7]));
  {
    const int r16 = lane & 15, hq = lane >> 4;
    const unsigned sb = lds_addr(smem);
    const unsigned ua = sb + ch * U_CH_B + (r16 & 3) * U_COPY_B + 2 * (4 + (r16 & ~3) + 8 * hq) + 64 * 4 * w2;
    const unsigned kb = sb + KR_OFF + (ch * 2) * KR_PART_B + 2 * (2032 - 16 * r16 + 8 * hq) - 64 * 56 + 64 * 4 * w2;
    ConvFrag f0, f1;
    conv_read_all<0, 0>(f0, ua, kb);
    conv_rho<0>(acc, f0, f1, ua, kb);
  }
  asm volatile("s_nop 15" : "+v"(acc[0]), "+v"(acc[1]), "+v"(acc[2]), "+v"(acc[3]), "+v"(acc[4]), "+v"(acc[5]), "+v"(acc[6]), "+v"(acc[7]));
  __syncthreads();
  {
    char* xw = smem + KR_OFF + (ch * 2 + w2) * 4096 + lane * 16;
    const char* xr = smem + KR_OFF + (ch * 2 + (w2 ^ 1)) * 4096 + lane * 16;
    f32x4 y[4];
    if (w2 == 0) {
#pragma unroll
      for (int i = 0; i < 4; ++i) { *(f32x4*)(xw + i * 1024) = acc[4 + i]; y[i] = acc[i]; }
    } else {
#pragma unroll
      for (int i = 0; i < 4; ++i) { *(f32x4*)(xw + i * 1024) = acc[i]; y[i] = acc[4 + i]; }
    }
    __syncthreads();
    const float Dk = bf2f(r_dsk);
#pragma unroll
    for (int i = 0; i < 4; ++i) {
      const f32x4 o = *(const f32x4*)(xr + i * 1024);
      const int l0 = 256 * (4 * w2 + i) + 16 * (lane & 15) + 4 * (lane >> 4);
      const u32x2 uu = *(const u32x2*)(smem + ch * U_CH_B + 3 * U_COPY_B + 2 * (16 + l0));
      const unsigned y01 = pk_bf16(y[i][0] + o[0], y[i][1] + o[1]), y23 = pk_bf16(y[i][2] + o[2], y[i][3] + o[3]);
      const unsigned s01 = pk_bf16(bf_lo(uu[0]) * Dk, bf_hi(uu[0]) * Dk), s23 = pk_bf16(bf_lo(uu[1]) * Dk, bf_hi(uu[1]) * Dk);
      *(u32x2*)(aT + (size_t)d * SEQ + l0) = u32x2{pk_bf16(bf_lo(y01) + bf_lo(s01), bf_hi(y01) + bf_hi(s01)),
                                                  pk_bf16(bf_lo(y23) + bf_lo(s23), bf_hi(y23) + bf_hi(s23))};
    }
  }
}

__device__ __forceinline__ float gelu_tanh(float x) {
  const float z = 0.7978845608028654f * (x + 0.044715f * x * x * x);
  const float e = __builtin_amdgcn_exp2f(-2.8853900817779268f * z);
  return x * __builtin_amdgcn_rcpf(1.0f + e);
}

constexpr int G1_WSLOT_B = 12288;
constexpr int G1_X_OFF = 3 * G1_WSLOT_B;
constexpr int G1_LDS = G1_X_OFF + 32768;

constexpr int g1_pos(bool isx, int id) {
  if (isx && id == 0) return 4;
  if (!isx && id == 0) return 7;
  if (!isx && id == 1) return 10;
  if (isx && id == 1) return 14;
  if (!isx && id == 2) return 17;
  if (!isx && id == 3) return 20;
  if (!isx && id == 4) return 23;
  if (isx && id == 2) return 27;
  int n = 27;
  for (int t = 0; t < 32; ++t) {
    if ((t & 1) == 0 && t / 2 + 5 < 16) { n += 3; if (!isx && t / 2 + 5 == id) return n; }
    if ((t & 3) == 1 && t / 4 + 3 < 8) { n += 4; if (isx && t / 4 + 3 == id) return n; }
  }
  return -1;
}
constexpr int g1_issued_before(int step) {
  int n = 27;
  for (int t = 0; t < step; ++t) {
    if ((t & 1) == 0 && t / 2 + 5 < 16) n += 3;
    if ((t & 3) == 1 && t / 4 + 3 < 8) n += 4;
  }
  return n;
}

#define GLOAD128(dst, ptr) asm volatile("global_load_dwordx4 %0, %1, off" : "=v"(dst) : "v"(ptr) : "memory")
#define GLOAD128S(dst, voff, sbase) asm volatile("global_load_dwordx4 %0, %1, %2" : "=v"(dst) : "v"(voff), "s"(sbase) : "memory")
#define GLOAD128SA(dst, voff, sbase) asm volatile("global_load_dwordx4 %0, %1, %2" : "=a"(dst) : "v"(voff), "s"(sbase) : "memory")
#define GLOAD128A(dst, ptr) asm volatile("global_load_dwordx4 %0, %1, off" : "=a"(dst) : "v"(ptr) : "memory")
#define DS_W128(addr, val, off) asm volatile("ds_write_b128 %0, %1 offset:%2" ::"v"(addr), "v"(val), "i"(off) : "memory")
#define MFMA32(acc, a, b) asm volatile("v_mfma_f32_32x32x16_bf16 %0, %1, %2, %0" : "+v"(acc) : "v"(a), "v"(b))

struct G1Ctx {
  const char* wbase;
  const char* xbase;
  unsigned wo[3], xo;
  unsigned woff, woff_o, ww[3], xw, xrd2[2][4];
  unsigned junk[4];
};
struct G1Frag {
  s16x4 w[3][2];
  bf16x8 x[2];
};

#define G1_RD_W(f, t, OFF)                                                        \
  do {                                                                            \
    if ((t) & 1) { DS_TR(f.w[t][0], c.woff_o, (OFF) + (t) * 64); DS_TR(f.w[t][1], c.woff_o, (OFF) + (t) * 64 + 4 * 384); } \
    else { DS_TR(f.w[t][0], c.woff, (OFF) + (t) * 64); DS_TR(f.w[t][1], c.woff, (OFF) + (t) * 64 + 4 * 384); }            \
  } while (0)
#define G1_RD_X(f, mi, N) DS_B128(f.x[mi], c.xrd2[mi][(N) & 3], (((N) >> 2) & 1) * 16384)
template <int N>
__device__ __forceinline__ void g1_read(G1Frag& f, const G1Ctx& c) {
  constexpr int OFF = ((N >> 1) % 3) * G1_WSLOT_B + (N & 1) * 16 * 384;
  G1_RD_X(f, 0, N); G1_RD_X(f, 1, N);
  G1_RD_W(f, 0, OFF); G1_RD_W(f, 1, OFF); G1_RD_W(f, 2, OFF);
}
#define G1_WAIT_FRAG(f, STR)                                                                                       \
  asm volatile(STR : "+v"(f.w[0][0]), "+v"(f.w[0][1]), "+v"(f.w[1][0]), "+v"(f.w[1][1]), "+v"(f.w[2][0]),        \
               "+v"(f.w[2][1]), "+v"(f.x[0]), "+v"(f.x[1])::"memory")

template <int N>
struct G1Step {
  static __device__ __forceinline__ void run(f32x16 (&acc)[6], u32x4 (&xr)[2][4], u32x4 (&wr)[3][3], G1Frag& cur,
                                             G1Frag& nxt, G1Ctx& c) {
    constexpr int ISS = g1_issued_before(N);
    constexpr int S = N >> 1;
    constexpr bool RD = N + 1 < 32;
    constexpr int OFF = (((N + 1) >> 1) % 3) * G1_WSLOT_B + ((N + 1) & 1) * 16 * 384;
    if constexpr ((N & 1) == 0 && N > 0) G1_WAIT_FRAG(cur, "s_waitcnt lgkmcnt(0)\n\ts_barrier");
    else G1_WAIT_FRAG(cur, "s_waitcnt lgkmcnt(0)");
    bf16x8 b[3];
#pragma unroll
    for (int t = 0; t < 3; ++t) b[t] = cat4(cur.w[t][0], cur.w[t][1]);
    MFMA32(acc[0], b[0], cur.x[0]);
    if constexpr (RD) { G1_RD_X(nxt, 0, N + 1); G1_RD_W(nxt, 0, OFF); }
    MFMA32(acc[3], b[0], cur.x[1]);
    if constexpr (RD) { G1_RD_X(nxt, 1, N + 1); G1_RD_W(nxt, 1, OFF); }
    MFMA32(acc[1], b[1], cur.x[0]);
    if constexpr (RD) { G1_RD_W(nxt, 2, OFF); }
    MFMA32(acc[4], b[1], cur.x[1]);
    if constexpr ((N & 1) == 0) {
      if constexpr (S + 2 < 16) {
        constexpr int R = (S + 2) % 3;
        constexpr int NW = ISS - g1_pos(false, S + 2);
        asm volatile("s_waitcnt vmcnt(%3)" : "+v"(wr[R][0]), "+v"(wr[R][1]), "+v"(wr[R][2]) : "i"(NW) : "memory");
#pragma unroll
        for (int i = 0; i < 3; ++i) DS_W128(c.ww[i], wr[R][i], R * G1_WSLOT_B);
      }
    }
    if constexpr ((N & 3) == 1) {
      constexpr int B = N / 4 + 1;
      if constexpr (B < 8) {
        constexpr int NW = ISS - g1_pos(true, B);
        asm volatile("s_waitcnt vmcnt(%4)" : "+v"(xr[B & 1][0]), "+v"(xr[B & 1][1]), "+v"(xr[B & 1][2]), "+v"(xr[B & 1][3]) : "i"(NW) : "memory");
#pragma unroll
        for (int i = 0; i < 4; ++i) DS_W128(c.xw, xr[B & 1][i], (B & 1) * 16384 + i * 1024);
      }
    }
    MFMA32(acc[2], b[2], cur.x[0]);
    if constexpr ((N & 1) == 0 && S + 5 < 16) {
#pragma unroll
      for (int i = 0; i < 3; ++i) GLOAD128S(wr[(S + 5) % 3][i], c.wo[i], c.wbase + (size_t)(S + 5) * 32 * FF * 2);
    }
    if constexpr ((N & 3) == 1 && N / 4 + 3 < 8) {
      constexpr int B = N / 4 + 1;
#pragma unroll
      for (int i = 0; i < 4; ++i) GLOAD128S(xr[B & 1][i], c.xo, c.xbase + (size_t)(8 * i) * DM * 2 + (B + 2) * 128);
    }
    MFMA32(acc[5], b[2], cur.x[1]);
    if constexpr (N + 1 < 32) G1Step<N + 1>::run(acc, xr, wr, nxt, cur, c);
  }
};

__device__ __forceinline__ void gemm1_tile(int t, const u16* __restrict__ x, const u16* __restrict__ wg,
                                           const u16* __restrict__ wu, u16* __restrict__ hf, char* smem DBG_ARG) {
  const int tid = threadIdx.x, lane = tid & 63;
  const int wave = __builtin_amdgcn_readfirstlane(tid >> 6);
  const int fb = t >> 4, mb = t & 15;
  const int f0 = 96 * fb;
  const int wm = wave & 1, isup = wave >> 1;
  G1Ctx c;
  const unsigned sbase = lds_addr(smem);
  c.wbase = (const char*)(isup ? wu : wg);
#pragma unroll
  for (int i = 0; i < 3; ++i) {
    const int id = 192 * (wave & 1) + 64 * i + lane;
    const int row = id / 12, cc = id % 12;
    int col = f0 + 8 * cc;
    if (col > FF - 8) col = FF - 8;
    c.wo[i] = (unsigned)(row * FF + col) * 2u;
    c.ww[i] = sbase + row * 384 + ((cc + 12 * isup) ^ (((row >> 1) & 1) << 2)) * 16;
  }
  c.xbase = (const char*)(x + (size_t)(128 * mb + 32 * wave) * DM);
  c.xo = (unsigned)((lane >> 3) * DM + (lane & 7) * 8) * 2u;
  const unsigned xl = sbase + G1_X_OFF;
  c.xw = xl + (32 * wave + (lane >> 3)) * 128 + (((lane & 7) ^ (lane >> 3)) * 16);
  const int r = lane & 31, h = lane >> 5;
#pragma unroll
  for (int i = 0; i < 4; ++i) {
    c.xrd2[0][i] = xl + (64 * wm + 32 * isup + r) * 128 + (((2 * i + h) ^ (r & 7)) * 16);
    c.xrd2[1][i] = xl + (64 * wm + 32 * (1 - isup) + r) * 128 + (((2 * i + h) ^ (r & 7)) * 16);
  }
  const int blk = (lane >> 4) & 1, q = (lane & 15) >> 2, p = lane & 3;
  const int sw = (q >> 1) & 1;
  {
    const unsigned e = sbase + (8 * h + q) * 384 + (2 * blk + (p >> 1) + 4 * sw) * 16 + 8 * (p & 1);
    const unsigned o = sbase + (8 * h + q) * 384 + (2 * blk + (p >> 1) - 4 * sw) * 16 + 8 * (p & 1);
    c.woff = isup ? o + 192 : e;
    c.woff_o = isup ? e + 192 : o;
  }

  f32x16 acc[6];
#pragma unroll
  for (int i = 0; i < 6; ++i) acc[i] = f32x16{};
  asm volatile("s_nop 4" : "+v"(acc[0]), "+v"(acc[1]), "+v"(acc[2]), "+v"(acc[3]), "+v"(acc[4]), "+v"(acc[5]));
  u32x4 xr[2][4], wr[3][3];
  G1Frag fa, fb2;
#define G1_ISSUE_X(R, B) _Pragma("unroll") for (int i = 0; i < 4; ++i) GLOAD128S(xr[R][i], c.xo, c.xbase + (size_t)(8 * i) * DM * 2 + (B) * 128)
#define G1_ISSUE_W(R, S) _Pragma("unroll") for (int i = 0; i < 3; ++i) GLOAD128S(wr[R][i], c.wo[i], c.wbase + (size_t)(S) * 32 * FF * 2)
  G1_ISSUE_X(0, 0); G1_ISSUE_W(0, 0); G1_ISSUE_W(1, 1); G1_ISSUE_X(1, 1); G1_ISSUE_W(2, 2);
#define G1_EXTRA 0
  asm volatile("s_waitcnt vmcnt(%10)"
               : "+v"(wr[0][0]), "+v"(wr[0][1]), "+v"(wr[0][2]), "+v"(wr[1][0]), "+v"(wr[1][1]), "+v"(wr[1][2]),
                 "+v"(xr[0][0]), "+v"(xr[0][1]), "+v"(xr[0][2]), "+v"(xr[0][3]) : "i"(7 + G1_EXTRA) : "memory");
#pragma unroll
  for (int i = 0; i < 3; ++i) { DS_W128(c.ww[i], wr[0][i], 0); DS_W128(c.ww[i], wr[1][i], G1_WSLOT_B); }
#pragma unroll
  for (int i = 0; i < 4; ++i) DS_W128(c.xw, xr[0][i], i * 1024);
  G1_ISSUE_W(0, 3); G1_ISSUE_W(1, 4); G1_ISSUE_X(0, 2);
  asm volatile("s_waitcnt lgkmcnt(0)\n\ts_barrier" ::: "memory");
  g1_read<0>(fa, c);
  G1Step<0>::run(acc, xr, wr, fa, fb2, c);
  asm volatile("s_nop 15\n\ts_nop 15" : "+v"(acc[0]), "+v"(acc[1]), "+v"(acc[2]), "+v"(acc[3]), "+v"(acc[4]), "+v"(acc[5]));

  unsigned keep[3][8];
  {
    unsigned give[3][8];
#pragma unroll
    for (int tt = 0; tt < 3; ++tt)
#pragma unroll
      for (int e = 0; e < 8; ++e) give[tt][e] = pk_bf16(acc[3 + tt][2 * e], acc[3 + tt][2 * e + 1]);
    __syncthreads();
    char* xo = smem + wave * 6144 + lane * 16;
#pragma unroll
    for (int tt = 0; tt < 3; ++tt) {
      *(u32x4*)(xo + (2 * tt) * 1024) = u32x4{give[tt][0], give[tt][1], give[tt][2], give[tt][3]};
      *(u32x4*)(xo + (2 * tt + 1) * 1024) = u32x4{give[tt][4], give[tt][5], give[tt][6], give[tt][7]};
    }
  }
#pragma unroll
  for (int tt = 0; tt < 3; ++tt)
#pragma unroll
    for (int e = 0; e < 8; ++e) keep[tt][e] = pk_bf16(acc[tt][2 * e], acc[tt][2 * e + 1]);
  __syncthreads();
  {
    const char* xi = smem + (wave ^ 2) * 6144 + lane * 16;
    const int mt = 4 * mb + 2 * wm + isup;
    u16* hp = hf + ((size_t)(mt * NKS + 6 * fb) * 64 + lane) * 8;
    auto finish = [&](auto ISUP) {
#pragma unroll
      for (int tt = 0; tt < 3; ++tt) {
        const int ft = 3 * fb + tt;
        if (ft < FF / 32) {
          const u32x4 o0 = *(const u32x4*)(xi + (2 * tt) * 1024);
          const u32x4 o1 = *(const u32x4*)(xi + (2 * tt + 1) * 1024);
          unsigned gp[8], up[8];
#pragma unroll
          for (int e = 0; e < 8; ++e) {
            const unsigned ot = e < 4 ? o0[e & 3] : o1[e & 3];
            gp[e] = decltype(ISUP)::value ? ot : keep[tt][e];
            up[e] = decltype(ISUP)::value ? keep[tt][e] : ot;
          }
          f32x2 xg[8], ar[8];
#pragma unroll
          for (int e = 0; e < 8; ++e) xg[e] = f32x2{bf_lo(gp[e]), bf_hi(gp[e])};
#pragma unroll
          for (int e = 0; e < 8; ++e) ar[e] = xg[e] * xg[e];
#pragma unroll
          for (int e = 0; e < 8; ++e) ar[e] = __builtin_elementwise_fma(ar[e], f32x2{-0.10294323958f, -0.10294323958f}, f32x2{-2.30220819814f, -2.30220819814f});
#pragma unroll
          for (int e = 0; e < 8; ++e) ar[e] = ar[e] * xg[e];
#pragma unroll
          for (int e = 0; e < 8; ++e) ar[e] = f32x2{__builtin_amdgcn_exp2f(ar[e][0]), __builtin_amdgcn_exp2f(ar[e][1])};
#pragma unroll
          for (int e = 0; e < 8; ++e) ar[e] = ar[e] + f32x2{1.0f, 1.0f};
#pragma unroll
          for (int e = 0; e < 8; ++e) ar[e] = f32x2{__builtin_amdgcn_rcpf(ar[e][0]), __builtin_amdgcn_rcpf(ar[e][1])};
#pragma unroll
          for (int e = 0; e < 8; ++e) ar[e] = ar[e] * xg[e];
          unsigned o[8];
#pragma unroll
          for (int e = 0; e < 8; ++e) o[e] = pk_bf16(ar[e][0], ar[e][1]);
#pragma unroll
          for (int e = 0; e < 8; ++e) ar[e] = f32x2{bf_lo(o[e]), bf_hi(o[e])} * f32x2{bf_lo(up[e]), bf_hi(up[e])};
#pragma unroll
          for (int e = 0; e < 8; ++e) o[e] = pk_bf16(ar[e][0], ar[e][1]);
          *(u32x4*)(hp + (2 * tt) * 512) = u32x4{o[0], o[1], o[2], o[3]};
          *(u32x4*)(hp + (2 * tt + 1) * 512) = u32x4{o[4], o[5], o[6], o[7]};
        } else if (ft < NKS / 2) {
          *(u32x4*)(hp + (2 * tt) * 512) = u32x4{0, 0, 0, 0};
          *(u32x4*)(hp + (2 * tt + 1) * 512) = u32x4{0, 0, 0, 0};
        }
      }
    };
    if (isup) finish(std::integral_constant<bool, true>{});
    else finish(std::integral_constant<bool, false>{});
  }
}

__device__ __forceinline__ void gate_unit(int b, const u16* __restrict__ x, const u16* __restrict__ gw,
                                          float* __restrict__ gout) {
  const int tid = threadIdx.x, lane = tid & 63, wave = tid >> 6;
  const int j = lane & 7, rr = lane >> 3;
  u32x4 gv[8];
#pragma unroll
  for (int i = 0; i < 8; ++i) gv[i] = *(const u32x4*)(gw + (j + 8 * i) * 8);
  const int row0 = b * 128 + wave * 32 + rr;
  u32x4 xv[4][8];
#pragma unroll
  for (int ps = 0; ps < 4; ++ps)
#pragma unroll
    for (int i = 0; i < 8; ++i) xv[ps][i] = *(const u32x4*)(x + (size_t)(row0 + 8 * ps) * DM + (j + 8 * i) * 8);
#pragma unroll
  for (int ps = 0; ps < 4; ++ps) {
    float s0 = 0.f, s1 = 0.f;
#pragma unroll
    for (int i = 0; i < 8; ++i)
#pragma unroll
      for (int e = 0; e < 4; ++e) {
        const unsigned pr = pk_bf16(bf_lo(xv[ps][i][e]) * bf_lo(gv[i][e]), bf_hi(xv[ps][i][e]) * bf_hi(gv[i][e]));
        s0 += bf_lo(pr);
        s1 += bf_hi(pr);
      }
    float s = s0 + s1;
    s += __shfl_xor(s, 1);
    s += __shfl_xor(s, 2);
    s += __shfl_xor(s, 4);
    if (j == 0) {
      const float sb = rbf(s);
      gout[row0 + 8 * ps] = rbf(1.0f / (1.0f + expf(-sb)));
    }
  }
}

__global__ __launch_bounds__(256, 2) void k1_kernel(const u16* __restrict__ u, const u16* __restrict__ logAr,
                                                   const u16* __restrict__ Aim, const u16* __restrict__ Cre,
                                                   const u16* __restrict__ Cim, const u16* __restrict__ logdt,
                                                   const u16* __restrict__ Dskip, const u16* __restrict__ wg,
                                                   const u16* __restrict__ wu, const u16* __restrict__ gw,
                                                   const u16* __restrict__ wd, u16* __restrict__ hbuf, u16* __restrict__ aT,
                                                   float* __restrict__ gout DBG_ARG
                                                   ) {
  __shared__ __attribute__((aligned(4096))) char smem[G1_LDS > SSM_LDS ? G1_LDS : SSM_LDS];
  const int bid = blockIdx.x;
  if (bid < NSSM) {
    ssm_unit(bid, u, logAr, Aim, Cre, Cim, logdt, Dskip, aT, smem DBG_PASS);
    unsigned acc = 0;
    for (int i = (bid >> 3) * 256 + threadIdx.x; i < 11008; i += 32 * 256) acc += ((const unsigned*)wd)[i * 32];
    asm volatile("" ::"v"(acc));
  } else {
    const int xcd = bid & 7, idx = (bid - NSSM) >> 3;
    if (idx < 30) {
      gemm1_tile(30 * xcd + idx, u, wg, wu, hbuf, smem DBG_PASS);
    }
    else gate_unit(2 * xcd + idx - 30, u, gw, gout);
  }
}

constexpr int G2_SLOT_B = 4096;
constexpr int G2_WAVE_B = 2 * G2_SLOT_B;
constexpr int G2_PITCH = 272;
constexpr int G2_A_OFF = 4 * 64 * G2_PITCH;
constexpr int K2_LDS = G2_A_OFF + 8192;
#define G2D 3
constexpr int G2_DEPTH = G2D;

struct G2Ctx {
  const u16* hsrc;
  const u16* wd;
  int wrow0, wcol;
  unsigned wb32, woff, ww;
};

template <int ST>
struct G2Step {
  static __device__ __forceinline__ void issue(bf16x8 (&hfr)[11][4], u32x4 (&wr)[11][4], const G2Ctx& c) {
#pragma unroll
    for (int i = 0; i < 4; ++i) {
      int kr = c.wrow0 + ST * 32 + 8 * i;
      kr = kr < FF ? kr : FF - 1;
      GLOAD128(wr[ST][i], c.wd + (size_t)kr * DM + c.wcol);
    }
#pragma unroll
    for (int mi = 0; mi < 2; ++mi)
#pragma unroll
      for (int i = 0; i < 2; ++i)
        GLOAD128A(hfr[ST][2 * mi + i], c.hsrc + (size_t)(mi * NKS + 2 * ST + i) * 512);

  }
  static __device__ __forceinline__ void run(f32x16 (&acc)[2][2], bf16x8 (&hfr)[11][4], u32x4 (&wr)[11][4], const G2Ctx& c) {
    constexpr int LAST = (ST + G2_DEPTH - 1 < 10) ? ST + G2_DEPTH - 1 : 10;
    constexpr int NW = 8 * (LAST - ST);
    asm volatile("s_waitcnt vmcnt(%4)" : "+v"(wr[ST][0]), "+v"(wr[ST][1]), "+v"(wr[ST][2]), "+v"(wr[ST][3]) : "i"(NW + 4) : "memory");
#pragma unroll
    for (int i = 0; i < 4; ++i) DS_W128(c.ww, wr[ST][i], (ST & 1) * G2_SLOT_B + i * 1024);
    asm volatile("s_waitcnt lgkmcnt(0)" ::: "memory");
    const unsigned sb = c.wb32 + c.woff;
    s16x4 w[2][2][2];
#pragma unroll
    for (int i = 0; i < 2; ++i)
#pragma unroll
      for (int t = 0; t < 2; ++t) {
        DS_TR(w[i][t][0], sb ^ (t * 64), (ST & 1) * G2_SLOT_B + i * 2048);
        DS_TR(w[i][t][1], sb ^ (t * 64), (ST & 1) * G2_SLOT_B + i * 2048 + 1024);
      }
    asm volatile("s_waitcnt vmcnt(%4)" : "+a"(hfr[ST][0]), "+a"(hfr[ST][1]), "+a"(hfr[ST][2]), "+a"(hfr[ST][3]) : "i"(NW) : "memory");
    if constexpr (ST + G2_DEPTH <= 10) G2Step<ST + G2_DEPTH>::issue(hfr, wr, c);
    asm volatile("s_waitcnt lgkmcnt(0)"
                 : "+v"(w[0][0][0]), "+v"(w[0][0][1]), "+v"(w[0][1][0]), "+v"(w[0][1][1]), "+v"(w[1][0][0]), "+v"(w[1][0][1]),
                   "+v"(w[1][1][0]), "+v"(w[1][1][1])::"memory");
#pragma unroll
    for (int i = 0; i < 2; ++i)
#pragma unroll
      for (int t = 0; t < 2; ++t)
#pragma unroll
        for (int mi = 0; mi < 2; ++mi)
          acc[t][mi] = __builtin_amdgcn_mfma_f32_32x32x16_bf16(cat4(w[i][t][0], w[i][t][1]), hfr[ST][2 * mi + i], acc[t][mi], 0, 0, 0);
    if constexpr (ST + 1 <= 10) G2Step<ST + 1>::run(acc, hfr, wr, c);
  }
};

__global__ __launch_bounds__(256, 1) void k2_kernel(const u16* __restrict__ hf, const u16* __restrict__ wd,
                                                   const u16* __restrict__ aT, const float* __restrict__ gin,
                                                   u16* __restrict__ out DBG2_ARG) {
  __shared__ __attribute__((aligned(4096))) char smem[K2_LDS];
  const int tid = threadIdx.x, lane = tid & 63;
  STAMP2(0);
  const int wave = __builtin_amdgcn_readfirstlane(tid >> 6);
  const int bid = blockIdx.x;
  const int xcd = bid & 7, idx = bid >> 3;
  const int m0 = 64 * (4 * xcd + (idx >> 3)), n0 = 64 * (idx & 7);

#pragma unroll
  for (int i = 0; i < 2; ++i) {
    const int piece = 2 * wave + i;
    const int drow = 8 * piece + (lane >> 3);
    glds16(aT + (size_t)(n0 + drow) * SEQ + m0 + (lane & 7) * 8, smem + G2_A_OFF + piece * 1024 + lane * 16);
  }

  G2Ctx c;
  c.wb32 = lds_addr(smem) + wave * G2_WAVE_B;
  c.hsrc = hf + ((size_t)((m0 >> 5) * NKS + 22 * wave) * 64 + lane) * 8;
  c.wd = wd;
  c.wcol = n0 + 8 * (lane & 7);
  c.wrow0 = 352 * wave + (lane >> 3);
  c.ww = c.wb32 + (lane >> 3) * 128 + (((lane & 7) ^ (((lane >> 4) & 1) << 2)) * 16);

  const int r = lane & 31, h = lane >> 5;
  const int blk = (lane >> 4) & 1, q = (lane & 15) >> 2, p = lane & 3;
  c.woff = (4 * h + q) * 128 + (((2 * blk + (p >> 1)) ^ (((q >> 1) & 1) << 2)) * 16) + 8 * (p & 1);

  f32x16 acc[2][2];
#pragma unroll
  for (int i = 0; i < 2; ++i)
#pragma unroll
    for (int j = 0; j < 2; ++j) acc[i][j] = f32x16{};
  bf16x8 hfr[11][4];
  u32x4 wr[11][4];

  G2Step<0>::issue(hfr, wr, c);
  G2Step<1>::issue(hfr, wr, c);
  if constexpr (G2_DEPTH > 2) G2Step<2>::issue(hfr, wr, c);
  if constexpr (G2_DEPTH > 3) G2Step<3>::issue(hfr, wr, c);
  if constexpr (G2_DEPTH > 4) G2Step<4>::issue(hfr, wr, c);
  if constexpr (G2_DEPTH > 5) G2Step<5>::issue(hfr, wr, c);
  if constexpr (G2_DEPTH > 6) G2Step<6>::issue(hfr, wr, c);
  STAMP2(1);
  STAMP2(2);
  G2Step<0>::run(acc, hfr, wr, c);
  STAMP2(3);
  __syncthreads();
  {
    char* pw = smem + wave * (64 * G2_PITCH);
#pragma unroll
    for (int c = 0; c < 2; ++c)
#pragma unroll
      for (int mi = 0; mi < 2; ++mi)
#pragma unroll
        for (int gq = 0; gq < 4; ++gq) {
          const f32x4 v = {acc[c][mi][4 * gq], acc[c][mi][4 * gq + 1], acc[c][mi][4 * gq + 2], acc[c][mi][4 * gq + 3]};
          *(f32x4*)(pw + (32 * mi + r) * G2_PITCH + (32 * c + 8 * gq + 4 * h) * 4) = v;
        }
  }
  __syncthreads();
  STAMP2(4);
  {
    const int m = tid >> 2, nq = (tid & 3) * 16;
    const float g = gin[m0 + m];
    const float omg = rbf(1.0f - g);
    float b[16];
#pragma unroll
    for (int j = 0; j < 4; ++j) {
      f32x4 s = *(const f32x4*)(smem + m * G2_PITCH + (nq + 4 * j) * 4);
#pragma unroll
      for (int w = 1; w < 4; ++w) s += *(const f32x4*)(smem + w * (64 * G2_PITCH) + m * G2_PITCH + (nq + 4 * j) * 4);
#pragma unroll
      for (int e = 0; e < 4; ++e) b[4 * j + e] = s[e];
    }
    const u16* aimg = (const u16*)(smem + G2_A_OFF);
    unsigned o[8];
#pragma unroll
    for (int j = 0; j < 8; ++j) {
      const float a0 = bf2f(aimg[(nq + 2 * j) * 64 + m]);
      const float a1 = bf2f(aimg[(nq + 2 * j + 1) * 64 + m]);
      const float o0 = rbf(g * a0) + rbf(omg * rbf(b[2 * j]));
      const float o1 = rbf(g * a1) + rbf(omg * rbf(b[2 * j + 1]));
      o[j] = pk_bf16(o0, o1);
    }
    u16* op = out + (size_t)(m0 + m) * DM + n0 + nq;
    *(u32x4*)op = u32x4{o[0], o[1], o[2], o[3]};
    *(u32x4*)(op + 8) = u32x4{o[4], o[5], o[6], o[7]};
  }
  STAMP2(5);
}

extern "C" void kernel_launch(void* const* d_in, const int* in_sizes, int n_in, void* d_out, int out_size,
                              void* d_ws, size_t ws_size, hipStream_t stream) {
  const u16* u = (const u16*)d_in[0];
  const u16* logAr = (const u16*)d_in[1];
  const u16* Aim = (const u16*)d_in[2];
  const u16* Cre = (const u16*)d_in[3];
  const u16* Cim = (const u16*)d_in[4];
  const u16* logdt = (const u16*)d_in[5];
  const u16* Dskip = (const u16*)d_in[6];
  const u16* wg = (const u16*)d_in[8];
  const u16* wu = (const u16*)d_in[9];
  const u16* wd = (const u16*)d_in[10];
  const u16* gw = (const u16*)d_in[11];
  char* ws = (char*)d_ws;
  u16* hbuf = (u16*)ws;
  u16* aT = (u16*)(ws + (size_t)SEQ * FFP * 2);
  float* gbuf = (float*)(ws + (size_t)SEQ * FFP * 2 + (size_t)DM * SEQ * 2);
  k1_kernel<<<512, 256, 0, stream>>>(u, logAr, Aim, Cre, Cim, logdt, Dskip, wg, wu, gw, wd, hbuf, aT, gbuf
      );
  k2_kernel<<<256, 256, 0, stream>>>(hbuf, wd, aT, gbuf, (u16*)d_out
      );
}
```

```cpp
#include <hip/hip_runtime.h>
#include <hip/hip_bf16.h>
#include <stdint.h>
#include <type_traits>

typedef unsigned short u16;
typedef __attribute__((ext_vector_type(8))) short bf16x8;
typedef __attribute__((ext_vector_type(4))) short s16x4;
typedef __attribute__((ext_vector_type(4))) float f32x4;
typedef __attribute__((ext_vector_type(16))) float f32x16;
typedef __attribute__((ext_vector_type(4))) unsigned u32x4;
typedef __attribute__((ext_vector_type(2))) unsigned u32x2;
typedef __attribute__((ext_vector_type(2))) __bf16 bf16x2_t;
typedef __attribute__((ext_vector_type(2))) float f32x2;
typedef __attribute__((address_space(3))) s16x4* lds_s16x4_ptr;

constexpr int SEQ = 2048, DM = 512, NST = 128, FF = 1376, FFP = 1408;
constexpr int NSSM = 256;
constexpr int NKS = 88;

__device__ __forceinline__ unsigned pk_bf16(float lo, float hi) {
  f32x2 v = {lo, hi};
  bf16x2_t b = __builtin_convertvector(v, bf16x2_t);
  return __builtin_bit_cast(unsigned, b);
}
__device__ __forceinline__ float bf_lo(unsigned w) { return __uint_as_float(w << 16); }
__device__ __forceinline__ float bf_hi(unsigned w) { return __uint_as_float(w & 0xffff0000u); }
__device__ __forceinline__ float rbf(float x) { return bf_lo(pk_bf16(x, 0.f)); }
__device__ __forceinline__ float bf2f(u16 v) { return __uint_as_float(((unsigned)v) << 16); }

__device__ __forceinline__ bf16x8 mk8(unsigned a, unsigned b, unsigned c, unsigned d) {
  u32x4 v = {a, b, c, d};
  return __builtin_bit_cast(bf16x8, v);
}
__device__ __forceinline__ bf16x8 cat4(s16x4 a, s16x4 b) {
  return __builtin_shufflevector(a, b, 0, 1, 2, 3, 4, 5, 6, 7);
}
__device__ __forceinline__ s16x4 tr_read(const char* smem_ptr) {
  return __builtin_amdgcn_ds_read_tr16_b64_v4i16((lds_s16x4_ptr)(smem_ptr));
}
__device__ __forceinline__ unsigned lds_addr(const void* p) {
  return (unsigned)(size_t)(const __attribute__((address_space(3))) char*)p;
}
__device__ __forceinline__ void glds16(const void* g, char* l) {
  __builtin_amdgcn_global_load_lds((const unsigned*)g, (unsigned*)l, 16, 0, 0);
}

#define STAMP2(i)
#define DBG2_ARG
#define DBG2_PASS
#define STAMP(i)
#define DBG_ARG
#define DBG_PASS
constexpr int U_COPY_B = 4160;
constexpr int U_CH_B = 4 * U_COPY_B;
constexpr int U_REGION_B = 2 * U_CH_B;
constexpr int KR_PART_B = 4576;
constexpr int KR_OFF = U_REGION_B;
constexpr int KQ_B = 8208;
constexpr int KX_OFF = 2 * KQ_B;
constexpr int SSM_LDS = KR_OFF + 4 * KR_PART_B;
static_assert(KX_OFF + 4 * 1536 <= U_REGION_B, "scratch must fit the U region");

#define DS_TR(dst, addr, off) asm volatile("ds_read_b64_tr_b16 %0, %1 offset:%2" : "=v"(dst) : "v"(addr), "i"(off) : "memory")
#define DS_B128(dst, addr, off) asm volatile("ds_read_b128 %0, %1 offset:%2" : "=v"(dst) : "v"(addr), "i"(off) : "memory")
#define DS_B64(dst, addr, off) asm volatile("ds_read_b64 %0, %1 offset:%2" : "=v"(dst) : "v"(addr), "i"(off) : "memory")
#define MFMA16(acc, a, b) asm volatile("v_mfma_f32_16x16x32_bf16 %0, %1, %2, %0" : "+v"(acc) : "v"(a), "v"(b))

struct ConvFrag {
  s16x4 a[8][2];
  bf16x8 bh[8], bl[8];
};
template <int RI, int K>
__device__ __forceinline__ void conv_read(ConvFrag& f, unsigned ua, unsigned kb) {
  if constexpr (K < 16) {
    DS_B64(f.a[K >> 1][K & 1], ua, 64 * (8 * (K >> 1) + RI) + 8 * (K & 1));
  } else if constexpr (K < 24) {
    DS_B128(f.bh[K - 16], kb, 64 * (56 - 8 * (K - 16) + RI));
  } else {
    DS_B128(f.bl[K - 24], kb, 64 * (56 - 8 * (K - 24) + RI) + KR_PART_B);
  }
}
template <int RI, int K>
__device__ __forceinline__ void conv_read_all(ConvFrag& f, unsigned ua, unsigned kb) {
  conv_read<RI, K>(f, ua, kb);
  if constexpr (K + 1 < 32) conv_read_all<RI, K + 1>(f, ua, kb);
}
#define CONV_WAIT(f)                                                                                                  \
  do {                                                                                                                \
    asm volatile("s_waitcnt lgkmcnt(0)"                                                                               \
                 : "+v"(f.a[0][0]), "+v"(f.a[0][1]), "+v"(f.a[1][0]), "+v"(f.a[1][1]), "+v"(f.a[2][0]), "+v"(f.a[2][1]), \
                   "+v"(f.a[3][0]), "+v"(f.a[3][1]), "+v"(f.a[4][0]), "+v"(f.a[4][1]), "+v"(f.a[5][0]), "+v"(f.a[5][1]), \
                   "+v"(f.a[6][0]), "+v"(f.a[6][1]), "+v"(f.a[7][0]), "+v"(f.a[7][1])::"memory");                     \
    asm volatile("" : "+v"(f.bh[0]), "+v"(f.bh[1]), "+v"(f.bh[2]), "+v"(f.bh[3]), "+v"(f.bh[4]), "+v"(f.bh[5]),        \
                 "+v"(f.bh[6]), "+v"(f.bh[7]), "+v"(f.bl[0]), "+v"(f.bl[1]), "+v"(f.bl[2]), "+v"(f.bl[3]),            \
                 "+v"(f.bl[4]), "+v"(f.bl[5]), "+v"(f.bl[6]), "+v"(f.bl[7])::"memory");                              \
  } while (0)
constexpr int conv_tau(int P) { int t = 0; while (P >= 8 - t) { P -= 8 - t; ++t; } return t; }
constexpr int conv_sig(int P) { int t = 0; while (P >= 8 - t) { P -= 8 - t; ++t; } return P; }
template <int RI, int P>
__device__ __forceinline__ void conv_pairs(f32x4 (&acc)[8], const bf16x8 (&A)[8], ConvFrag& cur, ConvFrag& nxt, unsigned ua,
                                           unsigned kb) {
  constexpr int T = conv_tau(P), SG = conv_sig(P);
  MFMA16(acc[T + SG], A[SG], cur.bl[T]);
  if constexpr (RI < 3 && P < 32) conv_read<RI + 1, P>(nxt, ua, kb);
  MFMA16(acc[T + SG], A[SG], cur.bh[T]);
  if constexpr (P + 1 < 36) conv_pairs<RI, P + 1>(acc, A, cur, nxt, ua, kb);
}
template <int RI>
__device__ __forceinline__ void conv_rho(f32x4 (&acc)[8], ConvFrag& cur, ConvFrag& nxt, unsigned ua, unsigned kb) {
  CONV_WAIT(cur);
  bf16x8 A[8];
#pragma unroll
  for (int i = 0; i < 8; ++i) A[i] = cat4(cur.a[i][0], cur.a[i][1]);
  conv_pairs<RI, 0>(acc, A, cur, nxt, ua, kb);
  if constexpr (RI < 3) conv_rho<RI + 1>(acc, nxt, cur, ua, kb);
}

__device__ __forceinline__ void split_pair(f32x2 v, unsigned& hi, unsigned& lo) {
  hi = pk_bf16(v[0], v[1]);
  const f32x2 r = v - f32x2{bf_lo(hi), bf_hi(hi)};
  lo = pk_bf16(r[0], r[1]);
}

__device__ __forceinline__ void ssm_unit(int w, const u16* __restrict__ u, const u16* __restrict__ logAr,
                                         const u16* __restrict__ Aim, const u16* __restrict__ Cre,
                                         const u16* __restrict__ Cim, const u16* __restrict__ logdt,
                                         const u16* __restrict__ Dskip, u16* __restrict__ aT,
                                         unsigned* __restrict__ flags, char* smem DBG_ARG) {
  const int tid = threadIdx.x, lane = tid & 63;
  const int wave = __builtin_amdgcn_readfirstlane(tid >> 6);
  const int ch = wave >> 1, w2 = wave & 1;
  const int d0 = 2 * w;
  const int d = d0 + ch;

  const int pidx = d0 * NST + tid;
  const u16 r_lar = logAr[pidx], r_aim = Aim[pidx], r_cre = Cre[pidx], r_cim = Cim[pidx], r_ldt = logdt[d], r_dsk = Dskip[d];
  const unsigned* u32p = (const unsigned*)(u + d0);
  unsigned uw[8], up[3];
#pragma unroll
  for (int i = 0; i < 3; ++i) up[i] = 0;
  if (lane == 0 && tid > 0) {
#pragma unroll
    for (int i = 0; i < 3; ++i) up[i] = u32p[(size_t)(8 * tid - 3 + i) * (DM / 2)];
  }
  const unsigned uoff = (unsigned)tid * 8192u;
  {
    const float dt = rbf(expf(bf2f(r_ldt)));
    const float are = rbf(expf(bf2f(r_lar)));
    f32x4 prm;
    prm[0] = (-are * dt) * 1.44269504088896340736f;
    prm[1] = (bf2f(r_aim) * dt) * 0.15915494309189533577f;
    prm[2] = 2.0f * (bf2f(r_cre) * dt);
    prm[3] = 2.0f * (bf2f(r_cim) * dt);
    *(f32x4*)(smem + KR_OFF + tid * 16) = prm;
  }
  __syncthreads();

  const int r = lane & 31, h = lane >> 5;
  f32x16 dacc = {};
  {
    int p1i;
    if (r < 8) p1i = r;
    else if (r < 12) p1i = 256 + 2 * (r - 8);
    else if (r < 16) p1i = 512 + 256 * ((r - 12) >> 1) + 4 * ((r - 12) & 1);
    else if (r < 21) p1i = 1024 + 256 * (r - 16);
    else p1i = 0;
    const float p1 = (float)p1i, p2 = (float)(8 * r);
    const f32x2 P1 = {p1, p1}, P2 = {p2, p2};
    const char* prm_base = smem + KR_OFF + (ch * 128 + 64 * w2 + 4 * h) * 16;
#pragma unroll
    for (int ks = 0; ks < 8; ++ks) {
      if (ks >= 4) {
        asm volatile("global_load_dword %0, %1, %2 offset:%3" : "=v"(uw[2 * ks - 8]) : "v"(uoff), "s"((const char*)u32p + ((2 * ks - 8) >> 2) * 4096), "i"(((2 * ks - 8) & 3) * 1024) : "memory");
        asm volatile("global_load_dword %0, %1, %2 offset:%3" : "=v"(uw[2 * ks - 7]) : "v"(uoff), "s"((const char*)u32p + ((2 * ks - 7) >> 2) * 4096), "i"(((2 * ks - 7) & 3) * 1024) : "memory");
      }
      f32x4 prm[4];
#pragma unroll
      for (int q = 0; q < 4; ++q) prm[q] = *(const f32x4*)(prm_base + (8 * ks + q) * 16);
      f32x2 za[4], zb[4];
#pragma unroll
      for (int q = 0; q < 4; ++q) { za[q] = f32x2{prm[q][0], prm[q][1]} * P1; zb[q] = f32x2{prm[q][0], prm[q][1]} * P2; }
      float ea[4], eb[4], fa[4], fb[4];
#pragma unroll
      for (int q = 0; q < 4; ++q) {
        ea[q] = __builtin_amdgcn_exp2f(za[q][0]); eb[q] = __builtin_amdgcn_exp2f(zb[q][0]);
        fa[q] = __builtin_amdgcn_fractf(za[q][1]); fb[q] = __builtin_amdgcn_fractf(zb[q][1]);
      }
      f32x2 ca[4], cb[4];
#pragma unroll
      for (int q = 0; q < 4; ++q) {
        ca[q] = f32x2{__builtin_amdgcn_cosf(fa[q]), __builtin_amdgcn_sinf(fa[q])};
        cb[q] = f32x2{__builtin_amdgcn_cosf(fb[q]), __builtin_amdgcn_sinf(fb[q])};
      }
      unsigned ah[4], al[4], bh[4], bl[4];
#pragma unroll
      for (int q = 0; q < 4; ++q) {
        const f32x2 ce = f32x2{prm[q][2], prm[q][3]} * f32x2{ea[q], ea[q]};
        f32x2 g = f32x2{ce[0], -ce[0]} * ca[q];
        g = __builtin_elementwise_fma(f32x2{-ce[1], -ce[1]}, f32x2{ca[q][1], ca[q][0]}, g);
        split_pair(g, ah[q], al[q]);
        split_pair(cb[q] * f32x2{eb[q], eb[q]}, bh[q], bl[q]);
      }
      const bf16x8 Ah = mk8(ah[0], ah[1], ah[2], ah[3]), Al = mk8(al[0], al[1], al[2], al[3]);
      const bf16x8 Bh = mk8(bh[0], bh[1], bh[2], bh[3]), Bl = mk8(bl[0], bl[1], bl[2], bl[3]);
      dacc = __builtin_amdgcn_mfma_f32_32x32x16_bf16(Al, Bh, dacc, 0, 0, 0);
      dacc = __builtin_amdgcn_mfma_f32_32x32x16_bf16(Ah, Bl, dacc, 0, 0, 0);
      dacc = __builtin_amdgcn_mfma_f32_32x32x16_bf16(Ah, Bh, dacc, 0, 0, 0);
    }
  }
  {
    float* xo = (float*)(smem + KX_OFF + wave * 1536) + lane;
    const float* xi = (const float*)(smem + KX_OFF + (wave ^ 1) * 1536) + lane;
    float mine[6];
    if (w2 == 0) {
#pragma unroll
      for (int e = 0; e < 6; ++e) { xo[e * 64] = dacc[6 + e]; mine[e] = dacc[e]; }
    } else {
#pragma unroll
      for (int e = 0; e < 6; ++e) { xo[e * 64] = dacc[e]; mine[e] = dacc[6 + e]; }
    }
    __syncthreads();
    unsigned* Kq = (unsigned*)(smem + ch * KQ_B);
    unsigned pq[6];
#pragma unroll
    for (int e = 0; e < 6; ++e) {
      const float v = mine[e] + xi[e * 64];
      const unsigned hi = pk_bf16(v, 0.f);
      pq[e] = pk_bf16(0.f, v - bf_lo(hi)) | hi;
    }
    if (w2 == 0) {
#pragma unroll
      for (int e = 0; e < 4; ++e) Kq[4 * h + e + 8 * r] = pq[e];
      Kq[(h ? 512 : 256) + 8 * r] = pq[4];
      Kq[(h ? 516 : 258) + 8 * r] = pq[5];
    } else {
      Kq[(h ? 768 : 260) + 8 * r] = pq[0];
      Kq[(h ? 772 : 262) + 8 * r] = pq[1];
      if (h == 0) {
#pragma unroll
        for (int e = 0; e < 4; ++e) Kq[1024 + 256 * e + 8 * r] = pq[2 + e];
      } else if (r == 0) {
        Kq[2048] = pq[2];
      }
    }
  }
  __syncthreads();

  {
    const int t = tid & 127;
    const unsigned* Kq = (const unsigned*)(smem + ch * KQ_B);
    unsigned* krh = (unsigned*)(smem + KR_OFF + (ch * 2) * KR_PART_B);
    unsigned* krl = (unsigned*)((char*)krh + KR_PART_B);
    unsigned q0[8], q1[8];
#pragma unroll
    for (int m = 0; m < 8; ++m) {
      const int sh = m < 4 ? 3 : (m < 6 ? 2 : (m < 7 ? 1 : 0));
      const int l0 = 2047 - 2 * (t + 128 * m), l1 = l0 - 1;
      int pa = l0, pb = l1;
      if (sh) {
        pa = (l0 + (1 << (sh - 1)) - 1 + ((l0 >> sh) & 1)) & ~((1 << sh) - 1);
        pb = (l1 + (1 << (sh - 1)) - 1 + ((l1 >> sh) & 1)) & ~((1 << sh) - 1);
      }
      q0[m] = Kq[pa];
      q1[m] = Kq[pb];
    }
#pragma unroll
    for (int m = 0; m < 8; ++m) {
      krh[t + 128 * m] = __builtin_amdgcn_perm(q1[m], q0[m], 0x05040100u);
      krl[t + 128 * m] = __builtin_amdgcn_perm(q1[m], q0[m], 0x07060302u);
    }
    if (t < 120) { krh[1024 + t] = 0; krl[1024 + t] = 0; }
  }
  __syncthreads();

  asm volatile("s_waitcnt vmcnt(0)" : "+v"(uw[0]), "+v"(uw[1]), "+v"(uw[2]), "+v"(uw[3]), "+v"(uw[4]), "+v"(uw[5]), "+v"(uw[6]), "+v"(uw[7])::"memory");
  {
    unsigned e[11];
#pragma unroll
    for (int i = 0; i < 3; ++i) {
      const unsigned nb = __shfl_up(uw[5 + i], 1);
      e[i] = (lane == 0) ? up[i] : nb;
    }
#pragma unroll
    for (int i = 0; i < 8; ++i) e[3 + i] = uw[i];
    unsigned p0[10], p1[10];
#pragma unroll
    for (int i = 0; i < 10; ++i) {
      p0[i] = __builtin_amdgcn_perm(e[i + 1], e[i], 0x05040100u);
      p1[i] = __builtin_amdgcn_perm(e[i + 1], e[i], 0x07060302u);
    }
#pragma unroll
    for (int c = 0; c < 4; ++c) {
      *(u32x4*)(smem + c * U_COPY_B + (tid + 2) * 16) = u32x4{p0[c], p0[c + 2], p0[c + 4], p0[c + 6]};
      *(u32x4*)(smem + U_CH_B + c * U_COPY_B + (tid + 2) * 16) = u32x4{p1[c], p1[c + 2], p1[c + 4], p1[c + 6]};
    }
    if (tid < 16) *(u32x4*)(smem + (tid >> 1) * U_COPY_B + (tid & 1) * 16) = u32x4{0, 0, 0, 0};
  }
  __syncthreads();

  f32x4 acc[8];
#pragma unroll
  for (int t = 0; t < 8; ++t) acc[t] = f32x4{0.f, 0.f, 0.f, 0.f};
  asm volatile("s_nop 4" : "+v"(acc[0]), "+v"(acc[1]), "+v"(acc[2]), "+v"(acc[3]), "+v"(acc[4]), "+v"(acc[5]), "+v"(acc[6]), "+v"(acc[7]));
  {
    const int r16 = lane & 15, hq = lane >> 4;
    const unsigned sb = lds_addr(smem);
    const unsigned ua = sb + ch * U_CH_B + (r16 & 3) * U_COPY_B + 2 * (4 + (r16 & ~3) + 8 * hq) + 64 * 4 * w2;
    const unsigned kb = sb + KR_OFF + (ch * 2) * KR_PART_B + 2 * (2032 - 16 * r16 + 8 * hq) - 64 * 56 + 64 * 4 * w2;
    ConvFrag f0, f1;
    conv_read_all<0, 0>(f0, ua, kb);
    conv_rho<0>(acc, f0, f1, ua, kb);
  }
  asm volatile("s_nop 15" : "+v"(acc[0]), "+v"(acc[1]), "+v"(acc[2]), "+v"(acc[3]), "+v"(acc[4]), "+v"(acc[5]), "+v"(acc[6]), "+v"(acc[7]));
  __syncthreads();
  {
    char* xw = smem + KR_OFF + (ch * 2 + w2) * 4096 + lane * 16;
    const char* xr = smem + KR_OFF + (ch * 2 + (w2 ^ 1)) * 4096 + lane * 16;
    f32x4 y[4];
    if (w2 == 0) {
#pragma unroll
      for (int i = 0; i < 4; ++i) { *(f32x4*)(xw + i * 1024) = acc[4 + i]; y[i] = acc[i]; }
    } else {
#pragma unroll
      for (int i = 0; i < 4; ++i) { *(f32x4*)(xw + i * 1024) = acc[i]; y[i] = acc[4 + i]; }
    }
    __syncthreads();
    const float Dk = bf2f(r_dsk);
#pragma unroll
    for (int i = 0; i < 4; ++i) {
      const f32x4 o = *(const f32x4*)(xr + i * 1024);
      const int l0 = 256 * (4 * w2 + i) + 16 * (lane & 15) + 4 * (lane >> 4);
      const u32x2 uu = *(const u32x2*)(smem + ch * U_CH_B + 3 * U_COPY_B + 2 * (16 + l0));
      const unsigned y01 = pk_bf16(y[i][0] + o[0], y[i][1] + o[1]), y23 = pk_bf16(y[i][2] + o[2], y[i][3] + o[3]);
      const unsigned s01 = pk_bf16(bf_lo(uu[0]) * Dk, bf_hi(uu[0]) * Dk), s23 = pk_bf16(bf_lo(uu[1]) * Dk, bf_hi(uu[1]) * Dk);
      const u32x2 av = u32x2{pk_bf16(bf_lo(y01) + bf_lo(s01), bf_hi(y01) + bf_hi(s01)),
                             pk_bf16(bf_lo(y23) + bf_lo(s23), bf_hi(y23) + bf_hi(s23))};
      asm volatile("global_store_dwordx2 %0, %1, off sc1\n\ts_nop 1" ::"v"(aT + (size_t)d * SEQ + l0), "v"(av) : "memory");
    }
    asm volatile("s_waitcnt vmcnt(0)" ::: "memory");
    __syncthreads();
    if (tid == 0) __hip_atomic_store(flags + w, 1u, __ATOMIC_RELAXED, __HIP_MEMORY_SCOPE_AGENT);
  }
}

__device__ __forceinline__ float gelu_tanh(float x) {
  const float z = 0.7978845608028654f * (x + 0.044715f * x * x * x);
  const float e = __builtin_amdgcn_exp2f(-2.8853900817779268f * z);
  return x * __builtin_amdgcn_rcpf(1.0f + e);
}

constexpr int G1_WSLOT_B = 12288;
constexpr int G1_X_OFF = 3 * G1_WSLOT_B;
constexpr int G1_LDS = G1_X_OFF + 32768;

constexpr int g1_pos(bool isx, int id) {
  if (isx && id == 0) return 4;
  if (!isx && id == 0) return 7;
  if (!isx && id == 1) return 10;
  if (isx && id == 1) return 14;
  if (!isx && id == 2) return 17;
  if (!isx && id == 3) return 20;
  if (!isx && id == 4) return 23;
  if (isx && id == 2) return 27;
  int n = 27;
  for (int t = 0; t < 32; ++t) {
    if ((t & 1) == 0 && t / 2 + 5 < 16) { n += 3; if (!isx && t / 2 + 5 == id) return n; }
    if ((t & 3) == 1 && t / 4 + 3 < 8) { n += 4; if (isx && t / 4 + 3 == id) return n; }
  }
  return -1;
}
constexpr int g1_issued_before(int step) {
  int n = 27;
  for (int t = 0; t < step; ++t) {
    if ((t & 1) == 0 && t / 2 + 5 < 16) n += 3;
    if ((t & 3) == 1 && t / 4 + 3 < 8) n += 4;
  }
  return n;
}

#define GLOAD128(dst, ptr) asm volatile("global_load_dwordx4 %0, %1, off" : "=v"(dst) : "v"(ptr) : "memory")
#define GLOAD128S(dst, voff, sbase) asm volatile("global_load_dwordx4 %0, %1, %2" : "=v"(dst) : "v"(voff), "s"(sbase) : "memory")
#define GLOAD128SA(dst, voff, sbase) asm volatile("global_load_dwordx4 %0, %1, %2" : "=a"(dst) : "v"(voff), "s"(sbase) : "memory")
#define GLOAD128A(dst, ptr) asm volatile("global_load_dwordx4 %0, %1, off" : "=a"(dst) : "v"(ptr) : "memory")
#define DS_W128(addr, val, off) asm volatile("ds_write_b128 %0, %1 offset:%2" ::"v"(addr), "v"(val), "i"(off) : "memory")
#define MFMA32(acc, a, b) asm volatile("v_mfma_f32_32x32x16_bf16 %0, %1, %2, %0" : "+v"(acc) : "v"(a), "v"(b))

struct G1Ctx {
  const char* wbase;
  const char* xbase;
  unsigned wo[3], xo;
  unsigned woff, woff_o, ww[3], xw, xrd2[2][4];
  unsigned junk[4];
};
struct G1Frag {
  s16x4 w[3][2];
  bf16x8 x[2];
};

#define G1_RD_W(f, t, OFF)                                                        \
  do {                                                                            \
    if ((t) & 1) { DS_TR(f.w[t][0], c.woff_o, (OFF) + (t) * 64); DS_TR(f.w[t][1], c.woff_o, (OFF) + (t) * 64 + 4 * 384); } \
    else { DS_TR(f.w[t][0], c.woff, (OFF) + (t) * 64); DS_TR(f.w[t][1], c.woff, (OFF) + (t) * 64 + 4 * 384); }            \
  } while (0)
#define G1_RD_X(f, mi, N) DS_B128(f.x[mi], c.xrd2[mi][(N) & 3], (((N) >> 2) & 1) * 16384)
template <int N>
__device__ __forceinline__ void g1_read(G1Frag& f, const G1Ctx& c) {
  constexpr int OFF = ((N >> 1) % 3) * G1_WSLOT_B + (N & 1) * 16 * 384;
  G1_RD_X(f, 0, N); G1_RD_X(f, 1, N);
  G1_RD_W(f, 0, OFF); G1_RD_W(f, 1, OFF); G1_RD_W(f, 2, OFF);
}
#define G1_WAIT_FRAG(f, STR)                                                                                       \
  asm volatile(STR : "+v"(f.w[0][0]), "+v"(f.w[0][1]), "+v"(f.w[1][0]), "+v"(f.w[1][1]), "+v"(f.w[2][0]),        \
               "+v"(f.w[2][1]), "+v"(f.x[0]), "+v"(f.x[1])::"memory")

template <int N>
struct G1Step {
  static __device__ __forceinline__ void run(f32x16 (&acc)[6], u32x4 (&xr)[2][4], u32x4 (&wr)[3][3], G1Frag& cur,
                                             G1Frag& nxt, G1Ctx& c) {
    constexpr int ISS = g1_issued_before(N);
    constexpr int S = N >> 1;
    constexpr bool RD = N + 1 < 32;
    constexpr int OFF = (((N + 1) >> 1) % 3) * G1_WSLOT_B + ((N + 1) & 1) * 16 * 384;
    if constexpr ((N & 1) == 0 && N > 0) G1_WAIT_FRAG(cur, "s_waitcnt lgkmcnt(0)\n\ts_barrier");
    else G1_WAIT_FRAG(cur, "s_waitcnt lgkmcnt(0)");
    bf16x8 b[3];
#pragma unroll
    for (int t = 0; t < 3; ++t) b[t] = cat4(cur.w[t][0], cur.w[t][1]);
    MFMA32(acc[0], b[0], cur.x[0]);
    if constexpr (RD) { G1_RD_X(nxt, 0, N + 1); G1_RD_W(nxt, 0, OFF); }
    MFMA32(acc[3], b[0], cur.x[1]);
    if constexpr (RD) { G1_RD_X(nxt, 1, N + 1); G1_RD_W(nxt, 1, OFF); }
    MFMA32(acc[1], b[1], cur.x[0]);
    if constexpr (RD) { G1_RD_W(nxt, 2, OFF); }
    MFMA32(acc[4], b[1], cur.x[1]);
    if constexpr ((N & 1) == 0) {
      if constexpr (S + 2 < 16) {
        constexpr int R = (S + 2) % 3;
        constexpr int NW = ISS - g1_pos(false, S + 2);
        asm volatile("s_waitcnt vmcnt(%3)" : "+v"(wr[R][0]), "+v"(wr[R][1]), "+v"(wr[R][2]) : "i"(NW) : "memory");
#pragma unroll
        for (int i = 0; i < 3; ++i) DS_W128(c.ww[i], wr[R][i], R * G1_WSLOT_B);
      }
    }
    if constexpr ((N & 3) == 1) {
      constexpr int B = N / 4 + 1;
      if constexpr (B < 8) {
        constexpr int NW = ISS - g1_pos(true, B);
        asm volatile("s_waitcnt vmcnt(%4)" : "+v"(xr[B & 1][0]), "+v"(xr[B & 1][1]), "+v"(xr[B & 1][2]), "+v"(xr[B & 1][3]) : "i"(NW) : "memory");
#pragma unroll
        for (int i = 0; i < 4; ++i) DS_W128(c.xw, xr[B & 1][i], (B & 1) * 16384 + i * 1024);
      }
    }
    MFMA32(acc[2], b[2], cur.x[0]);
    if constexpr ((N & 1) == 0 && S + 5 < 16) {
#pragma unroll
      for (int i = 0; i < 3; ++i) GLOAD128S(wr[(S + 5) % 3][i], c.wo[i], c.wbase + (size_t)(S + 5) * 32 * FF * 2);
    }
    if constexpr ((N & 3) == 1 && N / 4 + 3 < 8) {
      constexpr int B = N / 4 + 1;
#pragma unroll
      for (int i = 0; i < 4; ++i) GLOAD128S(xr[B & 1][i], c.xo, c.xbase + (size_t)(8 * i) * DM * 2 + (B + 2) * 128);
    }
    MFMA32(acc[5], b[2], cur.x[1]);
    if constexpr (N + 1 < 32) G1Step<N + 1>::run(acc, xr, wr, nxt, cur, c);
  }
};

__device__ __forceinline__ void gemm1_tile(int t, const u16* __restrict__ x, const u16* __restrict__ wg,
                                           const u16* __restrict__ wu, u16* __restrict__ hf, char* smem DBG_ARG) {
  const int tid = threadIdx.x, lane = tid & 63;
  const int wave = __builtin_amdgcn_readfirstlane(tid >> 6);
  const int fb = t >> 4, mb = t & 15;
  const int f0 = 96 * fb;
  const int wm = wave & 1, isup = wave >> 1;
  G1Ctx c;
  const unsigned sbase = lds_addr(smem);
  c.wbase = (const char*)(isup ? wu : wg);
#pragma unroll
  for (int i = 0; i < 3; ++i) {
    const int id = 192 * (wave & 1) + 64 * i + lane;
    const int row = id / 12, cc = id % 12;
    int col = f0 + 8 * cc;
    if (col > FF - 8) col = FF - 8;
    c.wo[i] = (unsigned)(row * FF + col) * 2u;
    c.ww[i] = sbase + row * 384 + ((cc + 12 * isup) ^ (((row >> 1) & 1) << 2)) * 16;
  }
  c.xbase = (const char*)(x + (size_t)(128 * mb + 32 * wave) * DM);
  c.xo = (unsigned)((lane >> 3) * DM + (lane & 7) * 8) * 2u;
  const unsigned xl = sbase + G1_X_OFF;
  c.xw = xl + (32 * wave + (lane >> 3)) * 128 + (((lane & 7) ^ (lane >> 3)) * 16);
  const int r = lane & 31, h = lane >> 5;
#pragma unroll
  for (int i = 0; i < 4; ++i) {
    c.xrd2[0][i] = xl + (64 * wm + 32 * isup + r) * 128 + (((2 * i + h) ^ (r & 7)) * 16);
    c.xrd2[1][i] = xl + (64 * wm + 32 * (1 - isup) + r) * 128 + (((2 * i + h) ^ (r & 7)) * 16);
  }
  const int blk = (lane >> 4) & 1, q = (lane & 15) >> 2, p = lane & 3;
  const int sw = (q >> 1) & 1;
  {
    const unsigned e = sbase + (8 * h + q) * 384 + (2 * blk + (p >> 1) + 4 * sw) * 16 + 8 * (p & 1);
    const unsigned o = sbase + (8 * h + q) * 384 + (2 * blk + (p >> 1) - 4 * sw) * 16 + 8 * (p & 1);
    c.woff = isup ? o + 192 : e;
    c.woff_o = isup ? e + 192 : o;
  }

  f32x16 acc[6];
#pragma unroll
  for (int i = 0; i < 6; ++i) acc[i] = f32x16{};
  asm volatile("s_nop 4" : "+v"(acc[0]), "+v"(acc[1]), "+v"(acc[2]), "+v"(acc[3]), "+v"(acc[4]), "+v"(acc[5]));
  u32x4 xr[2][4], wr[3][3];
  G1Frag fa, fb2;
#define G1_ISSUE_X(R, B) _Pragma("unroll") for (int i = 0; i < 4; ++i) GLOAD128S(xr[R][i], c.xo, c.xbase + (size_t)(8 * i) * DM * 2 + (B) * 128)
#define G1_ISSUE_W(R, S) _Pragma("unroll") for (int i = 0; i < 3; ++i) GLOAD128S(wr[R][i], c.wo[i], c.wbase + (size_t)(S) * 32 * FF * 2)
  G1_ISSUE_X(0, 0); G1_ISSUE_W(0, 0); G1_ISSUE_W(1, 1); G1_ISSUE_X(1, 1); G1_ISSUE_W(2, 2);
#define G1_EXTRA 0
  asm volatile("s_waitcnt vmcnt(%10)"
               : "+v"(wr[0][0]), "+v"(wr[0][1]), "+v"(wr[0][2]), "+v"(wr[1][0]), "+v"(wr[1][1]), "+v"(wr[1][2]),
                 "+v"(xr[0][0]), "+v"(xr[0][1]), "+v"(xr[0][2]), "+v"(xr[0][3]) : "i"(7 + G1_EXTRA) : "memory");
#pragma unroll
  for (int i = 0; i < 3; ++i) { DS_W128(c.ww[i], wr[0][i], 0); DS_W128(c.ww[i], wr[1][i], G1_WSLOT_B); }
#pragma unroll
  for (int i = 0; i < 4; ++i) DS_W128(c.xw, xr[0][i], i * 1024);
  G1_ISSUE_W(0, 3); G1_ISSUE_W(1, 4); G1_ISSUE_X(0, 2);
  asm volatile("s_waitcnt lgkmcnt(0)\n\ts_barrier" ::: "memory");
  g1_read<0>(fa, c);
  G1Step<0>::run(acc, xr, wr, fa, fb2, c);
  asm volatile("s_nop 15\n\ts_nop 15" : "+v"(acc[0]), "+v"(acc[1]), "+v"(acc[2]), "+v"(acc[3]), "+v"(acc[4]), "+v"(acc[5]));

  unsigned keep[3][8];
  {
    unsigned give[3][8];
#pragma unroll
    for (int tt = 0; tt < 3; ++tt)
#pragma unroll
      for (int e = 0; e < 8; ++e) give[tt][e] = pk_bf16(acc[3 + tt][2 * e], acc[3 + tt][2 * e + 1]);
    __syncthreads();
    char* xo = smem + wave * 6144 + lane * 16;
#pragma unroll
    for (int tt = 0; tt < 3; ++tt) {
      *(u32x4*)(xo + (2 * tt) * 1024) = u32x4{give[tt][0], give[tt][1], give[tt][2], give[tt][3]};
      *(u32x4*)(xo + (2 * tt + 1) * 1024) = u32x4{give[tt][4], give[tt][5], give[tt][6], give[tt][7]};
    }
  }
#pragma unroll
  for (int tt = 0; tt < 3; ++tt)
#pragma unroll
    for (int e = 0; e < 8; ++e) keep[tt][e] = pk_bf16(acc[tt][2 * e], acc[tt][2 * e + 1]);
  __syncthreads();
  {
    const char* xi = smem + (wave ^ 2) * 6144 + lane * 16;
    const int mt = 4 * mb + 2 * wm + isup;
    u16* hp = hf + ((size_t)(mt * NKS + 6 * fb) * 64 + lane) * 8;
    auto finish = [&](auto ISUP) {
#pragma unroll
      for (int tt = 0; tt < 3; ++tt) {
        const int ft = 3 * fb + tt;
        if (ft < FF / 32) {
          const u32x4 o0 = *(const u32x4*)(xi + (2 * tt) * 1024);
          const u32x4 o1 = *(const u32x4*)(xi + (2 * tt + 1) * 1024);
          unsigned gp[8], up[8];
#pragma unroll
          for (int e = 0; e < 8; ++e) {
            const unsigned ot = e < 4 ? o0[e & 3] : o1[e & 3];
            gp[e] = decltype(ISUP)::value ? ot : keep[tt][e];
            up[e] = decltype(ISUP)::value ? keep[tt][e] : ot;
          }
          f32x2 xg[8], ar[8];
#pragma unroll
          for (int e = 0; e < 8; ++e) xg[e] = f32x2{bf_lo(gp[e]), bf_hi(gp[e])};
#pragma unroll
          for (int e = 0; e < 8; ++e) ar[e] = xg[e] * xg[e];
#pragma unroll
          for (int e = 0; e < 8; ++e) ar[e] = __builtin_elementwise_fma(ar[e], f32x2{-0.10294323958f, -0.10294323958f}, f32x2{-2.30220819814f, -2.30220819814f});
#pragma unroll
          for (int e = 0; e < 8; ++e) ar[e] = ar[e] * xg[e];
#pragma unroll
          for (int e = 0; e < 8; ++e) ar[e] = f32x2{__builtin_amdgcn_exp2f(ar[e][0]), __builtin_amdgcn_exp2f(ar[e][1])};
#pragma unroll
          for (int e = 0; e < 8; ++e) ar[e] = ar[e] + f32x2{1.0f, 1.0f};
#pragma unroll
          for (int e = 0; e < 8; ++e) ar[e] = f32x2{__builtin_amdgcn_rcpf(ar[e][0]), __builtin_amdgcn_rcpf(ar[e][1])};
#pragma unroll
          for (int e = 0; e < 8; ++e) ar[e] = ar[e] * xg[e];
          unsigned o[8];
#pragma unroll
          for (int e = 0; e < 8; ++e) o[e] = pk_bf16(ar[e][0], ar[e][1]);
#pragma unroll
          for (int e = 0; e < 8; ++e) ar[e] = f32x2{bf_lo(o[e]), bf_hi(o[e])} * f32x2{bf_lo(up[e]), bf_hi(up[e])};
#pragma unroll
          for (int e = 0; e < 8; ++e) o[e] = pk_bf16(ar[e][0], ar[e][1]);
          *(u32x4*)(hp + (2 * tt) * 512) = u32x4{o[0], o[1], o[2], o[3]};
          *(u32x4*)(hp + (2 * tt + 1) * 512) = u32x4{o[4], o[5], o[6], o[7]};
        } else if (ft < NKS / 2) {
          *(u32x4*)(hp + (2 * tt) * 512) = u32x4{0, 0, 0, 0};
          *(u32x4*)(hp + (2 * tt + 1) * 512) = u32x4{0, 0, 0, 0};
        }
      }
    };
    if (isup) finish(std::integral_constant<bool, true>{});
    else finish(std::integral_constant<bool, false>{});
  }
}

__device__ __forceinline__ void gate_unit(int b, const u16* __restrict__ x, const u16* __restrict__ gw,
                                          float* __restrict__ gout) {
  const int tid = threadIdx.x, lane = tid & 63, wave = tid >> 6;
  const int j = lane & 7, rr = lane >> 3;
  u32x4 gv[8];
#pragma unroll
  for (int i = 0; i < 8; ++i) gv[i] = *(const u32x4*)(gw + (j + 8 * i) * 8);
  const int row0 = b * 128 + wave * 32 + rr;
  u32x4 xv[4][8];
#pragma unroll
  for (int ps = 0; ps < 4; ++ps)
#pragma unroll
    for (int i = 0; i < 8; ++i) xv[ps][i] = *(const u32x4*)(x + (size_t)(row0 + 8 * ps) * DM + (j + 8 * i) * 8);
#pragma unroll
  for (int ps = 0; ps < 4; ++ps) {
    float s0 = 0.f, s1 = 0.f;
#pragma unroll
    for (int i = 0; i < 8; ++i)
#pragma unroll
      for (int e = 0; e < 4; ++e) {
        const unsigned pr = pk_bf16(bf_lo(xv[ps][i][e]) * bf_lo(gv[i][e]), bf_hi(xv[ps][i][e]) * bf_hi(gv[i][e]));
        s0 += bf_lo(pr);
        s1 += bf_hi(pr);
      }
    float s = s0 + s1;
    s += __shfl_xor(s, 1);
    s += __shfl_xor(s, 2);
    s += __shfl_xor(s, 4);
    if (j == 0) {
      const float sb = rbf(s);
      gout[row0 + 8 * ps] = rbf(1.0f / (1.0f + expf(-sb)));
    }
  }
}

__global__ __launch_bounds__(256, 1) void k1_kernel(const u16* __restrict__ u, const u16* __restrict__ wg,
                                                   const u16* __restrict__ wu, const u16* __restrict__ gw,
                                                   const u16* __restrict__ wd, u16* __restrict__ hbuf,
                                                   float* __restrict__ gout, unsigned* __restrict__ flags DBG_ARG
                                                   ) {
  __shared__ __attribute__((aligned(4096))) char smem[G1_LDS];
  const int bid = blockIdx.x;
  const int xcd = bid & 7, idx = bid >> 3;
  if (idx < 30) {
    gemm1_tile(30 * xcd + idx, u, wg, wu, hbuf, smem DBG_PASS);
  } else {
    gate_unit(2 * xcd + idx - 30, u, gw, gout);
    if (bid == 255) flags[threadIdx.x] = 0u;
    unsigned acc = 0;
#pragma unroll 4
    for (int i = (idx - 30) * 256 + threadIdx.x; i < 11008; i += 512) acc += ((const unsigned*)wd)[i * 32];
    asm volatile("" ::"v"(acc));
  }
}

constexpr int G2_SLOT_B = 4096;
constexpr int G2_WAVE_B = 2 * G2_SLOT_B;
constexpr int G2_PITCH = 272;
constexpr int G2_A_OFF = 4 * 64 * G2_PITCH;
constexpr int K2_LDS = G2_A_OFF + 8192;
#define G2D 3
constexpr int G2_DEPTH = G2D;

struct G2Ctx {
  const u16* hsrc;
  const u16* wd;
  int wrow0, wcol;
  unsigned wb32, woff, ww;
};

template <int ST>
struct G2Step {
  static __device__ __forceinline__ void issue(bf16x8 (&hfr)[11][4], u32x4 (&wr)[11][4], const G2Ctx& c) {
#pragma unroll
    for (int i = 0; i < 4; ++i) {
      int kr = c.wrow0 + ST * 32 + 8 * i;
      kr = kr < FF ? kr : FF - 1;
      GLOAD128(wr[ST][i], c.wd + (size_t)kr * DM + c.wcol);
    }
#pragma unroll
    for (int mi = 0; mi < 2; ++mi)
#pragma unroll
      for (int i = 0; i < 2; ++i)
        GLOAD128(hfr[ST][2 * mi + i], c.hsrc + (size_t)(mi * NKS + 2 * ST + i) * 512);

  }
  static __device__ __forceinline__ void run(f32x16 (&acc)[2][2], bf16x8 (&hfr)[11][4], u32x4 (&wr)[11][4], const G2Ctx& c) {
    constexpr int LAST = (ST + G2_DEPTH - 1 < 10) ? ST + G2_DEPTH - 1 : 10;
    constexpr int NW = 8 * (LAST - ST);
    asm volatile("s_waitcnt vmcnt(%4)" : "+v"(wr[ST][0]), "+v"(wr[ST][1]), "+v"(wr[ST][2]), "+v"(wr[ST][3]) : "i"(NW + 4) : "memory");
#pragma unroll
    for (int i = 0; i < 4; ++i) DS_W128(c.ww, wr[ST][i], (ST & 1) * G2_SLOT_B + i * 1024);
    asm volatile("s_waitcnt lgkmcnt(0)" ::: "memory");
    const unsigned sb = c.wb32 + c.woff;
    s16x4 w[2][2][2];
#pragma unroll
    for (int i = 0; i < 2; ++i)
#pragma unroll
      for (int t = 0; t < 2; ++t) {
        DS_TR(w[i][t][0], sb ^ (t * 64), (ST & 1) * G2_SLOT_B + i * 2048);
        DS_TR(w[i][t][1], sb ^ (t * 64), (ST & 1) * G2_SLOT_B + i * 2048 + 1024);
      }
    asm volatile("s_waitcnt vmcnt(%4)" : "+v"(hfr[ST][0]), "+v"(hfr[ST][1]), "+v"(hfr[ST][2]), "+v"(hfr[ST][3]) : "i"(NW) : "memory");
    if constexpr (ST + G2_DEPTH <= 10) G2Step<ST + G2_DEPTH>::issue(hfr, wr, c);
    asm volatile("s_waitcnt lgkmcnt(0)"
                 : "+v"(w[0][0][0]), "+v"(w[0][0][1]), "+v"(w[0][1][0]), "+v"(w[0][1][1]), "+v"(w[1][0][0]), "+v"(w[1][0][1]),
                   "+v"(w[1][1][0]), "+v"(w[1][1][1])::"memory");
#pragma unroll
    for (int i = 0; i < 2; ++i)
#pragma unroll
      for (int t = 0; t < 2; ++t)
#pragma unroll
        for (int mi = 0; mi < 2; ++mi)
          acc[t][mi] = __builtin_amdgcn_mfma_f32_32x32x16_bf16(cat4(w[i][t][0], w[i][t][1]), hfr[ST][2 * mi + i], acc[t][mi], 0, 0, 0);
    if constexpr (ST + 1 <= 10) G2Step<ST + 1>::run(acc, hfr, wr, c);
  }
};

__device__ __forceinline__ void gemm2_tile(int bid, const u16* __restrict__ hf, const u16* __restrict__ wd,
                                           const u16* aT, const float* __restrict__ gin, unsigned* flags,
                                           u16* __restrict__ out, char* smem) {
  const int tid = threadIdx.x, lane = tid & 63;
  const int wave = __builtin_amdgcn_readfirstlane(tid >> 6);
  const int xcd = bid & 7, idx = bid >> 3;
  const int m0 = 64 * (4 * xcd + (idx >> 3)), n0 = 64 * (idx & 7);

  G2Ctx c;
  c.wb32 = lds_addr(smem) + wave * G2_WAVE_B;
  c.hsrc = hf + ((size_t)((m0 >> 5) * NKS + 22 * wave) * 64 + lane) * 8;
  c.wd = wd;
  c.wcol = n0 + 8 * (lane & 7);
  c.wrow0 = 352 * wave + (lane >> 3);
  c.ww = c.wb32 + (lane >> 3) * 128 + (((lane & 7) ^ (((lane >> 4) & 1) << 2)) * 16);

  const int r = lane & 31, h = lane >> 5;
  const int blk = (lane >> 4) & 1, q = (lane & 15) >> 2, p = lane & 3;
  c.woff = (4 * h + q) * 128 + (((2 * blk + (p >> 1)) ^ (((q >> 1) & 1) << 2)) * 16) + 8 * (p & 1);

  f32x16 acc[2][2];
#pragma unroll
  for (int i = 0; i < 2; ++i)
#pragma unroll
    for (int j = 0; j < 2; ++j) acc[i][j] = f32x16{};
  bf16x8 hfr[11][4];
  u32x4 wr[11][4];

  G2Step<0>::issue(hfr, wr, c);
  G2Step<1>::issue(hfr, wr, c);
  if constexpr (G2_DEPTH > 2) G2Step<2>::issue(hfr, wr, c);
  if constexpr (G2_DEPTH > 3) G2Step<3>::issue(hfr, wr, c);
  if constexpr (G2_DEPTH > 4) G2Step<4>::issue(hfr, wr, c);
  if constexpr (G2_DEPTH > 5) G2Step<5>::issue(hfr, wr, c);
  if constexpr (G2_DEPTH > 6) G2Step<6>::issue(hfr, wr, c);
  G2Step<0>::run(acc, hfr, wr, c);
  __syncthreads();
  {
    char* pw = smem + wave * (64 * G2_PITCH);
#pragma unroll
    for (int c = 0; c < 2; ++c)
#pragma unroll
      for (int mi = 0; mi < 2; ++mi)
#pragma unroll
        for (int gq = 0; gq < 4; ++gq) {
          const f32x4 v = {acc[c][mi][4 * gq], acc[c][mi][4 * gq + 1], acc[c][mi][4 * gq + 2], acc[c][mi][4 * gq + 3]};
          *(f32x4*)(pw + (32 * mi + r) * G2_PITCH + (32 * c + 8 * gq + 4 * h) * 4) = v;
        }
  }
  if (wave == 0) {
    unsigned* fp = flags + (n0 >> 1) + (lane & 31);
    for (int spin = 0; spin < (1 << 20); ++spin) {
      const unsigned v = __hip_atomic_load(fp, __ATOMIC_RELAXED, __HIP_MEMORY_SCOPE_AGENT);
      if (__all(v != 0u)) break;
      __builtin_amdgcn_s_sleep(2);
    }
  }
  __syncthreads();
  {
    u32x4 av[2];
#pragma unroll
    for (int i = 0; i < 2; ++i) {
      const int drow = 8 * (2 * wave + i) + (lane >> 3);
      asm volatile("global_load_dwordx4 %0, %1, off sc1" : "=v"(av[i]) : "v"(aT + (size_t)(n0 + drow) * SEQ + m0 + (lane & 7) * 8) : "memory");
    }
    asm volatile("s_waitcnt vmcnt(0)" : "+v"(av[0]), "+v"(av[1])::"memory");
#pragma unroll
    for (int i = 0; i < 2; ++i) *(u32x4*)(smem + G2_A_OFF + (2 * wave + i) * 1024 + lane * 16) = av[i];
  }
  __syncthreads();
  {
    const int m = tid >> 2, nq = (tid & 3) * 16;
    const float g = gin[m0 + m];
    const float omg = rbf(1.0f - g);
    float b[16];
#pragma unroll
    for (int j = 0; j < 4; ++j) {
      f32x4 s = *(const f32x4*)(smem + m * G2_PITCH + (nq + 4 * j) * 4);
#pragma unroll
      for (int w = 1; w < 4; ++w) s += *(const f32x4*)(smem + w * (64 * G2_PITCH) + m * G2_PITCH + (nq + 4 * j) * 4);
#pragma unroll
      for (int e = 0; e < 4; ++e) b[4 * j + e] = s[e];
    }
    const u16* aimg = (const u16*)(smem + G2_A_OFF);
    unsigned o[8];
#pragma unroll
    for (int j = 0; j < 8; ++j) {
      const float a0 = bf2f(aimg[(nq + 2 * j) * 64 + m]);
      const float a1 = bf2f(aimg[(nq + 2 * j + 1) * 64 + m]);
      const float o0 = rbf(g * a0) + rbf(omg * rbf(b[2 * j]));
      const float o1 = rbf(g * a1) + rbf(omg * rbf(b[2 * j + 1]));
      o[j] = pk_bf16(o0, o1);
    }
    u16* op = out + (size_t)(m0 + m) * DM + n0 + nq;
    *(u32x4*)op = u32x4{o[0], o[1], o[2], o[3]};
    *(u32x4*)(op + 8) = u32x4{o[4], o[5], o[6], o[7]};
  }
}

__global__ __launch_bounds__(256, 2) void k2_kernel(const u16* __restrict__ u, const u16* __restrict__ logAr,
                                                   const u16* __restrict__ Aim, const u16* __restrict__ Cre,
                                                   const u16* __restrict__ Cim, const u16* __restrict__ logdt,
                                                   const u16* __restrict__ Dskip, const u16* __restrict__ hf,
                                                   const u16* __restrict__ wd, u16* aT, const float* __restrict__ gin,
                                                   unsigned* flags, u16* __restrict__ out DBG_ARG
                                                   ) {
  __shared__ __attribute__((aligned(4096))) char smem[K2_LDS > SSM_LDS ? K2_LDS : SSM_LDS];
  const int bid = blockIdx.x;
  if (bid < NSSM) ssm_unit(bid, u, logAr, Aim, Cre, Cim, logdt, Dskip, aT, flags, smem DBG_PASS);
  else gemm2_tile(bid - NSSM, hf, wd, aT, gin, flags, out, smem);
}

extern "C" void kernel_launch(void* const* d_in, const int* in_sizes, int n_in, void* d_out, int out_size,
                              void* d_ws, size_t ws_size, hipStream_t stream) {
  const u16* u = (const u16*)d_in[0];
  const u16* logAr = (const u16*)d_in[1];
  const u16* Aim = (const u16*)d_in[2];
  const u16* Cre = (const u16*)d_in[3];
  const u16* Cim = (const u16*)d_in[4];
  const u16* logdt = (const u16*)d_in[5];
  const u16* Dskip = (const u16*)d_in[6];
  const u16* wg = (const u16*)d_in[8];
  const u16* wu = (const u16*)d_in[9];
  const u16* wd = (const u16*)d_in[10];
  const u16* gw = (const u16*)d_in[11];
  char* ws = (char*)d_ws;
  u16* hbuf = (u16*)ws;
  u16* aT = (u16*)(ws + (size_t)SEQ * FFP * 2);
  float* gbuf = (float*)(ws + (size_t)SEQ * FFP * 2 + (size_t)DM * SEQ * 2);
  unsigned* flags = (unsigned*)(ws + (size_t)SEQ * FFP * 2 + (size_t)DM * SEQ * 2 + SEQ * 4);
  k1_kernel<<<256, 256, 0, stream>>>(u, wg, wu, gw, wd, hbuf, gbuf, flags
      );
  k2_kernel<<<512, 256, 0, stream>>>(u, logAr, Aim, Cre, Cim, logdt, Dskip, hbuf, wd, aT, gbuf, flags, (u16*)d_out
      );
}
```

```cpp
#include <hip/hip_runtime.h>
#include <hip/hip_bf16.h>
#include <stdint.h>
#include <type_traits>

typedef unsigned short u16;
typedef __attribute__((ext_vector_type(8))) short bf16x8;
typedef __attribute__((ext_vector_type(4))) short s16x4;
typedef __attribute__((ext_vector_type(4))) float f32x4;
typedef __attribute__((ext_vector_type(16))) float f32x16;
typedef __attribute__((ext_vector_type(4))) unsigned u32x4;
typedef __attribute__((ext_vector_type(2))) unsigned u32x2;
typedef __attribute__((ext_vector_type(2))) __bf16 bf16x2_t;
typedef __attribute__((ext_vector_type(2))) float f32x2;
typedef __attribute__((address_space(3))) s16x4* lds_s16x4_ptr;

constexpr int SEQ = 2048, DM = 512, NST = 128, FF = 1376, FFP = 1408;
constexpr int NSSM = 256;
constexpr int NKS = 88;

__device__ __forceinline__ unsigned pk_bf16(float lo, float hi) {
  f32x2 v = {lo, hi};
  bf16x2_t b = __builtin_convertvector(v, bf16x2_t);
  return __builtin_bit_cast(unsigned, b);
}
__device__ __forceinline__ float bf_lo(unsigned w) { return __uint_as_float(w << 16); }
__device__ __forceinline__ float bf_hi(unsigned w) { return __uint_as_float(w & 0xffff0000u); }
__device__ __forceinline__ float rbf(float x) { return bf_lo(pk_bf16(x, 0.f)); }
__device__ __forceinline__ float bf2f(u16 v) { return __uint_as_float(((unsigned)v) << 16); }

__device__ __forceinline__ bf16x8 mk8(unsigned a, unsigned b, unsigned c, unsigned d) {
  u32x4 v = {a, b, c, d};
  return __builtin_bit_cast(bf16x8, v);
}
__device__ __forceinline__ bf16x8 cat4(s16x4 a, s16x4 b) {
  return __builtin_shufflevector(a, b, 0, 1, 2, 3, 4, 5, 6, 7);
}
__device__ __forceinline__ s16x4 tr_read(const char* smem_ptr) {
  return __builtin_amdgcn_ds_read_tr16_b64_v4i16((lds_s16x4_ptr)(smem_ptr));
}
__device__ __forceinline__ unsigned lds_addr(const void* p) {
  return (unsigned)(size_t)(const __attribute__((address_space(3))) char*)p;
}
__device__ __forceinline__ void glds16(const void* g, char* l) {
  __builtin_amdgcn_global_load_lds((const unsigned*)g, (unsigned*)l, 16, 0, 0);
}

#define STAMP2(i)
#define DBG2_ARG
#define DBG2_PASS
#define STAMP(i)
#define DBG_ARG
#define DBG_PASS
constexpr int U_COPY_B = 4160;
constexpr int U_CH_B = 4 * U_COPY_B;
constexpr int U_REGION_B = 2 * U_CH_B;
constexpr int KR_PART_B = 4576;
constexpr int KR_OFF = U_REGION_B;
constexpr int KQ_B = 8208;
constexpr int KX_OFF = 2 * KQ_B;
constexpr int SSM_LDS = KR_OFF + 4 * KR_PART_B;
static_assert(KX_OFF + 4 * 1536 <= U_REGION_B, "scratch must fit the U region");

#define DS_TR(dst, addr, off) asm volatile("ds_read_b64_tr_b16 %0, %1 offset:%2" : "=v"(dst) : "v"(addr), "i"(off) : "memory")
#define DS_B128(dst, addr, off) asm volatile("ds_read_b128 %0, %1 offset:%2" : "=v"(dst) : "v"(addr), "i"(off) : "memory")
#define DS_B64(dst, addr, off) asm volatile("ds_read_b64 %0, %1 offset:%2" : "=v"(dst) : "v"(addr), "i"(off) : "memory")
#define MFMA16(acc, a, b) asm volatile("v_mfma_f32_16x16x32_bf16 %0, %1, %2, %0" : "+v"(acc) : "v"(a), "v"(b))

struct ConvFrag {
  s16x4 a[8][2];
  bf16x8 bh[8], bl[8];
};
template <int RI, int K>
__device__ __forceinline__ void conv_read(ConvFrag& f, unsigned ua, unsigned kb) {
  if constexpr (K < 16) {
    DS_B64(f.a[K >> 1][K & 1], ua, 64 * (8 * (K >> 1) + RI) + 8 * (K & 1));
  } else if constexpr (K < 24) {
    DS_B128(f.bh[K - 16], kb, 64 * (56 - 8 * (K - 16) + RI));
  } else {
    DS_B128(f.bl[K - 24], kb, 64 * (56 - 8 * (K - 24) + RI) + KR_PART_B);
  }
}
template <int RI, int K>
__device__ __forceinline__ void conv_read_all(ConvFrag& f, unsigned ua, unsigned kb) {
  conv_read<RI, K>(f, ua, kb);
  if constexpr (K + 1 < 32) conv_read_all<RI, K + 1>(f, ua, kb);
}
#define CONV_WAIT(f)                                                                                                  \
  do {                                                                                                                \
    asm volatile("s_waitcnt lgkmcnt(0)"                                                                               \
                 : "+v"(f.a[0][0]), "+v"(f.a[0][1]), "+v"(f.a[1][0]), "+v"(f.a[1][1]), "+v"(f.a[2][0]), "+v"(f.a[2][1]), \
                   "+v"(f.a[3][0]), "+v"(f.a[3][1]), "+v"(f.a[4][0]), "+v"(f.a[4][1]), "+v"(f.a[5][0]), "+v"(f.a[5][1]), \
                   "+v"(f.a[6][0]), "+v"(f.a[6][1]), "+v"(f.a[7][0]), "+v"(f.a[7][1])::"memory");                     \
    asm volatile("" : "+v"(f.bh[0]), "+v"(f.bh[1]), "+v"(f.bh[2]), "+v"(f.bh[3]), "+v"(f.bh[4]), "+v"(f.bh[5]),        \
                 "+v"(f.bh[6]), "+v"(f.bh[7]), "+v"(f.bl[0]), "+v"(f.bl[1]), "+v"(f.bl[2]), "+v"(f.bl[3]),            \
                 "+v"(f.bl[4]), "+v"(f.bl[5]), "+v"(f.bl[6]), "+v"(f.bl[7])::"memory");                              \
  } while (0)
constexpr int conv_tau(int P) { int t = 0; while (P >= 8 - t) { P -= 8 - t; ++t; } return t; }
constexpr int conv_sig(int P) { int t = 0; while (P >= 8 - t) { P -= 8 - t; ++t; } return P; }
template <int RI, int P>
__device__ __forceinline__ void conv_pairs(f32x4 (&acc)[8], const bf16x8 (&A)[8], ConvFrag& cur, ConvFrag& nxt, unsigned ua,
                                           unsigned kb) {
  constexpr int T = conv_tau(P), SG = conv_sig(P);
  MFMA16(acc[T + SG], A[SG], cur.bl[T]);
  if constexpr (RI < 3 && P < 32) conv_read<RI + 1, P>(nxt, ua, kb);
  MFMA16(acc[T + SG], A[SG], cur.bh[T]);
  if constexpr (P + 1 < 36) conv_pairs<RI, P + 1>(acc, A, cur, nxt, ua, kb);
}
template <int RI>
__device__ __forceinline__ void conv_rho(f32x4 (&acc)[8], ConvFrag& cur, ConvFrag& nxt, unsigned ua, unsigned kb) {
  CONV_WAIT(cur);
  bf16x8 A[8];
#pragma unroll
  for (int i = 0; i < 8; ++i) A[i] = cat4(cur.a[i][0], cur.a[i][1]);
  conv_pairs<RI, 0>(acc, A, cur, nxt, ua, kb);
  if constexpr (RI < 3) conv_rho<RI + 1>(acc, nxt, cur, ua, kb);
}

__device__ __forceinline__ void split_pair(f32x2 v, unsigned& hi, unsigned& lo) {
  hi = pk_bf16(v[0], v[1]);
  const f32x2 r = v - f32x2{bf_lo(hi), bf_hi(hi)};
  lo = pk_bf16(r[0], r[1]);
}

constexpr int KG_PRM_OFF = 24576;
constexpr int KG_TR_OFF = 28672;
constexpr int KG_LDS = KG_TR_OFF + 128 * 80;
constexpr int KR_CH_ELEMS = 2 * 2288;

__device__ __forceinline__ void kgen_unit(int w, const u16* __restrict__ u, const u16* __restrict__ logAr,
                                          const u16* __restrict__ Aim, const u16* __restrict__ Cre,
                                          const u16* __restrict__ Cim, const u16* __restrict__ logdt,
                                          u16* __restrict__ krg, u16* __restrict__ uT, char* smem DBG_ARG) {
  const int tid = threadIdx.x, lane = tid & 63;
  const int wave = __builtin_amdgcn_readfirstlane(tid >> 6);
  const int ch = wave >> 1, w2 = wave & 1;
  const int d0 = 2 * w;
  const int d = d0 + ch;
  const int pidx = d0 * NST + tid;
  const u16 r_lar = logAr[pidx], r_aim = Aim[pidx], r_cre = Cre[pidx], r_cim = Cim[pidx], r_ldt = logdt[d];
  const int trow0 = 128 * (w >> 4), tcol0 = 32 * (w & 15);
  u32x4 tv[2];
#pragma unroll
  for (int i = 0; i < 2; ++i) tv[i] = *(const u32x4*)(u + (size_t)(trow0 + 64 * i + (tid >> 2)) * DM + tcol0 + (tid & 3) * 8);
  {
    const float dt = rbf(expf(bf2f(r_ldt)));
    const float are = rbf(expf(bf2f(r_lar)));
    f32x4 prm;
    prm[0] = (-are * dt) * 1.44269504088896340736f;
    prm[1] = (bf2f(r_aim) * dt) * 0.15915494309189533577f;
    prm[2] = 2.0f * (bf2f(r_cre) * dt);
    prm[3] = 2.0f * (bf2f(r_cim) * dt);
    *(f32x4*)(smem + KG_PRM_OFF + tid * 16) = prm;
  }
#pragma unroll
  for (int i = 0; i < 2; ++i) *(u32x4*)(smem + KG_TR_OFF + (64 * i + (tid >> 2)) * 80 + (tid & 3) * 16) = tv[i];
  __syncthreads();
  {
    const int g = lane >> 4, q = (lane & 15) >> 2, p = lane & 3;
    const unsigned ta = lds_addr(smem) + KG_TR_OFF + (32 * wave + 16 * (g >> 1) + q) * 80 + (g & 1) * 32 + p * 8;
    s16x4 t0, t1, t2, t3;
    DS_TR(t0, ta, 0); DS_TR(t1, ta, 4 * 80); DS_TR(t2, ta, 8 * 80); DS_TR(t3, ta, 12 * 80);
    asm volatile("s_waitcnt lgkmcnt(0)" : "+v"(t0), "+v"(t1), "+v"(t2), "+v"(t3)::"memory");
    u16* dst = uT + (size_t)(tcol0 + 16 * (g & 1) + (lane & 15)) * SEQ + trow0 + 32 * wave + 16 * (g >> 1);
    *(bf16x8*)dst = cat4(t0, t1);
    *(bf16x8*)(dst + 8) = cat4(t2, t3);
  }

  const int r = lane & 31, h = lane >> 5;
  f32x16 dacc = {};
  {
    int p1i;
    if (r < 8) p1i = r;
    else if (r < 12) p1i = 256 + 2 * (r - 8);
    else if (r < 16) p1i = 512 + 256 * ((r - 12) >> 1) + 4 * ((r - 12) & 1);
    else if (r < 21) p1i = 1024 + 256 * (r - 16);
    else p1i = 0;
    const float p1 = (float)p1i, p2 = (float)(8 * r);
    const f32x2 P1 = {p1, p1}, P2 = {p2, p2};
    const char* prm_base = smem + KG_PRM_OFF + (ch * 128 + 64 * w2 + 4 * h) * 16;
#pragma unroll
    for (int ks = 0; ks < 8; ++ks) {
      f32x4 prm[4];
#pragma unroll
      for (int q = 0; q < 4; ++q) prm[q] = *(const f32x4*)(prm_base + (8 * ks + q) * 16);
      f32x2 za[4], zb[4];
#pragma unroll
      for (int q = 0; q < 4; ++q) { za[q] = f32x2{prm[q][0], prm[q][1]} * P1; zb[q] = f32x2{prm[q][0], prm[q][1]} * P2; }
      float ea[4], eb[4], fa[4], fb[4];
#pragma unroll
      for (int q = 0; q < 4; ++q) {
        ea[q] = __builtin_amdgcn_exp2f(za[q][0]); eb[q] = __builtin_amdgcn_exp2f(zb[q][0]);
        fa[q] = __builtin_amdgcn_fractf(za[q][1]); fb[q] = __builtin_amdgcn_fractf(zb[q][1]);
      }
      f32x2 ca[4], cb[4];
#pragma unroll
      for (int q = 0; q < 4; ++q) {
        ca[q] = f32x2{__builtin_amdgcn_cosf(fa[q]), __builtin_amdgcn_sinf(fa[q])};
        cb[q] = f32x2{__builtin_amdgcn_cosf(fb[q]), __builtin_amdgcn_sinf(fb[q])};
      }
      unsigned ah[4], al[4], bh[4], bl[4];
#pragma unroll
      for (int q = 0; q < 4; ++q) {
        const f32x2 ce = f32x2{prm[q][2], prm[q][3]} * f32x2{ea[q], ea[q]};
        f32x2 g = f32x2{ce[0], -ce[0]} * ca[q];
        g = __builtin_elementwise_fma(f32x2{-ce[1], -ce[1]}, f32x2{ca[q][1], ca[q][0]}, g);
        split_pair(g, ah[q], al[q]);
        split_pair(cb[q] * f32x2{eb[q], eb[q]}, bh[q], bl[q]);
      }
      const bf16x8 Ah = mk8(ah[0], ah[1], ah[2], ah[3]), Al = mk8(al[0], al[1], al[2], al[3]);
      const bf16x8 Bh = mk8(bh[0], bh[1], bh[2], bh[3]), Bl = mk8(bl[0], bl[1], bl[2], bl[3]);
      dacc = __builtin_amdgcn_mfma_f32_32x32x16_bf16(Al, Bh, dacc, 0, 0, 0);
      dacc = __builtin_amdgcn_mfma_f32_32x32x16_bf16(Ah, Bl, dacc, 0, 0, 0);
      dacc = __builtin_amdgcn_mfma_f32_32x32x16_bf16(Ah, Bh, dacc, 0, 0, 0);
    }
  }
  {
    float* xo = (float*)(smem + KX_OFF + wave * 1536) + lane;
    const float* xi = (const float*)(smem + KX_OFF + (wave ^ 1) * 1536) + lane;
    float mine[6];
    if (w2 == 0) {
#pragma unroll
      for (int e = 0; e < 6; ++e) { xo[e * 64] = dacc[6 + e]; mine[e] = dacc[e]; }
    } else {
#pragma unroll
      for (int e = 0; e < 6; ++e) { xo[e * 64] = dacc[e]; mine[e] = dacc[6 + e]; }
    }
    __syncthreads();
    unsigned* Kq = (unsigned*)(smem + ch * KQ_B);
    unsigned pq[6];
#pragma unroll
    for (int e = 0; e < 6; ++e) {
      const float v = mine[e] + xi[e * 64];
      const unsigned hi = pk_bf16(v, 0.f);
      pq[e] = pk_bf16(0.f, v - bf_lo(hi)) | hi;
    }
    if (w2 == 0) {
#pragma unroll
      for (int e = 0; e < 4; ++e) Kq[4 * h + e + 8 * r] = pq[e];
      Kq[(h ? 512 : 256) + 8 * r] = pq[4];
      Kq[(h ? 516 : 258) + 8 * r] = pq[5];
    } else {
      Kq[(h ? 768 : 260) + 8 * r] = pq[0];
      Kq[(h ? 772 : 262) + 8 * r] = pq[1];
      if (h == 0) {
#pragma unroll
        for (int e = 0; e < 4; ++e) Kq[1024 + 256 * e + 8 * r] = pq[2 + e];
      } else if (r == 0) {
        Kq[2048] = pq[2];
      }
    }
  }
  __syncthreads();

  {
    const int t = tid & 127;
    const unsigned* Kq = (const unsigned*)(smem + ch * KQ_B);
    unsigned* krh = (unsigned*)(krg + (size_t)d * KR_CH_ELEMS);
    unsigned* krl = krh + 1144;
    unsigned q0[8], q1[8];
#pragma unroll
    for (int m = 0; m < 8; ++m) {
      const int sh = m < 4 ? 3 : (m < 6 ? 2 : (m < 7 ? 1 : 0));
      const int l0 = 2047 - 2 * (t + 128 * m), l1 = l0 - 1;
      int pa = l0, pb = l1;
      if (sh) {
        pa = (l0 + (1 << (sh - 1)) - 1 + ((l0 >> sh) & 1)) & ~((1 << sh) - 1);
        pb = (l1 + (1 << (sh - 1)) - 1 + ((l1 >> sh) & 1)) & ~((1 << sh) - 1);
      }
      q0[m] = Kq[pa];
      q1[m] = Kq[pb];
    }
#pragma unroll
    for (int m = 0; m < 8; ++m) {
      krh[t + 128 * m] = __builtin_amdgcn_perm(q1[m], q0[m], 0x05040100u);
      krl[t + 128 * m] = __builtin_amdgcn_perm(q1[m], q0[m], 0x07060302u);
    }
    if (t < 120) { krh[1024 + t] = 0; krl[1024 + t] = 0; }
  }
}

__device__ __forceinline__ void conv_unit(int w, const u16* __restrict__ uT, const u16* __restrict__ krg,
                                          const u16* __restrict__ Dskip, u16* aT, unsigned* flags, char* smem DBG_ARG) {
  const int tid = threadIdx.x, lane = tid & 63;
  const int wave = __builtin_amdgcn_readfirstlane(tid >> 6);
  const int ch = wave >> 1, w2 = wave & 1;
  const int d0 = 2 * w;
  const int d = d0 + ch;
  const u16 r_dsk = Dskip[d];
  u32x4 uv[2];
  u32x2 pv[2];
#pragma unroll
  for (int c = 0; c < 2; ++c) {
    uv[c] = *(const u32x4*)(uT + (size_t)(d0 + c) * SEQ + 8 * tid);
    pv[c] = u32x2{0u, 0u};
    if (tid > 0) pv[c] = *(const u32x2*)(uT + (size_t)(d0 + c) * SEQ + 8 * tid - 4);
  }
  u32x4 kv[5];
  const u32x4* ksrc = (const u32x4*)(krg + (size_t)d0 * KR_CH_ELEMS);
#pragma unroll
  for (int i = 0; i < 4; ++i) kv[i] = ksrc[tid + 256 * i];
  kv[4] = u32x4{0, 0, 0, 0};
  if (tid < 120) kv[4] = ksrc[1024 + tid];
#pragma unroll
  for (int c = 0; c < 2; ++c) {
    const unsigned m2 = pv[c][0], m1 = pv[c][1], w0 = uv[c][0], w1 = uv[c][1], w2_ = uv[c][2], w3 = uv[c][3];
    const unsigned s0 = __builtin_amdgcn_alignbit(m1, m2, 16), s1 = __builtin_amdgcn_alignbit(w0, m1, 16),
                   s2 = __builtin_amdgcn_alignbit(w1, w0, 16), s3 = __builtin_amdgcn_alignbit(w2_, w1, 16),
                   s4 = __builtin_amdgcn_alignbit(w3, w2_, 16);
    char* ub = smem + c * U_CH_B + (tid + 2) * 16;
    *(u32x4*)(ub + 0 * U_COPY_B) = u32x4{s0, s1, s2, s3};
    *(u32x4*)(ub + 1 * U_COPY_B) = u32x4{m1, w0, w1, w2_};
    *(u32x4*)(ub + 2 * U_COPY_B) = u32x4{s1, s2, s3, s4};
    *(u32x4*)(ub + 3 * U_COPY_B) = uv[c];
  }
  if (tid < 16) *(u32x4*)(smem + (tid >> 1) * U_COPY_B + (tid & 1) * 16) = u32x4{0, 0, 0, 0};
#pragma unroll
  for (int i = 0; i < 4; ++i) *(u32x4*)(smem + KR_OFF + (tid + 256 * i) * 16) = kv[i];
  if (tid < 120) *(u32x4*)(smem + KR_OFF + (1024 + tid) * 16) = kv[4];
  __syncthreads();

  f32x4 acc[8];
#pragma unroll
  for (int t = 0; t < 8; ++t) acc[t] = f32x4{0.f, 0.f, 0.f, 0.f};
  asm volatile("s_nop 4" : "+v"(acc[0]), "+v"(acc[1]), "+v"(acc[2]), "+v"(acc[3]), "+v"(acc[4]), "+v"(acc[5]), "+v"(acc[6]), "+v"(acc[7]));
  {
    const int r16 = lane & 15, hq = lane >> 4;
    const unsigned sb = lds_addr(smem);
    const unsigned ua = sb + ch * U_CH_B + (r16 & 3) * U_COPY_B + 2 * (4 + (r16 & ~3) + 8 * hq) + 64 * 4 * w2;
    const unsigned kb = sb + KR_OFF + (ch * 2) * KR_PART_B + 2 * (2032 - 16 * r16 + 8 * hq) - 64 * 56 + 64 * 4 * w2;
    ConvFrag f0, f1;
    conv_read_all<0, 0>(f0, ua, kb);
    conv_rho<0>(acc, f0, f1, ua, kb);
  }
  asm volatile("s_nop 15" : "+v"(acc[0]), "+v"(acc[1]), "+v"(acc[2]), "+v"(acc[3]), "+v"(acc[4]), "+v"(acc[5]), "+v"(acc[6]), "+v"(acc[7]));
  __syncthreads();
  {
    char* xw = smem + KR_OFF + (ch * 2 + w2) * 4096 + lane * 16;
    const char* xr = smem + KR_OFF + (ch * 2 + (w2 ^ 1)) * 4096 + lane * 16;
    f32x4 y[4];
    if (w2 == 0) {
#pragma unroll
      for (int i = 0; i < 4; ++i) { *(f32x4*)(xw + i * 1024) = acc[4 + i]; y[i] = acc[i]; }
    } else {
#pragma unroll
      for (int i = 0; i < 4; ++i) { *(f32x4*)(xw + i * 1024) = acc[i]; y[i] = acc[4 + i]; }
    }
    __syncthreads();
    const float Dk = bf2f(r_dsk);
#pragma unroll
    for (int i = 0; i < 4; ++i) {
      const f32x4 o = *(const f32x4*)(xr + i * 1024);
      const int l0 = 256 * (4 * w2 + i) + 16 * (lane & 15) + 4 * (lane >> 4);
      const u32x2 uu = *(const u32x2*)(smem + ch * U_CH_B + 3 * U_COPY_B + 2 * (16 + l0));
      const unsigned y01 = pk_bf16(y[i][0] + o[0], y[i][1] + o[1]), y23 = pk_bf16(y[i][2] + o[2], y[i][3] + o[3]);
      const unsigned s01 = pk_bf16(bf_lo(uu[0]) * Dk, bf_hi(uu[0]) * Dk), s23 = pk_bf16(bf_lo(uu[1]) * Dk, bf_hi(uu[1]) * Dk);
      const u32x2 av = u32x2{pk_bf16(bf_lo(y01) + bf_lo(s01), bf_hi(y01) + bf_hi(s01)),
                             pk_bf16(bf_lo(y23) + bf_lo(s23), bf_hi(y23) + bf_hi(s23))};
      asm volatile("global_store_dwordx2 %0, %1, off sc1\n\ts_nop 1" ::"v"(aT + (size_t)d * SEQ + l0), "v"(av) : "memory");
    }
    asm volatile("s_waitcnt vmcnt(0)" ::: "memory");
    __syncthreads();
    if (tid == 0) __hip_atomic_store(flags + w, 1u, __ATOMIC_RELAXED, __HIP_MEMORY_SCOPE_AGENT);
  }
}

__device__ __forceinline__ float gelu_tanh(float x) {
  const float z = 0.7978845608028654f * (x + 0.044715f * x * x * x);
  const float e = __builtin_amdgcn_exp2f(-2.8853900817779268f * z);
  return x * __builtin_amdgcn_rcpf(1.0f + e);
}

constexpr int G1_WSLOT_B = 12288;
constexpr int G1_X_OFF = 3 * G1_WSLOT_B;
constexpr int G1_LDS = G1_X_OFF + 32768;

constexpr int g1_pos(bool isx, int id) {
  if (isx && id == 0) return 4;
  if (!isx && id == 0) return 7;
  if (!isx && id == 1) return 10;
  if (isx && id == 1) return 14;
  if (!isx && id == 2) return 17;
  if (!isx && id == 3) return 20;
  if (!isx && id == 4) return 23;
  if (isx && id == 2) return 27;
  int n = 27;
  for (int t = 0; t < 32; ++t) {
    if ((t & 1) == 0 && t / 2 + 5 < 16) { n += 3; if (!isx && t / 2 + 5 == id) return n; }
    if ((t & 3) == 1 && t / 4 + 3 < 8) { n += 4; if (isx && t / 4 + 3 == id) return n; }
  }
  return -1;
}
constexpr int g1_issued_before(int step) {
  int n = 27;
  for (int t = 0; t < step; ++t) {
    if ((t & 1) == 0 && t / 2 + 5 < 16) n += 3;
    if ((t & 3) == 1 && t / 4 + 3 < 8) n += 4;
  }
  return n;
}

#define GLOAD128(dst, ptr) asm volatile("global_load_dwordx4 %0, %1, off" : "=v"(dst) : "v"(ptr) : "memory")
#define GLOAD128S(dst, voff, sbase) asm volatile("global_load_dwordx4 %0, %1, %2" : "=v"(dst) : "v"(voff), "s"(sbase) : "memory")
#define GLOAD128SA(dst, voff, sbase) asm volatile("global_load_dwordx4 %0, %1, %2" : "=a"(dst) : "v"(voff), "s"(sbase) : "memory")
#define GLOAD128A(dst, ptr) asm volatile("global_load_dwordx4 %0, %1, off" : "=a"(dst) : "v"(ptr) : "memory")
#define DS_W128(addr, val, off) asm volatile("ds_write_b128 %0, %1 offset:%2" ::"v"(addr), "v"(val), "i"(off) : "memory")
#define MFMA32(acc, a, b) asm volatile("v_mfma_f32_32x32x16_bf16 %0, %1, %2, %0" : "+v"(acc) : "v"(a), "v"(b))

struct G1Ctx {
  const char* wbase;
  const char* xbase;
  unsigned wo[3], xo;
  unsigned woff, woff_o, ww[3], xw, xrd2[2][4];
  unsigned junk[4];
};
struct G1Frag {
  s16x4 w[3][2];
  bf16x8 x[2];
};

#define G1_RD_W(f, t, OFF)                                                        \
  do {                                                                            \
    if ((t) & 1) { DS_TR(f.w[t][0], c.woff_o, (OFF) + (t) * 64); DS_TR(f.w[t][1], c.woff_o, (OFF) + (t) * 64 + 4 * 384); } \
    else { DS_TR(f.w[t][0], c.woff, (OFF) + (t) * 64); DS_TR(f.w[t][1], c.woff, (OFF) + (t) * 64 + 4 * 384); }            \
  } while (0)
#define G1_RD_X(f, mi, N) DS_B128(f.x[mi], c.xrd2[mi][(N) & 3], (((N) >> 2) & 1) * 16384)
template <int N>
__device__ __forceinline__ void g1_read(G1Frag& f, const G1Ctx& c) {
  constexpr int OFF = ((N >> 1) % 3) * G1_WSLOT_B + (N & 1) * 16 * 384;
  G1_RD_X(f, 0, N); G1_RD_X(f, 1, N);
  G1_RD_W(f, 0, OFF); G1_RD_W(f, 1, OFF); G1_RD_W(f, 2, OFF);
}
#define G1_WAIT_FRAG(f, STR)                                                                                       \
  asm volatile(STR : "+v"(f.w[0][0]), "+v"(f.w[0][1]), "+v"(f.w[1][0]), "+v"(f.w[1][1]), "+v"(f.w[2][0]),        \
               "+v"(f.w[2][1]), "+v"(f.x[0]), "+v"(f.x[1])::"memory")

template <int N>
struct G1Step {
  static __device__ __forceinline__ void run(f32x16 (&acc)[6], u32x4 (&xr)[2][4], u32x4 (&wr)[3][3], G1Frag& cur,
                                             G1Frag& nxt, G1Ctx& c) {
    constexpr int ISS = g1_issued_before(N);
    constexpr int S = N >> 1;
    constexpr bool RD = N + 1 < 32;
    constexpr int OFF = (((N + 1) >> 1) % 3) * G1_WSLOT_B + ((N + 1) & 1) * 16 * 384;
    if constexpr ((N & 1) == 0 && N > 0) G1_WAIT_FRAG(cur, "s_waitcnt lgkmcnt(0)\n\ts_barrier");
    else G1_WAIT_FRAG(cur, "s_waitcnt lgkmcnt(0)");
    bf16x8 b[3];
#pragma unroll
    for (int t = 0; t < 3; ++t) b[t] = cat4(cur.w[t][0], cur.w[t][1]);
    MFMA32(acc[0], b[0], cur.x[0]);
    if constexpr (RD) { G1_RD_X(nxt, 0, N + 1); G1_RD_W(nxt, 0, OFF); }
    MFMA32(acc[3], b[0], cur.x[1]);
    if constexpr (RD) { G1_RD_X(nxt, 1, N + 1); G1_RD_W(nxt, 1, OFF); }
    MFMA32(acc[1], b[1], cur.x[0]);
    if constexpr (RD) { G1_RD_W(nxt, 2, OFF); }
    MFMA32(acc[4], b[1], cur.x[1]);
    if constexpr ((N & 1) == 0) {
      if constexpr (S + 2 < 16) {
        constexpr int R = (S + 2) % 3;
        constexpr int NW = ISS - g1_pos(false, S + 2);
        asm volatile("s_waitcnt vmcnt(%3)" : "+v"(wr[R][0]), "+v"(wr[R][1]), "+v"(wr[R][2]) : "i"(NW) : "memory");
#pragma unroll
        for (int i = 0; i < 3; ++i) DS_W128(c.ww[i], wr[R][i], R * G1_WSLOT_B);
      }
    }
    if constexpr ((N & 3) == 1) {
      constexpr int B = N / 4 + 1;
      if constexpr (B < 8) {
        constexpr int NW = ISS - g1_pos(true, B);
        asm volatile("s_waitcnt vmcnt(%4)" : "+v"(xr[B & 1][0]), "+v"(xr[B & 1][1]), "+v"(xr[B & 1][2]), "+v"(xr[B & 1][3]) : "i"(NW) : "memory");
#pragma unroll
        for (int i = 0; i < 4; ++i) DS_W128(c.xw, xr[B & 1][i], (B & 1) * 16384 + i * 1024);
      }
    }
    MFMA32(acc[2], b[2], cur.x[0]);
    if constexpr ((N & 1) == 0 && S + 5 < 16) {
#pragma unroll
      for (int i = 0; i < 3; ++i) GLOAD128S(wr[(S + 5) % 3][i], c.wo[i], c.wbase + (size_t)(S + 5) * 32 * FF * 2);
    }
    if constexpr ((N & 3) == 1 && N / 4 + 3 < 8) {
      constexpr int B = N / 4 + 1;
#pragma unroll
      for (int i = 0; i < 4; ++i) GLOAD128S(xr[B & 1][i], c.xo, c.xbase + (size_t)(8 * i) * DM * 2 + (B + 2) * 128);
    }
    MFMA32(acc[5], b[2], cur.x[1]);
    if constexpr (N + 1 < 32) G1Step<N + 1>::run(acc, xr, wr, nxt, cur, c);
  }
};

__device__ __forceinline__ void gemm1_tile(int t, const u16* __restrict__ x, const u16* __restrict__ wg,
                                           const u16* __restrict__ wu, u16* __restrict__ hf, char* smem DBG_ARG) {
  const int tid = threadIdx.x, lane = tid & 63;
  const int wave = __builtin_amdgcn_readfirstlane(tid >> 6);
  const int fb = t >> 4, mb = t & 15;
  const int f0 = 96 * fb;
  const int wm = wave & 1, isup = wave >> 1;
  G1Ctx c;
  const unsigned sbase = lds_addr(smem);
  c.wbase = (const char*)(isup ? wu : wg);
#pragma unroll
  for (int i = 0; i < 3; ++i) {
    const int id = 192 * (wave & 1) + 64 * i + lane;
    const int row = id / 12, cc = id % 12;
    int col = f0 + 8 * cc;
    if (col > FF - 8) col = FF - 8;
    c.wo[i] = (unsigned)(row * FF + col) * 2u;
    c.ww[i] = sbase + row * 384 + ((cc + 12 * isup) ^ (((row >> 1) & 1) << 2)) * 16;
  }
  c.xbase = (const char*)(x + (size_t)(128 * mb + 32 * wave) * DM);
  c.xo = (unsigned)((lane >> 3) * DM + (lane & 7) * 8) * 2u;
  const unsigned xl = sbase + G1_X_OFF;
  c.xw = xl + (32 * wave + (lane >> 3)) * 128 + (((lane & 7) ^ (lane >> 3)) * 16);
  const int r = lane & 31, h = lane >> 5;
#pragma unroll
  for (int i = 0; i < 4; ++i) {
    c.xrd2[0][i] = xl + (64 * wm + 32 * isup + r) * 128 + (((2 * i + h) ^ (r & 7)) * 16);
    c.xrd2[1][i] = xl + (64 * wm + 32 * (1 - isup) + r) * 128 + (((2 * i + h) ^ (r & 7)) * 16);
  }
  const int blk = (lane >> 4) & 1, q = (lane & 15) >> 2, p = lane & 3;
  const int sw = (q >> 1) & 1;
  {
    const unsigned e = sbase + (8 * h + q) * 384 + (2 * blk + (p >> 1) + 4 * sw) * 16 + 8 * (p & 1);
    const unsigned o = sbase + (8 * h + q) * 384 + (2 * blk + (p >> 1) - 4 * sw) * 16 + 8 * (p & 1);
    c.woff = isup ? o + 192 : e;
    c.woff_o = isup ? e + 192 : o;
  }

  f32x16 acc[6];
#pragma unroll
  for (int i = 0; i < 6; ++i) acc[i] = f32x16{};
  asm volatile("s_nop 4" : "+v"(acc[0]), "+v"(acc[1]), "+v"(acc[2]), "+v"(acc[3]), "+v"(acc[4]), "+v"(acc[5]));
  u32x4 xr[2][4], wr[3][3];
  G1Frag fa, fb2;
#define G1_ISSUE_X(R, B) _Pragma("unroll") for (int i = 0; i < 4; ++i) GLOAD128S(xr[R][i], c.xo, c.xbase + (size_t)(8 * i) * DM * 2 + (B) * 128)
#define G1_ISSUE_W(R, S) _Pragma("unroll") for (int i = 0; i < 3; ++i) GLOAD128S(wr[R][i], c.wo[i], c.wbase + (size_t)(S) * 32 * FF * 2)
  G1_ISSUE_X(0, 0); G1_ISSUE_W(0, 0); G1_ISSUE_W(1, 1); G1_ISSUE_X(1, 1); G1_ISSUE_W(2, 2);
#define G1_EXTRA 0
  asm volatile("s_waitcnt vmcnt(%10)"
               : "+v"(wr[0][0]), "+v"(wr[0][1]), "+v"(wr[0][2]), "+v"(wr[1][0]), "+v"(wr[1][1]), "+v"(wr[1][2]),
                 "+v"(xr[0][0]), "+v"(xr[0][1]), "+v"(xr[0][2]), "+v"(xr[0][3]) : "i"(7 + G1_EXTRA) : "memory");
#pragma unroll
  for (int i = 0; i < 3; ++i) { DS_W128(c.ww[i], wr[0][i], 0); DS_W128(c.ww[i], wr[1][i], G1_WSLOT_B); }
#pragma unroll
  for (int i = 0; i < 4; ++i) DS_W128(c.xw, xr[0][i], i * 1024);
  G1_ISSUE_W(0, 3); G1_ISSUE_W(1, 4); G1_ISSUE_X(0, 2);
  asm volatile("s_waitcnt lgkmcnt(0)\n\ts_barrier" ::: "memory");
  g1_read<0>(fa, c);
  G1Step<0>::run(acc, xr, wr, fa, fb2, c);
  asm volatile("s_nop 15\n\ts_nop 15" : "+v"(acc[0]), "+v"(acc[1]), "+v"(acc[2]), "+v"(acc[3]), "+v"(acc[4]), "+v"(acc[5]));

  unsigned keep[3][8];
  {
    unsigned give[3][8];
#pragma unroll
    for (int tt = 0; tt < 3; ++tt)
#pragma unroll
      for (int e = 0; e < 8; ++e) give[tt][e] = pk_bf16(acc[3 + tt][2 * e], acc[3 + tt][2 * e + 1]);
    __syncthreads();
    char* xo = smem + wave * 6144 + lane * 16;
#pragma unroll
    for (int tt = 0; tt < 3; ++tt) {
      *(u32x4*)(xo + (2 * tt) * 1024) = u32x4{give[tt][0], give[tt][1], give[tt][2], give[tt][3]};
      *(u32x4*)(xo + (2 * tt + 1) * 1024) = u32x4{give[tt][4], give[tt][5], give[tt][6], give[tt][7]};
    }
  }
#pragma unroll
  for (int tt = 0; tt < 3; ++tt)
#pragma unroll
    for (int e = 0; e < 8; ++e) keep[tt][e] = pk_bf16(acc[tt][2 * e], acc[tt][2 * e + 1]);
  __syncthreads();
  {
    const char* xi = smem + (wave ^ 2) * 6144 + lane * 16;
    const int mt = 4 * mb + 2 * wm + isup;
    u16* hp = hf + ((size_t)(mt * NKS + 6 * fb) * 64 + lane) * 8;
    auto finish = [&](auto ISUP) {
#pragma unroll
      for (int tt = 0; tt < 3; ++tt) {
        const int ft = 3 * fb + tt;
        if (ft < FF / 32) {
          const u32x4 o0 = *(const u32x4*)(xi + (2 * tt) * 1024);
          const u32x4 o1 = *(const u32x4*)(xi + (2 * tt + 1) * 1024);
          unsigned gp[8], up[8];
#pragma unroll
          for (int e = 0; e < 8; ++e) {
            const unsigned ot = e < 4 ? o0[e & 3] : o1[e & 3];
            gp[e] = decltype(ISUP)::value ? ot : keep[tt][e];
            up[e] = decltype(ISUP)::value ? keep[tt][e] : ot;
          }
          f32x2 xg[8], ar[8];
#pragma unroll
          for (int e = 0; e < 8; ++e) xg[e] = f32x2{bf_lo(gp[e]), bf_hi(gp[e])};
#pragma unroll
          for (int e = 0; e < 8; ++e) ar[e] = xg[e] * xg[e];
#pragma unroll
          for (int e = 0; e < 8; ++e) ar[e] = __builtin_elementwise_fma(ar[e], f32x2{-0.10294323958f, -0.10294323958f}, f32x2{-2.30220819814f, -2.30220819814f});
#pragma unroll
          for (int e = 0; e < 8; ++e) ar[e] = ar[e] * xg[e];
#pragma unroll
          for (int e = 0; e < 8; ++e) ar[e] = f32x2{__builtin_amdgcn_exp2f(ar[e][0]), __builtin_amdgcn_exp2f(ar[e][1])};
#pragma unroll
          for (int e = 0; e < 8; ++e) ar[e] = ar[e] + f32x2{1.0f, 1.0f};
#pragma unroll
          for (int e = 0; e < 8; ++e) ar[e] = f32x2{__builtin_amdgcn_rcpf(ar[e][0]), __builtin_amdgcn_rcpf(ar[e][1])};
#pragma unroll
          for (int e = 0; e < 8; ++e) ar[e] = ar[e] * xg[e];
          unsigned o[8];
#pragma unroll
          for (int e = 0; e < 8; ++e) o[e] = pk_bf16(ar[e][0], ar[e][1]);
#pragma unroll
          for (int e = 0; e < 8; ++e) ar[e] = f32x2{bf_lo(o[e]), bf_hi(o[e])} * f32x2{bf_lo(up[e]), bf_hi(up[e])};
#pragma unroll
          for (int e = 0; e < 8; ++e) o[e] = pk_bf16(ar[e][0], ar[e][1]);
          *(u32x4*)(hp + (2 * tt) * 512) = u32x4{o[0], o[1], o[2], o[3]};
          *(u32x4*)(hp + (2 * tt + 1) * 512) = u32x4{o[4], o[5], o[6], o[7]};
        } else if (ft < NKS / 2) {
          *(u32x4*)(hp + (2 * tt) * 512) = u32x4{0, 0, 0, 0};
          *(u32x4*)(hp + (2 * tt + 1) * 512) = u32x4{0, 0, 0, 0};
        }
      }
    };
    if (isup) finish(std::integral_constant<bool, true>{});
    else finish(std::integral_constant<bool, false>{});
  }
}

__device__ __forceinline__ void gate_unit(int b, const u16* __restrict__ x, const u16* __restrict__ gw,
                                          float* __restrict__ gout) {
  const int tid = threadIdx.x, lane = tid & 63, wave = tid >> 6;
  const int j = lane & 7, rr = lane >> 3;
  u32x4 gv[8];
#pragma unroll
  for (int i = 0; i < 8; ++i) gv[i] = *(const u32x4*)(gw + (j + 8 * i) * 8);
  const int row0 = b * 128 + wave * 32 + rr;
  u32x4 xv[4][8];
#pragma unroll
  for (int ps = 0; ps < 4; ++ps)
#pragma unroll
    for (int i = 0; i < 8; ++i) xv[ps][i] = *(const u32x4*)(x + (size_t)(row0 + 8 * ps) * DM + (j + 8 * i) * 8);
#pragma unroll
  for (int ps = 0; ps < 4; ++ps) {
    float s0 = 0.f, s1 = 0.f;
#pragma unroll
    for (int i = 0; i < 8; ++i)
#pragma unroll
      for (int e = 0; e < 4; ++e) {
        const unsigned pr = pk_bf16(bf_lo(xv[ps][i][e]) * bf_lo(gv[i][e]), bf_hi(xv[ps][i][e]) * bf_hi(gv[i][e]));
        s0 += bf_lo(pr);
        s1 += bf_hi(pr);
      }
    float s = s0 + s1;
    s += __shfl_xor(s, 1);
    s += __shfl_xor(s, 2);
    s += __shfl_xor(s, 4);
    if (j == 0) {
      const float sb = rbf(s);
      gout[row0 + 8 * ps] = rbf(1.0f / (1.0f + expf(-sb)));
    }
  }
}

__global__ __launch_bounds__(256, 2) void k1_kernel(const u16* __restrict__ u, const u16* __restrict__ logAr,
                                                   const u16* __restrict__ Aim, const u16* __restrict__ Cre,
                                                   const u16* __restrict__ Cim, const u16* __restrict__ logdt,
                                                   const u16* __restrict__ wg, const u16* __restrict__ wu,
                                                   const u16* __restrict__ gw, const u16* __restrict__ wd,
                                                   u16* __restrict__ hbuf, u16* __restrict__ krg, u16* __restrict__ uT,
                                                   float* __restrict__ gout, unsigned* __restrict__ flags DBG_ARG
                                                   ) {
  __shared__ __attribute__((aligned(4096))) char smem[G1_LDS > KG_LDS ? G1_LDS : KG_LDS];
  const int bid = blockIdx.x;
  if (bid < NSSM) {
    kgen_unit(bid, u, logAr, Aim, Cre, Cim, logdt, krg, uT, smem DBG_PASS);
    if (threadIdx.x == 0) flags[bid] = 0u;
    unsigned a0 = ((const unsigned*)wd)[((bid >> 3) * 256 + threadIdx.x) * 32], a1 = 0;
    if ((bid >> 3) * 256 + threadIdx.x + 8192 < 11008) a1 = ((const unsigned*)wd)[((bid >> 3) * 256 + threadIdx.x + 8192) * 32];
    asm volatile("" ::"v"(a0), "v"(a1));
  } else {
    const int xcd = bid & 7, idx = (bid - NSSM) >> 3;
    if (idx < 30) gemm1_tile(30 * xcd + idx, u, wg, wu, hbuf, smem DBG_PASS);
    else gate_unit(2 * xcd + idx - 30, u, gw, gout);
  }
}

constexpr int G2_SLOT_B = 4096;
constexpr int G2_WAVE_B = 2 * G2_SLOT_B;
constexpr int G2_PITCH = 272;
constexpr int G2_A_OFF = 4 * 64 * G2_PITCH;
constexpr int K2_LDS = G2_A_OFF + 8192;
#define G2D 3
constexpr int G2_DEPTH = G2D;

struct G2Ctx {
  const u16* hsrc;
  const u16* wd;
  int wrow0, wcol;
  unsigned wb32, woff, ww;
};

template <int ST>
struct G2Step {
  static __device__ __forceinline__ void issue(bf16x8 (&hfr)[11][4], u32x4 (&wr)[11][4], const G2Ctx& c) {
#pragma unroll
    for (int i = 0; i < 4; ++i) {
      int kr = c.wrow0 + ST * 32 + 8 * i;
      kr = kr < FF ? kr : FF - 1;
      GLOAD128(wr[ST][i], c.wd + (size_t)kr * DM + c.wcol);
    }
#pragma unroll
    for (int mi = 0; mi < 2; ++mi)
#pragma unroll
      for (int i = 0; i < 2; ++i)
        GLOAD128(hfr[ST][2 * mi + i], c.hsrc + (size_t)(mi * NKS + 2 * ST + i) * 512);

  }
  static __device__ __forceinline__ void run(f32x16 (&acc)[2][2], bf16x8 (&hfr)[11][4], u32x4 (&wr)[11][4], const G2Ctx& c) {
    constexpr int LAST = (ST + G2_DEPTH - 1 < 10) ? ST + G2_DEPTH - 1 : 10;
    constexpr int NW = 8 * (LAST - ST);
    asm volatile("s_waitcnt vmcnt(%4)" : "+v"(wr[ST][0]), "+v"(wr[ST][1]), "+v"(wr[ST][2]), "+v"(wr[ST][3]) : "i"(NW + 4) : "memory");
#pragma unroll
    for (int i = 0; i < 4; ++i) DS_W128(c.ww, wr[ST][i], (ST & 1) * G2_SLOT_B + i * 1024);
    asm volatile("s_waitcnt lgkmcnt(0)" ::: "memory");
    const unsigned sb = c.wb32 + c.woff;
    s16x4 w[2][2][2];
#pragma unroll
    for (int i = 0; i < 2; ++i)
#pragma unroll
      for (int t = 0; t < 2; ++t) {
        DS_TR(w[i][t][0], sb ^ (t * 64), (ST & 1) * G2_SLOT_B + i * 2048);
        DS_TR(w[i][t][1], sb ^ (t * 64), (ST & 1) * G2_SLOT_B + i * 2048 + 1024);
      }
    asm volatile("s_waitcnt vmcnt(%4)" : "+v"(hfr[ST][0]), "+v"(hfr[ST][1]), "+v"(hfr[ST][2]), "+v"(hfr[ST][3]) : "i"(NW) : "memory");
    if constexpr (ST + G2_DEPTH <= 10) G2Step<ST + G2_DEPTH>::issue(hfr, wr, c);
    asm volatile("s_waitcnt lgkmcnt(0)"
                 : "+v"(w[0][0][0]), "+v"(w[0][0][1]), "+v"(w[0][1][0]), "+v"(w[0][1][1]), "+v"(w[1][0][0]), "+v"(w[1][0][1]),
                   "+v"(w[1][1][0]), "+v"(w[1][1][1])::"memory");
#pragma unroll
    for (int i = 0; i < 2; ++i)
#pragma unroll
      for (int t = 0; t < 2; ++t)
#pragma unroll
        for (int mi = 0; mi < 2; ++mi)
          acc[t][mi] = __builtin_amdgcn_mfma_f32_32x32x16_bf16(cat4(w[i][t][0], w[i][t][1]), hfr[ST][2 * mi + i], acc[t][mi], 0, 0, 0);
    if constexpr (ST + 1 <= 10) G2Step<ST + 1>::run(acc, hfr, wr, c);
  }
};

__device__ __forceinline__ void gemm2_tile(int bid, const u16* __restrict__ hf, const u16* __restrict__ wd,
                                           const u16* aT, const float* __restrict__ gin, unsigned* flags,
                                           u16* __restrict__ out, char* smem) {
  const int tid = threadIdx.x, lane = tid & 63;
  const int wave = __builtin_amdgcn_readfirstlane(tid >> 6);
  const int xcd = bid & 7, idx = bid >> 3;
  const int m0 = 64 * (4 * xcd + (idx >> 3)), n0 = 64 * (idx & 7);

  G2Ctx c;
  c.wb32 = lds_addr(smem) + wave * G2_WAVE_B;
  c.hsrc = hf + ((size_t)((m0 >> 5) * NKS + 22 * wave) * 64 + lane) * 8;
  c.wd = wd;
  c.wcol = n0 + 8 * (lane & 7);
  c.wrow0 = 352 * wave + (lane >> 3);
  c.ww = c.wb32 + (lane >> 3) * 128 + (((lane & 7) ^ (((lane >> 4) & 1) << 2)) * 16);

  const int r = lane & 31, h = lane >> 5;
  const int blk = (lane >> 4) & 1, q = (lane & 15) >> 2, p = lane & 3;
  c.woff = (4 * h + q) * 128 + (((2 * blk + (p >> 1)) ^ (((q >> 1) & 1) << 2)) * 16) + 8 * (p & 1);

  f32x16 acc[2][2];
#pragma unroll
  for (int i = 0; i < 2; ++i)
#pragma unroll
    for (int j = 0; j < 2; ++j) acc[i][j] = f32x16{};
  bf16x8 hfr[11][4];
  u32x4 wr[11][4];

  G2Step<0>::issue(hfr, wr, c);
  G2Step<1>::issue(hfr, wr, c);
  if constexpr (G2_DEPTH > 2) G2Step<2>::issue(hfr, wr, c);
  if constexpr (G2_DEPTH > 3) G2Step<3>::issue(hfr, wr, c);
  if constexpr (G2_DEPTH > 4) G2Step<4>::issue(hfr, wr, c);
  if constexpr (G2_DEPTH > 5) G2Step<5>::issue(hfr, wr, c);
  if constexpr (G2_DEPTH > 6) G2Step<6>::issue(hfr, wr, c);
  G2Step<0>::run(acc, hfr, wr, c);
  __syncthreads();
  {
    char* pw = smem + wave * (64 * G2_PITCH);
#pragma unroll
    for (int c = 0; c < 2; ++c)
#pragma unroll
      for (int mi = 0; mi < 2; ++mi)
#pragma unroll
        for (int gq = 0; gq < 4; ++gq) {
          const f32x4 v = {acc[c][mi][4 * gq], acc[c][mi][4 * gq + 1], acc[c][mi][4 * gq + 2], acc[c][mi][4 * gq + 3]};
          *(f32x4*)(pw + (32 * mi + r) * G2_PITCH + (32 * c + 8 * gq + 4 * h) * 4) = v;
        }
  }
  if (wave == 0) {
    unsigned* fp = flags + (n0 >> 1) + (lane & 31);
    for (int spin = 0; spin < (1 << 20); ++spin) {
      const unsigned v = __hip_atomic_load(fp, __ATOMIC_RELAXED, __HIP_MEMORY_SCOPE_AGENT);
      if (__all(v != 0u)) break;
      __builtin_amdgcn_s_sleep(2);
    }
  }
  __syncthreads();
  {
    u32x4 av[2];
#pragma unroll
    for (int i = 0; i < 2; ++i) {
      const int drow = 8 * (2 * wave + i) + (lane >> 3);
      asm volatile("global_load_dwordx4 %0, %1, off sc1" : "=v"(av[i]) : "v"(aT + (size_t)(n0 + drow) * SEQ + m0 + (lane & 7) * 8) : "memory");
    }
    asm volatile("s_waitcnt vmcnt(0)" : "+v"(av[0]), "+v"(av[1])::"memory");
#pragma unroll
    for (int i = 0; i < 2; ++i) *(u32x4*)(smem + G2_A_OFF + (2 * wave + i) * 1024 + lane * 16) = av[i];
  }
  __syncthreads();
  {
    const int m = tid >> 2, nq = (tid & 3) * 16;
    const float g = gin[m0 + m];
    const float omg = rbf(1.0f - g);
    float b[16];
#pragma unroll
    for (int j = 0; j < 4; ++j) {
      f32x4 s = *(const f32x4*)(smem + m * G2_PITCH + (nq + 4 * j) * 4);
#pragma unroll
      for (int w = 1; w < 4; ++w) s += *(const f32x4*)(smem + w * (64 * G2_PITCH) + m * G2_PITCH + (nq + 4 * j) * 4);
#pragma unroll
      for (int e = 0; e < 4; ++e) b[4 * j + e] = s[e];
    }
    const u16* aimg = (const u16*)(smem + G2_A_OFF);
    unsigned o[8];
#pragma unroll
    for (int j = 0; j < 8; ++j) {
      const float a0 = bf2f(aimg[(nq + 2 * j) * 64 + m]);
      const float a1 = bf2f(aimg[(nq + 2 * j + 1) * 64 + m]);
      const float o0 = rbf(g * a0) + rbf(omg * rbf(b[2 * j]));
      const float o1 = rbf(g * a1) + rbf(omg * rbf(b[2 * j + 1]));
      o[j] = pk_bf16(o0, o1);
    }
    u16* op = out + (size_t)(m0 + m) * DM + n0 + nq;
    *(u32x4*)op = u32x4{o[0], o[1], o[2], o[3]};
    *(u32x4*)(op + 8) = u32x4{o[4], o[5], o[6], o[7]};
  }
}

__global__ __launch_bounds__(256, 2) void k2_kernel(const u16* __restrict__ uT, const u16* __restrict__ krg,
                                                   const u16* __restrict__ Dskip, const u16* __restrict__ hf,
                                                   const u16* __restrict__ wd, u16* aT, const float* __restrict__ gin,
                                                   unsigned* flags, u16* __restrict__ out DBG_ARG
                                                   ) {
  __shared__ __attribute__((aligned(4096))) char smem[K2_LDS > SSM_LDS ? K2_LDS : SSM_LDS];
  const int bid = blockIdx.x;
  if (bid < NSSM) conv_unit(bid, uT, krg, Dskip, aT, flags, smem DBG_PASS);
  else gemm2_tile(bid - NSSM, hf, wd, aT, gin, flags, out, smem);
}

extern "C" void kernel_launch(void* const* d_in, const int* in_sizes, int n_in, void* d_out, int out_size,
                              void* d_ws, size_t ws_size, hipStream_t stream) {
  const u16* u = (const u16*)d_in[0];
  const u16* logAr = (const u16*)d_in[1];
  const u16* Aim = (const u16*)d_in[2];
  const u16* Cre = (const u16*)d_in[3];
  const u16* Cim = (const u16*)d_in[4];
  const u16* logdt = (const u16*)d_in[5];
  const u16* Dskip = (const u16*)d_in[6];
  const u16* wg = (const u16*)d_in[8];
  const u16* wu = (const u16*)d_in[9];
  const u16* wd = (const u16*)d_in[10];
  const u16* gw = (const u16*)d_in[11];
  char* ws = (char*)d_ws;
  u16* hbuf = (u16*)ws;
  u16* aT = (u16*)(ws + (size_t)SEQ * FFP * 2);
  float* gbuf = (float*)(ws + (size_t)SEQ * FFP * 2 + (size_t)DM * SEQ * 2);
  unsigned* flags = (unsigned*)(ws + (size_t)SEQ * FFP * 2 + (size_t)DM * SEQ * 2 + SEQ * 4);
  u16* uT = (u16*)(ws + (16u << 20));
  u16* krg = (u16*)(ws + (20u << 20));
  k1_kernel<<<512, 256, 0, stream>>>(u, logAr, Aim, Cre, Cim, logdt, wg, wu, gw, wd, hbuf, krg, uT, gbuf, flags
      );
  k2_kernel<<<512, 256, 0, stream>>>(uT, krg, Dskip, hbuf, wd, aT, gbuf, flags, (u16*)d_out
      );
}
```

```cpp
#include <hip/hip_runtime.h>
#include <hip/hip_bf16.h>
#include <stdint.h>
#include <type_traits>

typedef unsigned short u16;
typedef __attribute__((ext_vector_type(8))) short bf16x8;
typedef __attribute__((ext_vector_type(4))) short s16x4;
typedef __attribute__((ext_vector_type(4))) float f32x4;
typedef __attribute__((ext_vector_type(16))) float f32x16;
typedef __attribute__((ext_vector_type(4))) unsigned u32x4;
typedef __attribute__((ext_vector_type(2))) unsigned u32x2;
typedef __attribute__((ext_vector_type(2))) __bf16 bf16x2_t;
typedef __attribute__((ext_vector_type(2))) float f32x2;

constexpr int SEQ = 2048, DM = 512, NST = 128, FF = 1376;
constexpr int NSSM = 256;
constexpr int NKS = 88;

__device__ __forceinline__ unsigned pk_bf16(float lo, float hi) {
  f32x2 v = {lo, hi};
  bf16x2_t b = __builtin_convertvector(v, bf16x2_t);
  return __builtin_bit_cast(unsigned, b);
}
__device__ __forceinline__ float bf_lo(unsigned w) { return __uint_as_float(w << 16); }
__device__ __forceinline__ float bf_hi(unsigned w) { return __uint_as_float(w & 0xffff0000u); }
__device__ __forceinline__ float rbf(float x) { return bf_lo(pk_bf16(x, 0.f)); }
__device__ __forceinline__ float bf2f(u16 v) { return __uint_as_float(((unsigned)v) << 16); }
__device__ __forceinline__ bf16x8 mk8(unsigned a, unsigned b, unsigned c, unsigned d) {
  u32x4 v = {a, b, c, d};
  return __builtin_bit_cast(bf16x8, v);
}
__device__ __forceinline__ bf16x8 cat4(s16x4 a, s16x4 b) { return __builtin_shufflevector(a, b, 0, 1, 2, 3, 4, 5, 6, 7); }
__device__ __forceinline__ unsigned lds_addr(const void* p) {
  return (unsigned)(size_t)(const __attribute__((address_space(3))) char*)p;
}
__device__ __forceinline__ void split_pair(f32x2 v, unsigned& hi, unsigned& lo) {
  hi = pk_bf16(v[0], v[1]);
  const f32x2 r = v - f32x2{bf_lo(hi), bf_hi(hi)};
  lo = pk_bf16(r[0], r[1]);
}

#define DS_TR(dst, addr, off) asm volatile("ds_read_b64_tr_b16 %0, %1 offset:%2" : "=v"(dst) : "v"(addr), "i"(off) : "memory")
#define DS_B128(dst, addr, off) asm volatile("ds_read_b128 %0, %1 offset:%2" : "=v"(dst) : "v"(addr), "i"(off) : "memory")
#define DS_B64(dst, addr, off) asm volatile("ds_read_b64 %0, %1 offset:%2" : "=v"(dst) : "v"(addr), "i"(off) : "memory")
#define DS_W128(addr, val, off) asm volatile("ds_write_b128 %0, %1 offset:%2" ::"v"(addr), "v"(val), "i"(off) : "memory")
#define GLOAD128S(dst, voff, sbase) asm volatile("global_load_dwordx4 %0, %1, %2" : "=v"(dst) : "v"(voff), "s"(sbase) : "memory")
#define MFMA16(acc, a, b) asm volatile("v_mfma_f32_16x16x32_bf16 %0, %1, %2, %0" : "+v"(acc) : "v"(a), "v"(b))
#define MFMA32(acc, a, b) asm volatile("v_mfma_f32_32x32x16_bf16 %0, %1, %2, %0" : "+v"(acc) : "v"(a), "v"(b))
#define WAIT_LGKM1(N, a) asm volatile("s_waitcnt lgkmcnt(%1)" : "+v"(a) : "i"(N) : "memory")
#define WAIT_LGKM2(N, a, b) asm volatile("s_waitcnt lgkmcnt(%2)" : "+v"(a), "+v"(b) : "i"(N) : "memory")
#define WAIT_LGKM3(N, a, b, c) asm volatile("s_waitcnt lgkmcnt(%3)" : "+v"(a), "+v"(b), "+v"(c) : "i"(N) : "memory")

#define DBG_ARG
#define DBG_PASS
#define TS_DECL
#define TS(i)
#define TS_STORE()

constexpr int KQ_B = 8208;
constexpr int KX_OFF = 2 * KQ_B;
constexpr int KG_PRM_OFF = KX_OFF + 8 * 3072;
constexpr int KG_TR_OFF = KG_PRM_OFF + 4096;
constexpr int KG_LDS = KG_TR_OFF + 128 * 80;
constexpr int KR_CH_ELEMS = 2 * 2288;

__device__ __forceinline__ void kgen_unit(int w, const u16* __restrict__ u, const u16* __restrict__ logAr,
                                          const u16* __restrict__ Aim, const u16* __restrict__ Cre,
                                          const u16* __restrict__ Cim, const u16* __restrict__ logdt,
                                          u16* __restrict__ krg, u16* __restrict__ uT, char* smem DBG_ARG) {
  const int tid = threadIdx.x, lane = tid & 63;
  const int wave = __builtin_amdgcn_readfirstlane(tid >> 6);
  const int ch = wave >> 2, wq = wave & 3;
  const int d0 = 2 * w;
  const int d = d0 + ch;
  TS_DECL;
  TS(0);
  const int trow0 = 128 * (w >> 4), tcol0 = 32 * (w & 15);
  const u32x4 tv = *(const u32x4*)(u + (size_t)(trow0 + (tid >> 2)) * DM + tcol0 + (tid & 3) * 8);
  if (tid < 256) {
    const int pidx = d0 * NST + tid;
    const float dt = rbf(expf(bf2f(logdt[d0 + (tid >> 7)])));
    const float are = rbf(expf(bf2f(logAr[pidx])));
    f32x4 prm;
    prm[0] = (-are * dt) * 1.44269504088896340736f;
    prm[1] = (bf2f(Aim[pidx]) * dt) * 0.15915494309189533577f;
    prm[2] = 2.0f * (bf2f(Cre[pidx]) * dt);
    prm[3] = 2.0f * (bf2f(Cim[pidx]) * dt);
    *(f32x4*)(smem + KG_PRM_OFF + tid * 16) = prm;
  }
  *(u32x4*)(smem + KG_TR_OFF + (tid >> 2) * 80 + (tid & 3) * 16) = tv;
  __syncthreads();
  {
    const int g = lane >> 4, q = (lane & 15) >> 2, p = lane & 3;
    const unsigned ta = lds_addr(smem) + KG_TR_OFF + (16 * wave + 8 * (g >> 1) + q) * 80 + (g & 1) * 32 + p * 8;
    s16x4 t0, t1;
    DS_TR(t0, ta, 0); DS_TR(t1, ta, 4 * 80);
    WAIT_LGKM2(0, t0, t1);
    *(bf16x8*)(uT + (size_t)(tcol0 + 16 * (g & 1) + (lane & 15)) * SEQ + trow0 + 16 * wave + 8 * (g >> 1)) = cat4(t0, t1);
  }
  TS(1);

  const int r = lane & 31, h = lane >> 5;
  f32x16 dacc = {};
  {
    int p1i;
    if (r < 8) p1i = r;
    else if (r < 12) p1i = 256 + 2 * (r - 8);
    else if (r < 16) p1i = 512 + 256 * ((r - 12) >> 1) + 4 * ((r - 12) & 1);
    else if (r < 21) p1i = 1024 + 256 * (r - 16);
    else p1i = 0;
    const float p1 = (float)p1i, p2 = (float)(8 * r);
    const f32x2 P1 = {p1, p1}, P2 = {p2, p2};
    const char* prm_base = smem + KG_PRM_OFF + (ch * 128 + 32 * wq + 4 * h) * 16;
#pragma unroll 2
    for (int ks = 0; ks < 4; ++ks) {
      f32x4 prm[4];
#pragma unroll
      for (int q = 0; q < 4; ++q) prm[q] = *(const f32x4*)(prm_base + (8 * ks + q) * 16);
      f32x2 za[4], zb[4];
#pragma unroll
      for (int q = 0; q < 4; ++q) { za[q] = f32x2{prm[q][0], prm[q][1]} * P1; zb[q] = f32x2{prm[q][0], prm[q][1]} * P2; }
      float ea[4], eb[4], fa[4], fb[4];
#pragma unroll
      for (int q = 0; q < 4; ++q) {
        ea[q] = __builtin_amdgcn_exp2f(za[q][0]); eb[q] = __builtin_amdgcn_exp2f(zb[q][0]);
        fa[q] = __builtin_amdgcn_fractf(za[q][1]); fb[q] = __builtin_amdgcn_fractf(zb[q][1]);
      }
      f32x2 ca[4], cb[4];
#pragma unroll
      for (int q = 0; q < 4; ++q) {
        ca[q] = f32x2{__builtin_amdgcn_cosf(fa[q]), __builtin_amdgcn_sinf(fa[q])};
        cb[q] = f32x2{__builtin_amdgcn_cosf(fb[q]), __builtin_amdgcn_sinf(fb[q])};
      }
      unsigned ah[4], al[4], bh[4], bl[4];
#pragma unroll
      for (int q = 0; q < 4; ++q) {
        const f32x2 ce = f32x2{prm[q][2], prm[q][3]} * f32x2{ea[q], ea[q]};
        f32x2 g = f32x2{ce[0], -ce[0]} * ca[q];
        g = __builtin_elementwise_fma(f32x2{-ce[1], -ce[1]}, f32x2{ca[q][1], ca[q][0]}, g);
        split_pair(g, ah[q], al[q]);
        split_pair(cb[q] * f32x2{eb[q], eb[q]}, bh[q], bl[q]);
      }
      const bf16x8 Ah = mk8(ah[0], ah[1], ah[2], ah[3]), Al = mk8(al[0], al[1], al[2], al[3]);
      const bf16x8 Bh = mk8(bh[0], bh[1], bh[2], bh[3]), Bl = mk8(bl[0], bl[1], bl[2], bl[3]);
      dacc = __builtin_amdgcn_mfma_f32_32x32x16_bf16(Al, Bh, dacc, 0, 0, 0);
      dacc = __builtin_amdgcn_mfma_f32_32x32x16_bf16(Ah, Bl, dacc, 0, 0, 0);
      dacc = __builtin_amdgcn_mfma_f32_32x32x16_bf16(Ah, Bh, dacc, 0, 0, 0);
    }
  }
  TS(2);
  {
    float* xo = (float*)(smem + KX_OFF + wave * 3072) + lane;
#pragma unroll
    for (int e = 0; e < 12; ++e) xo[e * 64] = dacc[e];
    __syncthreads();
    const float* xi = (const float*)(smem + KX_OFF + (ch * 4) * 3072) + (3 * wq) * 64 + lane;
    unsigned* Kq = (unsigned*)(smem + ch * KQ_B);
#pragma unroll
    for (int e = 0; e < 3; ++e) {
      const float v = (xi[e * 64] + xi[768 + e * 64]) + (xi[1536 + e * 64] + xi[2304 + e * 64]);
      const unsigned hi = pk_bf16(v, 0.f);
      const unsigned pq = pk_bf16(0.f, v - bf_lo(hi)) | hi;
      const int reg = 3 * wq + e;
      const int row = (reg & 3) + 8 * (reg >> 2) + 4 * h;
      int p1i;
      if (row < 8) p1i = row;
      else if (row < 12) p1i = 256 + 2 * (row - 8);
      else if (row < 16) p1i = 512 + 256 * ((row - 12) >> 1) + 4 * ((row - 12) & 1);
      else p1i = 1024 + 256 * (row - 16);
      if (row < 20 || (row == 20 && r == 0)) Kq[p1i + 8 * r] = pq;
    }
  }
  __syncthreads();
  {
    const int t = tid & 255;
    const unsigned* Kq = (const unsigned*)(smem + ch * KQ_B);
    unsigned* krh = (unsigned*)(krg + (size_t)d * KR_CH_ELEMS);
    unsigned* krl = krh + 1144;
    unsigned q0[4], q1[4];
#pragma unroll
    for (int m = 0; m < 4; ++m) {
      const int l0 = 2047 - 2 * (t + 256 * m), l1 = l0 - 1;
      int pa, pb;
      if (m < 3) {
        const int sh = m < 2 ? 3 : 2;
        pa = (l0 + (1 << (sh - 1)) - 1 + ((l0 >> sh) & 1)) & ~((1 << sh) - 1);
        pb = (l1 + (1 << (sh - 1)) - 1 + ((l1 >> sh) & 1)) & ~((1 << sh) - 1);
      } else {
        pb = l1;
        pa = l0 < 256 ? l0 : ((l0 + ((l0 >> 1) & 1)) & ~1);
      }
      q0[m] = Kq[pa];
      q1[m] = Kq[pb];
    }
#pragma unroll
    for (int m = 0; m < 4; ++m) {
      krh[t + 256 * m] = __builtin_amdgcn_perm(q1[m], q0[m], 0x05040100u);
      krl[t + 256 * m] = __builtin_amdgcn_perm(q1[m], q0[m], 0x07060302u);
    }
    if (t < 120) { krh[1024 + t] = 0; krl[1024 + t] = 0; }
  }
  TS(3);
  TS_STORE();
}

constexpr int G1_WSLOT_B = 24576;
constexpr int G1_X_OFF = 2 * G1_WSLOT_B;
constexpr int G1_XSLOT_B = 16384;
constexpr int G1_LDS = G1_X_OFF + 2 * G1_XSLOT_B;

struct G1Ctx {
  const char* wbase;
  const char* xbase;
  unsigned wo[3], xo;
  unsigned woff, woff_o, ww[3], xw, xrd;
};
struct G1Regs { u32x4 w[3], x[2]; };

template <int S>
__device__ __forceinline__ void g1_issue(G1Regs& g, const G1Ctx& c) {
#pragma unroll
  for (int i = 0; i < 3; ++i) GLOAD128S(g.w[i], c.wo[i], c.wbase + (size_t)S * 64 * FF * 2);
#pragma unroll
  for (int i = 0; i < 2; ++i) GLOAD128S(g.x[i], c.xo, c.xbase + (size_t)(8 * i) * DM * 2 + S * 128);
}
template <int SLOT, int NW>
__device__ __forceinline__ void g1_store(G1Regs& g, const G1Ctx& c) {
  asm volatile("s_waitcnt vmcnt(%5)" : "+v"(g.w[0]), "+v"(g.w[1]), "+v"(g.w[2]), "+v"(g.x[0]), "+v"(g.x[1]) : "i"(NW) : "memory");
#pragma unroll
  for (int i = 0; i < 3; ++i) DS_W128(c.ww[i], g.w[i], SLOT * G1_WSLOT_B);
#pragma unroll
  for (int i = 0; i < 2; ++i) DS_W128(c.xw, g.x[i], SLOT * G1_XSLOT_B + i * 1024);
}
#define G1_RDW(t, OFF)                                                                                   \
  do {                                                                                                   \
    if ((t) & 1) { DS_TR(w[t][0], c.woff_o, (OFF) + (t) * 64); DS_TR(w[t][1], c.woff_o, (OFF) + (t) * 64 + 4 * 384); } \
    else { DS_TR(w[t][0], c.woff, (OFF) + (t) * 64); DS_TR(w[t][1], c.woff, (OFF) + (t) * 64 + 4 * 384); }            \
  } while (0)

template <int S>
struct G1Stage {
  static __device__ __forceinline__ void run(f32x16 (&acc)[3], G1Regs (&gr)[2], const G1Ctx& c) {
    constexpr int WB = (S & 1) * G1_WSLOT_B, XB = (S & 1) * G1_XSLOT_B;
    s16x4 w[3][2];
    bf16x8 xa, xb;
    const unsigned x0 = c.xrd, x1 = c.xrd ^ 32u, x2 = c.xrd ^ 64u, x3 = c.xrd ^ 96u;
    DS_B128(xa, x0, XB);
    G1_RDW(0, WB); G1_RDW(1, WB); G1_RDW(2, WB);
#define G1_STEP(J, XC, XN, XADDR)                                                                        \
    {                                                                                                    \
      constexpr bool NX = (J) < 3;                                                                       \
      constexpr int OFF = WB + ((J) + 1) * 16 * 384;                                                     \
      WAIT_LGKM3(4, XC, w[0][0], w[0][1]);                                                               \
      { const bf16x8 b = cat4(w[0][0], w[0][1]); MFMA32(acc[0], b, XC); }                                \
      if constexpr (NX) { DS_B128(XN, XADDR, XB); G1_RDW(0, OFF); }                                      \
      WAIT_LGKM2(NX ? 5 : 2, w[1][0], w[1][1]);                                                          \
      { const bf16x8 b = cat4(w[1][0], w[1][1]); MFMA32(acc[1], b, XC); }                                \
      if constexpr (NX) { G1_RDW(1, OFF); }                                                              \
      WAIT_LGKM2(NX ? 5 : 0, w[2][0], w[2][1]);                                                          \
      { const bf16x8 b = cat4(w[2][0], w[2][1]); MFMA32(acc[2], b, XC); }                                \
      if constexpr (NX) { G1_RDW(2, OFF); }                                                              \
    }
    G1_STEP(0, xa, xb, x1)
    G1_STEP(1, xb, xa, x2)
    G1_STEP(2, xa, xb, x3)
    G1_STEP(3, xb, xa, x0)
#undef G1_STEP
    if constexpr (S + 1 < 8) {
      g1_store<(S + 1) & 1, (S + 2 < 8) ? 5 : 0>(gr[(S + 1) & 1], c);
      if constexpr (S + 3 < 8) g1_issue<S + 3>(gr[(S + 1) & 1], c);
      asm volatile("s_waitcnt lgkmcnt(0)\n\ts_barrier" ::: "memory");
      G1Stage<S + 1>::run(acc, gr, c);
    }
  }
};

__device__ __forceinline__ void gemm1_tile(int t, const u16* __restrict__ x, const u16* __restrict__ wg,
                                           const u16* __restrict__ wu, u16* __restrict__ hf, char* smem DBG_ARG) {
  const int tid = threadIdx.x, lane = tid & 63;
  const int wave = __builtin_amdgcn_readfirstlane(tid >> 6);
  const int fb = t >> 4, mb = t & 15;
  const int f0 = 96 * fb;
  const int wq = wave & 3, isup = wave >> 2;
  TS_DECL;
  TS(0);
  G1Ctx c;
  const unsigned sbase = lds_addr(smem);
  c.wbase = (const char*)(isup ? wu : wg);
#pragma unroll
  for (int i = 0; i < 3; ++i) {
    const int id = 192 * wq + 64 * i + lane;
    const int row = id / 12, cc = id % 12;
    int col = f0 + 8 * cc;
    if (col > FF - 8) col = FF - 8;
    c.wo[i] = (unsigned)(row * FF + col) * 2u;
    c.ww[i] = sbase + row * 384 + ((cc + 12 * isup) ^ (((row >> 1) & 1) << 2)) * 16;
  }
  c.xbase = (const char*)(x + (size_t)(128 * mb + 16 * wave) * DM);
  c.xo = (unsigned)((lane >> 3) * DM + (lane & 7) * 8) * 2u;
  G1Regs gr[2];
  g1_issue<0>(gr[0], c);
  g1_issue<1>(gr[1], c);
  const unsigned xl = sbase + G1_X_OFF;
  c.xw = xl + (16 * wave + (lane >> 3)) * 128 + (((lane & 7) ^ (lane >> 3)) * 16);
  const int r = lane & 31, h = lane >> 5;
  c.xrd = xl + (32 * wq + r) * 128 + ((h ^ (r & 7)) * 16);
  const int blk = (lane >> 4) & 1, q = (lane & 15) >> 2, p = lane & 3;
  const int sw = (q >> 1) & 1;
  {
    const unsigned e = sbase + (8 * h + q) * 384 + (2 * blk + (p >> 1) + 4 * sw) * 16 + 8 * (p & 1);
    const unsigned o = sbase + (8 * h + q) * 384 + (2 * blk + (p >> 1) - 4 * sw) * 16 + 8 * (p & 1);
    c.woff = isup ? o + 192 : e;
    c.woff_o = isup ? e + 192 : o;
  }
  f32x16 acc[3];
#pragma unroll
  for (int i = 0; i < 3; ++i) acc[i] = f32x16{};
  asm volatile("s_nop 4" : "+v"(acc[0]), "+v"(acc[1]), "+v"(acc[2]));
  g1_store<0, 5>(gr[0], c);
  g1_issue<2>(gr[0], c);
  asm volatile("s_waitcnt lgkmcnt(0)\n\ts_barrier" ::: "memory");
  TS(1);
  G1Stage<0>::run(acc, gr, c);
  asm volatile("s_nop 15\n\ts_nop 15" : "+v"(acc[0]), "+v"(acc[1]), "+v"(acc[2]));
  TS(2);

  auto finish = [&](auto ISUP) {
    constexpr int K = decltype(ISUP)::value ? 1 : 0;
    unsigned keep[3][4];
    __syncthreads();
    {
      char* xo = smem + wave * 3072 + lane * 16;
#pragma unroll
      for (int tt = 0; tt < 3; ++tt) {
        unsigned give[4];
#pragma unroll
        for (int e = 0; e < 4; ++e) {
          give[e] = pk_bf16(acc[tt][8 * (1 - K) + 2 * e], acc[tt][8 * (1 - K) + 2 * e + 1]);
          keep[tt][e] = pk_bf16(acc[tt][8 * K + 2 * e], acc[tt][8 * K + 2 * e + 1]);
        }
        *(u32x4*)(xo + tt * 1024) = u32x4{give[0], give[1], give[2], give[3]};
      }
    }
    __syncthreads();
    const char* xi = smem + (wave ^ 4) * 3072 + lane * 16;
    const int mt = 4 * mb + wq;
    u16* hp = hf + ((size_t)(mt * NKS + 6 * fb + K) * 64 + lane) * 8;
    unsigned gp[12], up[12];
#pragma unroll
    for (int tt = 0; tt < 3; ++tt) {
      const u32x4 ot = *(const u32x4*)(xi + tt * 1024);
#pragma unroll
      for (int e = 0; e < 4; ++e) {
        gp[4 * tt + e] = K ? ot[e] : keep[tt][e];
        up[4 * tt + e] = K ? keep[tt][e] : ot[e];
      }
    }
    f32x2 xg[12], ar[12];
#pragma unroll
    for (int e = 0; e < 12; ++e) xg[e] = f32x2{bf_lo(gp[e]), bf_hi(gp[e])};
#pragma unroll
    for (int e = 0; e < 12; ++e) ar[e] = xg[e] * xg[e];
#pragma unroll
    for (int e = 0; e < 12; ++e) ar[e] = __builtin_elementwise_fma(ar[e], f32x2{-0.10294323958f, -0.10294323958f}, f32x2{-2.30220819814f, -2.30220819814f});
#pragma unroll
    for (int e = 0; e < 12; ++e) ar[e] = ar[e] * xg[e];
#pragma unroll
    for (int e = 0; e < 12; ++e) ar[e] = f32x2{__builtin_amdgcn_exp2f(ar[e][0]), __builtin_amdgcn_exp2f(ar[e][1])};
#pragma unroll
    for (int e = 0; e < 12; ++e) ar[e] = ar[e] + f32x2{1.0f, 1.0f};
#pragma unroll
    for (int e = 0; e < 12; ++e) ar[e] = f32x2{__builtin_amdgcn_rcpf(ar[e][0]), __builtin_amdgcn_rcpf(ar[e][1])};
#pragma unroll
    for (int e = 0; e < 12; ++e) ar[e] = ar[e] * xg[e];
    unsigned o[12];
#pragma unroll
    for (int e = 0; e < 12; ++e) o[e] = pk_bf16(ar[e][0], ar[e][1]);
#pragma unroll
    for (int e = 0; e < 12; ++e) ar[e] = f32x2{bf_lo(o[e]), bf_hi(o[e])} * f32x2{bf_lo(up[e]), bf_hi(up[e])};
#pragma unroll
    for (int e = 0; e < 12; ++e) o[e] = pk_bf16(ar[e][0], ar[e][1]);
#pragma unroll
    for (int tt = 0; tt < 3; ++tt) {
      const int ft = 3 * fb + tt;
      if (ft < FF / 32) *(u32x4*)(hp + (2 * tt) * 512) = u32x4{o[4 * tt], o[4 * tt + 1], o[4 * tt + 2], o[4 * tt + 3]};
      else if (ft < NKS / 2) *(u32x4*)(hp + (2 * tt) * 512) = u32x4{0, 0, 0, 0};
    }
  };
  if (isup) finish(std::integral_constant<bool, true>{});
  else finish(std::integral_constant<bool, false>{});
  TS(3);
  TS_STORE();
}

__device__ __forceinline__ void gate_unit(int b, const u16* __restrict__ x, const u16* __restrict__ gw,
                                          float* __restrict__ gout) {
  const int tid = threadIdx.x, lane = tid & 63, wave = tid >> 6;
  const int j = lane & 7, rr = lane >> 3;
  const int row0 = b * 128 + wave * 16 + rr;
#pragma unroll 1
  for (int ps = 0; ps < 2; ++ps) {
    u32x4 gv[8], xv[8];
#pragma unroll
    for (int i = 0; i < 8; ++i) xv[i] = *(const u32x4*)(x + (size_t)(row0 + 8 * ps) * DM + (j + 8 * i) * 8);
#pragma unroll
    for (int i = 0; i < 8; ++i) gv[i] = *(const u32x4*)(gw + (j + 8 * i) * 8);
    float s0 = 0.f, s1 = 0.f;
#pragma unroll
    for (int i = 0; i < 8; ++i)
#pragma unroll
      for (int e = 0; e < 4; ++e) {
        const unsigned pr = pk_bf16(bf_lo(xv[i][e]) * bf_lo(gv[i][e]), bf_hi(xv[i][e]) * bf_hi(gv[i][e]));
        s0 += bf_lo(pr);
        s1 += bf_hi(pr);
      }
    float s = s0 + s1;
    s += __shfl_xor(s, 1);
    s += __shfl_xor(s, 2);
    s += __shfl_xor(s, 4);
    if (j == 0) {
      const float sb = rbf(s);
      gout[row0 + 8 * ps] = rbf(1.0f / (1.0f + expf(-sb)));
    }
  }
}

__global__ __launch_bounds__(512, 4) void k1_kernel(const u16* __restrict__ u, const u16* __restrict__ logAr,
                                                   const u16* __restrict__ Aim, const u16* __restrict__ Cre,
                                                   const u16* __restrict__ Cim, const u16* __restrict__ logdt,
                                                   const u16* __restrict__ wg, const u16* __restrict__ wu,
                                                   const u16* __restrict__ gw, const u16* __restrict__ wd,
                                                   u16* __restrict__ hbuf, u16* __restrict__ krg, u16* __restrict__ uT,
                                                   float* __restrict__ gout, unsigned* __restrict__ flags DBG_ARG
                                                   ) {
  __shared__ __attribute__((aligned(4096))) char smem[G1_LDS > KG_LDS ? G1_LDS : KG_LDS];
  const int bid = blockIdx.x;
  if (bid < NSSM) {
    kgen_unit(bid, u, logAr, Aim, Cre, Cim, logdt, krg, uT, smem DBG_PASS);
    if (threadIdx.x == 0) flags[bid] = 0u;
    const int li = (bid >> 3) * 512 + threadIdx.x;
    unsigned a0 = 0;
    if (li < 11008) a0 = ((const unsigned*)wd)[li * 32];
    asm volatile("" ::"v"(a0));
  } else {
    const int xcd = bid & 7, idx = (bid - NSSM) >> 3;
    if (idx < 30) gemm1_tile(30 * xcd + idx, u, wg, wu, hbuf, smem DBG_PASS);
    else gate_unit(2 * xcd + idx - 30, u, gw, gout);
  }
}

constexpr int U_COPY_B = 4160;
constexpr int U_CH_B = 4 * U_COPY_B;
constexpr int KR_PART_B = 4576;
constexpr int KR_OFF = 2 * U_CH_B;
constexpr int CV_X_OFF = KR_OFF + 4 * KR_PART_B;
constexpr int CV_LDS = CV_X_OFF + 24576;

constexpr int conv_tau(int P) { int t = 0; while (P >= 8 - t) { P -= 8 - t; ++t; } return t; }
constexpr int conv_sig(int P) { int t = 0; while (P >= 8 - t) { P -= 8 - t; ++t; } return P; }

struct ConvB { bf16x8 h, l; };
template <int RI, int T>
struct ConvTau {
  static __device__ __forceinline__ void run(f32x4 (&acc)[8], const bf16x8 (&A)[8], ConvB (&B)[3], unsigned kb) {
    ConvB& cur = B[T % 3];
    WAIT_LGKM2((T + 1 < 8) ? 2 : 0, cur.h, cur.l);
    MFMA16(acc[T], A[0], cur.l);
    if constexpr (T + 2 < 8) DS_B128(B[(T + 2) % 3].h, kb, 64 * (56 - 8 * (T + 2) + RI));
    MFMA16(acc[T], A[0], cur.h);
    if constexpr (T + 2 < 8) DS_B128(B[(T + 2) % 3].l, kb, 64 * (56 - 8 * (T + 2) + RI) + KR_PART_B);
#pragma unroll
    for (int sg = 1; sg + T < 8; ++sg) {
      MFMA16(acc[T + sg], A[sg], cur.l);
      MFMA16(acc[T + sg], A[sg], cur.h);
    }
    if constexpr (T + 1 < 8) ConvTau<RI, T + 1>::run(acc, A, B, kb);
  }
};
template <int RI>
__device__ __forceinline__ void conv_rho(f32x4 (&acc)[8], unsigned ua, unsigned kb) {
  s16x4 a[8][2];
  ConvB B[3];
#pragma unroll
  for (int sg = 0; sg < 8; ++sg) {
    DS_B64(a[sg][0], ua, 64 * (8 * sg + RI));
    DS_B64(a[sg][1], ua, 64 * (8 * sg + RI) + 8);
  }
  asm volatile("s_waitcnt lgkmcnt(0)"
               : "+v"(a[0][0]), "+v"(a[0][1]), "+v"(a[1][0]), "+v"(a[1][1]), "+v"(a[2][0]), "+v"(a[2][1]), "+v"(a[3][0]),
                 "+v"(a[3][1]), "+v"(a[4][0]), "+v"(a[4][1]), "+v"(a[5][0]), "+v"(a[5][1]), "+v"(a[6][0]), "+v"(a[6][1]),
                 "+v"(a[7][0]), "+v"(a[7][1])::"memory");
  DS_B128(B[0].h, kb, 64 * (56 + RI)); DS_B128(B[0].l, kb, 64 * (56 + RI) + KR_PART_B);
  DS_B128(B[1].h, kb, 64 * (48 + RI)); DS_B128(B[1].l, kb, 64 * (48 + RI) + KR_PART_B);
  bf16x8 A[8];
#pragma unroll
  for (int i = 0; i < 8; ++i) A[i] = cat4(a[i][0], a[i][1]);
  ConvTau<RI, 0>::run(acc, A, B, kb);
}

__device__ __forceinline__ void conv_unit(int w, const u16* __restrict__ uT, const u16* __restrict__ krg,
                                          const u16* __restrict__ Dskip, u16* aT, unsigned* flags, char* smem DBG_ARG) {
  const int tid = threadIdx.x, lane = tid & 63;
  const int wave = __builtin_amdgcn_readfirstlane(tid >> 6);
  const int ch = wave >> 2, wq = wave & 3;
  const int d0 = 2 * w;
  const int d = d0 + ch;
  TS_DECL;
  TS(0);
  const u16 r_dsk = Dskip[d];
  const int t = tid & 255;
  const u32x4 uv = *(const u32x4*)(uT + (size_t)d * SEQ + 8 * t);
  u32x2 pv = u32x2{0u, 0u};
  if (t > 0) pv = *(const u32x2*)(uT + (size_t)d * SEQ + 8 * t - 4);
  u32x4 kv[3];
  const u32x4* ksrc = (const u32x4*)(krg + (size_t)d0 * KR_CH_ELEMS);
  kv[0] = ksrc[tid];
  kv[1] = ksrc[tid + 512];
  kv[2] = u32x4{0, 0, 0, 0};
  if (tid < 120) kv[2] = ksrc[1024 + tid];
  {
    const unsigned m2 = pv[0], m1 = pv[1], w0 = uv[0], w1 = uv[1], w2 = uv[2], w3 = uv[3];
    const unsigned s0 = __builtin_amdgcn_alignbit(m1, m2, 16), s1 = __builtin_amdgcn_alignbit(w0, m1, 16),
                   s2 = __builtin_amdgcn_alignbit(w1, w0, 16), s3 = __builtin_amdgcn_alignbit(w2, w1, 16),
                   s4 = __builtin_amdgcn_alignbit(w3, w2, 16);
    char* ub = smem + ch * U_CH_B + (t + 2) * 16;
    *(u32x4*)(ub + 0 * U_COPY_B) = u32x4{s0, s1, s2, s3};
    *(u32x4*)(ub + 1 * U_COPY_B) = u32x4{m1, w0, w1, w2};
    *(u32x4*)(ub + 2 * U_COPY_B) = u32x4{s1, s2, s3, s4};
    *(u32x4*)(ub + 3 * U_COPY_B) = uv;
  }
  if (tid < 16) *(u32x4*)(smem + (tid >> 1) * U_COPY_B + (tid & 1) * 16) = u32x4{0, 0, 0, 0};
  *(u32x4*)(smem + KR_OFF + tid * 16) = kv[0];
  *(u32x4*)(smem + KR_OFF + (tid + 512) * 16) = kv[1];
  if (tid < 120) *(u32x4*)(smem + KR_OFF + (1024 + tid) * 16) = kv[2];
  __syncthreads();
  TS(1);

  f32x4 acc[8];
#pragma unroll
  for (int i = 0; i < 8; ++i) acc[i] = f32x4{0.f, 0.f, 0.f, 0.f};
  asm volatile("s_nop 4" : "+v"(acc[0]), "+v"(acc[1]), "+v"(acc[2]), "+v"(acc[3]), "+v"(acc[4]), "+v"(acc[5]), "+v"(acc[6]), "+v"(acc[7]));
  {
    const int r16 = lane & 15, hq = lane >> 4;
    const unsigned sb = lds_addr(smem);
    const unsigned ua = sb + ch * U_CH_B + (r16 & 3) * U_COPY_B + 2 * (4 + (r16 & ~3) + 8 * hq) + 64 * 2 * wq;
    const unsigned kb = sb + KR_OFF + (ch * 2) * KR_PART_B + 2 * (2032 - 16 * r16 + 8 * hq) - 64 * 56 + 64 * 2 * wq;
    conv_rho<0>(acc, ua, kb);
    conv_rho<1>(acc, ua, kb);
  }
  asm volatile("s_nop 15" : "+v"(acc[0]), "+v"(acc[1]), "+v"(acc[2]), "+v"(acc[3]), "+v"(acc[4]), "+v"(acc[5]), "+v"(acc[6]), "+v"(acc[7]));
  TS(2);
  __syncthreads();
  {
    auto slot = [&](int wv, int tile) -> char* {
      const int sidx = wv * 6 + (tile < 2 * (wv & 3) ? tile : tile - 2);
      return sidx < 36 ? smem + KR_OFF + sidx * 1024 : smem + (sidx - 36) * 1024;
    };
    f32x4 y[2];
#pragma unroll
    for (int i = 0; i < 8; ++i) {
      if ((i >> 1) != wq) *(f32x4*)(slot(wave, i) + lane * 16) = acc[i];
    }
    if (wq == 0) { y[0] = acc[0]; y[1] = acc[1]; }
    else if (wq == 1) { y[0] = acc[2]; y[1] = acc[3]; }
    else if (wq == 2) { y[0] = acc[4]; y[1] = acc[5]; }
    else { y[0] = acc[6]; y[1] = acc[7]; }
    __syncthreads();
    const float Dk = bf2f(r_dsk);
#pragma unroll
    for (int i = 0; i < 2; ++i) {
      const int tile = 2 * wq + i;
#pragma unroll
      for (int o = 1; o < 4; ++o) y[i] += *(const f32x4*)(slot(ch * 4 + ((wq + o) & 3), tile) + lane * 16);
      const int l0 = 256 * tile + 16 * (lane & 15) + 4 * (lane >> 4);
      const u32x2 uu = *(const u32x2*)(smem + ch * U_CH_B + 3 * U_COPY_B + 2 * (16 + l0));
      const unsigned y01 = pk_bf16(y[i][0], y[i][1]), y23 = pk_bf16(y[i][2], y[i][3]);
      const unsigned s01 = pk_bf16(bf_lo(uu[0]) * Dk, bf_hi(uu[0]) * Dk), s23 = pk_bf16(bf_lo(uu[1]) * Dk, bf_hi(uu[1]) * Dk);
      const u32x2 av = u32x2{pk_bf16(bf_lo(y01) + bf_lo(s01), bf_hi(y01) + bf_hi(s01)),
                             pk_bf16(bf_lo(y23) + bf_lo(s23), bf_hi(y23) + bf_hi(s23))};
      asm volatile("global_store_dwordx2 %0, %1, off sc1\n\ts_nop 1" ::"v"(aT + (size_t)d * SEQ + l0), "v"(av) : "memory");
    }
    asm volatile("s_waitcnt vmcnt(0)" ::: "memory");
    __syncthreads();
    if (tid == 0) __hip_atomic_store(flags + w, 1u, __ATOMIC_RELAXED, __HIP_MEMORY_SCOPE_AGENT);
  }
  TS(3);
  TS_STORE();
}

constexpr int G2_PITCH = 272;
constexpr int G2_PART_B = 64 * G2_PITCH;
constexpr int G2_A_OFF = 4 * G2_PART_B;
constexpr int G2_LDS = G2_A_OFF + 8192;
#define G2D 2
constexpr int G2_DEPTH = G2D;

struct G2Ctx {
  const char* hbase;
  const char* wbase;
  unsigned ho, wo, wrd, ww;
  int klim;
};
struct G2Regs { u32x4 w[2]; bf16x8 h[2]; };

template <int J>
struct G2Step {
  static __device__ __forceinline__ void issue(G2Regs (&g)[G2_DEPTH], const G2Ctx& c) {
    G2Regs& r = g[J % G2_DEPTH];
    const int k0 = (16 * J < c.klim) ? 16 * J : 0;
#pragma unroll
    for (int i = 0; i < 2; ++i) GLOAD128S(r.w[i], c.wo, c.wbase + (size_t)(k0 + 8 * i) * DM * 2);
#pragma unroll
    for (int mi = 0; mi < 2; ++mi) GLOAD128S(r.h[mi], c.ho, c.hbase + (size_t)(mi * NKS + J) * 1024);
  }
  static __device__ __forceinline__ void run(f32x16 (&acc)[2][2], G2Regs (&g)[G2_DEPTH], const G2Ctx& c) {
    constexpr int LAST = (J + G2_DEPTH - 1 < 10) ? J + G2_DEPTH - 1 : 10;
    constexpr int NW = 4 * (LAST - J);
    G2Regs& r = g[J % G2_DEPTH];
    asm volatile("s_waitcnt vmcnt(%2)" : "+v"(r.w[0]), "+v"(r.w[1]) : "i"(NW + 2) : "memory");
    DS_W128(c.ww, r.w[0], (J & 1) * 2048);
    DS_W128(c.ww, r.w[1], (J & 1) * 2048 + 1024);
    asm volatile("s_waitcnt lgkmcnt(0)" ::: "memory");
    s16x4 w[2][2];
    const unsigned w1 = c.wrd ^ 64u;
    DS_TR(w[0][0], c.wrd, (J & 1) * 2048); DS_TR(w[0][1], c.wrd, (J & 1) * 2048 + 1024);
    DS_TR(w[1][0], w1, (J & 1) * 2048); DS_TR(w[1][1], w1, (J & 1) * 2048 + 1024);
    asm volatile("s_waitcnt vmcnt(%2)" : "+v"(r.h[0]), "+v"(r.h[1]) : "i"(NW) : "memory");
    WAIT_LGKM2(2, w[0][0], w[0][1]);
    { const bf16x8 b = cat4(w[0][0], w[0][1]); MFMA32(acc[0][0], b, r.h[0]); MFMA32(acc[0][1], b, r.h[1]); }
    WAIT_LGKM2(0, w[1][0], w[1][1]);
    { const bf16x8 b = cat4(w[1][0], w[1][1]); MFMA32(acc[1][0], b, r.h[0]); MFMA32(acc[1][1], b, r.h[1]); }
    if constexpr (J + G2_DEPTH <= 10) G2Step<J + G2_DEPTH>::issue(g, c);
    if constexpr (J + 1 <= 10) G2Step<J + 1>::run(acc, g, c);
  }
};

__device__ __forceinline__ void gemm2_tile(int bid, const u16* __restrict__ hf, const u16* __restrict__ wd,
                                           const u16* aT, const float* __restrict__ gin, unsigned* flags,
                                           u16* __restrict__ out, char* smem DBG_ARG) {
  const int tid = threadIdx.x, lane = tid & 63;
  const int wave = __builtin_amdgcn_readfirstlane(tid >> 6);
  const int xcd = bid & 7, idx = bid >> 3;
  const int m0 = 64 * (4 * xcd + (idx >> 3)), n0 = 64 * (idx & 7);
  TS_DECL;
  TS(0);
  G2Ctx c;
  c.hbase = (const char*)(hf + (size_t)((m0 >> 5) * NKS + 11 * wave) * 512);
  c.ho = (unsigned)lane * 16u;
  c.wbase = (const char*)(wd + (size_t)(176 * wave) * DM + n0);
  c.wo = (unsigned)((lane >> 3) * DM + (lane & 7) * 8) * 2u;
  c.klim = FF - 176 * wave;
  G2Regs g[G2_DEPTH];
  G2Step<0>::issue(g, c);
  G2Step<1>::issue(g, c);
  if constexpr (G2_DEPTH > 2) G2Step<2>::issue(g, c);
  const unsigned wb = lds_addr(smem) + wave * 4096;
  c.ww = wb + (lane >> 3) * 128 + (((lane & 7) ^ (((lane >> 4) & 1) << 2)) * 16);
  const int r = lane & 31, h = lane >> 5;
  const int blk = (lane >> 4) & 1, q = (lane & 15) >> 2, p = lane & 3;
  c.wrd = wb + (4 * h + q) * 128 + (((2 * blk + (p >> 1)) ^ (((q >> 1) & 1) << 2)) * 16) + 8 * (p & 1);
  f32x16 acc[2][2];
#pragma unroll
  for (int i = 0; i < 2; ++i)
#pragma unroll
    for (int j = 0; j < 2; ++j) acc[i][j] = f32x16{};
  asm volatile("s_nop 4" : "+v"(acc[0][0]), "+v"(acc[0][1]), "+v"(acc[1][0]), "+v"(acc[1][1]));
  G2Step<0>::run(acc, g, c);
  asm volatile("s_nop 15\n\ts_nop 15" : "+v"(acc[0][0]), "+v"(acc[0][1]), "+v"(acc[1][0]), "+v"(acc[1][1]));
  TS(1);
  char* pw = smem + (wave & 3) * G2_PART_B + r * G2_PITCH + 16 * h;
  auto put = [&]() {
#pragma unroll
    for (int t = 0; t < 2; ++t)
#pragma unroll
      for (int mi = 0; mi < 2; ++mi)
#pragma unroll
        for (int gq = 0; gq < 4; ++gq)
          *(f32x4*)(pw + (32 * mi) * G2_PITCH + (32 * t + 8 * gq) * 4) =
              f32x4{acc[t][mi][4 * gq], acc[t][mi][4 * gq + 1], acc[t][mi][4 * gq + 2], acc[t][mi][4 * gq + 3]};
  };
  __syncthreads();
  if (wave >= 4) put();
  __syncthreads();
  if (wave < 4) {
#pragma unroll
    for (int t = 0; t < 2; ++t)
#pragma unroll
      for (int mi = 0; mi < 2; ++mi)
#pragma unroll
        for (int gq = 0; gq < 4; ++gq) {
          const f32x4 v = *(const f32x4*)(pw + (32 * mi) * G2_PITCH + (32 * t + 8 * gq) * 4);
#pragma unroll
          for (int e = 0; e < 4; ++e) acc[t][mi][4 * gq + e] += v[e];
        }
    put();
  } else if (wave == 7) {
    unsigned* fp = flags + (n0 >> 1) + (lane & 31);
    for (int spin = 0; spin < (1 << 20); ++spin) {
      const unsigned v = __hip_atomic_load(fp, __ATOMIC_RELAXED, __HIP_MEMORY_SCOPE_AGENT);
      if (__all(v != 0u)) break;
      __builtin_amdgcn_s_sleep(4);
    }
  }
  __syncthreads();
  TS(2);
  {
    u32x4 av;
    asm volatile("global_load_dwordx4 %0, %1, off sc1" : "=v"(av) : "v"(aT + (size_t)(n0 + 8 * wave + (lane >> 3)) * SEQ + m0 + (lane & 7) * 8) : "memory");
    const int m = tid >> 3, nq = (tid & 7) * 8;
    const float g0 = gin[m0 + m];
    const float omg = rbf(1.0f - g0);
    float b[8];
#pragma unroll
    for (int j = 0; j < 2; ++j) {
      f32x4 s = *(const f32x4*)(smem + m * G2_PITCH + (nq + 4 * j) * 4);
#pragma unroll
      for (int w = 1; w < 4; ++w) s += *(const f32x4*)(smem + w * G2_PART_B + m * G2_PITCH + (nq + 4 * j) * 4);
#pragma unroll
      for (int e = 0; e < 4; ++e) b[4 * j + e] = s[e];
    }
    unsigned bt[4];
#pragma unroll
    for (int j = 0; j < 4; ++j) {
      const unsigned bp = pk_bf16(b[2 * j], b[2 * j + 1]);
      bt[j] = pk_bf16(omg * bf_lo(bp), omg * bf_hi(bp));
    }
    asm volatile("s_waitcnt vmcnt(0)" : "+v"(av)::"memory");
    *(u32x4*)(smem + G2_A_OFF + wave * 1024 + lane * 16) = av;
    __syncthreads();
    TS(3);
    const u16* aimg = (const u16*)(smem + G2_A_OFF);
    unsigned o[4];
#pragma unroll
    for (int j = 0; j < 4; ++j) {
      const float a0 = bf2f(aimg[(nq + 2 * j) * 64 + m]);
      const float a1 = bf2f(aimg[(nq + 2 * j + 1) * 64 + m]);
      const unsigned ga = pk_bf16(g0 * a0, g0 * a1);
      o[j] = pk_bf16(bf_lo(ga) + bf_lo(bt[j]), bf_hi(ga) + bf_hi(bt[j]));
    }
    *(u32x4*)(out + (size_t)(m0 + m) * DM + n0 + nq) = u32x4{o[0], o[1], o[2], o[3]};
  }
  TS(4);
  TS_STORE();
}

__global__ __launch_bounds__(512, 4) void k2_kernel(const u16* __restrict__ uT, const u16* __restrict__ krg,
                                                   const u16* __restrict__ Dskip, const u16* __restrict__ hf,
                                                   const u16* __restrict__ wd, u16* aT, const float* __restrict__ gin,
                                                   unsigned* flags, u16* __restrict__ out DBG_ARG
                                                   ) {
  __shared__ __attribute__((aligned(4096))) char smem[G2_LDS > CV_LDS ? G2_LDS : CV_LDS];
  const int bid = blockIdx.x;
  if (bid < NSSM) conv_unit(bid, uT, krg, Dskip, aT, flags, smem DBG_PASS);
  else gemm2_tile(bid - NSSM, hf, wd, aT, gin, flags, out, smem DBG_PASS);
}

extern "C" void kernel_launch(void* const* d_in, const int* in_sizes, int n_in, void* d_out, int out_size,
                              void* d_ws, size_t ws_size, hipStream_t stream) {
  const u16* u = (const u16*)d_in[0];
  const u16* logAr = (const u16*)d_in[1];
  const u16* Aim = (const u16*)d_in[2];
  const u16* Cre = (const u16*)d_in[3];
  const u16* Cim = (const u16*)d_in[4];
  const u16* logdt = (const u16*)d_in[5];
  const u16* Dskip = (const u16*)d_in[6];
  const u16* wg = (const u16*)d_in[8];
  const u16* wu = (const u16*)d_in[9];
  const u16* wd = (const u16*)d_in[10];
  const u16* gw = (const u16*)d_in[11];
  char* ws = (char*)d_ws;
  u16* hbuf = (u16*)ws;
  u16* aT = (u16*)(ws + (8u << 20));
  float* gbuf = (float*)(ws + (12u << 20));
  unsigned* flags = (unsigned*)(ws + (13u << 20));
  u16* uT = (u16*)(ws + (16u << 20));
  u16* krg = (u16*)(ws + (20u << 20));
  k1_kernel<<<512, 512, 0, stream>>>(u, logAr, Aim, Cre, Cim, logdt, wg, wu, gw, wd, hbuf, krg, uT, gbuf, flags
      );
  k2_kernel<<<512, 512, 0, stream>>>(uT, krg, Dskip, hbuf, wd, aT, gbuf, flags, (u16*)d_out
      );
}
```

```cpp
#include <hip/hip_runtime.h>
#include <hip/hip_bf16.h>
#include <stdint.h>
#include <type_traits>

typedef unsigned short u16;
typedef __attribute__((ext_vector_type(8))) short bf16x8;
typedef __attribute__((ext_vector_type(4))) short s16x4;
typedef __attribute__((ext_vector_type(4))) float f32x4;
typedef __attribute__((ext_vector_type(16))) float f32x16;
typedef __attribute__((ext_vector_type(4))) unsigned u32x4;
typedef __attribute__((ext_vector_type(2))) unsigned u32x2;
typedef __attribute__((ext_vector_type(2))) __bf16 bf16x2_t;
typedef __attribute__((ext_vector_type(2))) float f32x2;
typedef __attribute__((address_space(3))) s16x4* lds_s16x4_ptr;

constexpr int SEQ = 2048, DM = 512, NST = 128, FF = 1376, FFP = 1408;
constexpr int NSSM = 256;
constexpr int NKS = 88;

__device__ __forceinline__ unsigned pk_bf16(float lo, float hi) {
  f32x2 v = {lo, hi};
  bf16x2_t b = __builtin_convertvector(v, bf16x2_t);
  return __builtin_bit_cast(unsigned, b);
}
__device__ __forceinline__ float bf_lo(unsigned w) { return __uint_as_float(w << 16); }
__device__ __forceinline__ float bf_hi(unsigned w) { return __uint_as_float(w & 0xffff0000u); }
__device__ __forceinline__ float rbf(float x) { return bf_lo(pk_bf16(x, 0.f)); }
__device__ __forceinline__ float bf2f(u16 v) { return __uint_as_float(((unsigned)v) << 16); }

__device__ __forceinline__ float smul(float a, float b) { float r; asm("v_mul_f32 %0, %1, %2" : "=v"(r) : "v"(a), "v"(b)); return r; }
__device__ __forceinline__ float sadd(float a, float b) { float r; asm("v_add_f32 %0, %1, %2" : "=v"(r) : "v"(a), "v"(b)); return r; }
__device__ __forceinline__ float ssub(float a, float b) { float r; asm("v_sub_f32 %0, %1, %2" : "=v"(r) : "v"(a), "v"(b)); return r; }
__device__ __forceinline__ float sfma(float a, float b, float c) { float r; asm("v_fma_f32 %0, %1, %2, %3" : "=v"(r) : "v"(a), "v"(b), "v"(c)); return r; }
__device__ __forceinline__ float sfms(float a, float b, float c) { float r; asm("v_fma_f32 %0, -%1, %2, %3" : "=v"(r) : "v"(a), "v"(b), "v"(c)); return r; }
__device__ __forceinline__ float sfnn(float a, float b, float c) { float r; asm("v_fma_f32 %0, -%1, %2, -%3" : "=v"(r) : "v"(a), "v"(b), "v"(c)); return r; }
#define TRANS_FENCE8(a, o) asm volatile("s_nop 1" : "+v"(a[o]), "+v"(a[o + 1]), "+v"(a[o + 2]), "+v"(a[o + 3]), "+v"(a[o + 4]), "+v"(a[o + 5]), "+v"(a[o + 6]), "+v"(a[o + 7]))
__device__ __forceinline__ bf16x8 mk8(unsigned a, unsigned b, unsigned c, unsigned d) {
  u32x4 v = {a, b, c, d};
  return __builtin_bit_cast(bf16x8, v);
}
__device__ __forceinline__ bf16x8 cat4(s16x4 a, s16x4 b) {
  return __builtin_shufflevector(a, b, 0, 1, 2, 3, 4, 5, 6, 7);
}
__device__ __forceinline__ s16x4 tr_read(const char* smem_ptr) {
  return __builtin_amdgcn_ds_read_tr16_b64_v4i16((lds_s16x4_ptr)(smem_ptr));
}
__device__ __forceinline__ unsigned lds_addr(const void* p) {
  return (unsigned)(size_t)(const __attribute__((address_space(3))) char*)p;
}
__device__ __forceinline__ void glds16(const void* g, char* l) {
  __builtin_amdgcn_global_load_lds((const unsigned*)g, (unsigned*)l, 16, 0, 0);
}

#define STAMP2(i)
#define DBG2_ARG
#define DBG2_PASS
#define STAMP(i)
#define DBG_ARG
#define DBG_PASS
constexpr int U_COPY_B = 4160;
constexpr int U_CH_B = 4 * U_COPY_B;
constexpr int U_REGION_B = 2 * U_CH_B;
constexpr int KR_PART_B = 4576;
constexpr int KR_OFF = U_REGION_B;
constexpr int KQ_B = 8208;
constexpr int KX_OFF = 2 * KQ_B;
constexpr int SSM_LDS = KR_OFF + 4 * KR_PART_B;
static_assert(KX_OFF + 4 * 1536 <= U_REGION_B, "scratch must fit the U region");

#define DS_TR(dst, addr, off) asm volatile("ds_read_b64_tr_b16 %0, %1 offset:%2" : "=v"(dst) : "v"(addr), "i"(off) : "memory")
#define DS_B128(dst, addr, off) asm volatile("ds_read_b128 %0, %1 offset:%2" : "=v"(dst) : "v"(addr), "i"(off) : "memory")
#define DS_B64(dst, addr, off) asm volatile("ds_read_b64 %0, %1 offset:%2" : "=v"(dst) : "v"(addr), "i"(off) : "memory")
#define MFMA16(acc, a, b) asm volatile("v_mfma_f32_16x16x32_bf16 %0, %1, %2, %0" : "+v"(acc) : "v"(a), "v"(b))

struct ConvFrag {
  s16x4 a[8][2];
  bf16x8 bh[8], bl[8];
};
template <int RI, int K>
__device__ __forceinline__ void conv_read(ConvFrag& f, unsigned ua, unsigned kb) {
  if constexpr (K < 16) {
    DS_B64(f.a[K >> 1][K & 1], ua, 64 * (8 * (K >> 1) + RI) + 8 * (K & 1));
  } else if constexpr (K < 24) {
    DS_B128(f.bh[K - 16], kb, 64 * (56 - 8 * (K - 16) + RI));
  } else {
    DS_B128(f.bl[K - 24], kb, 64 * (56 - 8 * (K - 24) + RI) + KR_PART_B);
  }
}
template <int RI, int K>
__device__ __forceinline__ void conv_read_all(ConvFrag& f, unsigned ua, unsigned kb) {
  conv_read<RI, K>(f, ua, kb);
  if constexpr (K + 1 < 32) conv_read_all<RI, K + 1>(f, ua, kb);
}
#define CONV_WAIT(f)                                                                                                  \
  do {                                                                                                                \
    asm volatile("s_waitcnt lgkmcnt(0)"                                                                               \
                 : "+v"(f.a[0][0]), "+v"(f.a[0][1]), "+v"(f.a[1][0]), "+v"(f.a[1][1]), "+v"(f.a[2][0]), "+v"(f.a[2][1]), \
                   "+v"(f.a[3][0]), "+v"(f.a[3][1]), "+v"(f.a[4][0]), "+v"(f.a[4][1]), "+v"(f.a[5][0]), "+v"(f.a[5][1]), \
                   "+v"(f.a[6][0]), "+v"(f.a[6][1]), "+v"(f.a[7][0]), "+v"(f.a[7][1])::"memory");                     \
    asm volatile("" : "+v"(f.bh[0]), "+v"(f.bh[1]), "+v"(f.bh[2]), "+v"(f.bh[3]), "+v"(f.bh[4]), "+v"(f.bh[5]),        \
                 "+v"(f.bh[6]), "+v"(f.bh[7]), "+v"(f.bl[0]), "+v"(f.bl[1]), "+v"(f.bl[2]), "+v"(f.bl[3]),            \
                 "+v"(f.bl[4]), "+v"(f.bl[5]), "+v"(f.bl[6]), "+v"(f.bl[7])::"memory");                              \
  } while (0)
constexpr int conv_tau(int P) { int t = 0; while (P >= 8 - t) { P -= 8 - t; ++t; } return t; }
constexpr int conv_sig(int P) { int t = 0; while (P >= 8 - t) { P -= 8 - t; ++t; } return P; }
template <int RI, int P>
__device__ __forceinline__ void conv_pairs(f32x4 (&acc)[8], const bf16x8 (&A)[8], ConvFrag& cur, ConvFrag& nxt, unsigned ua,
                                           unsigned kb) {
  constexpr int T = conv_tau(P), SG = conv_sig(P);
  MFMA16(acc[T + SG], A[SG], cur.bl[T]);
  if constexpr (RI < 3 && P < 32) conv_read<RI + 1, P>(nxt, ua, kb);
  MFMA16(acc[T + SG], A[SG], cur.bh[T]);
  if constexpr (P + 1 < 36) conv_pairs<RI, P + 1>(acc, A, cur, nxt, ua, kb);
}
template <int RI>
__device__ __forceinline__ void conv_rho(f32x4 (&acc)[8], ConvFrag& cur, ConvFrag& nxt, unsigned ua, unsigned kb) {
  CONV_WAIT(cur);
  bf16x8 A[8];
#pragma unroll
  for (int i = 0; i < 8; ++i) A[i] = cat4(cur.a[i][0], cur.a[i][1]);
  conv_pairs<RI, 0>(acc, A, cur, nxt, ua, kb);
  if constexpr (RI < 3) conv_rho<RI + 1>(acc, nxt, cur, ua, kb);
}

__device__ __forceinline__ void split_pair(float v0, float v1, unsigned& hi, unsigned& lo) {
  hi = pk_bf16(v0, v1);
  lo = pk_bf16(ssub(v0, bf_lo(hi)), ssub(v1, bf_hi(hi)));
}

__device__ __forceinline__ void ssm_unit(int w, const u16* __restrict__ u, const u16* __restrict__ logAr,
                                         const u16* __restrict__ Aim, const u16* __restrict__ Cre,
                                         const u16* __restrict__ Cim, const u16* __restrict__ logdt,
                                         const u16* __restrict__ Dskip, u16* __restrict__ aT, char* smem DBG_ARG) {
  const int tid = threadIdx.x, lane = tid & 63;
  const int wave = __builtin_amdgcn_readfirstlane(tid >> 6);
  const int ch = wave >> 1, w2 = wave & 1;
  const int d0 = 2 * w;
  const int d = d0 + ch;

  const int pidx = d0 * NST + tid;
  const u16 r_lar = logAr[pidx], r_aim = Aim[pidx], r_cre = Cre[pidx], r_cim = Cim[pidx], r_ldt = logdt[d], r_dsk = Dskip[d];
  const unsigned* u32p = (const unsigned*)(u + d0);
  unsigned uw[8], up[3];
#pragma unroll
  for (int i = 0; i < 3; ++i) up[i] = 0;
  if (lane == 0 && tid > 0) {
#pragma unroll
    for (int i = 0; i < 3; ++i) up[i] = u32p[(size_t)(8 * tid - 3 + i) * (DM / 2)];
  }
  const unsigned uoff = (unsigned)tid * 8192u;
  {
    const float dt = rbf(expf(bf2f(r_ldt)));
    const float are = rbf(expf(bf2f(r_lar)));
    f32x4 prm;
    prm[0] = (-are * dt) * 1.44269504088896340736f;
    prm[1] = (bf2f(r_aim) * dt) * 0.15915494309189533577f;
    prm[2] = 2.0f * (bf2f(r_cre) * dt);
    prm[3] = 2.0f * (bf2f(r_cim) * dt);
    *(f32x4*)(smem + KR_OFF + tid * 16) = prm;
  }
  __syncthreads();

  const int r = lane & 31, h = lane >> 5;
  f32x16 dacc = {};
  {
    int p1i;
    if (r < 8) p1i = r;
    else if (r < 12) p1i = 256 + 2 * (r - 8);
    else if (r < 16) p1i = 512 + 256 * ((r - 12) >> 1) + 4 * ((r - 12) & 1);
    else if (r < 21) p1i = 1024 + 256 * (r - 16);
    else p1i = 0;
    const float p1 = (float)p1i, p2 = (float)(8 * r);
    const char* prm_base = smem + KR_OFF + (ch * 128 + 64 * w2 + 4 * h) * 16;
#pragma unroll
    for (int ks = 0; ks < 8; ++ks) {
      if (ks >= 4) {
        asm volatile("global_load_dword %0, %1, %2 offset:%3" : "=v"(uw[2 * ks - 8]) : "v"(uoff), "s"((const char*)u32p + ((2 * ks - 8) >> 2) * 4096), "i"(((2 * ks - 8) & 3) * 1024) : "memory");
        asm volatile("global_load_dword %0, %1, %2 offset:%3" : "=v"(uw[2 * ks - 7]) : "v"(uoff), "s"((const char*)u32p + ((2 * ks - 7) >> 2) * 4096), "i"(((2 * ks - 7) & 3) * 1024) : "memory");
      }
      f32x4 prm[4];
#pragma unroll
      for (int q = 0; q < 4; ++q) prm[q] = *(const f32x4*)(prm_base + (8 * ks + q) * 16);
      float ea[4], eb[4], fa[4], fb[4];
#pragma unroll
      for (int q = 0; q < 4; ++q) {
        ea[q] = __builtin_amdgcn_exp2f(smul(prm[q][0], p1)); eb[q] = __builtin_amdgcn_exp2f(smul(prm[q][0], p2));
        fa[q] = __builtin_amdgcn_fractf(smul(prm[q][1], p1)); fb[q] = __builtin_amdgcn_fractf(smul(prm[q][1], p2));
      }
      float ac[4], as[4], bc[4], bs[4];
#pragma unroll
      for (int q = 0; q < 4; ++q) {
        ac[q] = __builtin_amdgcn_cosf(fa[q]); as[q] = __builtin_amdgcn_sinf(fa[q]);
        bc[q] = __builtin_amdgcn_cosf(fb[q]); bs[q] = __builtin_amdgcn_sinf(fb[q]);
      }
      asm volatile("s_nop 1" : "+v"(ea[0]), "+v"(ea[1]), "+v"(ea[2]), "+v"(ea[3]), "+v"(eb[0]), "+v"(eb[1]), "+v"(eb[2]), "+v"(eb[3]),
                   "+v"(ac[0]), "+v"(ac[1]), "+v"(ac[2]), "+v"(ac[3]), "+v"(as[0]), "+v"(as[1]), "+v"(as[2]), "+v"(as[3]),
                   "+v"(bc[0]), "+v"(bc[1]), "+v"(bc[2]), "+v"(bc[3]), "+v"(bs[0]), "+v"(bs[1]), "+v"(bs[2]), "+v"(bs[3]));
      unsigned ah[4], al[4], bh[4], bl[4];
#pragma unroll
      for (int q = 0; q < 4; ++q) {
        const float cr = smul(prm[q][2], ea[q]), ci = smul(prm[q][3], ea[q]);
        const float gre = sfms(ci, as[q], smul(cr, ac[q]));
        const float gmi = sfnn(ci, ac[q], smul(cr, as[q]));
        split_pair(gre, gmi, ah[q], al[q]);
        split_pair(smul(bc[q], eb[q]), smul(bs[q], eb[q]), bh[q], bl[q]);
      }
      const bf16x8 Ah = mk8(ah[0], ah[1], ah[2], ah[3]), Al = mk8(al[0], al[1], al[2], al[3]);
      const bf16x8 Bh = mk8(bh[0], bh[1], bh[2], bh[3]), Bl = mk8(bl[0], bl[1], bl[2], bl[3]);
      dacc = __builtin_amdgcn_mfma_f32_32x32x16_bf16(Al, Bh, dacc, 0, 0, 0);
      dacc = __builtin_amdgcn_mfma_f32_32x32x16_bf16(Ah, Bl, dacc, 0, 0, 0);
      dacc = __builtin_amdgcn_mfma_f32_32x32x16_bf16(Ah, Bh, dacc, 0, 0, 0);
    }
  }
  {
    float* xo = (float*)(smem + KX_OFF + wave * 1536) + lane;
    const float* xi = (const float*)(smem + KX_OFF + (wave ^ 1) * 1536) + lane;
    float mine[6];
    if (w2 == 0) {
#pragma unroll
      for (int e = 0; e < 6; ++e) { xo[e * 64] = dacc[6 + e]; mine[e] = dacc[e]; }
    } else {
#pragma unroll
      for (int e = 0; e < 6; ++e) { xo[e * 64] = dacc[e]; mine[e] = dacc[6 + e]; }
    }
    __syncthreads();
    unsigned* Kq = (unsigned*)(smem + ch * KQ_B);
    unsigned pq[6];
#pragma unroll
    for (int e = 0; e < 6; ++e) {
      const float v = sadd(mine[e], xi[e * 64]);
      const unsigned hi = pk_bf16(v, 0.f);
      pq[e] = pk_bf16(0.f, ssub(v, bf_lo(hi))) | hi;
    }
    if (w2 == 0) {
#pragma unroll
      for (int e = 0; e < 4; ++e) Kq[4 * h + e + 8 * r] = pq[e];
      Kq[(h ? 512 : 256) + 8 * r] = pq[4];
      Kq[(h ? 516 : 258) + 8 * r] = pq[5];
    } else {
      Kq[(h ? 768 : 260) + 8 * r] = pq[0];
      Kq[(h ? 772 : 262) + 8 * r] = pq[1];
      if (h == 0) {
#pragma unroll
        for (int e = 0; e < 4; ++e) Kq[1024 + 256 * e + 8 * r] = pq[2 + e];
      } else if (r == 0) {
        Kq[2048] = pq[2];
      }
    }
  }
  __syncthreads();

  {
    const int t = tid & 127;
    const unsigned* Kq = (const unsigned*)(smem + ch * KQ_B);
    unsigned* krh = (unsigned*)(smem + KR_OFF + (ch * 2) * KR_PART_B);
    unsigned* krl = (unsigned*)((char*)krh + KR_PART_B);
    unsigned q0[8], q1[8];
#pragma unroll
    for (int m = 0; m < 8; ++m) {
      const int sh = m < 4 ? 3 : (m < 6 ? 2 : (m < 7 ? 1 : 0));
      const int l0 = 2047 - 2 * (t + 128 * m), l1 = l0 - 1;
      int pa = l0, pb = l1;
      if (sh) {
        pa = (l0 + (1 << (sh - 1)) - 1 + ((l0 >> sh) & 1)) & ~((1 << sh) - 1);
        pb = (l1 + (1 << (sh - 1)) - 1 + ((l1 >> sh) & 1)) & ~((1 << sh) - 1);
      }
      q0[m] = Kq[pa];
      q1[m] = Kq[pb];
    }
#pragma unroll
    for (int m = 0; m < 8; ++m) {
      krh[t + 128 * m] = __builtin_amdgcn_perm(q1[m], q0[m], 0x05040100u);
      krl[t + 128 * m] = __builtin_amdgcn_perm(q1[m], q0[m], 0x07060302u);
    }
    if (t < 120) { krh[1024 + t] = 0; krl[1024 + t] = 0; }
  }
  __syncthreads();

  asm volatile("s_waitcnt vmcnt(0)" : "+v"(uw[0]), "+v"(uw[1]), "+v"(uw[2]), "+v"(uw[3]), "+v"(uw[4]), "+v"(uw[5]), "+v"(uw[6]), "+v"(uw[7])::"memory");
  {
    unsigned e[11];
#pragma unroll
    for (int i = 0; i < 3; ++i) {
      const unsigned nb = __shfl_up(uw[5 + i], 1);
      e[i] = (lane == 0) ? up[i] : nb;
    }
#pragma unroll
    for (int i = 0; i < 8; ++i) e[3 + i] = uw[i];
    unsigned p0[10], p1[10];
#pragma unroll
    for (int i = 0; i < 10; ++i) {
      p0[i] = __builtin_amdgcn_perm(e[i + 1], e[i], 0x05040100u);
      p1[i] = __builtin_amdgcn_perm(e[i + 1], e[i], 0x07060302u);
    }
#pragma unroll
    for (int c = 0; c < 4; ++c) {
      *(u32x4*)(smem + c * U_COPY_B + (tid + 2) * 16) = u32x4{p0[c], p0[c + 2], p0[c + 4], p0[c + 6]};
      *(u32x4*)(smem + U_CH_B + c * U_COPY_B + (tid + 2) * 16) = u32x4{p1[c], p1[c + 2], p1[c + 4], p1[c + 6]};
    }
    if (tid < 16) *(u32x4*)(smem + (tid >> 1) * U_COPY_B + (tid & 1) * 16) = u32x4{0, 0, 0, 0};
  }
  __syncthreads();

  f32x4 acc[8];
#pragma unroll
  for (int t = 0; t < 8; ++t) acc[t] = f32x4{0.f, 0.f, 0.f, 0.f};
  asm volatile("s_nop 4" : "+v"(acc[0]), "+v"(acc[1]), "+v"(acc[2]), "+v"(acc[3]), "+v"(acc[4]), "+v"(acc[5]), "+v"(acc[6]), "+v"(acc[7]));
  {
    const int r16 = lane & 15, hq = lane >> 4;
    const unsigned sb = lds_addr(smem);
    const unsigned ua = sb + ch * U_CH_B + (r16 & 3) * U_COPY_B + 2 * (4 + (r16 & ~3) + 8 * hq) + 64 * 4 * w2;
    const unsigned kb = sb + KR_OFF + (ch * 2) * KR_PART_B + 2 * (2032 - 16 * r16 + 8 * hq) - 64 * 56 + 64 * 4 * w2;
    ConvFrag f0, f1;
    conv_read_all<0, 0>(f0, ua, kb);
    conv_rho<0>(acc, f0, f1, ua, kb);
  }
  asm volatile("s_nop 15" : "+v"(acc[0]), "+v"(acc[1]), "+v"(acc[2]), "+v"(acc[3]), "+v"(acc[4]), "+v"(acc[5]), "+v"(acc[6]), "+v"(acc[7]));
  __syncthreads();
  {
    char* xw = smem + KR_OFF + (ch * 2 + w2) * 4096 + lane * 16;
    const char* xr = smem + KR_OFF + (ch * 2 + (w2 ^ 1)) * 4096 + lane * 16;
    f32x4 y[4];
    if (w2 == 0) {
#pragma unroll
      for (int i = 0; i < 4; ++i) { *(f32x4*)(xw + i * 1024) = acc[4 + i]; y[i] = acc[i]; }
    } else {
#pragma unroll
      for (int i = 0; i < 4; ++i) { *(f32x4*)(xw + i * 1024) = acc[i]; y[i] = acc[4 + i]; }
    }
    __syncthreads();
    const float Dk = bf2f(r_dsk);
#pragma unroll
    for (int i = 0; i < 4; ++i) {
      const f32x4 o = *(const f32x4*)(xr + i * 1024);
      const int l0 = 256 * (4 * w2 + i) + 16 * (lane & 15) + 4 * (lane >> 4);
      const u32x2 uu = *(const u32x2*)(smem + ch * U_CH_B + 3 * U_COPY_B + 2 * (16 + l0));
      const unsigned y01 = pk_bf16(sadd(y[i][0], o[0]), sadd(y[i][1], o[1])), y23 = pk_bf16(sadd(y[i][2], o[2]), sadd(y[i][3], o[3]));
      const unsigned s01 = pk_bf16(smul(bf_lo(uu[0]), Dk), smul(bf_hi(uu[0]), Dk)), s23 = pk_bf16(smul(bf_lo(uu[1]), Dk), smul(bf_hi(uu[1]), Dk));
      *(u32x2*)(aT + (size_t)d * SEQ + l0) = u32x2{pk_bf16(sadd(bf_lo(y01), bf_lo(s01)), sadd(bf_hi(y01), bf_hi(s01))),
                                                  pk_bf16(sadd(bf_lo(y23), bf_lo(s23)), sadd(bf_hi(y23), bf_hi(s23)))};
    }
  }
}

__device__ __forceinline__ float gelu_tanh(float x) {
  const float z = 0.7978845608028654f * (x + 0.044715f * x * x * x);
  const float e = __builtin_amdgcn_exp2f(-2.8853900817779268f * z);
  return x * __builtin_amdgcn_rcpf(1.0f + e);
}

constexpr int G1_WSLOT_B = 12288;
constexpr int G1_X_OFF = 3 * G1_WSLOT_B;
constexpr int G1_LDS = G1_X_OFF + 32768;

constexpr int g1_pos(bool isx, int id) {
  if (isx && id == 0) return 4;
  if (!isx && id == 0) return 7;
  if (!isx && id == 1) return 10;
  if (isx && id == 1) return 14;
  if (!isx && id == 2) return 17;
  if (!isx && id == 3) return 20;
  if (!isx && id == 4) return 23;
  if (isx && id == 2) return 27;
  int n = 27;
  for (int t = 0; t < 32; ++t) {
    if ((t & 1) == 0 && t / 2 + 5 < 16) { n += 3; if (!isx && t / 2 + 5 == id) return n; }
    if ((t & 3) == 1 && t / 4 + 3 < 8) { n += 4; if (isx && t / 4 + 3 == id) return n; }
  }
  return -1;
}
constexpr int g1_issued_before(int step) {
  int n = 27;
  for (int t = 0; t < step; ++t) {
    if ((t & 1) == 0 && t / 2 + 5 < 16) n += 3;
    if ((t & 3) == 1 && t / 4 + 3 < 8) n += 4;
  }
  return n;
}

#define GLOAD128(dst, ptr) asm volatile("global_load_dwordx4 %0, %1, off" : "=v"(dst) : "v"(ptr) : "memory")
#define GLOAD128S(dst, voff, sbase) asm volatile("global_load_dwordx4 %0, %1, %2" : "=v"(dst) : "v"(voff), "s"(sbase) : "memory")
#define GLOAD128SA(dst, voff, sbase) asm volatile("global_load_dwordx4 %0, %1, %2" : "=a"(dst) : "v"(voff), "s"(sbase) : "memory")
#define GLOAD128A(dst, ptr) asm volatile("global_load_dwordx4 %0, %1, off" : "=a"(dst) : "v"(ptr) : "memory")
#define DS_W128(addr, val, off) asm volatile("ds_write_b128 %0, %1 offset:%2" ::"v"(addr), "v"(val), "i"(off) : "memory")
#define MFMA32(acc, a, b) asm volatile("v_mfma_f32_32x32x16_bf16 %0, %1, %2, %0" : "+v"(acc) : "v"(a), "v"(b))

struct G1Ctx {
  const char* wbase;
  const char* xbase;
  unsigned wo[3], xo;
  unsigned woff, woff_o, ww[3], xw, xrd2[2][4];
  unsigned junk[4];
};
struct G1Frag {
  s16x4 w[3][2];
  bf16x8 x[2];
};

#define G1_RD_W(f, t, OFF)                                                        \
  do {                                                                            \
    if ((t) & 1) { DS_TR(f.w[t][0], c.woff_o, (OFF) + (t) * 64); DS_TR(f.w[t][1], c.woff_o, (OFF) + (t) * 64 + 4 * 384); } \
    else { DS_TR(f.w[t][0], c.woff, (OFF) + (t) * 64); DS_TR(f.w[t][1], c.woff, (OFF) + (t) * 64 + 4 * 384); }            \
  } while (0)
#define G1_RD_X(f, mi, N) DS_B128(f.x[mi], c.xrd2[mi][(N) & 3], (((N) >> 2) & 1) * 16384)
template <int N>
__device__ __forceinline__ void g1_read(G1Frag& f, const G1Ctx& c) {
  constexpr int OFF = ((N >> 1) % 3) * G1_WSLOT_B + (N & 1) * 16 * 384;
  G1_RD_X(f, 0, N); G1_RD_X(f, 1, N);
  G1_RD_W(f, 0, OFF); G1_RD_W(f, 1, OFF); G1_RD_W(f, 2, OFF);
}
#define G1_WAIT_FRAG(f, STR)                                                                                       \
  asm volatile(STR : "+v"(f.w[0][0]), "+v"(f.w[0][1]), "+v"(f.w[1][0]), "+v"(f.w[1][1]), "+v"(f.w[2][0]),        \
               "+v"(f.w[2][1]), "+v"(f.x[0]), "+v"(f.x[1])::"memory")

template <int N>
struct G1Step {
  static __device__ __forceinline__ void run(f32x16 (&acc)[6], u32x4 (&xr)[2][4], u32x4 (&wr)[3][3], G1Frag& cur,
                                             G1Frag& nxt, G1Ctx& c) {
    constexpr int ISS = g1_issued_before(N);
    constexpr int S = N >> 1;
    constexpr bool RD = N + 1 < 32;
    constexpr int OFF = (((N + 1) >> 1) % 3) * G1_WSLOT_B + ((N + 1) & 1) * 16 * 384;
    if constexpr ((N & 1) == 0 && N > 0) G1_WAIT_FRAG(cur, "s_waitcnt lgkmcnt(0)\n\ts_barrier");
    else G1_WAIT_FRAG(cur, "s_waitcnt lgkmcnt(0)");
    bf16x8 b[3];
#pragma unroll
    for (int t = 0; t < 3; ++t) b[t] = cat4(cur.w[t][0], cur.w[t][1]);
    MFMA32(acc[0], b[0], cur.x[0]);
    if constexpr (RD) { G1_RD_X(nxt, 0, N + 1); G1_RD_W(nxt, 0, OFF); }
    MFMA32(acc[3], b[0], cur.x[1]);
    if constexpr (RD) { G1_RD_X(nxt, 1, N + 1); G1_RD_W(nxt, 1, OFF); }
    MFMA32(acc[1], b[1], cur.x[0]);
    if constexpr (RD) { G1_RD_W(nxt, 2, OFF); }
    MFMA32(acc[4], b[1], cur.x[1]);
    if constexpr ((N & 1) == 0) {
      if constexpr (S + 2 < 16) {
        constexpr int R = (S + 2) % 3;
        constexpr int NW = ISS - g1_pos(false, S + 2);
        asm volatile("s_waitcnt vmcnt(%3)" : "+v"(wr[R][0]), "+v"(wr[R][1]), "+v"(wr[R][2]) : "i"(NW) : "memory");
#pragma unroll
        for (int i = 0; i < 3; ++i) DS_W128(c.ww[i], wr[R][i], R * G1_WSLOT_B);
      }
    }
    if constexpr ((N & 3) == 1) {
      constexpr int B = N / 4 + 1;
      if constexpr (B < 8) {
        constexpr int NW = ISS - g1_pos(true, B);
        asm volatile("s_waitcnt vmcnt(%4)" : "+v"(xr[B & 1][0]), "+v"(xr[B & 1][1]), "+v"(xr[B & 1][2]), "+v"(xr[B & 1][3]) : "i"(NW) : "memory");
#pragma unroll
        for (int i = 0; i < 4; ++i) DS_W128(c.xw, xr[B & 1][i], (B & 1) * 16384 + i * 1024);
      }
    }
    MFMA32(acc[2], b[2], cur.x[0]);
    if constexpr ((N & 1) == 0 && S + 5 < 16) {
#pragma unroll
      for (int i = 0; i < 3; ++i) GLOAD128S(wr[(S + 5) % 3][i], c.wo[i], c.wbase + (size_t)(S + 5) * 32 * FF * 2);
    }
    if constexpr ((N & 3) == 1 && N / 4 + 3 < 8) {
      constexpr int B = N / 4 + 1;
#pragma unroll
      for (int i = 0; i < 4; ++i) GLOAD128S(xr[B & 1][i], c.xo, c.xbase + (size_t)(8 * i) * DM * 2 + (B + 2) * 128);
    }
    MFMA32(acc[5], b[2], cur.x[1]);
    if constexpr (N + 1 < 32) G1Step<N + 1>::run(acc, xr, wr, nxt, cur, c);
  }
};

__device__ __forceinline__ void gemm1_tile(int t, const u16* __restrict__ x, const u16* __restrict__ wg,
                                           const u16* __restrict__ wu, u16* __restrict__ hf, char* smem DBG_ARG) {
  const int tid = threadIdx.x, lane = tid & 63;
  const int wave = __builtin_amdgcn_readfirstlane(tid >> 6);
  const int fb = t >> 4, mb = t & 15;
  const int f0 = 96 * fb;
  const int wm = wave & 1, isup = wave >> 1;
  G1Ctx c;
  const unsigned sbase = lds_addr(smem);
  c.wbase = (const char*)(isup ? wu : wg);
#pragma unroll
  for (int i = 0; i < 3; ++i) {
    const int id = 192 * (wave & 1) + 64 * i + lane;
    const int row = id / 12, cc = id % 12;
    int col = f0 + 8 * cc;
    if (col > FF - 8) col = FF - 8;
    c.wo[i] = (unsigned)(row * FF + col) * 2u;
    c.ww[i] = sbase + row * 384 + ((cc + 12 * isup) ^ (((row >> 1) & 1) << 2)) * 16;
  }
  c.xbase = (const char*)(x + (size_t)(128 * mb + 32 * wave) * DM);
  c.xo = (unsigned)((lane >> 3) * DM + (lane & 7) * 8) * 2u;
  const unsigned xl = sbase + G1_X_OFF;
  c.xw = xl + (32 * wave + (lane >> 3)) * 128 + (((lane & 7) ^ (lane >> 3)) * 16);
  const int r = lane & 31, h = lane >> 5;
#pragma unroll
  for (int i = 0; i < 4; ++i) {
    c.xrd2[0][i] = xl + (64 * wm + 32 * isup + r) * 128 + (((2 * i + h) ^ (r & 7)) * 16);
    c.xrd2[1][i] = xl + (64 * wm + 32 * (1 - isup) + r) * 128 + (((2 * i + h) ^ (r & 7)) * 16);
  }
  const int blk = (lane >> 4) & 1, q = (lane & 15) >> 2, p = lane & 3;
  const int sw = (q >> 1) & 1;
  {
    const unsigned e = sbase + (8 * h + q) * 384 + (2 * blk + (p >> 1) + 4 * sw) * 16 + 8 * (p & 1);
    const unsigned o = sbase + (8 * h + q) * 384 + (2 * blk + (p >> 1) - 4 * sw) * 16 + 8 * (p & 1);
    c.woff = isup ? o + 192 : e;
    c.woff_o = isup ? e + 192 : o;
  }

  f32x16 acc[6];
#pragma unroll
  for (int i = 0; i < 6; ++i) acc[i] = f32x16{};
  asm volatile("s_nop 4" : "+v"(acc[0]), "+v"(acc[1]), "+v"(acc[2]), "+v"(acc[3]), "+v"(acc[4]), "+v"(acc[5]));
  u32x4 xr[2][4], wr[3][3];
  G1Frag fa, fb2;
#define G1_ISSUE_X(R, B) _Pragma("unroll") for (int i = 0; i < 4; ++i) GLOAD128S(xr[R][i], c.xo, c.xbase + (size_t)(8 * i) * DM * 2 + (B) * 128)
#define G1_ISSUE_W(R, S) _Pragma("unroll") for (int i = 0; i < 3; ++i) GLOAD128S(wr[R][i], c.wo[i], c.wbase + (size_t)(S) * 32 * FF * 2)
  G1_ISSUE_X(0, 0); G1_ISSUE_W(0, 0); G1_ISSUE_W(1, 1); G1_ISSUE_X(1, 1); G1_ISSUE_W(2, 2);
#define G1_EXTRA 0
  asm volatile("s_waitcnt vmcnt(%10)"
               : "+v"(wr[0][0]), "+v"(wr[0][1]), "+v"(wr[0][2]), "+v"(wr[1][0]), "+v"(wr[1][1]), "+v"(wr[1][2]),
                 "+v"(xr[0][0]), "+v"(xr[0][1]), "+v"(xr[0][2]), "+v"(xr[0][3]) : "i"(7 + G1_EXTRA) : "memory");
#pragma unroll
  for (int i = 0; i < 3; ++i) { DS_W128(c.ww[i], wr[0][i], 0); DS_W128(c.ww[i], wr[1][i], G1_WSLOT_B); }
#pragma unroll
  for (int i = 0; i < 4; ++i) DS_W128(c.xw, xr[0][i], i * 1024);
  G1_ISSUE_W(0, 3); G1_ISSUE_W(1, 4); G1_ISSUE_X(0, 2);
  asm volatile("s_waitcnt lgkmcnt(0)\n\ts_barrier" ::: "memory");
  g1_read<0>(fa, c);
  G1Step<0>::run(acc, xr, wr, fa, fb2, c);
  asm volatile("s_nop 15\n\ts_nop 15" : "+v"(acc[0]), "+v"(acc[1]), "+v"(acc[2]), "+v"(acc[3]), "+v"(acc[4]), "+v"(acc[5]));

  unsigned keep[3][8];
  {
    unsigned give[3][8];
#pragma unroll
    for (int tt = 0; tt < 3; ++tt)
#pragma unroll
      for (int e = 0; e < 8; ++e) give[tt][e] = pk_bf16(acc[3 + tt][2 * e], acc[3 + tt][2 * e + 1]);
    __syncthreads();
    char* xo = smem + wave * 6144 + lane * 16;
#pragma unroll
    for (int tt = 0; tt < 3; ++tt) {
      *(u32x4*)(xo + (2 * tt) * 1024) = u32x4{give[tt][0], give[tt][1], give[tt][2], give[tt][3]};
      *(u32x4*)(xo + (2 * tt + 1) * 1024) = u32x4{give[tt][4], give[tt][5], give[tt][6], give[tt][7]};
    }
  }
#pragma unroll
  for (int tt = 0; tt < 3; ++tt)
#pragma unroll
    for (int e = 0; e < 8; ++e) keep[tt][e] = pk_bf16(acc[tt][2 * e], acc[tt][2 * e + 1]);
  __syncthreads();
  {
    const char* xi = smem + (wave ^ 2) * 6144 + lane * 16;
    const int mt = 4 * mb + 2 * wm + isup;
    u16* hp = hf + ((size_t)(mt * NKS + 6 * fb) * 64 + lane) * 8;
    auto finish = [&](auto ISUP) {
#pragma unroll
      for (int tt = 0; tt < 3; ++tt) {
        const int ft = 3 * fb + tt;
        if (ft < FF / 32) {
          const u32x4 o0 = *(const u32x4*)(xi + (2 * tt) * 1024);
          const u32x4 o1 = *(const u32x4*)(xi + (2 * tt + 1) * 1024);
          unsigned gp[8], up[8];
#pragma unroll
          for (int e = 0; e < 8; ++e) {
            const unsigned ot = e < 4 ? o0[e & 3] : o1[e & 3];
            gp[e] = decltype(ISUP)::value ? ot : keep[tt][e];
            up[e] = decltype(ISUP)::value ? keep[tt][e] : ot;
          }
          float xg[16], ar[16];
#pragma unroll
          for (int e = 0; e < 8; ++e) { xg[2 * e] = bf_lo(gp[e]); xg[2 * e + 1] = bf_hi(gp[e]); }
          const float c1 = -2.30220819814f, c2 = -0.10294323958f, one = 1.0f;
#pragma unroll
          for (int e = 0; e < 16; ++e) ar[e] = smul(xg[e], xg[e]);
#pragma unroll
          for (int e = 0; e < 16; ++e) ar[e] = sfma(ar[e], c2, c1);
#pragma unroll
          for (int e = 0; e < 16; ++e) ar[e] = __builtin_amdgcn_exp2f(smul(ar[e], xg[e]));
          TRANS_FENCE8(ar, 0);
          TRANS_FENCE8(ar, 8);
#pragma unroll
          for (int e = 0; e < 16; ++e) ar[e] = __builtin_amdgcn_rcpf(sadd(ar[e], one));
          TRANS_FENCE8(ar, 0);
          TRANS_FENCE8(ar, 8);
#pragma unroll
          for (int e = 0; e < 16; ++e) ar[e] = smul(ar[e], xg[e]);
          unsigned o[8];
#pragma unroll
          for (int e = 0; e < 8; ++e) o[e] = pk_bf16(ar[2 * e], ar[2 * e + 1]);
#pragma unroll
          for (int e = 0; e < 8; ++e) o[e] = pk_bf16(smul(bf_lo(o[e]), bf_lo(up[e])), smul(bf_hi(o[e]), bf_hi(up[e])));
          *(u32x4*)(hp + (2 * tt) * 512) = u32x4{o[0], o[1], o[2], o[3]};
          *(u32x4*)(hp + (2 * tt + 1) * 512) = u32x4{o[4], o[5], o[6], o[7]};
        } else if (ft < NKS / 2) {
          *(u32x4*)(hp + (2 * tt) * 512) = u32x4{0, 0, 0, 0};
          *(u32x4*)(hp + (2 * tt + 1) * 512) = u32x4{0, 0, 0, 0};
        }
      }
    };
    if (isup) finish(std::integral_constant<bool, true>{});
    else finish(std::integral_constant<bool, false>{});
  }
}

__device__ __forceinline__ void gate_unit(int b, const u16* __restrict__ x, const u16* __restrict__ gw,
                                          float* __restrict__ gout) {
  const int tid = threadIdx.x, lane = tid & 63, wave = tid >> 6;
  const int j = lane & 7, rr = lane >> 3;
  u32x4 gv[8];
#pragma unroll
  for (int i = 0; i < 8; ++i) gv[i] = *(const u32x4*)(gw + (j + 8 * i) * 8);
  const int row0 = b * 128 + wave * 32 + rr;
  u32x4 xv[4][8];
#pragma unroll
  for (int ps = 0; ps < 4; ++ps)
#pragma unroll
    for (int i = 0; i < 8; ++i) xv[ps][i] = *(const u32x4*)(x + (size_t)(row0 + 8 * ps) * DM + (j + 8 * i) * 8);
#pragma unroll
  for (int ps = 0; ps < 4; ++ps) {
    float s0 = 0.f, s1 = 0.f;
#pragma unroll
    for (int i = 0; i < 8; ++i)
#pragma unroll
      for (int e = 0; e < 4; ++e) {
        const unsigned pr = pk_bf16(smul(bf_lo(xv[ps][i][e]), bf_lo(gv[i][e])), smul(bf_hi(xv[ps][i][e]), bf_hi(gv[i][e])));
        s0 = sadd(s0, bf_lo(pr));
        s1 = sadd(s1, bf_hi(pr));
      }
    float s = s0 + s1;
    s += __shfl_xor(s, 1);
    s += __shfl_xor(s, 2);
    s += __shfl_xor(s, 4);
    if (j == 0) {
      const float sb = rbf(s);
      gout[row0 + 8 * ps] = rbf(1.0f / (1.0f + expf(-sb)));
    }
  }
}

__global__ __launch_bounds__(256, 2) void k1_kernel(const u16* __restrict__ u, const u16* __restrict__ logAr,
                                                   const u16* __restrict__ Aim, const u16* __restrict__ Cre,
                                                   const u16* __restrict__ Cim, const u16* __restrict__ logdt,
                                                   const u16* __restrict__ Dskip, const u16* __restrict__ wg,
                                                   const u16* __restrict__ wu, const u16* __restrict__ gw,
                                                   const u16* __restrict__ wd, u16* __restrict__ hbuf, u16* __restrict__ aT,
                                                   float* __restrict__ gout DBG_ARG
                                                   ) {
  __shared__ __attribute__((aligned(4096))) char smem[G1_LDS > SSM_LDS ? G1_LDS : SSM_LDS];
  const int bid = blockIdx.x;
  if (bid < NSSM) {
    ssm_unit(bid, u, logAr, Aim, Cre, Cim, logdt, Dskip, aT, smem DBG_PASS);
    unsigned acc = 0;
    for (int i = (bid >> 3) * 256 + threadIdx.x; i < 11008; i += 32 * 256) acc += ((const unsigned*)wd)[i * 32];
    asm volatile("" ::"v"(acc));
  } else {
    const int xcd = bid & 7, idx = (bid - NSSM) >> 3;
    if (idx < 30) {
      gemm1_tile(30 * xcd + idx, u, wg, wu, hbuf, smem DBG_PASS);
    }
    else gate_unit(2 * xcd + idx - 30, u, gw, gout);
  }
}

constexpr int G2_SLOT_B = 4096;
constexpr int G2_WAVE_B = 2 * G2_SLOT_B;
constexpr int G2_PITCH = 272;
constexpr int G2_A_OFF = 4 * 64 * G2_PITCH;
constexpr int K2_LDS = G2_A_OFF + 8192;
#define G2D 3
constexpr int G2_DEPTH = G2D;

struct G2Ctx {
  const u16* hsrc;
  const u16* wd;
  int wrow0, wcol;
  unsigned wb32, woff, ww;
};

template <int ST>
struct G2Step {
  static __device__ __forceinline__ void issue(bf16x8 (&hfr)[11][4], u32x4 (&wr)[11][4], const G2Ctx& c) {
#pragma unroll
    for (int i = 0; i < 4; ++i) {
      int kr = c.wrow0 + ST * 32 + 8 * i;
      kr = kr < FF ? kr : FF - 1;
      GLOAD128(wr[ST][i], c.wd + (size_t)kr * DM + c.wcol);
    }
#pragma unroll
    for (int mi = 0; mi < 2; ++mi)
#pragma unroll
      for (int i = 0; i < 2; ++i)
        GLOAD128A(hfr[ST][2 * mi + i], c.hsrc + (size_t)(mi * NKS + 2 * ST + i) * 512);

  }
  static __device__ __forceinline__ void run(f32x16 (&acc)[2][2], bf16x8 (&hfr)[11][4], u32x4 (&wr)[11][4], const G2Ctx& c) {
    constexpr int LAST = (ST + G2_DEPTH - 1 < 10) ? ST + G2_DEPTH - 1 : 10;
    constexpr int NW = 8 * (LAST - ST);
    asm volatile("s_waitcnt vmcnt(%4)" : "+v"(wr[ST][0]), "+v"(wr[ST][1]), "+v"(wr[ST][2]), "+v"(wr[ST][3]) : "i"(NW + 4) : "memory");
#pragma unroll
    for (int i = 0; i < 4; ++i) DS_W128(c.ww, wr[ST][i], (ST & 1) * G2_SLOT_B + i * 1024);
    asm volatile("s_waitcnt lgkmcnt(0)" ::: "memory");
    const unsigned sb = c.wb32 + c.woff;
    s16x4 w[2][2][2];
#pragma unroll
    for (int i = 0; i < 2; ++i)
#pragma unroll
      for (int t = 0; t < 2; ++t) {
        DS_TR(w[i][t][0], sb ^ (t * 64), (ST & 1) * G2_SLOT_B + i * 2048);
        DS_TR(w[i][t][1], sb ^ (t * 64), (ST & 1) * G2_SLOT_B + i * 2048 + 1024);
      }
    asm volatile("s_waitcnt vmcnt(%4)" : "+a"(hfr[ST][0]), "+a"(hfr[ST][1]), "+a"(hfr[ST][2]), "+a"(hfr[ST][3]) : "i"(NW) : "memory");
    if constexpr (ST + G2_DEPTH <= 10) G2Step<ST + G2_DEPTH>::issue(hfr, wr, c);
    asm volatile("s_waitcnt lgkmcnt(0)"
                 : "+v"(w[0][0][0]), "+v"(w[0][0][1]), "+v"(w[0][1][0]), "+v"(w[0][1][1]), "+v"(w[1][0][0]), "+v"(w[1][0][1]),
                   "+v"(w[1][1][0]), "+v"(w[1][1][1])::"memory");
#pragma unroll
    for (int i = 0; i < 2; ++i)
#pragma unroll
      for (int t = 0; t < 2; ++t)
#pragma unroll
        for (int mi = 0; mi < 2; ++mi)
          acc[t][mi] = __builtin_amdgcn_mfma_f32_32x32x16_bf16(cat4(w[i][t][0], w[i][t][1]), hfr[ST][2 * mi + i], acc[t][mi], 0, 0, 0);
    if constexpr (ST + 1 <= 10) G2Step<ST + 1>::run(acc, hfr, wr, c);
  }
};

__global__ __launch_bounds__(256, 1) void k2_kernel(const u16* __restrict__ hf, const u16* __restrict__ wd,
                                                   const u16* __restrict__ aT, const float* __restrict__ gin,
                                                   u16* __restrict__ out DBG2_ARG) {
  __shared__ __attribute__((aligned(4096))) char smem[K2_LDS];
  const int tid = threadIdx.x, lane = tid & 63;
  STAMP2(0);
  const int wave = __builtin_amdgcn_readfirstlane(tid >> 6);
  const int bid = blockIdx.x;
  const int xcd = bid & 7, idx = bid >> 3;
  const int m0 = 64 * (4 * xcd + (idx >> 3)), n0 = 64 * (idx & 7);

#pragma unroll
  for (int i = 0; i < 2; ++i) {
    const int piece = 2 * wave + i;
    const int drow = 8 * piece + (lane >> 3);
    glds16(aT + (size_t)(n0 + drow) * SEQ + m0 + (lane & 7) * 8, smem + G2_A_OFF + piece * 1024 + lane * 16);
  }

  G2Ctx c;
  c.wb32 = lds_addr(smem) + wave * G2_WAVE_B;
  c.hsrc = hf + ((size_t)((m0 >> 5) * NKS + 22 * wave) * 64 + lane) * 8;
  c.wd = wd;
  c.wcol = n0 + 8 * (lane & 7);
  c.wrow0 = 352 * wave + (lane >> 3);
  c.ww = c.wb32 + (lane >> 3) * 128 + (((lane & 7) ^ (((lane >> 4) & 1) << 2)) * 16);

  const int r = lane & 31, h = lane >> 5;
  const int blk = (lane >> 4) & 1, q = (lane & 15) >> 2, p = lane & 3;
  c.woff = (4 * h + q) * 128 + (((2 * blk + (p >> 1)) ^ (((q >> 1) & 1) << 2)) * 16) + 8 * (p & 1);

  f32x16 acc[2][2];
#pragma unroll
  for (int i = 0; i < 2; ++i)
#pragma unroll
    for (int j = 0; j < 2; ++j) acc[i][j] = f32x16{};
  bf16x8 hfr[11][4];
  u32x4 wr[11][4];

  G2Step<0>::issue(hfr, wr, c);
  G2Step<1>::issue(hfr, wr, c);
  if constexpr (G2_DEPTH > 2) G2Step<2>::issue(hfr, wr, c);
  if constexpr (G2_DEPTH > 3) G2Step<3>::issue(hfr, wr, c);
  if constexpr (G2_DEPTH > 4) G2Step<4>::issue(hfr, wr, c);
  if constexpr (G2_DEPTH > 5) G2Step<5>::issue(hfr, wr, c);
  if constexpr (G2_DEPTH > 6) G2Step<6>::issue(hfr, wr, c);
  STAMP2(1);
  STAMP2(2);
  G2Step<0>::run(acc, hfr, wr, c);
  STAMP2(3);
  __syncthreads();
  {
    char* pw = smem + wave * (64 * G2_PITCH);
#pragma unroll
    for (int c = 0; c < 2; ++c)
#pragma unroll
      for (int mi = 0; mi < 2; ++mi)
#pragma unroll
        for (int gq = 0; gq < 4; ++gq) {
          const f32x4 v = {acc[c][mi][4 * gq], acc[c][mi][4 * gq + 1], acc[c][mi][4 * gq + 2], acc[c][mi][4 * gq + 3]};
          *(f32x4*)(pw + (32 * mi + r) * G2_PITCH + (32 * c + 8 * gq + 4 * h) * 4) = v;
        }
  }
  __syncthreads();
  STAMP2(4);
  {
    const int m = tid >> 2, nq = (tid & 3) * 16;
    const float g = gin[m0 + m];
    const float omg = rbf(1.0f - g);
    float b[16];
#pragma unroll
    for (int j = 0; j < 4; ++j) {
      f32x4 s = *(const f32x4*)(smem + m * G2_PITCH + (nq + 4 * j) * 4);
#pragma unroll
      for (int w = 1; w < 4; ++w) s += *(const f32x4*)(smem + w * (64 * G2_PITCH) + m * G2_PITCH + (nq + 4 * j) * 4);
#pragma unroll
      for (int e = 0; e < 4; ++e) b[4 * j + e] = s[e];
    }
    const u16* aimg = (const u16*)(smem + G2_A_OFF);
    unsigned o[8];
#pragma unroll
    for (int j = 0; j < 8; ++j) {
      const float a0 = bf2f(aimg[(nq + 2 * j) * 64 + m]);
      const float a1 = bf2f(aimg[(nq + 2 * j + 1) * 64 + m]);
      const float o0 = rbf(g * a0) + rbf(omg * rbf(b[2 * j]));
      const float o1 = rbf(g * a1) + rbf(omg * rbf(b[2 * j + 1]));
      o[j] = pk_bf16(o0, o1);
    }
    u16* op = out + (size_t)(m0 + m) * DM + n0 + nq;
    *(u32x4*)op = u32x4{o[0], o[1], o[2], o[3]};
    *(u32x4*)(op + 8) = u32x4{o[4], o[5], o[6], o[7]};
  }
  STAMP2(5);
}

extern "C" void kernel_launch(void* const* d_in, const int* in_sizes, int n_in, void* d_out, int out_size,
                              void* d_ws, size_t ws_size, hipStream_t stream) {
  const u16* u = (const u16*)d_in[0];
  const u16* logAr = (const u16*)d_in[1];
  const u16* Aim = (const u16*)d_in[2];
  const u16* Cre = (const u16*)d_in[3];
  const u16* Cim = (const u16*)d_in[4];
  const u16* logdt = (const u16*)d_in[5];
  const u16* Dskip = (const u16*)d_in[6];
  const u16* wg = (const u16*)d_in[8];
  const u16* wu = (const u16*)d_in[9];
  const u16* wd = (const u16*)d_in[10];
  const u16* gw = (const u16*)d_in[11];
  char* ws = (char*)d_ws;
  u16* hbuf = (u16*)ws;
  u16* aT = (u16*)(ws + (size_t)SEQ * FFP * 2);
  float* gbuf = (float*)(ws + (size_t)SEQ * FFP * 2 + (size_t)DM * SEQ * 2);
  k1_kernel<<<512, 256, 0, stream>>>(u, logAr, Aim, Cre, Cim, logdt, Dskip, wg, wu, gw, wd, hbuf, aT, gbuf
      );
  k2_kernel<<<256, 256, 0, stream>>>(hbuf, wd, aT, gbuf, (u16*)d_out
      );
}
```

```cpp
#include <hip/hip_runtime.h>
#include <hip/hip_bf16.h>
#include <stdint.h>
#include <type_traits>

typedef unsigned short u16;
typedef __attribute__((ext_vector_type(8))) short bf16x8;
typedef __attribute__((ext_vector_type(4))) short s16x4;
typedef __attribute__((ext_vector_type(4))) float f32x4;
typedef __attribute__((ext_vector_type(16))) float f32x16;
typedef __attribute__((ext_vector_type(4))) unsigned u32x4;
typedef __attribute__((ext_vector_type(2))) unsigned u32x2;
typedef __attribute__((ext_vector_type(2))) __bf16 bf16x2_t;
typedef __attribute__((ext_vector_type(2))) float f32x2;
typedef __attribute__((address_space(3))) s16x4* lds_s16x4_ptr;

constexpr int SEQ = 2048, DM = 512, NST = 128, FF = 1376, FFP = 1408;
constexpr int NSSM = 256;
constexpr int NKS = 88;

__device__ __forceinline__ unsigned pk_bf16(float lo, float hi) {
  f32x2 v = {lo, hi};
  bf16x2_t b = __builtin_convertvector(v, bf16x2_t);
  return __builtin_bit_cast(unsigned, b);
}
__device__ __forceinline__ float bf_lo(unsigned w) { return __uint_as_float(w << 16); }
__device__ __forceinline__ float bf_hi(unsigned w) { return __uint_as_float(w & 0xffff0000u); }
__device__ __forceinline__ float rbf(float x) { return bf_lo(pk_bf16(x, 0.f)); }
__device__ __forceinline__ float bf2f(u16 v) { return __uint_as_float(((unsigned)v) << 16); }

__device__ __forceinline__ float smul(float a, float b) { float r; asm("v_mul_f32 %0, %1, %2" : "=v"(r) : "v"(a), "v"(b)); return r; }
__device__ __forceinline__ float sadd(float a, float b) { float r; asm("v_add_f32 %0, %1, %2" : "=v"(r) : "v"(a), "v"(b)); return r; }
__device__ __forceinline__ float ssub(float a, float b) { float r; asm("v_sub_f32 %0, %1, %2" : "=v"(r) : "v"(a), "v"(b)); return r; }
__device__ __forceinline__ float sfma(float a, float b, float c) { float r; asm("v_fma_f32 %0, %1, %2, %3" : "=v"(r) : "v"(a), "v"(b), "v"(c)); return r; }
__device__ __forceinline__ float sfms(float a, float b, float c) { float r; asm("v_fma_f32 %0, -%1, %2, %3" : "=v"(r) : "v"(a), "v"(b), "v"(c)); return r; }
__device__ __forceinline__ float sfnn(float a, float b, float c) { float r; asm("v_fma_f32 %0, -%1, %2, -%3" : "=v"(r) : "v"(a), "v"(b), "v"(c)); return r; }
#define TRANS_FENCE8(a, o) asm volatile("s_nop 1" : "+v"(a[o]), "+v"(a[o + 1]), "+v"(a[o + 2]), "+v"(a[o + 3]), "+v"(a[o + 4]), "+v"(a[o + 5]), "+v"(a[o + 6]), "+v"(a[o + 7]))
__device__ __forceinline__ bf16x8 mk8(unsigned a, unsigned b, unsigned c, unsigned d) {
  u32x4 v = {a, b, c, d};
  return __builtin_bit_cast(bf16x8, v);
}
__device__ __forceinline__ bf16x8 cat4(s16x4 a, s16x4 b) {
  return __builtin_shufflevector(a, b, 0, 1, 2, 3, 4, 5, 6, 7);
}
__device__ __forceinline__ s16x4 tr_read(const char* smem_ptr) {
  return __builtin_amdgcn_ds_read_tr16_b64_v4i16((lds_s16x4_ptr)(smem_ptr));
}
__device__ __forceinline__ unsigned lds_addr(const void* p) {
  return (unsigned)(size_t)(const __attribute__((address_space(3))) char*)p;
}
__device__ __forceinline__ void glds16(const void* g, char* l) {
  __builtin_amdgcn_global_load_lds((const unsigned*)g, (unsigned*)l, 16, 0, 0);
}

#define STAMP2(i)
#define DBG2_ARG
#define DBG2_PASS
#define STAMP(i)
#define DBG_ARG
#define DBG_PASS
constexpr int U_COPY_B = 4160;
constexpr int U_CH_B = 4 * U_COPY_B;
constexpr int U_REGION_B = 2 * U_CH_B;
constexpr int KR_PART_B = 4576;
constexpr int KR_OFF = U_REGION_B;
constexpr int KQ_B = 8208;
constexpr int KX_OFF = 2 * KQ_B;
constexpr int SSM_LDS = KR_OFF + 4 * KR_PART_B;
static_assert(KX_OFF + 4 * 1536 <= U_REGION_B, "scratch must fit the U region");

#define DS_TR(dst, addr, off) asm volatile("ds_read_b64_tr_b16 %0, %1 offset:%2" : "=v"(dst) : "v"(addr), "i"(off) : "memory")
#define DS_B128(dst, addr, off) asm volatile("ds_read_b128 %0, %1 offset:%2" : "=v"(dst) : "v"(addr), "i"(off) : "memory")
#define DS_B64(dst, addr, off) asm volatile("ds_read_b64 %0, %1 offset:%2" : "=v"(dst) : "v"(addr), "i"(off) : "memory")
#define MFMA16(acc, a, b) asm volatile("v_mfma_f32_16x16x32_bf16 %0, %1, %2, %0" : "+v"(acc) : "v"(a), "v"(b))

struct ConvFrag {
  s16x4 a[8][2];
  bf16x8 bh[8], bl[8];
};
template <int RI, int K>
__device__ __forceinline__ void conv_read(ConvFrag& f, unsigned ua, unsigned kb) {
  if constexpr (K < 16) {
    DS_B64(f.a[K >> 1][K & 1], ua, 64 * (8 * (K >> 1) + RI) + 8 * (K & 1));
  } else if constexpr (K < 24) {
    DS_B128(f.bh[K - 16], kb, 64 * (56 - 8 * (K - 16) + RI));
  } else {
    DS_B128(f.bl[K - 24], kb, 64 * (56 - 8 * (K - 24) + RI) + KR_PART_B);
  }
}
template <int RI, int K>
__device__ __forceinline__ void conv_read_all(ConvFrag& f, unsigned ua, unsigned kb) {
  conv_read<RI, K>(f, ua, kb);
  if constexpr (K + 1 < 32) conv_read_all<RI, K + 1>(f, ua, kb);
}
#define CONV_WAIT(f)                                                                                                  \
  do {                                                                                                                \
    asm volatile("s_waitcnt lgkmcnt(0)"                                                                               \
                 : "+v"(f.a[0][0]), "+v"(f.a[0][1]), "+v"(f.a[1][0]), "+v"(f.a[1][1]), "+v"(f.a[2][0]), "+v"(f.a[2][1]), \
                   "+v"(f.a[3][0]), "+v"(f.a[3][1]), "+v"(f.a[4][0]), "+v"(f.a[4][1]), "+v"(f.a[5][0]), "+v"(f.a[5][1]), \
                   "+v"(f.a[6][0]), "+v"(f.a[6][1]), "+v"(f.a[7][0]), "+v"(f.a[7][1])::"memory");                     \
    asm volatile("" : "+v"(f.bh[0]), "+v"(f.bh[1]), "+v"(f.bh[2]), "+v"(f.bh[3]), "+v"(f.bh[4]), "+v"(f.bh[5]),        \
                 "+v"(f.bh[6]), "+v"(f.bh[7]), "+v"(f.bl[0]), "+v"(f.bl[1]), "+v"(f.bl[2]), "+v"(f.bl[3]),            \
                 "+v"(f.bl[4]), "+v"(f.bl[5]), "+v"(f.bl[6]), "+v"(f.bl[7])::"memory");                              \
  } while (0)
constexpr int conv_tau(int P) { int t = 0; while (P >= 8 - t) { P -= 8 - t; ++t; } return t; }
constexpr int conv_sig(int P) { int t = 0; while (P >= 8 - t) { P -= 8 - t; ++t; } return P; }
template <int RI, int P>
__device__ __forceinline__ void conv_pairs(f32x4 (&acc)[8], const bf16x8 (&A)[8], ConvFrag& cur, ConvFrag& nxt, unsigned ua,
                                           unsigned kb) {
  constexpr int T = conv_tau(P), SG = conv_sig(P);
  MFMA16(acc[T + SG], A[SG], cur.bl[T]);
  if constexpr (RI < 3 && P < 32) conv_read<RI + 1, P>(nxt, ua, kb);
  MFMA16(acc[T + SG], A[SG], cur.bh[T]);
  if constexpr (P + 1 < 36) conv_pairs<RI, P + 1>(acc, A, cur, nxt, ua, kb);
}
template <int RI>
__device__ __forceinline__ void conv_rho(f32x4 (&acc)[8], ConvFrag& cur, ConvFrag& nxt, unsigned ua, unsigned kb) {
  CONV_WAIT(cur);
  bf16x8 A[8];
#pragma unroll
  for (int i = 0; i < 8; ++i) A[i] = cat4(cur.a[i][0], cur.a[i][1]);
  conv_pairs<RI, 0>(acc, A, cur, nxt, ua, kb);
  if constexpr (RI < 3) conv_rho<RI + 1>(acc, nxt, cur, ua, kb);
}

__device__ __forceinline__ void split_pair(float v0, float v1, unsigned& hi, unsigned& lo) {
  hi = pk_bf16(v0, v1);
  lo = pk_bf16(ssub(v0, bf_lo(hi)), ssub(v1, bf_hi(hi)));
}

__device__ __forceinline__ void ssm_unit(int w, const u16* __restrict__ u, const u16* __restrict__ logAr,
                                         const u16* __restrict__ Aim, const u16* __restrict__ Cre,
                                         const u16* __restrict__ Cim, const u16* __restrict__ logdt,
                                         const u16* __restrict__ Dskip, u16* __restrict__ aT, char* smem DBG_ARG) {
  const int tid = threadIdx.x, lane = tid & 63;
  const int wave = __builtin_amdgcn_readfirstlane(tid >> 6);
  const int ch = wave >> 1, w2 = wave & 1;
  const int d0 = 2 * w;
  const int d = d0 + ch;

  const int pidx = d0 * NST + tid;
  const u16 r_lar = logAr[pidx], r_aim = Aim[pidx], r_cre = Cre[pidx], r_cim = Cim[pidx], r_ldt = logdt[d], r_dsk = Dskip[d];
  const unsigned* u32p = (const unsigned*)(u + d0);
  unsigned uw[8], up[3];
#pragma unroll
  for (int i = 0; i < 3; ++i) up[i] = 0;
  if (lane == 0 && tid > 0) {
#pragma unroll
    for (int i = 0; i < 3; ++i) up[i] = u32p[(size_t)(8 * tid - 3 + i) * (DM / 2)];
  }
  const unsigned uoff = (unsigned)tid * 8192u;
  {
    const float dt = rbf(expf(bf2f(r_ldt)));
    const float are = rbf(expf(bf2f(r_lar)));
    f32x4 prm;
    prm[0] = (-are * dt) * 1.44269504088896340736f;
    prm[1] = (bf2f(r_aim) * dt) * 0.15915494309189533577f;
    prm[2] = 2.0f * (bf2f(r_cre) * dt);
    prm[3] = 2.0f * (bf2f(r_cim) * dt);
    *(f32x4*)(smem + KR_OFF + tid * 16) = prm;
  }
  __syncthreads();

  const int r = lane & 31, h = lane >> 5;
  f32x16 dacc = {};
  {
    int p1i;
    if (r < 8) p1i = r;
    else if (r < 12) p1i = 256 + 2 * (r - 8);
    else if (r < 16) p1i = 512 + 256 * ((r - 12) >> 1) + 4 * ((r - 12) & 1);
    else if (r < 21) p1i = 1024 + 256 * (r - 16);
    else p1i = 0;
    const float p1 = (float)p1i, p2 = (float)(8 * r);
    const char* prm_base = smem + KR_OFF + (ch * 128 + 64 * w2 + 4 * h) * 16;
#pragma unroll
    for (int ks = 0; ks < 8; ++ks) {
      if (ks >= 4) {
        asm volatile("global_load_dword %0, %1, %2 offset:%3" : "=v"(uw[2 * ks - 8]) : "v"(uoff), "s"((const char*)u32p + ((2 * ks - 8) >> 2) * 4096), "i"(((2 * ks - 8) & 3) * 1024) : "memory");
        asm volatile("global_load_dword %0, %1, %2 offset:%3" : "=v"(uw[2 * ks - 7]) : "v"(uoff), "s"((const char*)u32p + ((2 * ks - 7) >> 2) * 4096), "i"(((2 * ks - 7) & 3) * 1024) : "memory");
      }
      f32x4 prm[4];
#pragma unroll
      for (int q = 0; q < 4; ++q) prm[q] = *(const f32x4*)(prm_base + (8 * ks + q) * 16);
      float ea[4], eb[4], fa[4], fb[4];
#pragma unroll
      for (int q = 0; q < 4; ++q) {
        ea[q] = __builtin_amdgcn_exp2f(smul(prm[q][0], p1)); eb[q] = __builtin_amdgcn_exp2f(smul(prm[q][0], p2));
        fa[q] = __builtin_amdgcn_fractf(smul(prm[q][1], p1)); fb[q] = __builtin_amdgcn_fractf(smul(prm[q][1], p2));
      }
      float ac[4], as[4], bc[4], bs[4];
#pragma unroll
      for (int q = 0; q < 4; ++q) {
        ac[q] = __builtin_amdgcn_cosf(fa[q]); as[q] = __builtin_amdgcn_sinf(fa[q]);
        bc[q] = __builtin_amdgcn_cosf(fb[q]); bs[q] = __builtin_amdgcn_sinf(fb[q]);
      }
      asm volatile("s_nop 1" : "+v"(ea[0]), "+v"(ea[1]), "+v"(ea[2]), "+v"(ea[3]), "+v"(eb[0]), "+v"(eb[1]), "+v"(eb[2]), "+v"(eb[3]),
                   "+v"(ac[0]), "+v"(ac[1]), "+v"(ac[2]), "+v"(ac[3]), "+v"(as[0]), "+v"(as[1]), "+v"(as[2]), "+v"(as[3]),
                   "+v"(bc[0]), "+v"(bc[1]), "+v"(bc[2]), "+v"(bc[3]), "+v"(bs[0]), "+v"(bs[1]), "+v"(bs[2]), "+v"(bs[3]));
      unsigned ah[4], al[4], bh[4], bl[4];
#pragma unroll
      for (int q = 0; q < 4; ++q) {
        const float cr = smul(prm[q][2], ea[q]), ci = smul(prm[q][3], ea[q]);
        const float gre = sfms(ci, as[q], smul(cr, ac[q]));
        const float gmi = sfnn(ci, ac[q], smul(cr, as[q]));
        split_pair(gre, gmi, ah[q], al[q]);
        split_pair(smul(bc[q], eb[q]), smul(bs[q], eb[q]), bh[q], bl[q]);
      }
      const bf16x8 Ah = mk8(ah[0], ah[1], ah[2], ah[3]), Al = mk8(al[0], al[1], al[2], al[3]);
      const bf16x8 Bh = mk8(bh[0], bh[1], bh[2], bh[3]), Bl = mk8(bl[0], bl[1], bl[2], bl[3]);
      dacc = __builtin_amdgcn_mfma_f32_32x32x16_bf16(Al, Bh, dacc, 0, 0, 0);
      dacc = __builtin_amdgcn_mfma_f32_32x32x16_bf16(Ah, Bl, dacc, 0, 0, 0);
      dacc = __builtin_amdgcn_mfma_f32_32x32x16_bf16(Ah, Bh, dacc, 0, 0, 0);
    }
  }
  {
    float* xo = (float*)(smem + KX_OFF + wave * 1536) + lane;
    const float* xi = (const float*)(smem + KX_OFF + (wave ^ 1) * 1536) + lane;
    float mine[6];
    if (w2 == 0) {
#pragma unroll
      for (int e = 0; e < 6; ++e) { xo[e * 64] = dacc[6 + e]; mine[e] = dacc[e]; }
    } else {
#pragma unroll
      for (int e = 0; e < 6; ++e) { xo[e * 64] = dacc[e]; mine[e] = dacc[6 + e]; }
    }
    __syncthreads();
    unsigned* Kq = (unsigned*)(smem + ch * KQ_B);
    unsigned pq[6];
#pragma unroll
    for (int e = 0; e < 6; ++e) {
      const float v = sadd(mine[e], xi[e * 64]);
      const unsigned hi = pk_bf16(v, 0.f);
      pq[e] = pk_bf16(0.f, ssub(v, bf_lo(hi))) | hi;
    }
    if (w2 == 0) {
#pragma unroll
      for (int e = 0; e < 4; ++e) Kq[4 * h + e + 8 * r] = pq[e];
      Kq[(h ? 512 : 256) + 8 * r] = pq[4];
      Kq[(h ? 516 : 258) + 8 * r] = pq[5];
    } else {
      Kq[(h ? 768 : 260) + 8 * r] = pq[0];
      Kq[(h ? 772 : 262) + 8 * r] = pq[1];
      if (h == 0) {
#pragma unroll
        for (int e = 0; e < 4; ++e) Kq[1024 + 256 * e + 8 * r] = pq[2 + e];
      } else if (r == 0) {
        Kq[2048] = pq[2];
      }
    }
  }
  __syncthreads();

  {
    const int t = tid & 127;
    const unsigned* Kq = (const unsigned*)(smem + ch * KQ_B);
    unsigned* krh = (unsigned*)(smem + KR_OFF + (ch * 2) * KR_PART_B);
    unsigned* krl = (unsigned*)((char*)krh + KR_PART_B);
    unsigned q0[8], q1[8];
#pragma unroll
    for (int m = 0; m < 8; ++m) {
      const int sh = m < 4 ? 3 : (m < 6 ? 2 : (m < 7 ? 1 : 0));
      const int l0 = 2047 - 2 * (t + 128 * m), l1 = l0 - 1;
      int pa = l0, pb = l1;
      if (sh) {
        pa = (l0 + (1 << (sh - 1)) - 1 + ((l0 >> sh) & 1)) & ~((1 << sh) - 1);
        pb = (l1 + (1 << (sh - 1)) - 1 + ((l1 >> sh) & 1)) & ~((1 << sh) - 1);
      }
      q0[m] = Kq[pa];
      q1[m] = Kq[pb];
    }
#pragma unroll
    for (int m = 0; m < 8; ++m) {
      krh[t + 128 * m] = __builtin_amdgcn_perm(q1[m], q0[m], 0x05040100u);
      krl[t + 128 * m] = __builtin_amdgcn_perm(q1[m], q0[m], 0x07060302u);
    }
    if (t < 120) { krh[1024 + t] = 0; krl[1024 + t] = 0; }
  }
  __syncthreads();

  asm volatile("s_waitcnt vmcnt(0)" : "+v"(uw[0]), "+v"(uw[1]), "+v"(uw[2]), "+v"(uw[3]), "+v"(uw[4]), "+v"(uw[5]), "+v"(uw[6]), "+v"(uw[7])::"memory");
  {
    unsigned e[11];
#pragma unroll
    for (int i = 0; i < 3; ++i) {
      const unsigned nb = __shfl_up(uw[5 + i], 1);
      e[i] = (lane == 0) ? up[i] : nb;
    }
#pragma unroll
    for (int i = 0; i < 8; ++i) e[3 + i] = uw[i];
    unsigned p0[10], p1[10];
#pragma unroll
    for (int i = 0; i < 10; ++i) {
      p0[i] = __builtin_amdgcn_perm(e[i + 1], e[i], 0x05040100u);
      p1[i] = __builtin_amdgcn_perm(e[i + 1], e[i], 0x07060302u);
    }
#pragma unroll
    for (int c = 0; c < 4; ++c) {
      *(u32x4*)(smem + c * U_COPY_B + (tid + 2) * 16) = u32x4{p0[c], p0[c + 2], p0[c + 4], p0[c + 6]};
      *(u32x4*)(smem + U_CH_B + c * U_COPY_B + (tid + 2) * 16) = u32x4{p1[c], p1[c + 2], p1[c + 4], p1[c + 6]};
    }
    if (tid < 16) *(u32x4*)(smem + (tid >> 1) * U_COPY_B + (tid & 1) * 16) = u32x4{0, 0, 0, 0};
  }
  __syncthreads();

  f32x4 acc[8];
#pragma unroll
  for (int t = 0; t < 8; ++t) acc[t] = f32x4{0.f, 0.f, 0.f, 0.f};
  asm volatile("s_nop 4" : "+v"(acc[0]), "+v"(acc[1]), "+v"(acc[2]), "+v"(acc[3]), "+v"(acc[4]), "+v"(acc[5]), "+v"(acc[6]), "+v"(acc[7]));
  {
    const int r16 = lane & 15, hq = lane >> 4;
    const unsigned sb = lds_addr(smem);
    const unsigned ua = sb + ch * U_CH_B + (r16 & 3) * U_COPY_B + 2 * (4 + (r16 & ~3) + 8 * hq) + 64 * 4 * w2;
    const unsigned kb = sb + KR_OFF + (ch * 2) * KR_PART_B + 2 * (2032 - 16 * r16 + 8 * hq) - 64 * 56 + 64 * 4 * w2;
    ConvFrag f0, f1;
    conv_read_all<0, 0>(f0, ua, kb);
    conv_rho<0>(acc, f0, f1, ua, kb);
  }
  asm volatile("s_nop 15" : "+v"(acc[0]), "+v"(acc[1]), "+v"(acc[2]), "+v"(acc[3]), "+v"(acc[4]), "+v"(acc[5]), "+v"(acc[6]), "+v"(acc[7]));
  __syncthreads();
  {
    char* xw = smem + KR_OFF + (ch * 2 + w2) * 4096 + lane * 16;
    const char* xr = smem + KR_OFF + (ch * 2 + (w2 ^ 1)) * 4096 + lane * 16;
    f32x4 y[4];
    if (w2 == 0) {
#pragma unroll
      for (int i = 0; i < 4; ++i) { *(f32x4*)(xw + i * 1024) = acc[4 + i]; y[i] = acc[i]; }
    } else {
#pragma unroll
      for (int i = 0; i < 4; ++i) { *(f32x4*)(xw + i * 1024) = acc[i]; y[i] = acc[4 + i]; }
    }
    __syncthreads();
    const float Dk = bf2f(r_dsk);
#pragma unroll
    for (int i = 0; i < 4; ++i) {
      const f32x4 o = *(const f32x4*)(xr + i * 1024);
      const int l0 = 256 * (4 * w2 + i) + 16 * (lane & 15) + 4 * (lane >> 4);
      const u32x2 uu = *(const u32x2*)(smem + ch * U_CH_B + 3 * U_COPY_B + 2 * (16 + l0));
      const unsigned y01 = pk_bf16(sadd(y[i][0], o[0]), sadd(y[i][1], o[1])), y23 = pk_bf16(sadd(y[i][2], o[2]), sadd(y[i][3], o[3]));
      const unsigned s01 = pk_bf16(smul(bf_lo(uu[0]), Dk), smul(bf_hi(uu[0]), Dk)), s23 = pk_bf16(smul(bf_lo(uu[1]), Dk), smul(bf_hi(uu[1]), Dk));
      { const u32x2 av = u32x2{pk_bf16(sadd(bf_lo(y01), bf_lo(s01)), sadd(bf_hi(y01), bf_hi(s01))),
                                                  pk_bf16(sadd(bf_lo(y23), bf_lo(s23)), sadd(bf_hi(y23), bf_hi(s23)))};
        asm volatile("global_store_dwordx2 %0, %1, off sc1\n\ts_nop 1" ::"v"(aT + (size_t)d * SEQ + l0), "v"(av) : "memory"); }
    }
  }
}

__device__ __forceinline__ float gelu_tanh(float x) {
  const float z = 0.7978845608028654f * (x + 0.044715f * x * x * x);
  const float e = __builtin_amdgcn_exp2f(-2.8853900817779268f * z);
  return x * __builtin_amdgcn_rcpf(1.0f + e);
}

constexpr int G1_WSLOT_B = 12288;
constexpr int G1_X_OFF = 3 * G1_WSLOT_B;
constexpr int G1_LDS = G1_X_OFF + 32768;

constexpr int g1_pos(bool isx, int id) {
  if (isx && id == 0) return 4;
  if (!isx && id == 0) return 7;
  if (!isx && id == 1) return 10;
  if (isx && id == 1) return 14;
  if (!isx && id == 2) return 17;
  if (!isx && id == 3) return 20;
  if (!isx && id == 4) return 23;
  if (isx && id == 2) return 27;
  int n = 27;
  for (int t = 0; t < 32; ++t) {
    if ((t & 1) == 0 && t / 2 + 5 < 16) { n += 3; if (!isx && t / 2 + 5 == id) return n; }
    if ((t & 3) == 1 && t / 4 + 3 < 8) { n += 4; if (isx && t / 4 + 3 == id) return n; }
  }
  return -1;
}
constexpr int g1_issued_before(int step) {
  int n = 27;
  for (int t = 0; t < step; ++t) {
    if ((t & 1) == 0 && t / 2 + 5 < 16) n += 3;
    if ((t & 3) == 1 && t / 4 + 3 < 8) n += 4;
  }
  return n;
}

#define GLOAD128(dst, ptr) asm volatile("global_load_dwordx4 %0, %1, off" : "=v"(dst) : "v"(ptr) : "memory")
#define GLOAD128S(dst, voff, sbase) asm volatile("global_load_dwordx4 %0, %1, %2" : "=v"(dst) : "v"(voff), "s"(sbase) : "memory")
#define GLOAD128SA(dst, voff, sbase) asm volatile("global_load_dwordx4 %0, %1, %2" : "=a"(dst) : "v"(voff), "s"(sbase) : "memory")
#define GLOAD128A(dst, ptr) asm volatile("global_load_dwordx4 %0, %1, off" : "=a"(dst) : "v"(ptr) : "memory")
#define DS_W128(addr, val, off) asm volatile("ds_write_b128 %0, %1 offset:%2" ::"v"(addr), "v"(val), "i"(off) : "memory")
#define MFMA32(acc, a, b) asm volatile("v_mfma_f32_32x32x16_bf16 %0, %1, %2, %0" : "+v"(acc) : "v"(a), "v"(b))

struct G1Ctx {
  const char* wbase;
  const char* xbase;
  unsigned wo[3], xo;
  unsigned woff, woff_o, ww[3], xw, xrd2[2][4];
  unsigned junk[4];
};
struct G1Frag {
  s16x4 w[3][2];
  bf16x8 x[2];
};

#define G1_RD_W(f, t, OFF)                                                        \
  do {                                                                            \
    if ((t) & 1) { DS_TR(f.w[t][0], c.woff_o, (OFF) + (t) * 64); DS_TR(f.w[t][1], c.woff_o, (OFF) + (t) * 64 + 4 * 384); } \
    else { DS_TR(f.w[t][0], c.woff, (OFF) + (t) * 64); DS_TR(f.w[t][1], c.woff, (OFF) + (t) * 64 + 4 * 384); }            \
  } while (0)
#define G1_RD_X(f, mi, N) DS_B128(f.x[mi], c.xrd2[mi][(N) & 3], (((N) >> 2) & 1) * 16384)
template <int N>
__device__ __forceinline__ void g1_read(G1Frag& f, const G1Ctx& c) {
  constexpr int OFF = ((N >> 1) % 3) * G1_WSLOT_B + (N & 1) * 16 * 384;
  G1_RD_X(f, 0, N); G1_RD_X(f, 1, N);
  G1_RD_W(f, 0, OFF); G1_RD_W(f, 1, OFF); G1_RD_W(f, 2, OFF);
}
#define G1_WAIT_FRAG(f, STR)                                                                                       \
  asm volatile(STR : "+v"(f.w[0][0]), "+v"(f.w[0][1]), "+v"(f.w[1][0]), "+v"(f.w[1][1]), "+v"(f.w[2][0]),        \
               "+v"(f.w[2][1]), "+v"(f.x[0]), "+v"(f.x[1])::"memory")

template <int N>
struct G1Step {
  static __device__ __forceinline__ void run(f32x16 (&acc)[6], u32x4 (&xr)[2][4], u32x4 (&wr)[3][3], G1Frag& cur,
                                             G1Frag& nxt, G1Ctx& c) {
    constexpr int ISS = g1_issued_before(N);
    constexpr int S = N >> 1;
    constexpr bool RD = N + 1 < 32;
    constexpr int OFF = (((N + 1) >> 1) % 3) * G1_WSLOT_B + ((N + 1) & 1) * 16 * 384;
    if constexpr ((N & 1) == 0 && N > 0) G1_WAIT_FRAG(cur, "s_waitcnt lgkmcnt(0)\n\ts_barrier");
    else G1_WAIT_FRAG(cur, "s_waitcnt lgkmcnt(0)");
    bf16x8 b[3];
#pragma unroll
    for (int t = 0; t < 3; ++t) b[t] = cat4(cur.w[t][0], cur.w[t][1]);
    MFMA32(acc[0], b[0], cur.x[0]);
    if constexpr (RD) { G1_RD_X(nxt, 0, N + 1); G1_RD_W(nxt, 0, OFF); }
    MFMA32(acc[3], b[0], cur.x[1]);
    if constexpr (RD) { G1_RD_X(nxt, 1, N + 1); G1_RD_W(nxt, 1, OFF); }
    MFMA32(acc[1], b[1], cur.x[0]);
    if constexpr (RD) { G1_RD_W(nxt, 2, OFF); }
    MFMA32(acc[4], b[1], cur.x[1]);
    if constexpr ((N & 1) == 0) {
      if constexpr (S + 2 < 16) {
        constexpr int R = (S + 2) % 3;
        constexpr int NW = ISS - g1_pos(false, S + 2);
        asm volatile("s_waitcnt vmcnt(%3)" : "+v"(wr[R][0]), "+v"(wr[R][1]), "+v"(wr[R][2]) : "i"(NW) : "memory");
#pragma unroll
        for (int i = 0; i < 3; ++i) DS_W128(c.ww[i], wr[R][i], R * G1_WSLOT_B);
      }
    }
    if constexpr ((N & 3) == 1) {
      constexpr int B = N / 4 + 1;
      if constexpr (B < 8) {
        constexpr int NW = ISS - g1_pos(true, B);
        asm volatile("s_waitcnt vmcnt(%4)" : "+v"(xr[B & 1][0]), "+v"(xr[B & 1][1]), "+v"(xr[B & 1][2]), "+v"(xr[B & 1][3]) : "i"(NW) : "memory");
#pragma unroll
        for (int i = 0; i < 4; ++i) DS_W128(c.xw, xr[B & 1][i], (B & 1) * 16384 + i * 1024);
      }
    }
    MFMA32(acc[2], b[2], cur.x[0]);
    if constexpr ((N & 1) == 0 && S + 5 < 16) {
#pragma unroll
      for (int i = 0; i < 3; ++i) GLOAD128S(wr[(S + 5) % 3][i], c.wo[i], c.wbase + (size_t)(S + 5) * 32 * FF * 2);
    }
    if constexpr ((N & 3) == 1 && N / 4 + 3 < 8) {
      constexpr int B = N / 4 + 1;
#pragma unroll
      for (int i = 0; i < 4; ++i) GLOAD128S(xr[B & 1][i], c.xo, c.xbase + (size_t)(8 * i) * DM * 2 + (B + 2) * 128);
    }
    MFMA32(acc[5], b[2], cur.x[1]);
    if constexpr (N + 1 < 32) G1Step<N + 1>::run(acc, xr, wr, nxt, cur, c);
  }
};

__device__ __forceinline__ void gemm1_tile(int t, const u16* __restrict__ x, const u16* __restrict__ wg,
                                           const u16* __restrict__ wu, u16* __restrict__ hf, char* smem DBG_ARG) {
  const int tid = threadIdx.x, lane = tid & 63;
  const int wave = __builtin_amdgcn_readfirstlane(tid >> 6);
  const int fb = t >> 4, mb = t & 15;
  const int f0 = 96 * fb;
  const int wm = wave & 1, isup = wave >> 1;
  G1Ctx c;
  const unsigned sbase = lds_addr(smem);
  c.wbase = (const char*)(isup ? wu : wg);
#pragma unroll
  for (int i = 0; i < 3; ++i) {
    const int id = 192 * (wave & 1) + 64 * i + lane;
    const int row = id / 12, cc = id % 12;
    int col = f0 + 8 * cc;
    if (col > FF - 8) col = FF - 8;
    c.wo[i] = (unsigned)(row * FF + col) * 2u;
    c.ww[i] = sbase + row * 384 + ((cc + 12 * isup) ^ (((row >> 1) & 1) << 2)) * 16;
  }
  c.xbase = (const char*)(x + (size_t)(128 * mb + 32 * wave) * DM);
  c.xo = (unsigned)((lane >> 3) * DM + (lane & 7) * 8) * 2u;
  const unsigned xl = sbase + G1_X_OFF;
  c.xw = xl + (32 * wave + (lane >> 3)) * 128 + (((lane & 7) ^ (lane >> 3)) * 16);
  const int r = lane & 31, h = lane >> 5;
#pragma unroll
  for (int i = 0; i < 4; ++i) {
    c.xrd2[0][i] = xl + (64 * wm + 32 * isup + r) * 128 + (((2 * i + h) ^ (r & 7)) * 16);
    c.xrd2[1][i] = xl + (64 * wm + 32 * (1 - isup) + r) * 128 + (((2 * i + h) ^ (r & 7)) * 16);
  }
  const int blk = (lane >> 4) & 1, q = (lane & 15) >> 2, p = lane & 3;
  const int sw = (q >> 1) & 1;
  {
    const unsigned e = sbase + (8 * h + q) * 384 + (2 * blk + (p >> 1) + 4 * sw) * 16 + 8 * (p & 1);
    const unsigned o = sbase + (8 * h + q) * 384 + (2 * blk + (p >> 1) - 4 * sw) * 16 + 8 * (p & 1);
    c.woff = isup ? o + 192 : e;
    c.woff_o = isup ? e + 192 : o;
  }

  f32x16 acc[6];
#pragma unroll
  for (int i = 0; i < 6; ++i) acc[i] = f32x16{};
  asm volatile("s_nop 4" : "+v"(acc[0]), "+v"(acc[1]), "+v"(acc[2]), "+v"(acc[3]), "+v"(acc[4]), "+v"(acc[5]));
  u32x4 xr[2][4], wr[3][3];
  G1Frag fa, fb2;
#define G1_ISSUE_X(R, B) _Pragma("unroll") for (int i = 0; i < 4; ++i) GLOAD128S(xr[R][i], c.xo, c.xbase + (size_t)(8 * i) * DM * 2 + (B) * 128)
#define G1_ISSUE_W(R, S) _Pragma("unroll") for (int i = 0; i < 3; ++i) GLOAD128S(wr[R][i], c.wo[i], c.wbase + (size_t)(S) * 32 * FF * 2)
  G1_ISSUE_X(0, 0); G1_ISSUE_W(0, 0); G1_ISSUE_W(1, 1); G1_ISSUE_X(1, 1); G1_ISSUE_W(2, 2);
#define G1_EXTRA 0
  asm volatile("s_waitcnt vmcnt(%10)"
               : "+v"(wr[0][0]), "+v"(wr[0][1]), "+v"(wr[0][2]), "+v"(wr[1][0]), "+v"(wr[1][1]), "+v"(wr[1][2]),
                 "+v"(xr[0][0]), "+v"(xr[0][1]), "+v"(xr[0][2]), "+v"(xr[0][3]) : "i"(7 + G1_EXTRA) : "memory");
#pragma unroll
  for (int i = 0; i < 3; ++i) { DS_W128(c.ww[i], wr[0][i], 0); DS_W128(c.ww[i], wr[1][i], G1_WSLOT_B); }
#pragma unroll
  for (int i = 0; i < 4; ++i) DS_W128(c.xw, xr[0][i], i * 1024);
  G1_ISSUE_W(0, 3); G1_ISSUE_W(1, 4); G1_ISSUE_X(0, 2);
  asm volatile("s_waitcnt lgkmcnt(0)\n\ts_barrier" ::: "memory");
  g1_read<0>(fa, c);
  G1Step<0>::run(acc, xr, wr, fa, fb2, c);
  asm volatile("s_nop 15\n\ts_nop 15" : "+v"(acc[0]), "+v"(acc[1]), "+v"(acc[2]), "+v"(acc[3]), "+v"(acc[4]), "+v"(acc[5]));

  unsigned keep[3][8];
  {
    unsigned give[3][8];
#pragma unroll
    for (int tt = 0; tt < 3; ++tt)
#pragma unroll
      for (int e = 0; e < 8; ++e) give[tt][e] = pk_bf16(acc[3 + tt][2 * e], acc[3 + tt][2 * e + 1]);
    __syncthreads();
    char* xo = smem + wave * 6144 + lane * 16;
#pragma unroll
    for (int tt = 0; tt < 3; ++tt) {
      *(u32x4*)(xo + (2 * tt) * 1024) = u32x4{give[tt][0], give[tt][1], give[tt][2], give[tt][3]};
      *(u32x4*)(xo + (2 * tt + 1) * 1024) = u32x4{give[tt][4], give[tt][5], give[tt][6], give[tt][7]};
    }
  }
#pragma unroll
  for (int tt = 0; tt < 3; ++tt)
#pragma unroll
    for (int e = 0; e < 8; ++e) keep[tt][e] = pk_bf16(acc[tt][2 * e], acc[tt][2 * e + 1]);
  __syncthreads();
  {
    const char* xi = smem + (wave ^ 2) * 6144 + lane * 16;
    const int mt = 4 * mb + 2 * wm + isup;
    u16* hp = hf + ((size_t)(mt * NKS + 6 * fb) * 64 + lane) * 8;
    auto finish = [&](auto ISUP) {
#pragma unroll
      for (int tt = 0; tt < 3; ++tt) {
        const int ft = 3 * fb + tt;
        if (ft < FF / 32) {
          const u32x4 o0 = *(const u32x4*)(xi + (2 * tt) * 1024);
          const u32x4 o1 = *(const u32x4*)(xi + (2 * tt + 1) * 1024);
          unsigned gp[8], up[8];
#pragma unroll
          for (int e = 0; e < 8; ++e) {
            const unsigned ot = e < 4 ? o0[e & 3] : o1[e & 3];
            gp[e] = decltype(ISUP)::value ? ot : keep[tt][e];
            up[e] = decltype(ISUP)::value ? keep[tt][e] : ot;
          }
          float xg[16], ar[16];
#pragma unroll
          for (int e = 0; e < 8; ++e) { xg[2 * e] = bf_lo(gp[e]); xg[2 * e + 1] = bf_hi(gp[e]); }
          const float c1 = -2.30220819814f, c2 = -0.10294323958f, one = 1.0f;
#pragma unroll
          for (int e = 0; e < 16; ++e) ar[e] = smul(xg[e], xg[e]);
#pragma unroll
          for (int e = 0; e < 16; ++e) ar[e] = sfma(ar[e], c2, c1);
#pragma unroll
          for (int e = 0; e < 16; ++e) ar[e] = __builtin_amdgcn_exp2f(smul(ar[e], xg[e]));
          TRANS_FENCE8(ar, 0);
          TRANS_FENCE8(ar, 8);
#pragma unroll
          for (int e = 0; e < 16; ++e) ar[e] = __builtin_amdgcn_rcpf(sadd(ar[e], one));
          TRANS_FENCE8(ar, 0);
          TRANS_FENCE8(ar, 8);
#pragma unroll
          for (int e = 0; e < 16; ++e) ar[e] = smul(ar[e], xg[e]);
          unsigned o[8];
#pragma unroll
          for (int e = 0; e < 8; ++e) o[e] = pk_bf16(ar[2 * e], ar[2 * e + 1]);
#pragma unroll
          for (int e = 0; e < 8; ++e) o[e] = pk_bf16(smul(bf_lo(o[e]), bf_lo(up[e])), smul(bf_hi(o[e]), bf_hi(up[e])));
          { const u32x4 v0 = u32x4{o[0], o[1], o[2], o[3]}, v1 = u32x4{o[4], o[5], o[6], o[7]};
            asm volatile("global_store_dwordx4 %0, %1, off sc1\n\ts_nop 1" ::"v"(hp + (2 * tt) * 512), "v"(v0) : "memory");
            asm volatile("global_store_dwordx4 %0, %1, off sc1\n\ts_nop 1" ::"v"(hp + (2 * tt + 1) * 512), "v"(v1) : "memory"); }
        } else if (ft < NKS / 2) {
          *(u32x4*)(hp + (2 * tt) * 512) = u32x4{0, 0, 0, 0};
          *(u32x4*)(hp + (2 * tt + 1) * 512) = u32x4{0, 0, 0, 0};
        }
      }
    };
    if (isup) finish(std::integral_constant<bool, true>{});
    else finish(std::integral_constant<bool, false>{});
  }
}

__device__ __forceinline__ void gate_unit(int b, const u16* __restrict__ x, const u16* __restrict__ gw,
                                          float* __restrict__ gout) {
  const int tid = threadIdx.x, lane = tid & 63, wave = tid >> 6;
  const int j = lane & 7, rr = lane >> 3;
  u32x4 gv[8];
#pragma unroll
  for (int i = 0; i < 8; ++i) gv[i] = *(const u32x4*)(gw + (j + 8 * i) * 8);
  const int row0 = b * 128 + wave * 32 + rr;
  u32x4 xv[4][8];
#pragma unroll
  for (int ps = 0; ps < 4; ++ps)
#pragma unroll
    for (int i = 0; i < 8; ++i) xv[ps][i] = *(const u32x4*)(x + (size_t)(row0 + 8 * ps) * DM + (j + 8 * i) * 8);
#pragma unroll
  for (int ps = 0; ps < 4; ++ps) {
    float s0 = 0.f, s1 = 0.f;
#pragma unroll
    for (int i = 0; i < 8; ++i)
#pragma unroll
      for (int e = 0; e < 4; ++e) {
        const unsigned pr = pk_bf16(smul(bf_lo(xv[ps][i][e]), bf_lo(gv[i][e])), smul(bf_hi(xv[ps][i][e]), bf_hi(gv[i][e])));
        s0 = sadd(s0, bf_lo(pr));
        s1 = sadd(s1, bf_hi(pr));
      }
    float s = s0 + s1;
    s += __shfl_xor(s, 1);
    s += __shfl_xor(s, 2);
    s += __shfl_xor(s, 4);
    if (j == 0) {
      const float sb = rbf(s);
      gout[row0 + 8 * ps] = rbf(1.0f / (1.0f + expf(-sb)));
    }
  }
}

__global__ __launch_bounds__(256, 2) void k1_kernel(const u16* __restrict__ u, const u16* __restrict__ logAr,
                                                   const u16* __restrict__ Aim, const u16* __restrict__ Cre,
                                                   const u16* __restrict__ Cim, const u16* __restrict__ logdt,
                                                   const u16* __restrict__ Dskip, const u16* __restrict__ wg,
                                                   const u16* __restrict__ wu, const u16* __restrict__ gw,
                                                   const u16* __restrict__ wd, u16* __restrict__ hbuf, u16* __restrict__ aT,
                                                   float* __restrict__ gout DBG_ARG
                                                   ) {
  __shared__ __attribute__((aligned(4096))) char smem[G1_LDS > SSM_LDS ? G1_LDS : SSM_LDS];
  const int bid = blockIdx.x;
  if (bid < NSSM) {
    ssm_unit(bid, u, logAr, Aim, Cre, Cim, logdt, Dskip, aT, smem DBG_PASS);
    unsigned acc = 0;
    for (int i = (bid >> 3) * 256 + threadIdx.x; i < 11008; i += 32 * 256) acc += ((const unsigned*)wd)[i * 32];
    asm volatile("" ::"v"(acc));
  } else {
    const int xcd = bid & 7, idx = (bid - NSSM) >> 3;
    if (idx < 30) {
      gemm1_tile(30 * xcd + idx, u, wg, wu, hbuf, smem DBG_PASS);
    }
    else gate_unit(2 * xcd + idx - 30, u, gw, gout);
  }
}

constexpr int G2_SLOT_B = 4096;
constexpr int G2_WAVE_B = 2 * G2_SLOT_B;
constexpr int G2_PITCH = 272;
constexpr int G2_A_OFF = 4 * 64 * G2_PITCH;
constexpr int K2_LDS = G2_A_OFF + 8192;
#define G2D 3
constexpr int G2_DEPTH = G2D;

struct G2Ctx {
  const u16* hsrc;
  const u16* wd;
  int wrow0, wcol;
  unsigned wb32, woff, ww;
};

template <int ST>
struct G2Step {
  static __device__ __forceinline__ void issue(bf16x8 (&hfr)[11][4], u32x4 (&wr)[11][4], const G2Ctx& c) {
#pragma unroll
    for (int i = 0; i < 4; ++i) {
      int kr = c.wrow0 + ST * 32 + 8 * i;
      kr = kr < FF ? kr : FF - 1;
      GLOAD128(wr[ST][i], c.wd + (size_t)kr * DM + c.wcol);
    }
#pragma unroll
    for (int mi = 0; mi < 2; ++mi)
#pragma unroll
      for (int i = 0; i < 2; ++i)
        GLOAD128A(hfr[ST][2 * mi + i], c.hsrc + (size_t)(mi * NKS + 2 * ST + i) * 512);

  }
  static __device__ __forceinline__ void run(f32x16 (&acc)[2][2], bf16x8 (&hfr)[11][4], u32x4 (&wr)[11][4], const G2Ctx& c) {
    constexpr int LAST = (ST + G2_DEPTH - 1 < 10) ? ST + G2_DEPTH - 1 : 10;
    constexpr int NW = 8 * (LAST - ST);
    asm volatile("s_waitcnt vmcnt(%4)" : "+v"(wr[ST][0]), "+v"(wr[ST][1]), "+v"(wr[ST][2]), "+v"(wr[ST][3]) : "i"(NW + 4) : "memory");
#pragma unroll
    for (int i = 0; i < 4; ++i) DS_W128(c.ww, wr[ST][i], (ST & 1) * G2_SLOT_B + i * 1024);
    asm volatile("s_waitcnt lgkmcnt(0)" ::: "memory");
    const unsigned sb = c.wb32 + c.woff;
    s16x4 w[2][2][2];
#pragma unroll
    for (int i = 0; i < 2; ++i)
#pragma unroll
      for (int t = 0; t < 2; ++t) {
        DS_TR(w[i][t][0], sb ^ (t * 64), (ST & 1) * G2_SLOT_B + i * 2048);
        DS_TR(w[i][t][1], sb ^ (t * 64), (ST & 1) * G2_SLOT_B + i * 2048 + 1024);
      }
    asm volatile("s_waitcnt vmcnt(%4)" : "+a"(hfr[ST][0]), "+a"(hfr[ST][1]), "+a"(hfr[ST][2]), "+a"(hfr[ST][3]) : "i"(NW) : "memory");
    if constexpr (ST + G2_DEPTH <= 10) G2Step<ST + G2_DEPTH>::issue(hfr, wr, c);
    asm volatile("s_waitcnt lgkmcnt(0)"
                 : "+v"(w[0][0][0]), "+v"(w[0][0][1]), "+v"(w[0][1][0]), "+v"(w[0][1][1]), "+v"(w[1][0][0]), "+v"(w[1][0][1]),
                   "+v"(w[1][1][0]), "+v"(w[1][1][1])::"memory");
#pragma unroll
    for (int i = 0; i < 2; ++i)
#pragma unroll
      for (int t = 0; t < 2; ++t)
#pragma unroll
        for (int mi = 0; mi < 2; ++mi)
          acc[t][mi] = __builtin_amdgcn_mfma_f32_32x32x16_bf16(cat4(w[i][t][0], w[i][t][1]), hfr[ST][2 * mi + i], acc[t][mi], 0, 0, 0);
    if constexpr (ST + 1 <= 10) G2Step<ST + 1>::run(acc, hfr, wr, c);
  }
};

__global__ __launch_bounds__(256, 1) void k2_kernel(const u16* __restrict__ hf, const u16* __restrict__ wd,
                                                   const u16* __restrict__ aT, const float* __restrict__ gin,
                                                   u16* __restrict__ out DBG2_ARG) {
  __shared__ __attribute__((aligned(4096))) char smem[K2_LDS];
  const int tid = threadIdx.x, lane = tid & 63;
  STAMP2(0);
  const int wave = __builtin_amdgcn_readfirstlane(tid >> 6);
  const int bid = blockIdx.x;
  const int xcd = bid & 7, idx = bid >> 3;
  const int m0 = 64 * (4 * xcd + (idx >> 3)), n0 = 64 * (idx & 7);

#pragma unroll
  for (int i = 0; i < 2; ++i) {
    const int piece = 2 * wave + i;
    const int drow = 8 * piece + (lane >> 3);
    glds16(aT + (size_t)(n0 + drow) * SEQ + m0 + (lane & 7) * 8, smem + G2_A_OFF + piece * 1024 + lane * 16);
  }

  G2Ctx c;
  c.wb32 = lds_addr(smem) + wave * G2_WAVE_B;
  c.hsrc = hf + ((size_t)((m0 >> 5) * NKS + 22 * wave) * 64 + lane) * 8;
  c.wd = wd;
  c.wcol = n0 + 8 * (lane & 7);
  c.wrow0 = 352 * wave + (lane >> 3);
  c.ww = c.wb32 + (lane >> 3) * 128 + (((lane & 7) ^ (((lane >> 4) & 1) << 2)) * 16);

  const int r = lane & 31, h = lane >> 5;
  const int blk = (lane >> 4) & 1, q = (lane & 15) >> 2, p = lane & 3;
  c.woff = (4 * h + q) * 128 + (((2 * blk + (p >> 1)) ^ (((q >> 1) & 1) << 2)) * 16) + 8 * (p & 1);

  f32x16 acc[2][2];
#pragma unroll
  for (int i = 0; i < 2; ++i)
#pragma unroll
    for (int j = 0; j < 2; ++j) acc[i][j] = f32x16{};
  bf16x8 hfr[11][4];
  u32x4 wr[11][4];

  G2Step<0>::issue(hfr, wr, c);
  G2Step<1>::issue(hfr, wr, c);
  if constexpr (G2_DEPTH > 2) G2Step<2>::issue(hfr, wr, c);
  if constexpr (G2_DEPTH > 3) G2Step<3>::issue(hfr, wr, c);
  if constexpr (G2_DEPTH > 4) G2Step<4>::issue(hfr, wr, c);
  if constexpr (G2_DEPTH > 5) G2Step<5>::issue(hfr, wr, c);
  if constexpr (G2_DEPTH > 6) G2Step<6>::issue(hfr, wr, c);
  STAMP2(1);
  STAMP2(2);
  G2Step<0>::run(acc, hfr, wr, c);
  STAMP2(3);
  __syncthreads();
  {
    char* pw = smem + wave * (64 * G2_PITCH);
#pragma unroll
    for (int c = 0; c < 2; ++c)
#pragma unroll
      for (int mi = 0; mi < 2; ++mi)
#pragma unroll
        for (int gq = 0; gq < 4; ++gq) {
          const f32x4 v = {acc[c][mi][4 * gq], acc[c][mi][4 * gq + 1], acc[c][mi][4 * gq + 2], acc[c][mi][4 * gq + 3]};
          *(f32x4*)(pw + (32 * mi + r) * G2_PITCH + (32 * c + 8 * gq + 4 * h) * 4) = v;
        }
  }
  __syncthreads();
  STAMP2(4);
  {
    const int m = tid >> 2, nq = (tid & 3) * 16;
    const float g = gin[m0 + m];
    const float omg = rbf(1.0f - g);
    float b[16];
#pragma unroll
    for (int j = 0; j < 4; ++j) {
      f32x4 s = *(const f32x4*)(smem + m * G2_PITCH + (nq + 4 * j) * 4);
#pragma unroll
      for (int w = 1; w < 4; ++w) s += *(const f32x4*)(smem + w * (64 * G2_PITCH) + m * G2_PITCH + (nq + 4 * j) * 4);
#pragma unroll
      for (int e = 0; e < 4; ++e) b[4 * j + e] = s[e];
    }
    const u16* aimg = (const u16*)(smem + G2_A_OFF);
    unsigned o[8];
#pragma unroll
    for (int j = 0; j < 8; ++j) {
      const float a0 = bf2f(aimg[(nq + 2 * j) * 64 + m]);
      const float a1 = bf2f(aimg[(nq + 2 * j + 1) * 64 + m]);
      const float o0 = rbf(g * a0) + rbf(omg * rbf(b[2 * j]));
      const float o1 = rbf(g * a1) + rbf(omg * rbf(b[2 * j + 1]));
      o[j] = pk_bf16(o0, o1);
    }
    u16* op = out + (size_t)(m0 + m) * DM + n0 + nq;
    *(u32x4*)op = u32x4{o[0], o[1], o[2], o[3]};
    *(u32x4*)(op + 8) = u32x4{o[4], o[5], o[6], o[7]};
  }
  STAMP2(5);
}

extern "C" void kernel_launch(void* const* d_in, const int* in_sizes, int n_in, void* d_out, int out_size,
                              void* d_ws, size_t ws_size, hipStream_t stream) {
  const u16* u = (const u16*)d_in[0];
  const u16* logAr = (const u16*)d_in[1];
  const u16* Aim = (const u16*)d_in[2];
  const u16* Cre = (const u16*)d_in[3];
  const u16* Cim = (const u16*)d_in[4];
  const u16* logdt = (const u16*)d_in[5];
  const u16* Dskip = (const u16*)d_in[6];
  const u16* wg = (const u16*)d_in[8];
  const u16* wu = (const u16*)d_in[9];
  const u16* wd = (const u16*)d_in[10];
  const u16* gw = (const u16*)d_in[11];
  char* ws = (char*)d_ws;
  u16* hbuf = (u16*)ws;
  u16* aT = (u16*)(ws + (size_t)SEQ * FFP * 2);
  float* gbuf = (float*)(ws + (size_t)SEQ * FFP * 2 + (size_t)DM * SEQ * 2);
  k1_kernel<<<512, 256, 0, stream>>>(u, logAr, Aim, Cre, Cim, logdt, Dskip, wg, wu, gw, wd, hbuf, aT, gbuf
      );
  k2_kernel<<<256, 256, 0, stream>>>(hbuf, wd, aT, gbuf, (u16*)d_out
      );
}
```

```cpp
#include <hip/hip_runtime.h>
#include <hip/hip_bf16.h>
#include <stdint.h>
#include <type_traits>

typedef unsigned short u16;
typedef __attribute__((ext_vector_type(8))) short bf16x8;
typedef __attribute__((ext_vector_type(4))) short s16x4;
typedef __attribute__((ext_vector_type(4))) float f32x4;
typedef __attribute__((ext_vector_type(16))) float f32x16;
typedef __attribute__((ext_vector_type(4))) unsigned u32x4;
typedef __attribute__((ext_vector_type(2))) unsigned u32x2;
typedef __attribute__((ext_vector_type(2))) __bf16 bf16x2_t;
typedef __attribute__((ext_vector_type(2))) float f32x2;
typedef __attribute__((address_space(3))) s16x4* lds_s16x4_ptr;

constexpr int SEQ = 2048, DM = 512, NST = 128, FF = 1376, FFP = 1408;
constexpr int NSSM = 256;
constexpr int NKS = 88;

__device__ __forceinline__ unsigned pk_bf16(float lo, float hi) {
  f32x2 v = {lo, hi};
  bf16x2_t b = __builtin_convertvector(v, bf16x2_t);
  return __builtin_bit_cast(unsigned, b);
}
__device__ __forceinline__ float bf_lo(unsigned w) { return __uint_as_float(w << 16); }
__device__ __forceinline__ float bf_hi(unsigned w) { return __uint_as_float(w & 0xffff0000u); }
__device__ __forceinline__ float rbf(float x) { return bf_lo(pk_bf16(x, 0.f)); }
__device__ __forceinline__ float bf2f(u16 v) { return __uint_as_float(((unsigned)v) << 16); }

__device__ __forceinline__ float smul(float a, float b) { float r; asm("v_mul_f32 %0, %1, %2" : "=v"(r) : "v"(a), "v"(b)); return r; }
__device__ __forceinline__ float sadd(float a, float b) { float r; asm("v_add_f32 %0, %1, %2" : "=v"(r) : "v"(a), "v"(b)); return r; }
__device__ __forceinline__ float ssub(float a, float b) { float r; asm("v_sub_f32 %0, %1, %2" : "=v"(r) : "v"(a), "v"(b)); return r; }
__device__ __forceinline__ float sfma(float a, float b, float c) { float r; asm("v_fma_f32 %0, %1, %2, %3" : "=v"(r) : "v"(a), "v"(b), "v"(c)); return r; }
__device__ __forceinline__ float sfms(float a, float b, float c) { float r; asm("v_fma_f32 %0, -%1, %2, %3" : "=v"(r) : "v"(a), "v"(b), "v"(c)); return r; }
__device__ __forceinline__ float sfnn(float a, float b, float c) { float r; asm("v_fma_f32 %0, -%1, %2, -%3" : "=v"(r) : "v"(a), "v"(b), "v"(c)); return r; }
#define TRANS_FENCE8(a, o) asm volatile("s_nop 1" : "+v"(a[o]), "+v"(a[o + 1]), "+v"(a[o + 2]), "+v"(a[o + 3]), "+v"(a[o + 4]), "+v"(a[o + 5]), "+v"(a[o + 6]), "+v"(a[o + 7]))
__device__ __forceinline__ bf16x8 mk8(unsigned a, unsigned b, unsigned c, unsigned d) {
  u32x4 v = {a, b, c, d};
  return __builtin_bit_cast(bf16x8, v);
}
__device__ __forceinline__ bf16x8 cat4(s16x4 a, s16x4 b) {
  return __builtin_shufflevector(a, b, 0, 1, 2, 3, 4, 5, 6, 7);
}
__device__ __forceinline__ s16x4 tr_read(const char* smem_ptr) {
  return __builtin_amdgcn_ds_read_tr16_b64_v4i16((lds_s16x4_ptr)(smem_ptr));
}
__device__ __forceinline__ unsigned lds_addr(const void* p) {
  return (unsigned)(size_t)(const __attribute__((address_space(3))) char*)p;
}
__device__ __forceinline__ void glds16(const void* g, char* l) {
  __builtin_amdgcn_global_load_lds((const unsigned*)g, (unsigned*)l, 16, 0, 0);
}

#define STAMP2(i)
#define DBG2_ARG
#define DBG2_PASS
#define STAMP(i)
#define DBG_ARG
#define DBG_PASS
constexpr int U_COPY_B = 4160;
constexpr int U_CH_B = 4 * U_COPY_B;
constexpr int U_REGION_B = 2 * U_CH_B;
constexpr int KR_PART_B = 4576;
constexpr int KR_OFF = U_REGION_B;
constexpr int KQ_B = 8208;
constexpr int KX_OFF = 2 * KQ_B;
constexpr int SSM_LDS = KR_OFF + 4 * KR_PART_B;
static_assert(KX_OFF + 4 * 1536 <= U_REGION_B, "scratch must fit the U region");

#define DS_TR(dst, addr, off) asm volatile("ds_read_b64_tr_b16 %0, %1 offset:%2" : "=v"(dst) : "v"(addr), "i"(off) : "memory")
#define DS_B128(dst, addr, off) asm volatile("ds_read_b128 %0, %1 offset:%2" : "=v"(dst) : "v"(addr), "i"(off) : "memory")
#define DS_B64(dst, addr, off) asm volatile("ds_read_b64 %0, %1 offset:%2" : "=v"(dst) : "v"(addr), "i"(off) : "memory")
#define MFMA16(acc, a, b) asm volatile("v_mfma_f32_16x16x32_bf16 %0, %1, %2, %0" : "+v"(acc) : "v"(a), "v"(b))

struct ConvFrag {
  s16x4 a[8][2];
  bf16x8 bh[8], bl[8];
};
template <int RI, int K>
__device__ __forceinline__ void conv_read(ConvFrag& f, unsigned ua, unsigned kb) {
  if constexpr (K < 16) {
    DS_B64(f.a[K >> 1][K & 1], ua, 64 * (8 * (K >> 1) + RI) + 8 * (K & 1));
  } else if constexpr (K < 24) {
    DS_B128(f.bh[K - 16], kb, 64 * (56 - 8 * (K - 16) + RI));
  } else {
    DS_B128(f.bl[K - 24], kb, 64 * (56 - 8 * (K - 24) + RI) + KR_PART_B);
  }
}
template <int RI, int K>
__device__ __forceinline__ void conv_read_all(ConvFrag& f, unsigned ua, unsigned kb) {
  conv_read<RI, K>(f, ua, kb);
  if constexpr (K + 1 < 32) conv_read_all<RI, K + 1>(f, ua, kb);
}
#define CONV_WAIT(f)                                                                                                  \
  do {                                                                                                                \
    asm volatile("s_waitcnt lgkmcnt(0)"                                                                               \
                 : "+v"(f.a[0][0]), "+v"(f.a[0][1]), "+v"(f.a[1][0]), "+v"(f.a[1][1]), "+v"(f.a[2][0]), "+v"(f.a[2][1]), \
                   "+v"(f.a[3][0]), "+v"(f.a[3][1]), "+v"(f.a[4][0]), "+v"(f.a[4][1]), "+v"(f.a[5][0]), "+v"(f.a[5][1]), \
                   "+v"(f.a[6][0]), "+v"(f.a[6][1]), "+v"(f.a[7][0]), "+v"(f.a[7][1])::"memory");                     \
    asm volatile("" : "+v"(f.bh[0]), "+v"(f.bh[1]), "+v"(f.bh[2]), "+v"(f.bh[3]), "+v"(f.bh[4]), "+v"(f.bh[5]),        \
                 "+v"(f.bh[6]), "+v"(f.bh[7]), "+v"(f.bl[0]), "+v"(f.bl[1]), "+v"(f.bl[2]), "+v"(f.bl[3]),            \
                 "+v"(f.bl[4]), "+v"(f.bl[5]), "+v"(f.bl[6]), "+v"(f.bl[7])::"memory");                              \
  } while (0)
constexpr int conv_tau(int P) { int t = 0; while (P >= 8 - t) { P -= 8 - t; ++t; } return t; }
constexpr int conv_sig(int P) { int t = 0; while (P >= 8 - t) { P -= 8 - t; ++t; } return P; }
template <int RI, int P>
__device__ __forceinline__ void conv_pairs(f32x4 (&acc)[8], const bf16x8 (&A)[8], ConvFrag& cur, ConvFrag& nxt, unsigned ua,
                                           unsigned kb) {
  constexpr int T = conv_tau(P), SG = conv_sig(P);
  MFMA16(acc[T + SG], A[SG], cur.bl[T]);
  if constexpr (RI < 3 && P < 32) conv_read<RI + 1, P>(nxt, ua, kb);
  MFMA16(acc[T + SG], A[SG], cur.bh[T]);
  if constexpr (P + 1 < 36) conv_pairs<RI, P + 1>(acc, A, cur, nxt, ua, kb);
}
template <int RI>
__device__ __forceinline__ void conv_rho(f32x4 (&acc)[8], ConvFrag& cur, ConvFrag& nxt, unsigned ua, unsigned kb) {
  CONV_WAIT(cur);
  bf16x8 A[8];
#pragma unroll
  for (int i = 0; i < 8; ++i) A[i] = cat4(cur.a[i][0], cur.a[i][1]);
  conv_pairs<RI, 0>(acc, A, cur, nxt, ua, kb);
  if constexpr (RI < 3) conv_rho<RI + 1>(acc, nxt, cur, ua, kb);
}

__device__ __forceinline__ void split_pair(float v0, float v1, unsigned& hi, unsigned& lo) {
  hi = pk_bf16(v0, v1);
  lo = pk_bf16(ssub(v0, bf_lo(hi)), ssub(v1, bf_hi(hi)));
}

__device__ __forceinline__ void ssm_unit(int w, const u16* __restrict__ u, const u16* __restrict__ logAr,
                                         const u16* __restrict__ Aim, const u16* __restrict__ Cre,
                                         const u16* __restrict__ Cim, const u16* __restrict__ logdt,
                                         const u16* __restrict__ Dskip, u16* __restrict__ aT, char* smem DBG_ARG) {
  const int tid = threadIdx.x, lane = tid & 63;
  const int wave = __builtin_amdgcn_readfirstlane(tid >> 6);
  const int ch = wave >> 1, w2 = wave & 1;
  const int d0 = 64 * (w & 7) + 2 * (w >> 3);
  const int d = d0 + ch;

  const int pidx = d0 * NST + tid;
  const u16 r_lar = logAr[pidx], r_aim = Aim[pidx], r_cre = Cre[pidx], r_cim = Cim[pidx], r_ldt = logdt[d], r_dsk = Dskip[d];
  const unsigned* u32p = (const unsigned*)(u + d0);
  unsigned uw[8], up[3];
#pragma unroll
  for (int i = 0; i < 3; ++i) up[i] = 0;
  if (lane == 0 && tid > 0) {
#pragma unroll
    for (int i = 0; i < 3; ++i) up[i] = u32p[(size_t)(8 * tid - 3 + i) * (DM / 2)];
  }
  const unsigned uoff = (unsigned)tid * 8192u;
  {
    const float dt = rbf(expf(bf2f(r_ldt)));
    const float are = rbf(expf(bf2f(r_lar)));
    f32x4 prm;
    prm[0] = (-are * dt) * 1.44269504088896340736f;
    prm[1] = (bf2f(r_aim) * dt) * 0.15915494309189533577f;
    prm[2] = 2.0f * (bf2f(r_cre) * dt);
    prm[3] = 2.0f * (bf2f(r_cim) * dt);
    *(f32x4*)(smem + KR_OFF + tid * 16) = prm;
  }
  __syncthreads();

  const int r = lane & 31, h = lane >> 5;
  f32x16 dacc = {};
  {
    int p1i;
    if (r < 8) p1i = r;
    else if (r < 12) p1i = 256 + 2 * (r - 8);
    else if (r < 16) p1i = 512 + 256 * ((r - 12) >> 1) + 4 * ((r - 12) & 1);
    else if (r < 21) p1i = 1024 + 256 * (r - 16);
    else p1i = 0;
    const float p1 = (float)p1i, p2 = (float)(8 * r);
    const char* prm_base = smem + KR_OFF + (ch * 128 + 64 * w2 + 4 * h) * 16;
#pragma unroll
    for (int ks = 0; ks < 8; ++ks) {
#define EXP_ULATE
      f32x4 prm[4];
#pragma unroll
      for (int q = 0; q < 4; ++q) prm[q] = *(const f32x4*)(prm_base + (8 * ks + q) * 16);
      float ea[4], eb[4], fa[4], fb[4];
#pragma unroll
      for (int q = 0; q < 4; ++q) {
        ea[q] = __builtin_amdgcn_exp2f(smul(prm[q][0], p1)); eb[q] = __builtin_amdgcn_exp2f(smul(prm[q][0], p2));
        fa[q] = __builtin_amdgcn_fractf(smul(prm[q][1], p1)); fb[q] = __builtin_amdgcn_fractf(smul(prm[q][1], p2));
      }
      float ac[4], as[4], bc[4], bs[4];
#pragma unroll
      for (int q = 0; q < 4; ++q) {
        ac[q] = __builtin_amdgcn_cosf(fa[q]); as[q] = __builtin_amdgcn_sinf(fa[q]);
        bc[q] = __builtin_amdgcn_cosf(fb[q]); bs[q] = __builtin_amdgcn_sinf(fb[q]);
      }
      asm volatile("s_nop 1" : "+v"(ea[0]), "+v"(ea[1]), "+v"(ea[2]), "+v"(ea[3]), "+v"(eb[0]), "+v"(eb[1]), "+v"(eb[2]), "+v"(eb[3]),
                   "+v"(ac[0]), "+v"(ac[1]), "+v"(ac[2]), "+v"(ac[3]), "+v"(as[0]), "+v"(as[1]), "+v"(as[2]), "+v"(as[3]),
                   "+v"(bc[0]), "+v"(bc[1]), "+v"(bc[2]), "+v"(bc[3]), "+v"(bs[0]), "+v"(bs[1]), "+v"(bs[2]), "+v"(bs[3]));
      unsigned ah[4], al[4], bh[4], bl[4];
#pragma unroll
      for (int q = 0; q < 4; ++q) {
        const float cr = smul(prm[q][2], ea[q]), ci = smul(prm[q][3], ea[q]);
        const float gre = sfms(ci, as[q], smul(cr, ac[q]));
        const float gmi = sfnn(ci, ac[q], smul(cr, as[q]));
        split_pair(gre, gmi, ah[q], al[q]);
        split_pair(smul(bc[q], eb[q]), smul(bs[q], eb[q]), bh[q], bl[q]);
      }
      const bf16x8 Ah = mk8(ah[0], ah[1], ah[2], ah[3]), Al = mk8(al[0], al[1], al[2], al[3]);
      const bf16x8 Bh = mk8(bh[0], bh[1], bh[2], bh[3]), Bl = mk8(bl[0], bl[1], bl[2], bl[3]);
      dacc = __builtin_amdgcn_mfma_f32_32x32x16_bf16(Al, Bh, dacc, 0, 0, 0);
      dacc = __builtin_amdgcn_mfma_f32_32x32x16_bf16(Ah, Bl, dacc, 0, 0, 0);
      dacc = __builtin_amdgcn_mfma_f32_32x32x16_bf16(Ah, Bh, dacc, 0, 0, 0);
    }
  }
#pragma unroll
  for (int ks = 0; ks < 8; ++ks)
    asm volatile("global_load_dword %0, %1, %2 offset:%3" : "=v"(uw[ks]) : "v"(uoff), "s"((const char*)u32p + (ks >> 2) * 4096), "i"((ks & 3) * 1024) : "memory");
  {
    float* xo = (float*)(smem + KX_OFF + wave * 1536) + lane;
    const float* xi = (const float*)(smem + KX_OFF + (wave ^ 1) * 1536) + lane;
    float mine[6];
    if (w2 == 0) {
#pragma unroll
      for (int e = 0; e < 6; ++e) { xo[e * 64] = dacc[6 + e]; mine[e] = dacc[e]; }
    } else {
#pragma unroll
      for (int e = 0; e < 6; ++e) { xo[e * 64] = dacc[e]; mine[e] = dacc[6 + e]; }
    }
    __syncthreads();
    unsigned* Kq = (unsigned*)(smem + ch * KQ_B);
    unsigned pq[6];
#pragma unroll
    for (int e = 0; e < 6; ++e) {
      const float v = sadd(mine[e], xi[e * 64]);
      const unsigned hi = pk_bf16(v, 0.f);
      pq[e] = pk_bf16(0.f, ssub(v, bf_lo(hi))) | hi;
    }
    if (w2 == 0) {
#pragma unroll
      for (int e = 0; e < 4; ++e) Kq[4 * h + e + 8 * r] = pq[e];
      Kq[(h ? 512 : 256) + 8 * r] = pq[4];
      Kq[(h ? 516 : 258) + 8 * r] = pq[5];
    } else {
      Kq[(h ? 768 : 260) + 8 * r] = pq[0];
      Kq[(h ? 772 : 262) + 8 * r] = pq[1];
      if (h == 0) {
#pragma unroll
        for (int e = 0; e < 4; ++e) Kq[1024 + 256 * e + 8 * r] = pq[2 + e];
      } else if (r == 0) {
        Kq[2048] = pq[2];
      }
    }
  }
  __syncthreads();

  {
    const int t = tid & 127;
    const unsigned* Kq = (const unsigned*)(smem + ch * KQ_B);
    unsigned* krh = (unsigned*)(smem + KR_OFF + (ch * 2) * KR_PART_B);
    unsigned* krl = (unsigned*)((char*)krh + KR_PART_B);
    unsigned q0[8], q1[8];
#pragma unroll
    for (int m = 0; m < 8; ++m) {
      const int sh = m < 4 ? 3 : (m < 6 ? 2 : (m < 7 ? 1 : 0));
      const int l0 = 2047 - 2 * (t + 128 * m), l1 = l0 - 1;
      int pa = l0, pb = l1;
      if (sh) {
        pa = (l0 + (1 << (sh - 1)) - 1 + ((l0 >> sh) & 1)) & ~((1 << sh) - 1);
        pb = (l1 + (1 << (sh - 1)) - 1 + ((l1 >> sh) & 1)) & ~((1 << sh) - 1);
      }
      q0[m] = Kq[pa];
      q1[m] = Kq[pb];
    }
#pragma unroll
    for (int m = 0; m < 8; ++m) {
      krh[t + 128 * m] = __builtin_amdgcn_perm(q1[m], q0[m], 0x05040100u);
      krl[t + 128 * m] = __builtin_amdgcn_perm(q1[m], q0[m], 0x07060302u);
    }
    if (t < 120) { krh[1024 + t] = 0; krl[1024 + t] = 0; }
  }
  __syncthreads();

  asm volatile("s_waitcnt vmcnt(0)" : "+v"(uw[0]), "+v"(uw[1]), "+v"(uw[2]), "+v"(uw[3]), "+v"(uw[4]), "+v"(uw[5]), "+v"(uw[6]), "+v"(uw[7])::"memory");
  {
    unsigned e[11];
#pragma unroll
    for (int i = 0; i < 3; ++i) {
      const unsigned nb = __shfl_up(uw[5 + i], 1);
      e[i] = (lane == 0) ? up[i] : nb;
    }
#pragma unroll
    for (int i = 0; i < 8; ++i) e[3 + i] = uw[i];
    unsigned p0[10], p1[10];
#pragma unroll
    for (int i = 0; i < 10; ++i) {
      p0[i] = __builtin_amdgcn_perm(e[i + 1], e[i], 0x05040100u);
      p1[i] = __builtin_amdgcn_perm(e[i + 1], e[i], 0x07060302u);
    }
#pragma unroll
    for (int c = 0; c < 4; ++c) {
      *(u32x4*)(smem + c * U_COPY_B + (tid + 2) * 16) = u32x4{p0[c], p0[c + 2], p0[c + 4], p0[c + 6]};
      *(u32x4*)(smem + U_CH_B + c * U_COPY_B + (tid + 2) * 16) = u32x4{p1[c], p1[c + 2], p1[c + 4], p1[c + 6]};
    }
    if (tid < 16) *(u32x4*)(smem + (tid >> 1) * U_COPY_B + (tid & 1) * 16) = u32x4{0, 0, 0, 0};
  }
  __syncthreads();

  f32x4 acc[8];
#pragma unroll
  for (int t = 0; t < 8; ++t) acc[t] = f32x4{0.f, 0.f, 0.f, 0.f};
  asm volatile("s_nop 4" : "+v"(acc[0]), "+v"(acc[1]), "+v"(acc[2]), "+v"(acc[3]), "+v"(acc[4]), "+v"(acc[5]), "+v"(acc[6]), "+v"(acc[7]));
  {
    const int r16 = lane & 15, hq = lane >> 4;
    const unsigned sb = lds_addr(smem);
    const unsigned ua = sb + ch * U_CH_B + (r16 & 3) * U_COPY_B + 2 * (4 + (r16 & ~3) + 8 * hq) + 64 * 4 * w2;
    const unsigned kb = sb + KR_OFF + (ch * 2) * KR_PART_B + 2 * (2032 - 16 * r16 + 8 * hq) - 64 * 56 + 64 * 4 * w2;
    ConvFrag f0, f1;
    conv_read_all<0, 0>(f0, ua, kb);
    conv_rho<0>(acc, f0, f1, ua, kb);
  }
  asm volatile("s_nop 15" : "+v"(acc[0]), "+v"(acc[1]), "+v"(acc[2]), "+v"(acc[3]), "+v"(acc[4]), "+v"(acc[5]), "+v"(acc[6]), "+v"(acc[7]));
  __syncthreads();
  {
    char* xw = smem + KR_OFF + (ch * 2 + w2) * 4096 + lane * 16;
    const char* xr = smem + KR_OFF + (ch * 2 + (w2 ^ 1)) * 4096 + lane * 16;
    f32x4 y[4];
    if (w2 == 0) {
#pragma unroll
      for (int i = 0; i < 4; ++i) { *(f32x4*)(xw + i * 1024) = acc[4 + i]; y[i] = acc[i]; }
    } else {
#pragma unroll
      for (int i = 0; i < 4; ++i) { *(f32x4*)(xw + i * 1024) = acc[i]; y[i] = acc[4 + i]; }
    }
    __syncthreads();
    const float Dk = bf2f(r_dsk);
#pragma unroll
    for (int i = 0; i < 4; ++i) {
      const f32x4 o = *(const f32x4*)(xr + i * 1024);
      const int l0 = 256 * (4 * w2 + i) + 16 * (lane & 15) + 4 * (lane >> 4);
      const u32x2 uu = *(const u32x2*)(smem + ch * U_CH_B + 3 * U_COPY_B + 2 * (16 + l0));
      const unsigned y01 = pk_bf16(sadd(y[i][0], o[0]), sadd(y[i][1], o[1])), y23 = pk_bf16(sadd(y[i][2], o[2]), sadd(y[i][3], o[3]));
      const unsigned s01 = pk_bf16(smul(bf_lo(uu[0]), Dk), smul(bf_hi(uu[0]), Dk)), s23 = pk_bf16(smul(bf_lo(uu[1]), Dk), smul(bf_hi(uu[1]), Dk));
      { const u32x2 av = u32x2{pk_bf16(sadd(bf_lo(y01), bf_lo(s01)), sadd(bf_hi(y01), bf_hi(s01))),
                                                  pk_bf16(sadd(bf_lo(y23), bf_lo(s23)), sadd(bf_hi(y23), bf_hi(s23)))};
        asm volatile("global_store_dwordx2 %0, %1, off sc1\n\ts_nop 1" ::"v"(aT + (size_t)d * SEQ + l0), "v"(av) : "memory"); }
    }
  }
}

__device__ __forceinline__ float gelu_tanh(float x) {
  const float z = 0.7978845608028654f * (x + 0.044715f * x * x * x);
  const float e = __builtin_amdgcn_exp2f(-2.8853900817779268f * z);
  return x * __builtin_amdgcn_rcpf(1.0f + e);
}

constexpr int G1_WSLOT_B = 12288;
constexpr int G1_X_OFF = 3 * G1_WSLOT_B;
constexpr int G1_LDS = G1_X_OFF + 32768;

constexpr int g1_pos(bool isx, int id) {
  if (isx && id == 0) return 4;
  if (!isx && id == 0) return 7;
  if (!isx && id == 1) return 10;
  if (isx && id == 1) return 14;
  if (!isx && id == 2) return 17;
  if (!isx && id == 3) return 20;
  if (!isx && id == 4) return 23;
  if (isx && id == 2) return 27;
  int n = 27;
  for (int t = 0; t < 32; ++t) {
    if ((t & 1) == 0 && t / 2 + 5 < 16) { n += 3; if (!isx && t / 2 + 5 == id) return n; }
    if ((t & 3) == 1 && t / 4 + 3 < 8) { n += 4; if (isx && t / 4 + 3 == id) return n; }
  }
  return -1;
}
constexpr int g1_issued_before(int step) {
  int n = 27;
  for (int t = 0; t < step; ++t) {
    if ((t & 1) == 0 && t / 2 + 5 < 16) n += 3;
    if ((t & 3) == 1 && t / 4 + 3 < 8) n += 4;
  }
  return n;
}

#define GLOAD128(dst, ptr) asm volatile("global_load_dwordx4 %0, %1, off" : "=v"(dst) : "v"(ptr) : "memory")
#define GLOAD128S(dst, voff, sbase) asm volatile("global_load_dwordx4 %0, %1, %2" : "=v"(dst) : "v"(voff), "s"(sbase) : "memory")
#define GLOAD128SA(dst, voff, sbase) asm volatile("global_load_dwordx4 %0, %1, %2" : "=a"(dst) : "v"(voff), "s"(sbase) : "memory")
#define GLOAD128A(dst, ptr) asm volatile("global_load_dwordx4 %0, %1, off" : "=a"(dst) : "v"(ptr) : "memory")
#define DS_W128(addr, val, off) asm volatile("ds_write_b128 %0, %1 offset:%2" ::"v"(addr), "v"(val), "i"(off) : "memory")
#define MFMA32(acc, a, b) asm volatile("v_mfma_f32_32x32x16_bf16 %0, %1, %2, %0" : "+v"(acc) : "v"(a), "v"(b))

struct G1Ctx {
  const char* wbase;
  const char* xbase;
  unsigned wo[3], xo;
  unsigned woff, woff_o, ww[3], xw, xrd2[2][4];
  unsigned junk[4];
};
struct G1Frag {
  s16x4 w[3][2];
  bf16x8 x[2];
};

#define G1_RD_W(f, t, OFF)                                                        \
  do {                                                                            \
    if ((t) & 1) { DS_TR(f.w[t][0], c.woff_o, (OFF) + (t) * 64); DS_TR(f.w[t][1], c.woff_o, (OFF) + (t) * 64 + 4 * 384); } \
    else { DS_TR(f.w[t][0], c.woff, (OFF) + (t) * 64); DS_TR(f.w[t][1], c.woff, (OFF) + (t) * 64 + 4 * 384); }            \
  } while (0)
#define G1_RD_X(f, mi, N) DS_B128(f.x[mi], c.xrd2[mi][(N) & 3], (((N) >> 2) & 1) * 16384)
template <int N>
__device__ __forceinline__ void g1_read(G1Frag& f, const G1Ctx& c) {
  constexpr int OFF = ((N >> 1) % 3) * G1_WSLOT_B + (N & 1) * 16 * 384;
  G1_RD_X(f, 0, N); G1_RD_X(f, 1, N);
  G1_RD_W(f, 0, OFF); G1_RD_W(f, 1, OFF); G1_RD_W(f, 2, OFF);
}
#define G1_WAIT_FRAG(f, STR)                                                                                       \
  asm volatile(STR : "+v"(f.w[0][0]), "+v"(f.w[0][1]), "+v"(f.w[1][0]), "+v"(f.w[1][1]), "+v"(f.w[2][0]),        \
               "+v"(f.w[2][1]), "+v"(f.x[0]), "+v"(f.x[1])::"memory")

template <int N>
struct G1Step {
  static __device__ __forceinline__ void run(f32x16 (&acc)[6], u32x4 (&xr)[2][4], u32x4 (&wr)[3][3], G1Frag& cur,
                                             G1Frag& nxt, G1Ctx& c) {
    constexpr int ISS = g1_issued_before(N);
    constexpr int S = N >> 1;
    constexpr bool RD = N + 1 < 32;
    constexpr int OFF = (((N + 1) >> 1) % 3) * G1_WSLOT_B + ((N + 1) & 1) * 16 * 384;
    if constexpr ((N & 1) == 0 && N > 0) G1_WAIT_FRAG(cur, "s_waitcnt lgkmcnt(0)\n\ts_barrier");
    else G1_WAIT_FRAG(cur, "s_waitcnt lgkmcnt(0)");
    bf16x8 b[3];
#pragma unroll
    for (int t = 0; t < 3; ++t) b[t] = cat4(cur.w[t][0], cur.w[t][1]);
    MFMA32(acc[0], b[0], cur.x[0]);
    if constexpr (RD) { G1_RD_X(nxt, 0, N + 1); G1_RD_W(nxt, 0, OFF); }
    MFMA32(acc[3], b[0], cur.x[1]);
    if constexpr (RD) { G1_RD_X(nxt, 1, N + 1); G1_RD_W(nxt, 1, OFF); }
    MFMA32(acc[1], b[1], cur.x[0]);
    if constexpr (RD) { G1_RD_W(nxt, 2, OFF); }
    MFMA32(acc[4], b[1], cur.x[1]);
    if constexpr ((N & 1) == 0) {
      if constexpr (S + 2 < 16) {
        constexpr int R = (S + 2) % 3;
        constexpr int NW = ISS - g1_pos(false, S + 2);
        asm volatile("s_waitcnt vmcnt(%3)" : "+v"(wr[R][0]), "+v"(wr[R][1]), "+v"(wr[R][2]) : "i"(NW) : "memory");
#pragma unroll
        for (int i = 0; i < 3; ++i) DS_W128(c.ww[i], wr[R][i], R * G1_WSLOT_B);
      }
    }
    if constexpr ((N & 3) == 1) {
      constexpr int B = N / 4 + 1;
      if constexpr (B < 8) {
        constexpr int NW = ISS - g1_pos(true, B);
        asm volatile("s_waitcnt vmcnt(%4)" : "+v"(xr[B & 1][0]), "+v"(xr[B & 1][1]), "+v"(xr[B & 1][2]), "+v"(xr[B & 1][3]) : "i"(NW) : "memory");
#pragma unroll
        for (int i = 0; i < 4; ++i) DS_W128(c.xw, xr[B & 1][i], (B & 1) * 16384 + i * 1024);
      }
    }
    MFMA32(acc[2], b[2], cur.x[0]);
    if constexpr ((N & 1) == 0 && S + 5 < 16) {
#pragma unroll
      for (int i = 0; i < 3; ++i) GLOAD128S(wr[(S + 5) % 3][i], c.wo[i], c.wbase + (size_t)(S + 5) * 32 * FF * 2);
    }
    if constexpr ((N & 3) == 1 && N / 4 + 3 < 8) {
      constexpr int B = N / 4 + 1;
#pragma unroll
      for (int i = 0; i < 4; ++i) GLOAD128S(xr[B & 1][i], c.xo, c.xbase + (size_t)(8 * i) * DM * 2 + (B + 2) * 128);
    }
    MFMA32(acc[5], b[2], cur.x[1]);
    if constexpr (N + 1 < 32) G1Step<N + 1>::run(acc, xr, wr, nxt, cur, c);
  }
};

__device__ __forceinline__ void gemm1_tile(int t, const u16* __restrict__ x, const u16* __restrict__ wg,
                                           const u16* __restrict__ wu, u16* __restrict__ hf, char* smem DBG_ARG) {
  const int tid = threadIdx.x, lane = tid & 63;
  const int wave = __builtin_amdgcn_readfirstlane(tid >> 6);
  const int fb = t >> 4, mb = t & 15;
  const int f0 = 96 * fb;
  const int wm = wave & 1, isup = wave >> 1;
  G1Ctx c;
  const unsigned sbase = lds_addr(smem);
  c.wbase = (const char*)(isup ? wu : wg);
#pragma unroll
  for (int i = 0; i < 3; ++i) {
    const int id = 192 * (wave & 1) + 64 * i + lane;
    const int row = id / 12, cc = id % 12;
    int col = f0 + 8 * cc;
    if (col > FF - 8) col = FF - 8;
    c.wo[i] = (unsigned)(row * FF + col) * 2u;
    c.ww[i] = sbase + row * 384 + ((cc + 12 * isup) ^ (((row >> 1) & 1) << 2)) * 16;
  }
  c.xbase = (const char*)(x + (size_t)(128 * mb + 32 * wave) * DM);
  c.xo = (unsigned)((lane >> 3) * DM + (lane & 7) * 8) * 2u;
  const unsigned xl = sbase + G1_X_OFF;
  c.xw = xl + (32 * wave + (lane >> 3)) * 128 + (((lane & 7) ^ (lane >> 3)) * 16);
  const int r = lane & 31, h = lane >> 5;
#pragma unroll
  for (int i = 0; i < 4; ++i) {
    c.xrd2[0][i] = xl + (64 * wm + 32 * isup + r) * 128 + (((2 * i + h) ^ (r & 7)) * 16);
    c.xrd2[1][i] = xl + (64 * wm + 32 * (1 - isup) + r) * 128 + (((2 * i + h) ^ (r & 7)) * 16);
  }
  const int blk = (lane >> 4) & 1, q = (lane & 15) >> 2, p = lane & 3;
  const int sw = (q >> 1) & 1;
  {
    const unsigned e = sbase + (8 * h + q) * 384 + (2 * blk + (p >> 1) + 4 * sw) * 16 + 8 * (p & 1);
    const unsigned o = sbase + (8 * h + q) * 384 + (2 * blk + (p >> 1) - 4 * sw) * 16 + 8 * (p & 1);
    c.woff = isup ? o + 192 : e;
    c.woff_o = isup ? e + 192 : o;
  }

  f32x16 acc[6];
#pragma unroll
  for (int i = 0; i < 6; ++i) acc[i] = f32x16{};
  asm volatile("s_nop 4" : "+v"(acc[0]), "+v"(acc[1]), "+v"(acc[2]), "+v"(acc[3]), "+v"(acc[4]), "+v"(acc[5]));
  u32x4 xr[2][4], wr[3][3];
  G1Frag fa, fb2;
#define G1_ISSUE_X(R, B) _Pragma("unroll") for (int i = 0; i < 4; ++i) GLOAD128S(xr[R][i], c.xo, c.xbase + (size_t)(8 * i) * DM * 2 + (B) * 128)
#define G1_ISSUE_W(R, S) _Pragma("unroll") for (int i = 0; i < 3; ++i) GLOAD128S(wr[R][i], c.wo[i], c.wbase + (size_t)(S) * 32 * FF * 2)
  G1_ISSUE_X(0, 0); G1_ISSUE_W(0, 0); G1_ISSUE_W(1, 1); G1_ISSUE_X(1, 1); G1_ISSUE_W(2, 2);
#define G1_EXTRA 0
  asm volatile("s_waitcnt vmcnt(%10)"
               : "+v"(wr[0][0]), "+v"(wr[0][1]), "+v"(wr[0][2]), "+v"(wr[1][0]), "+v"(wr[1][1]), "+v"(wr[1][2]),
                 "+v"(xr[0][0]), "+v"(xr[0][1]), "+v"(xr[0][2]), "+v"(xr[0][3]) : "i"(7 + G1_EXTRA) : "memory");
#pragma unroll
  for (int i = 0; i < 3; ++i) { DS_W128(c.ww[i], wr[0][i], 0); DS_W128(c.ww[i], wr[1][i], G1_WSLOT_B); }
#pragma unroll
  for (int i = 0; i < 4; ++i) DS_W128(c.xw, xr[0][i], i * 1024);
  G1_ISSUE_W(0, 3); G1_ISSUE_W(1, 4); G1_ISSUE_X(0, 2);
  asm volatile("s_waitcnt lgkmcnt(0)\n\ts_barrier" ::: "memory");
  g1_read<0>(fa, c);
  G1Step<0>::run(acc, xr, wr, fa, fb2, c);
  asm volatile("s_nop 15\n\ts_nop 15" : "+v"(acc[0]), "+v"(acc[1]), "+v"(acc[2]), "+v"(acc[3]), "+v"(acc[4]), "+v"(acc[5]));

  unsigned keep[3][8];
  {
    unsigned give[3][8];
#pragma unroll
    for (int tt = 0; tt < 3; ++tt)
#pragma unroll
      for (int e = 0; e < 8; ++e) give[tt][e] = pk_bf16(acc[3 + tt][2 * e], acc[3 + tt][2 * e + 1]);
    __syncthreads();
    char* xo = smem + wave * 6144 + lane * 16;
#pragma unroll
    for (int tt = 0; tt < 3; ++tt) {
      *(u32x4*)(xo + (2 * tt) * 1024) = u32x4{give[tt][0], give[tt][1], give[tt][2], give[tt][3]};
      *(u32x4*)(xo + (2 * tt + 1) * 1024) = u32x4{give[tt][4], give[tt][5], give[tt][6], give[tt][7]};
    }
  }
#pragma unroll
  for (int tt = 0; tt < 3; ++tt)
#pragma unroll
    for (int e = 0; e < 8; ++e) keep[tt][e] = pk_bf16(acc[tt][2 * e], acc[tt][2 * e + 1]);
  __syncthreads();
  {
    const char* xi = smem + (wave ^ 2) * 6144 + lane * 16;
    const int mt = 4 * mb + 2 * wm + isup;
    u16* hp = hf + ((size_t)(mt * NKS + 6 * fb) * 64 + lane) * 8;
    auto finish = [&](auto ISUP) {
#pragma unroll
      for (int tt = 0; tt < 3; ++tt) {
        const int ft = 3 * fb + tt;
        if (ft < FF / 32) {
          const u32x4 o0 = *(const u32x4*)(xi + (2 * tt) * 1024);
          const u32x4 o1 = *(const u32x4*)(xi + (2 * tt + 1) * 1024);
          unsigned gp[8], up[8];
#pragma unroll
          for (int e = 0; e < 8; ++e) {
            const unsigned ot = e < 4 ? o0[e & 3] : o1[e & 3];
            gp[e] = decltype(ISUP)::value ? ot : keep[tt][e];
            up[e] = decltype(ISUP)::value ? keep[tt][e] : ot;
          }
          float xg[16], ar[16];
#pragma unroll
          for (int e = 0; e < 8; ++e) { xg[2 * e] = bf_lo(gp[e]); xg[2 * e + 1] = bf_hi(gp[e]); }
          const float c1 = -2.30220819814f, c2 = -0.10294323958f, one = 1.0f;
#pragma unroll
          for (int e = 0; e < 16; ++e) ar[e] = smul(xg[e], xg[e]);
#pragma unroll
          for (int e = 0; e < 16; ++e) ar[e] = sfma(ar[e], c2, c1);
#pragma unroll
          for (int e = 0; e < 16; ++e) ar[e] = __builtin_amdgcn_exp2f(smul(ar[e], xg[e]));
          TRANS_FENCE8(ar, 0);
          TRANS_FENCE8(ar, 8);
#pragma unroll
          for (int e = 0; e < 16; ++e) ar[e] = __builtin_amdgcn_rcpf(sadd(ar[e], one));
          TRANS_FENCE8(ar, 0);
          TRANS_FENCE8(ar, 8);
#pragma unroll
          for (int e = 0; e < 16; ++e) ar[e] = smul(ar[e], xg[e]);
          unsigned o[8];
#pragma unroll
          for (int e = 0; e < 8; ++e) o[e] = pk_bf16(ar[2 * e], ar[2 * e + 1]);
#pragma unroll
          for (int e = 0; e < 8; ++e) o[e] = pk_bf16(smul(bf_lo(o[e]), bf_lo(up[e])), smul(bf_hi(o[e]), bf_hi(up[e])));
          { const u32x4 v0 = u32x4{o[0], o[1], o[2], o[3]}, v1 = u32x4{o[4], o[5], o[6], o[7]};
            asm volatile("global_store_dwordx4 %0, %1, off sc1\n\ts_nop 1" ::"v"(hp + (2 * tt) * 512), "v"(v0) : "memory");
            asm volatile("global_store_dwordx4 %0, %1, off sc1\n\ts_nop 1" ::"v"(hp + (2 * tt + 1) * 512), "v"(v1) : "memory"); }
        } else if (ft < NKS / 2) {
          *(u32x4*)(hp + (2 * tt) * 512) = u32x4{0, 0, 0, 0};
          *(u32x4*)(hp + (2 * tt + 1) * 512) = u32x4{0, 0, 0, 0};
        }
      }
    };
    if (isup) finish(std::integral_constant<bool, true>{});
    else finish(std::integral_constant<bool, false>{});
  }
}

__device__ __forceinline__ void gate_unit(int b, const u16* __restrict__ x, const u16* __restrict__ gw,
                                          float* __restrict__ gout) {
  const int tid = threadIdx.x, lane = tid & 63, wave = tid >> 6;
  const int j = lane & 7, rr = lane >> 3;
  u32x4 gv[8];
#pragma unroll
  for (int i = 0; i < 8; ++i) gv[i] = *(const u32x4*)(gw + (j + 8 * i) * 8);
  const int row0 = b * 128 + wave * 32 + rr;
  u32x4 xv[4][8];
#pragma unroll
  for (int ps = 0; ps < 4; ++ps)
#pragma unroll
    for (int i = 0; i < 8; ++i) xv[ps][i] = *(const u32x4*)(x + (size_t)(row0 + 8 * ps) * DM + (j + 8 * i) * 8);
#pragma unroll
  for (int ps = 0; ps < 4; ++ps) {
    float s0 = 0.f, s1 = 0.f;
#pragma unroll
    for (int i = 0; i < 8; ++i)
#pragma unroll
      for (int e = 0; e < 4; ++e) {
        const unsigned pr = pk_bf16(smul(bf_lo(xv[ps][i][e]), bf_lo(gv[i][e])), smul(bf_hi(xv[ps][i][e]), bf_hi(gv[i][e])));
        s0 = sadd(s0, bf_lo(pr));
        s1 = sadd(s1, bf_hi(pr));
      }
    float s = s0 + s1;
    s += __shfl_xor(s, 1);
    s += __shfl_xor(s, 2);
    s += __shfl_xor(s, 4);
    if (j == 0) {
      const float sb = rbf(s);
      gout[row0 + 8 * ps] = rbf(1.0f / (1.0f + expf(-sb)));
    }
  }
}

__global__ __launch_bounds__(256, 2) void k1_kernel(const u16* __restrict__ u, const u16* __restrict__ logAr,
                                                   const u16* __restrict__ Aim, const u16* __restrict__ Cre,
                                                   const u16* __restrict__ Cim, const u16* __restrict__ logdt,
                                                   const u16* __restrict__ Dskip, const u16* __restrict__ wg,
                                                   const u16* __restrict__ wu, const u16* __restrict__ gw,
                                                   const u16* __restrict__ wd, u16* __restrict__ hbuf, u16* __restrict__ aT,
                                                   float* __restrict__ gout DBG_ARG
                                                   ) {
  __shared__ __attribute__((aligned(4096))) char smem[G1_LDS > SSM_LDS ? G1_LDS : SSM_LDS];
  const int bid = blockIdx.x;
  if (bid < NSSM) {
    ssm_unit(bid, u, logAr, Aim, Cre, Cim, logdt, Dskip, aT, smem DBG_PASS);
    unsigned acc = 0;
    for (int i = (bid >> 3) * 256 + threadIdx.x; i < 11008; i += 32 * 256) acc += ((const unsigned*)wd)[i * 32];
    asm volatile("" ::"v"(acc));
  } else {
    const int xcd = bid & 7, idx = (bid - NSSM) >> 3;
    if (idx < 30) {
      gemm1_tile(30 * xcd + idx, u, wg, wu, hbuf, smem DBG_PASS);
    }
    else gate_unit(2 * xcd + idx - 30, u, gw, gout);
  }
}

constexpr int G2_SLOT_B = 4096;
constexpr int G2_WAVE_B = 2 * G2_SLOT_B;
constexpr int G2_PITCH = 272;
constexpr int G2_A_OFF = 4 * 64 * G2_PITCH;
constexpr int K2_LDS = G2_A_OFF + 8192;
#define G2D 3
constexpr int G2_DEPTH = G2D;

struct G2Ctx {
  const u16* hsrc;
  const u16* wd;
  int wrow0, wcol;
  unsigned wb32, woff, ww;
};

template <int ST>
struct G2Step {
  static __device__ __forceinline__ void issue(bf16x8 (&hfr)[11][4], u32x4 (&wr)[11][4], const G2Ctx& c) {
#pragma unroll
    for (int i = 0; i < 4; ++i) {
      int kr = c.wrow0 + ST * 32 + 8 * i;
      kr = kr < FF ? kr : FF - 1;
      GLOAD128(wr[ST][i], c.wd + (size_t)kr * DM + c.wcol);
    }
#pragma unroll
    for (int mi = 0; mi < 2; ++mi)
#pragma unroll
      for (int i = 0; i < 2; ++i)
        GLOAD128A(hfr[ST][2 * mi + i], c.hsrc + (size_t)(mi * NKS + 2 * ST + i) * 512);

  }
  static __device__ __forceinline__ void run(f32x16 (&acc)[2][2], bf16x8 (&hfr)[11][4], u32x4 (&wr)[11][4], const G2Ctx& c) {
    constexpr int LAST = (ST + G2_DEPTH - 1 < 10) ? ST + G2_DEPTH - 1 : 10;
    constexpr int NW = 8 * (LAST - ST);
    asm volatile("s_waitcnt vmcnt(%4)" : "+v"(wr[ST][0]), "+v"(wr[ST][1]), "+v"(wr[ST][2]), "+v"(wr[ST][3]) : "i"(NW + 4) : "memory");
#pragma unroll
    for (int i = 0; i < 4; ++i) DS_W128(c.ww, wr[ST][i], (ST & 1) * G2_SLOT_B + i * 1024);
    asm volatile("s_waitcnt lgkmcnt(0)" ::: "memory");
    const unsigned sb = c.wb32 + c.woff;
    s16x4 w[2][2][2];
#pragma unroll
    for (int i = 0; i < 2; ++i)
#pragma unroll
      for (int t = 0; t < 2; ++t) {
        DS_TR(w[i][t][0], sb ^ (t * 64), (ST & 1) * G2_SLOT_B + i * 2048);
        DS_TR(w[i][t][1], sb ^ (t * 64), (ST & 1) * G2_SLOT_B + i * 2048 + 1024);
      }
    asm volatile("s_waitcnt vmcnt(%4)" : "+a"(hfr[ST][0]), "+a"(hfr[ST][1]), "+a"(hfr[ST][2]), "+a"(hfr[ST][3]) : "i"(NW) : "memory");
    if constexpr (ST + G2_DEPTH <= 10) G2Step<ST + G2_DEPTH>::issue(hfr, wr, c);
    asm volatile("s_waitcnt lgkmcnt(0)"
                 : "+v"(w[0][0][0]), "+v"(w[0][0][1]), "+v"(w[0][1][0]), "+v"(w[0][1][1]), "+v"(w[1][0][0]), "+v"(w[1][0][1]),
                   "+v"(w[1][1][0]), "+v"(w[1][1][1])::"memory");
#pragma unroll
    for (int i = 0; i < 2; ++i)
#pragma unroll
      for (int t = 0; t < 2; ++t)
#pragma unroll
        for (int mi = 0; mi < 2; ++mi)
          acc[t][mi] = __builtin_amdgcn_mfma_f32_32x32x16_bf16(cat4(w[i][t][0], w[i][t][1]), hfr[ST][2 * mi + i], acc[t][mi], 0, 0, 0);
    if constexpr (ST + 1 <= 10) G2Step<ST + 1>::run(acc, hfr, wr, c);
  }
};

__global__ __launch_bounds__(256, 1) void k2_kernel(const u16* __restrict__ hf, const u16* __restrict__ wd,
                                                   const u16* __restrict__ aT, const float* __restrict__ gin,
                                                   u16* __restrict__ out DBG2_ARG) {
  __shared__ __attribute__((aligned(4096))) char smem[K2_LDS];
  const int tid = threadIdx.x, lane = tid & 63;
  STAMP2(0);
  const int wave = __builtin_amdgcn_readfirstlane(tid >> 6);
  const int bid = blockIdx.x;
  const int xcd = bid & 7, idx = bid >> 3;
  const int m0 = 64 * (4 * xcd + (idx >> 3)), n0 = 64 * (idx & 7);

#pragma unroll
  for (int i = 0; i < 2; ++i) {
    const int piece = 2 * wave + i;
    const int drow = 8 * piece + (lane >> 3);
    glds16(aT + (size_t)(n0 + drow) * SEQ + m0 + (lane & 7) * 8, smem + G2_A_OFF + piece * 1024 + lane * 16);
  }

  G2Ctx c;
  c.wb32 = lds_addr(smem) + wave * G2_WAVE_B;
  c.hsrc = hf + ((size_t)((m0 >> 5) * NKS + 22 * wave) * 64 + lane) * 8;
  c.wd = wd;
  c.wcol = n0 + 8 * (lane & 7);
  c.wrow0 = 352 * wave + (lane >> 3);
  c.ww = c.wb32 + (lane >> 3) * 128 + (((lane & 7) ^ (((lane >> 4) & 1) << 2)) * 16);

  const int r = lane & 31, h = lane >> 5;
  const int blk = (lane >> 4) & 1, q = (lane & 15) >> 2, p = lane & 3;
  c.woff = (4 * h + q) * 128 + (((2 * blk + (p >> 1)) ^ (((q >> 1) & 1) << 2)) * 16) + 8 * (p & 1);

  f32x16 acc[2][2];
#pragma unroll
  for (int i = 0; i < 2; ++i)
#pragma unroll
    for (int j = 0; j < 2; ++j) acc[i][j] = f32x16{};
  bf16x8 hfr[11][4];
  u32x4 wr[11][4];

  G2Step<0>::issue(hfr, wr, c);
  G2Step<1>::issue(hfr, wr, c);
  if constexpr (G2_DEPTH > 2) G2Step<2>::issue(hfr, wr, c);
  if constexpr (G2_DEPTH > 3) G2Step<3>::issue(hfr, wr, c);
  if constexpr (G2_DEPTH > 4) G2Step<4>::issue(hfr, wr, c);
  if constexpr (G2_DEPTH > 5) G2Step<5>::issue(hfr, wr, c);
  if constexpr (G2_DEPTH > 6) G2Step<6>::issue(hfr, wr, c);
  STAMP2(1);
  STAMP2(2);
  G2Step<0>::run(acc, hfr, wr, c);
  STAMP2(3);
  __syncthreads();
  {
    char* pw = smem + wave * (64 * G2_PITCH);
#pragma unroll
    for (int c = 0; c < 2; ++c)
#pragma unroll
      for (int mi = 0; mi < 2; ++mi)
#pragma unroll
        for (int gq = 0; gq < 4; ++gq) {
          const f32x4 v = {acc[c][mi][4 * gq], acc[c][mi][4 * gq + 1], acc[c][mi][4 * gq + 2], acc[c][mi][4 * gq + 3]};
          *(f32x4*)(pw + (32 * mi + r) * G2_PITCH + (32 * c + 8 * gq + 4 * h) * 4) = v;
        }
  }
  __syncthreads();
  STAMP2(4);
  {
    const int m = tid >> 2, nq = (tid & 3) * 16;
    const float g = gin[m0 + m];
    const float omg = rbf(1.0f - g);
    float b[16];
#pragma unroll
    for (int j = 0; j < 4; ++j) {
      f32x4 s = *(const f32x4*)(smem + m * G2_PITCH + (nq + 4 * j) * 4);
#pragma unroll
      for (int w = 1; w < 4; ++w) s += *(const f32x4*)(smem + w * (64 * G2_PITCH) + m * G2_PITCH + (nq + 4 * j) * 4);
#pragma unroll
      for (int e = 0; e < 4; ++e) b[4 * j + e] = s[e];
    }
    const u16* aimg = (const u16*)(smem + G2_A_OFF);
    unsigned o[8];
#pragma unroll
    for (int j = 0; j < 8; ++j) {
      const float a0 = bf2f(aimg[(nq + 2 * j) * 64 + m]);
      const float a1 = bf2f(aimg[(nq + 2 * j + 1) * 64 + m]);
      const float o0 = rbf(g * a0) + rbf(omg * rbf(b[2 * j]));
      const float o1 = rbf(g * a1) + rbf(omg * rbf(b[2 * j + 1]));
      o[j] = pk_bf16(o0, o1);
    }
    u16* op = out + (size_t)(m0 + m) * DM + n0 + nq;
    *(u32x4*)op = u32x4{o[0], o[1], o[2], o[3]};
    *(u32x4*)(op + 8) = u32x4{o[4], o[5], o[6], o[7]};
  }
  STAMP2(5);
}

extern "C" void kernel_launch(void* const* d_in, const int* in_sizes, int n_in, void* d_out, int out_size,
                              void* d_ws, size_t ws_size, hipStream_t stream) {
  const u16* u = (const u16*)d_in[0];
  const u16* logAr = (const u16*)d_in[1];
  const u16* Aim = (const u16*)d_in[2];
  const u16* Cre = (const u16*)d_in[3];
  const u16* Cim = (const u16*)d_in[4];
  const u16* logdt = (const u16*)d_in[5];
  const u16* Dskip = (const u16*)d_in[6];
  const u16* wg = (const u16*)d_in[8];
  const u16* wu = (const u16*)d_in[9];
  const u16* wd = (const u16*)d_in[10];
  const u16* gw = (const u16*)d_in[11];
  char* ws = (char*)d_ws;
  u16* hbuf = (u16*)ws;
  u16* aT = (u16*)(ws + (size_t)SEQ * FFP * 2);
  float* gbuf = (float*)(ws + (size_t)SEQ * FFP * 2 + (size_t)DM * SEQ * 2);
  k1_kernel<<<512, 256, 0, stream>>>(u, logAr, Aim, Cre, Cim, logdt, Dskip, wg, wu, gw, wd, hbuf, aT, gbuf
      );
  k2_kernel<<<256, 256, 0, stream>>>(hbuf, wd, aT, gbuf, (u16*)d_out
      );
}
```

```cpp
#include <hip/hip_runtime.h>
#include <hip/hip_bf16.h>
#include <stdint.h>
#include <type_traits>

typedef unsigned short u16;
typedef __attribute__((ext_vector_type(8))) short bf16x8;
typedef __attribute__((ext_vector_type(4))) short s16x4;
typedef __attribute__((ext_vector_type(4))) float f32x4;
typedef __attribute__((ext_vector_type(16))) float f32x16;
typedef __attribute__((ext_vector_type(4))) unsigned u32x4;
typedef __attribute__((ext_vector_type(2))) unsigned u32x2;
typedef __attribute__((ext_vector_type(2))) __bf16 bf16x2_t;
typedef __attribute__((ext_vector_type(2))) float f32x2;
typedef __attribute__((address_space(3))) s16x4* lds_s16x4_ptr;

constexpr int SEQ = 2048, DM = 512, NST = 128, FF = 1376, FFP = 1408;
constexpr int NSSM = 256;
constexpr int NKS = 88;

__device__ __forceinline__ unsigned pk_bf16(float lo, float hi) {
  f32x2 v = {lo, hi};
  bf16x2_t b = __builtin_convertvector(v, bf16x2_t);
  return __builtin_bit_cast(unsigned, b);
}
__device__ __forceinline__ float bf_lo(unsigned w) { return __uint_as_float(w << 16); }
__device__ __forceinline__ float bf_hi(unsigned w) { return __uint_as_float(w & 0xffff0000u); }
__device__ __forceinline__ float rbf(float x) { return bf_lo(pk_bf16(x, 0.f)); }
__device__ __forceinline__ float bf2f(u16 v) { return __uint_as_float(((unsigned)v) << 16); }

__device__ __forceinline__ float smul(float a, float b) { float r; asm("v_mul_f32 %0, %1, %2" : "=v"(r) : "v"(a), "v"(b)); return r; }
__device__ __forceinline__ float sadd(float a, float b) { float r; asm("v_add_f32 %0, %1, %2" : "=v"(r) : "v"(a), "v"(b)); return r; }
__device__ __forceinline__ float ssub(float a, float b) { float r; asm("v_sub_f32 %0, %1, %2" : "=v"(r) : "v"(a), "v"(b)); return r; }
__device__ __forceinline__ float sfma(float a, float b, float c) { float r; asm("v_fma_f32 %0, %1, %2, %3" : "=v"(r) : "v"(a), "v"(b), "v"(c)); return r; }
__device__ __forceinline__ float sfms(float a, float b, float c) { float r; asm("v_fma_f32 %0, -%1, %2, %3" : "=v"(r) : "v"(a), "v"(b), "v"(c)); return r; }
__device__ __forceinline__ float sfnn(float a, float b, float c) { float r; asm("v_fma_f32 %0, -%1, %2, -%3" : "=v"(r) : "v"(a), "v"(b), "v"(c)); return r; }
#define TRANS_FENCE8(a, o) asm volatile("s_nop 1" : "+v"(a[o]), "+v"(a[o + 1]), "+v"(a[o + 2]), "+v"(a[o + 3]), "+v"(a[o + 4]), "+v"(a[o + 5]), "+v"(a[o + 6]), "+v"(a[o + 7]))
__device__ __forceinline__ bf16x8 mk8(unsigned a, unsigned b, unsigned c, unsigned d) {
  u32x4 v = {a, b, c, d};
  return __builtin_bit_cast(bf16x8, v);
}
__device__ __forceinline__ bf16x8 cat4(s16x4 a, s16x4 b) {
  return __builtin_shufflevector(a, b, 0, 1, 2, 3, 4, 5, 6, 7);
}
__device__ __forceinline__ s16x4 tr_read(const char* smem_ptr) {
  return __builtin_amdgcn_ds_read_tr16_b64_v4i16((lds_s16x4_ptr)(smem_ptr));
}
__device__ __forceinline__ unsigned lds_addr(const void* p) {
  return (unsigned)(size_t)(const __attribute__((address_space(3))) char*)p;
}
__device__ __forceinline__ void glds16(const void* g, char* l) {
  __builtin_amdgcn_global_load_lds((const unsigned*)g, (unsigned*)l, 16, 0, 0);
}

#define STAMP2(i)
#define DBG2_ARG
#define DBG2_PASS
#define STAMP(i)
#define DBG_ARG
#define DBG_PASS
constexpr int U_COPY_B = 4160;
constexpr int U_CH_B = 4 * U_COPY_B;
constexpr int U_REGION_B = 2 * U_CH_B;
constexpr int KR_PART_B = 4576;
constexpr int KR_OFF = U_REGION_B;
constexpr int KQ_B = 8208;
constexpr int KX_OFF = 2 * KQ_B;
constexpr int SSM_LDS = KR_OFF + 4 * KR_PART_B;
static_assert(KX_OFF + 4 * 1536 <= U_REGION_B, "scratch must fit the U region");

#define DS_TR(dst, addr, off) asm volatile("ds_read_b64_tr_b16 %0, %1 offset:%2" : "=v"(dst) : "v"(addr), "i"(off) : "memory")
#define DS_B128(dst, addr, off) asm volatile("ds_read_b128 %0, %1 offset:%2" : "=v"(dst) : "v"(addr), "i"(off) : "memory")
#define DS_B64(dst, addr, off) asm volatile("ds_read_b64 %0, %1 offset:%2" : "=v"(dst) : "v"(addr), "i"(off) : "memory")
#define MFMA16(acc, a, b) asm volatile("v_mfma_f32_16x16x32_bf16 %0, %1, %2, %0" : "+v"(acc) : "v"(a), "v"(b))

struct ConvFrag {
  s16x4 a[8][2];
  bf16x8 bh[8], bl[8];
};
template <int RI, int K>
__device__ __forceinline__ void conv_read(ConvFrag& f, unsigned ua, unsigned kb) {
  if constexpr (K < 16) {
    DS_B64(f.a[K >> 1][K & 1], ua, 64 * (8 * (K >> 1) + RI) + 8 * (K & 1));
  } else if constexpr (K < 24) {
    DS_B128(f.bh[K - 16], kb, 64 * (56 - 8 * (K - 16) + RI));
  } else {
    DS_B128(f.bl[K - 24], kb, 64 * (56 - 8 * (K - 24) + RI) + KR_PART_B);
  }
}
template <int RI, int K>
__device__ __forceinline__ void conv_read_all(ConvFrag& f, unsigned ua, unsigned kb) {
  conv_read<RI, K>(f, ua, kb);
  if constexpr (K + 1 < 32) conv_read_all<RI, K + 1>(f, ua, kb);
}
#define CONV_WAIT(f)                                                                                                  \
  do {                                                                                                                \
    asm volatile("s_waitcnt lgkmcnt(0)"                                                                               \
                 : "+v"(f.a[0][0]), "+v"(f.a[0][1]), "+v"(f.a[1][0]), "+v"(f.a[1][1]), "+v"(f.a[2][0]), "+v"(f.a[2][1]), \
                   "+v"(f.a[3][0]), "+v"(f.a[3][1]), "+v"(f.a[4][0]), "+v"(f.a[4][1]), "+v"(f.a[5][0]), "+v"(f.a[5][1]), \
                   "+v"(f.a[6][0]), "+v"(f.a[6][1]), "+v"(f.a[7][0]), "+v"(f.a[7][1])::"memory");                     \
    asm volatile("" : "+v"(f.bh[0]), "+v"(f.bh[1]), "+v"(f.bh[2]), "+v"(f.bh[3]), "+v"(f.bh[4]), "+v"(f.bh[5]),        \
                 "+v"(f.bh[6]), "+v"(f.bh[7]), "+v"(f.bl[0]), "+v"(f.bl[1]), "+v"(f.bl[2]), "+v"(f.bl[3]),            \
                 "+v"(f.bl[4]), "+v"(f.bl[5]), "+v"(f.bl[6]), "+v"(f.bl[7])::"memory");                              \
  } while (0)
constexpr int conv_tau(int P) { int t = 0; while (P >= 8 - t) { P -= 8 - t; ++t; } return t; }
constexpr int conv_sig(int P) { int t = 0; while (P >= 8 - t) { P -= 8 - t; ++t; } return P; }
template <int RI, int P>
__device__ __forceinline__ void conv_pairs(f32x4 (&acc)[8], const bf16x8 (&A)[8], ConvFrag& cur, ConvFrag& nxt, unsigned ua,
                                           unsigned kb) {
  constexpr int T = conv_tau(P), SG = conv_sig(P);
  MFMA16(acc[T + SG], A[SG], cur.bl[T]);
  if constexpr (RI < 3 && P < 32) conv_read<RI + 1, P>(nxt, ua, kb);
  MFMA16(acc[T + SG], A[SG], cur.bh[T]);
  if constexpr (P + 1 < 36) conv_pairs<RI, P + 1>(acc, A, cur, nxt, ua, kb);
}
template <int RI>
__device__ __forceinline__ void conv_rho(f32x4 (&acc)[8], ConvFrag& cur, ConvFrag& nxt, unsigned ua, unsigned kb) {
  CONV_WAIT(cur);
  bf16x8 A[8];
#pragma unroll
  for (int i = 0; i < 8; ++i) A[i] = cat4(cur.a[i][0], cur.a[i][1]);
  conv_pairs<RI, 0>(acc, A, cur, nxt, ua, kb);
  if constexpr (RI < 3) conv_rho<RI + 1>(acc, nxt, cur, ua, kb);
}

__device__ __forceinline__ void split_pair(float v0, float v1, unsigned& hi, unsigned& lo) {
  hi = pk_bf16(v0, v1);
  lo = pk_bf16(ssub(v0, bf_lo(hi)), ssub(v1, bf_hi(hi)));
}

__device__ __forceinline__ void ssm_unit(int w, const u16* __restrict__ u, const u16* __restrict__ logAr,
                                         const u16* __restrict__ Aim, const u16* __restrict__ Cre,
                                         const u16* __restrict__ Cim, const u16* __restrict__ logdt,
                                         const u16* __restrict__ Dskip, u16* __restrict__ aT, char* smem DBG_ARG) {
  const int tid = threadIdx.x, lane = tid & 63;
  const int wave = __builtin_amdgcn_readfirstlane(tid >> 6);
  const int ch = wave >> 1, w2 = wave & 1;
  const int d0 = 64 * (w & 7) + 2 * (w >> 3);
  const int d = d0 + ch;

  const int pidx = d0 * NST + tid;
  const u16 r_lar = logAr[pidx], r_aim = Aim[pidx], r_cre = Cre[pidx], r_cim = Cim[pidx], r_ldt = logdt[d], r_dsk = Dskip[d];
  const unsigned* u32p = (const unsigned*)(u + d0);
  unsigned uw[8], up[3];
#pragma unroll
  for (int i = 0; i < 3; ++i) up[i] = 0;
  if (lane == 0 && tid > 0) {
#pragma unroll
    for (int i = 0; i < 3; ++i) up[i] = u32p[(size_t)(8 * tid - 3 + i) * (DM / 2)];
  }
  const unsigned uoff = (unsigned)tid * 8192u;
  {
    const float dt = rbf(expf(bf2f(r_ldt)));
    const float are = rbf(expf(bf2f(r_lar)));
    f32x4 prm;
    prm[0] = (-are * dt) * 1.44269504088896340736f;
    prm[1] = (bf2f(r_aim) * dt) * 0.15915494309189533577f;
    prm[2] = 2.0f * (bf2f(r_cre) * dt);
    prm[3] = 2.0f * (bf2f(r_cim) * dt);
    *(f32x4*)(smem + KR_OFF + tid * 16) = prm;
  }
  __syncthreads();

  const int r = lane & 31, h = lane >> 5;
  f32x16 dacc = {};
  {
    int p1i;
    if (r < 8) p1i = r;
    else if (r < 12) p1i = 256 + 2 * (r - 8);
    else if (r < 16) p1i = 512 + 256 * ((r - 12) >> 1) + 4 * ((r - 12) & 1);
    else if (r < 21) p1i = 1024 + 256 * (r - 16);
    else p1i = 0;
    const float p1 = (float)p1i, p2 = (float)(8 * r);
    const char* prm_base = smem + KR_OFF + (ch * 128 + 64 * w2 + 4 * h) * 16;
#pragma unroll
    for (int ks = 0; ks < 8; ++ks) {
      if (ks >= 4) {
        asm volatile("global_load_dword %0, %1, %2 offset:%3" : "=v"(uw[2 * ks - 8]) : "v"(uoff), "s"((const char*)u32p + ((2 * ks - 8) >> 2) * 4096), "i"(((2 * ks - 8) & 3) * 1024) : "memory");
        asm volatile("global_load_dword %0, %1, %2 offset:%3" : "=v"(uw[2 * ks - 7]) : "v"(uoff), "s"((const char*)u32p + ((2 * ks - 7) >> 2) * 4096), "i"(((2 * ks - 7) & 3) * 1024) : "memory");
      }
      f32x4 prm[4];
#pragma unroll
      for (int q = 0; q < 4; ++q) prm[q] = *(const f32x4*)(prm_base + (8 * ks + q) * 16);
      float ea[4], eb[4], fa[4], fb[4];
#pragma unroll
      for (int q = 0; q < 4; ++q) {
        ea[q] = __builtin_amdgcn_exp2f(smul(prm[q][0], p1)); eb[q] = __builtin_amdgcn_exp2f(smul(prm[q][0], p2));
        fa[q] = __builtin_amdgcn_fractf(smul(prm[q][1], p1)); fb[q] = __builtin_amdgcn_fractf(smul(prm[q][1], p2));
      }
      float ac[4], as[4], bc[4], bs[4];
#pragma unroll
      for (int q = 0; q < 4; ++q) {
        ac[q] = __builtin_amdgcn_cosf(fa[q]); as[q] = __builtin_amdgcn_sinf(fa[q]);
        bc[q] = __builtin_amdgcn_cosf(fb[q]); bs[q] = __builtin_amdgcn_sinf(fb[q]);
      }
      asm volatile("s_nop 1" : "+v"(ea[0]), "+v"(ea[1]), "+v"(ea[2]), "+v"(ea[3]), "+v"(eb[0]), "+v"(eb[1]), "+v"(eb[2]), "+v"(eb[3]),
                   "+v"(ac[0]), "+v"(ac[1]), "+v"(ac[2]), "+v"(ac[3]), "+v"(as[0]), "+v"(as[1]), "+v"(as[2]), "+v"(as[3]),
                   "+v"(bc[0]), "+v"(bc[1]), "+v"(bc[2]), "+v"(bc[3]), "+v"(bs[0]), "+v"(bs[1]), "+v"(bs[2]), "+v"(bs[3]));
      unsigned ah[4], al[4], bh[4], bl[4];
#pragma unroll
      for (int q = 0; q < 4; ++q) {
        const float cr = smul(prm[q][2], ea[q]), ci = smul(prm[q][3], ea[q]);
        const float gre = sfms(ci, as[q], smul(cr, ac[q]));
        const float gmi = sfnn(ci, ac[q], smul(cr, as[q]));
        split_pair(gre, gmi, ah[q], al[q]);
        split_pair(smul(bc[q], eb[q]), smul(bs[q], eb[q]), bh[q], bl[q]);
      }
      const bf16x8 Ah = mk8(ah[0], ah[1], ah[2], ah[3]), Al = mk8(al[0], al[1], al[2], al[3]);
      const bf16x8 Bh = mk8(bh[0], bh[1], bh[2], bh[3]), Bl = mk8(bl[0], bl[1], bl[2], bl[3]);
      dacc = __builtin_amdgcn_mfma_f32_32x32x16_bf16(Al, Bh, dacc, 0, 0, 0);
      dacc = __builtin_amdgcn_mfma_f32_32x32x16_bf16(Ah, Bl, dacc, 0, 0, 0);
      dacc = __builtin_amdgcn_mfma_f32_32x32x16_bf16(Ah, Bh, dacc, 0, 0, 0);
    }
  }
  {
    float* xo = (float*)(smem + KX_OFF + wave * 1536) + lane;
    const float* xi = (const float*)(smem + KX_OFF + (wave ^ 1) * 1536) + lane;
    float mine[6];
    if (w2 == 0) {
#pragma unroll
      for (int e = 0; e < 6; ++e) { xo[e * 64] = dacc[6 + e]; mine[e] = dacc[e]; }
    } else {
#pragma unroll
      for (int e = 0; e < 6; ++e) { xo[e * 64] = dacc[e]; mine[e] = dacc[6 + e]; }
    }
    __syncthreads();
    unsigned* Kq = (unsigned*)(smem + ch * KQ_B);
    unsigned pq[6];
#pragma unroll
    for (int e = 0; e < 6; ++e) {
      const float v = sadd(mine[e], xi[e * 64]);
      const unsigned hi = pk_bf16(v, 0.f);
      pq[e] = pk_bf16(0.f, ssub(v, bf_lo(hi))) | hi;
    }
    if (w2 == 0) {
#pragma unroll
      for (int e = 0; e < 4; ++e) Kq[4 * h + e + 8 * r] = pq[e];
      Kq[(h ? 512 : 256) + 8 * r] = pq[4];
      Kq[(h ? 516 : 258) + 8 * r] = pq[5];
    } else {
      Kq[(h ? 768 : 260) + 8 * r] = pq[0];
      Kq[(h ? 772 : 262) + 8 * r] = pq[1];
      if (h == 0) {
#pragma unroll
        for (int e = 0; e < 4; ++e) Kq[1024 + 256 * e + 8 * r] = pq[2 + e];
      } else if (r == 0) {
        Kq[2048] = pq[2];
      }
    }
  }
  __syncthreads();

  {
    const int t = tid & 127;
    const unsigned* Kq = (const unsigned*)(smem + ch * KQ_B);
    unsigned* krh = (unsigned*)(smem + KR_OFF + (ch * 2) * KR_PART_B);
    unsigned* krl = (unsigned*)((char*)krh + KR_PART_B);
    unsigned q0[8], q1[8];
#pragma unroll
    for (int m = 0; m < 8; ++m) {
      const int sh = m < 4 ? 3 : (m < 6 ? 2 : (m < 7 ? 1 : 0));
      const int l0 = 2047 - 2 * (t + 128 * m), l1 = l0 - 1;
      int pa = l0, pb = l1;
      if (sh) {
        pa = (l0 + (1 << (sh - 1)) - 1 + ((l0 >> sh) & 1)) & ~((1 << sh) - 1);
        pb = (l1 + (1 << (sh - 1)) - 1 + ((l1 >> sh) & 1)) & ~((1 << sh) - 1);
      }
      q0[m] = Kq[pa];
      q1[m] = Kq[pb];
    }
#pragma unroll
    for (int m = 0; m < 8; ++m) {
      krh[t + 128 * m] = __builtin_amdgcn_perm(q1[m], q0[m], 0x05040100u);
      krl[t + 128 * m] = __builtin_amdgcn_perm(q1[m], q0[m], 0x07060302u);
    }
    if (t < 120) { krh[1024 + t] = 0; krl[1024 + t] = 0; }
  }
  __syncthreads();

  asm volatile("s_waitcnt vmcnt(0)" : "+v"(uw[0]), "+v"(uw[1]), "+v"(uw[2]), "+v"(uw[3]), "+v"(uw[4]), "+v"(uw[5]), "+v"(uw[6]), "+v"(uw[7])::"memory");
  {
    unsigned e[11];
#pragma unroll
    for (int i = 0; i < 3; ++i) {
      const unsigned nb = __shfl_up(uw[5 + i], 1);
      e[i] = (lane == 0) ? up[i] : nb;
    }
#pragma unroll
    for (int i = 0; i < 8; ++i) e[3 + i] = uw[i];
    unsigned p0[10], p1[10];
#pragma unroll
    for (int i = 0; i < 10; ++i) {
      p0[i] = __builtin_amdgcn_perm(e[i + 1], e[i], 0x05040100u);
      p1[i] = __builtin_amdgcn_perm(e[i + 1], e[i], 0x07060302u);
    }
#pragma unroll
    for (int c = 0; c < 4; ++c) {
      *(u32x4*)(smem + c * U_COPY_B + (tid + 2) * 16) = u32x4{p0[c], p0[c + 2], p0[c + 4], p0[c + 6]};
      *(u32x4*)(smem + U_CH_B + c * U_COPY_B + (tid + 2) * 16) = u32x4{p1[c], p1[c + 2], p1[c + 4], p1[c + 6]};
    }
    if (tid < 16) *(u32x4*)(smem + (tid >> 1) * U_COPY_B + (tid & 1) * 16) = u32x4{0, 0, 0, 0};
  }
  __syncthreads();

  f32x4 acc[8];
#pragma unroll
  for (int t = 0; t < 8; ++t) acc[t] = f32x4{0.f, 0.f, 0.f, 0.f};
  asm volatile("s_nop 4" : "+v"(acc[0]), "+v"(acc[1]), "+v"(acc[2]), "+v"(acc[3]), "+v"(acc[4]), "+v"(acc[5]), "+v"(acc[6]), "+v"(acc[7]));
  {
    const int r16 = lane & 15, hq = lane >> 4;
    const unsigned sb = lds_addr(smem);
    const unsigned ua = sb + ch * U_CH_B + (r16 & 3) * U_COPY_B + 2 * (4 + (r16 & ~3) + 8 * hq) + 64 * 4 * w2;
    const unsigned kb = sb + KR_OFF + (ch * 2) * KR_PART_B + 2 * (2032 - 16 * r16 + 8 * hq) - 64 * 56 + 64 * 4 * w2;
    ConvFrag f0, f1;
    conv_read_all<0, 0>(f0, ua, kb);
    conv_rho<0>(acc, f0, f1, ua, kb);
  }
  asm volatile("s_nop 15" : "+v"(acc[0]), "+v"(acc[1]), "+v"(acc[2]), "+v"(acc[3]), "+v"(acc[4]), "+v"(acc[5]), "+v"(acc[6]), "+v"(acc[7]));
  __syncthreads();
  {
    char* xw = smem + KR_OFF + (ch * 2 + w2) * 4096 + lane * 16;
    const char* xr = smem + KR_OFF + (ch * 2 + (w2 ^ 1)) * 4096 + lane * 16;
    f32x4 y[4];
    if (w2 == 0) {
#pragma unroll
      for (int i = 0; i < 4; ++i) { *(f32x4*)(xw + i * 1024) = acc[4 + i]; y[i] = acc[i]; }
    } else {
#pragma unroll
      for (int i = 0; i < 4; ++i) { *(f32x4*)(xw + i * 1024) = acc[i]; y[i] = acc[4 + i]; }
    }
    __syncthreads();
    const float Dk = bf2f(r_dsk);
#pragma unroll
    for (int i = 0; i < 4; ++i) {
      const f32x4 o = *(const f32x4*)(xr + i * 1024);
      const int l0 = 256 * (4 * w2 + i) + 16 * (lane & 15) + 4 * (lane >> 4);
      const u32x2 uu = *(const u32x2*)(smem + ch * U_CH_B + 3 * U_COPY_B + 2 * (16 + l0));
      const unsigned y01 = pk_bf16(sadd(y[i][0], o[0]), sadd(y[i][1], o[1])), y23 = pk_bf16(sadd(y[i][2], o[2]), sadd(y[i][3], o[3]));
      const unsigned s01 = pk_bf16(smul(bf_lo(uu[0]), Dk), smul(bf_hi(uu[0]), Dk)), s23 = pk_bf16(smul(bf_lo(uu[1]), Dk), smul(bf_hi(uu[1]), Dk));
      { const u32x2 av = u32x2{pk_bf16(sadd(bf_lo(y01), bf_lo(s01)), sadd(bf_hi(y01), bf_hi(s01))),
                                                  pk_bf16(sadd(bf_lo(y23), bf_lo(s23)), sadd(bf_hi(y23), bf_hi(s23)))};
        asm volatile("global_store_dwordx2 %0, %1, off sc1\n\ts_nop 1" ::"v"(aT + (size_t)d * SEQ + l0), "v"(av) : "memory"); }
    }
  }
}

__device__ __forceinline__ float gelu_tanh(float x) {
  const float z = 0.7978845608028654f * (x + 0.044715f * x * x * x);
  const float e = __builtin_amdgcn_exp2f(-2.8853900817779268f * z);
  return x * __builtin_amdgcn_rcpf(1.0f + e);
}

constexpr int G1_WSLOT_B = 12288;
constexpr int G1_X_OFF = 3 * G1_WSLOT_B;
constexpr int G1_LDS = G1_X_OFF + 32768;

constexpr int g1_pos(bool isx, int id) {
  if (isx && id == 0) return 4;
  if (!isx && id == 0) return 7;
  if (!isx && id == 1) return 10;
  if (isx && id == 1) return 14;
  if (!isx && id == 2) return 17;
  if (!isx && id == 3) return 20;
  if (!isx && id == 4) return 23;
  if (isx && id == 2) return 27;
  int n = 27;
  for (int t = 0; t < 32; ++t) {
    if ((t & 1) == 0 && t / 2 + 5 < 16) { n += 3; if (!isx && t / 2 + 5 == id) return n; }
    if ((t & 3) == 1 && t / 4 + 3 < 8) { n += 4; if (isx && t / 4 + 3 == id) return n; }
  }
  return -1;
}
constexpr int g1_issued_before(int step) {
  int n = 27;
  for (int t = 0; t < step; ++t) {
    if ((t & 1) == 0 && t / 2 + 5 < 16) n += 3;
    if ((t & 3) == 1 && t / 4 + 3 < 8) n += 4;
  }
  return n;
}

#define GLOAD128(dst, ptr) asm volatile("global_load_dwordx4 %0, %1, off" : "=v"(dst) : "v"(ptr) : "memory")
#define GLOAD128S(dst, voff, sbase) asm volatile("global_load_dwordx4 %0, %1, %2" : "=v"(dst) : "v"(voff), "s"(sbase) : "memory")
#define GLOAD128SA(dst, voff, sbase) asm volatile("global_load_dwordx4 %0, %1, %2" : "=a"(dst) : "v"(voff), "s"(sbase) : "memory")
#define GLOAD128A(dst, ptr) asm volatile("global_load_dwordx4 %0, %1, off" : "=a"(dst) : "v"(ptr) : "memory")
#define DS_W128(addr, val, off) asm volatile("ds_write_b128 %0, %1 offset:%2" ::"v"(addr), "v"(val), "i"(off) : "memory")
#define MFMA32(acc, a, b) asm volatile("v_mfma_f32_32x32x16_bf16 %0, %1, %2, %0" : "+v"(acc) : "v"(a), "v"(b))

struct G1Ctx {
  const char* wbase;
  const char* xbase;
  unsigned wo[3], xo;
  unsigned woff, woff_o, ww[3], xw, xrd2[2][4];
  unsigned junk[4];
};
struct G1Frag {
  s16x4 w[3][2];
  bf16x8 x[2];
};

#define G1_RD_W(f, t, OFF)                                                        \
  do {                                                                            \
    if ((t) & 1) { DS_TR(f.w[t][0], c.woff_o, (OFF) + (t) * 64); DS_TR(f.w[t][1], c.woff_o, (OFF) + (t) * 64 + 4 * 384); } \
    else { DS_TR(f.w[t][0], c.woff, (OFF) + (t) * 64); DS_TR(f.w[t][1], c.woff, (OFF) + (t) * 64 + 4 * 384); }            \
  } while (0)
#define G1_RD_X(f, mi, N) DS_B128(f.x[mi], c.xrd2[mi][(N) & 3], (((N) >> 2) & 1) * 16384)
template <int N>
__device__ __forceinline__ void g1_read(G1Frag& f, const G1Ctx& c) {
  constexpr int OFF = ((N >> 1) % 3) * G1_WSLOT_B + (N & 1) * 16 * 384;
  G1_RD_X(f, 0, N); G1_RD_X(f, 1, N);
  G1_RD_W(f, 0, OFF); G1_RD_W(f, 1, OFF); G1_RD_W(f, 2, OFF);
}
#define G1_WAIT_FRAG(f, STR)                                                                                       \
  asm volatile(STR : "+v"(f.w[0][0]), "+v"(f.w[0][1]), "+v"(f.w[1][0]), "+v"(f.w[1][1]), "+v"(f.w[2][0]),        \
               "+v"(f.w[2][1]), "+v"(f.x[0]), "+v"(f.x[1])::"memory")

template <int N>
struct G1Step {
  static __device__ __forceinline__ void run(f32x16 (&acc)[6], u32x4 (&xr)[2][4], u32x4 (&wr)[3][3], G1Frag& cur,
                                             G1Frag& nxt, G1Ctx& c) {
    constexpr int ISS = g1_issued_before(N);
    constexpr int S = N >> 1;
    constexpr bool RD = N + 1 < 32;
    constexpr int OFF = (((N + 1) >> 1) % 3) * G1_WSLOT_B + ((N + 1) & 1) * 16 * 384;
    if constexpr ((N & 1) == 0 && N > 0) G1_WAIT_FRAG(cur, "s_waitcnt lgkmcnt(0)\n\ts_barrier");
    else G1_WAIT_FRAG(cur, "s_waitcnt lgkmcnt(0)");
    bf16x8 b[3];
#pragma unroll
    for (int t = 0; t < 3; ++t) b[t] = cat4(cur.w[t][0], cur.w[t][1]);
    MFMA32(acc[0], b[0], cur.x[0]);
    if constexpr (RD) { G1_RD_X(nxt, 0, N + 1); G1_RD_W(nxt, 0, OFF); }
    MFMA32(acc[3], b[0], cur.x[1]);
    if constexpr (RD) { G1_RD_X(nxt, 1, N + 1); G1_RD_W(nxt, 1, OFF); }
    MFMA32(acc[1], b[1], cur.x[0]);
    if constexpr (RD) { G1_RD_W(nxt, 2, OFF); }
    MFMA32(acc[4], b[1], cur.x[1]);
    if constexpr ((N & 1) == 0) {
      if constexpr (S + 2 < 16) {
        constexpr int R = (S + 2) % 3;
        constexpr int NW = ISS - g1_pos(false, S + 2);
        asm volatile("s_waitcnt vmcnt(%3)" : "+v"(wr[R][0]), "+v"(wr[R][1]), "+v"(wr[R][2]) : "i"(NW) : "memory");
#pragma unroll
        for (int i = 0; i < 3; ++i) DS_W128(c.ww[i], wr[R][i], R * G1_WSLOT_B);
      }
    }
    if constexpr ((N & 3) == 1) {
      constexpr int B = N / 4 + 1;
      if constexpr (B < 8) {
        constexpr int NW = ISS - g1_pos(true, B);
        asm volatile("s_waitcnt vmcnt(%4)" : "+v"(xr[B & 1][0]), "+v"(xr[B & 1][1]), "+v"(xr[B & 1][2]), "+v"(xr[B & 1][3]) : "i"(NW) : "memory");
#pragma unroll
        for (int i = 0; i < 4; ++i) DS_W128(c.xw, xr[B & 1][i], (B & 1) * 16384 + i * 1024);
      }
    }
    MFMA32(acc[2], b[2], cur.x[0]);
    if constexpr ((N & 1) == 0 && S + 5 < 16) {
#pragma unroll
      for (int i = 0; i < 3; ++i) GLOAD128S(wr[(S + 5) % 3][i], c.wo[i], c.wbase + (size_t)(S + 5) * 32 * FF * 2);
    }
    if constexpr ((N & 3) == 1 && N / 4 + 3 < 8) {
      constexpr int B = N / 4 + 1;
#pragma unroll
      for (int i = 0; i < 4; ++i) GLOAD128S(xr[B & 1][i], c.xo, c.xbase + (size_t)(8 * i) * DM * 2 + (B + 2) * 128);
    }
    MFMA32(acc[5], b[2], cur.x[1]);
    if constexpr (N + 1 < 32) G1Step<N + 1>::run(acc, xr, wr, nxt, cur, c);
  }
};

__device__ __forceinline__ void gemm1_tile(int t, const u16* __restrict__ x, const u16* __restrict__ wg,
                                           const u16* __restrict__ wu, u16* __restrict__ hf, char* smem DBG_ARG) {
  const int tid = threadIdx.x, lane = tid & 63;
  const int wave = __builtin_amdgcn_readfirstlane(tid >> 6);
  const int fb = t >> 4, mb = t & 15;
  const int f0 = 96 * fb;
  const int wm = wave & 1, isup = wave >> 1;
  G1Ctx c;
  const unsigned sbase = lds_addr(smem);
  c.wbase = (const char*)(isup ? wu : wg);
#pragma unroll
  for (int i = 0; i < 3; ++i) {
    const int id = 192 * (wave & 1) + 64 * i + lane;
    const int row = id / 12, cc = id % 12;
    int col = f0 + 8 * cc;
    if (col > FF - 8) col = FF - 8;
    c.wo[i] = (unsigned)(row * FF + col) * 2u;
    c.ww[i] = sbase + row * 384 + ((cc + 12 * isup) ^ (((row >> 1) & 1) << 2)) * 16;
  }
  c.xbase = (const char*)(x + (size_t)(128 * mb + 32 * wave) * DM);
  c.xo = (unsigned)((lane >> 3) * DM + (lane & 7) * 8) * 2u;
  const unsigned xl = sbase + G1_X_OFF;
  c.xw = xl + (32 * wave + (lane >> 3)) * 128 + (((lane & 7) ^ (lane >> 3)) * 16);
  const int r = lane & 31, h = lane >> 5;
#pragma unroll
  for (int i = 0; i < 4; ++i) {
    c.xrd2[0][i] = xl + (64 * wm + 32 * isup + r) * 128 + (((2 * i + h) ^ (r & 7)) * 16);
    c.xrd2[1][i] = xl + (64 * wm + 32 * (1 - isup) + r) * 128 + (((2 * i + h) ^ (r & 7)) * 16);
  }
  const int blk = (lane >> 4) & 1, q = (lane & 15) >> 2, p = lane & 3;
  const int sw = (q >> 1) & 1;
  {
    const unsigned e = sbase + (8 * h + q) * 384 + (2 * blk + (p >> 1) + 4 * sw) * 16 + 8 * (p & 1);
    const unsigned o = sbase + (8 * h + q) * 384 + (2 * blk + (p >> 1) - 4 * sw) * 16 + 8 * (p & 1);
    c.woff = isup ? o + 192 : e;
    c.woff_o = isup ? e + 192 : o;
  }

  f32x16 acc[6];
#pragma unroll
  for (int i = 0; i < 6; ++i) acc[i] = f32x16{};
  asm volatile("s_nop 4" : "+v"(acc[0]), "+v"(acc[1]), "+v"(acc[2]), "+v"(acc[3]), "+v"(acc[4]), "+v"(acc[5]));
  u32x4 xr[2][4], wr[3][3];
  G1Frag fa, fb2;
#define G1_ISSUE_X(R, B) _Pragma("unroll") for (int i = 0; i < 4; ++i) GLOAD128S(xr[R][i], c.xo, c.xbase + (size_t)(8 * i) * DM * 2 + (B) * 128)
#define G1_ISSUE_W(R, S) _Pragma("unroll") for (int i = 0; i < 3; ++i) GLOAD128S(wr[R][i], c.wo[i], c.wbase + (size_t)(S) * 32 * FF * 2)
  G1_ISSUE_X(0, 0); G1_ISSUE_W(0, 0); G1_ISSUE_W(1, 1); G1_ISSUE_X(1, 1); G1_ISSUE_W(2, 2);
#define G1_EXTRA 0
  asm volatile("s_waitcnt vmcnt(%10)"
               : "+v"(wr[0][0]), "+v"(wr[0][1]), "+v"(wr[0][2]), "+v"(wr[1][0]), "+v"(wr[1][1]), "+v"(wr[1][2]),
                 "+v"(xr[0][0]), "+v"(xr[0][1]), "+v"(xr[0][2]), "+v"(xr[0][3]) : "i"(7 + G1_EXTRA) : "memory");
#pragma unroll
  for (int i = 0; i < 3; ++i) { DS_W128(c.ww[i], wr[0][i], 0); DS_W128(c.ww[i], wr[1][i], G1_WSLOT_B); }
#pragma unroll
  for (int i = 0; i < 4; ++i) DS_W128(c.xw, xr[0][i], i * 1024);
  G1_ISSUE_W(0, 3); G1_ISSUE_W(1, 4); G1_ISSUE_X(0, 2);
  asm volatile("s_waitcnt lgkmcnt(0)\n\ts_barrier" ::: "memory");
  g1_read<0>(fa, c);
  G1Step<0>::run(acc, xr, wr, fa, fb2, c);
  asm volatile("s_nop 15\n\ts_nop 15" : "+v"(acc[0]), "+v"(acc[1]), "+v"(acc[2]), "+v"(acc[3]), "+v"(acc[4]), "+v"(acc[5]));

  unsigned keep[3][8];
  {
    unsigned give[3][8];
#pragma unroll
    for (int tt = 0; tt < 3; ++tt)
#pragma unroll
      for (int e = 0; e < 8; ++e) give[tt][e] = pk_bf16(acc[3 + tt][2 * e], acc[3 + tt][2 * e + 1]);
    __syncthreads();
    char* xo = smem + wave * 6144 + lane * 16;
#pragma unroll
    for (int tt = 0; tt < 3; ++tt) {
      *(u32x4*)(xo + (2 * tt) * 1024) = u32x4{give[tt][0], give[tt][1], give[tt][2], give[tt][3]};
      *(u32x4*)(xo + (2 * tt + 1) * 1024) = u32x4{give[tt][4], give[tt][5], give[tt][6], give[tt][7]};
    }
  }
#pragma unroll
  for (int tt = 0; tt < 3; ++tt)
#pragma unroll
    for (int e = 0; e < 8; ++e) keep[tt][e] = pk_bf16(acc[tt][2 * e], acc[tt][2 * e + 1]);
  __syncthreads();
  {
    const char* xi = smem + (wave ^ 2) * 6144 + lane * 16;
    const int mt = 4 * mb + 2 * wm + isup;
    u16* hp = hf + ((size_t)(mt * NKS + 6 * fb) * 64 + lane) * 8;
    auto finish = [&](auto ISUP) {
#pragma unroll
      for (int tt = 0; tt < 3; ++tt) {
        const int ft = 3 * fb + tt;
        if (ft < FF / 32) {
          const u32x4 o0 = *(const u32x4*)(xi + (2 * tt) * 1024);
          const u32x4 o1 = *(const u32x4*)(xi + (2 * tt + 1) * 1024);
          unsigned gp[8], up[8];
#pragma unroll
          for (int e = 0; e < 8; ++e) {
            const unsigned ot = e < 4 ? o0[e & 3] : o1[e & 3];
            gp[e] = decltype(ISUP)::value ? ot : keep[tt][e];
            up[e] = decltype(ISUP)::value ? keep[tt][e] : ot;
          }
          float xg[16], ar[16];
#pragma unroll
          for (int e = 0; e < 8; ++e) { xg[2 * e] = bf_lo(gp[e]); xg[2 * e + 1] = bf_hi(gp[e]); }
          const float c1 = -2.30220819814f, c2 = -0.10294323958f, one = 1.0f;
#pragma unroll
          for (int e = 0; e < 16; ++e) ar[e] = smul(xg[e], xg[e]);
#pragma unroll
          for (int e = 0; e < 16; ++e) ar[e] = sfma(ar[e], c2, c1);
#pragma unroll
          for (int e = 0; e < 16; ++e) ar[e] = __builtin_amdgcn_exp2f(smul(ar[e], xg[e]));
          TRANS_FENCE8(ar, 0);
          TRANS_FENCE8(ar, 8);
#pragma unroll
          for (int e = 0; e < 16; ++e) ar[e] = __builtin_amdgcn_rcpf(sadd(ar[e], one));
          TRANS_FENCE8(ar, 0);
          TRANS_FENCE8(ar, 8);
#pragma unroll
          for (int e = 0; e < 16; ++e) ar[e] = smul(ar[e], xg[e]);
          unsigned o[8];
#pragma unroll
          for (int e = 0; e < 8; ++e) o[e] = pk_bf16(ar[2 * e], ar[2 * e + 1]);
#pragma unroll
          for (int e = 0; e < 8; ++e) o[e] = pk_bf16(smul(bf_lo(o[e]), bf_lo(up[e])), smul(bf_hi(o[e]), bf_hi(up[e])));
          { const u32x4 v0 = u32x4{o[0], o[1], o[2], o[3]}, v1 = u32x4{o[4], o[5], o[6], o[7]};
            asm volatile("global_store_dwordx4 %0, %1, off sc1\n\ts_nop 1" ::"v"(hp + (2 * tt) * 512), "v"(v0) : "memory");
            asm volatile("global_store_dwordx4 %0, %1, off sc1\n\ts_nop 1" ::"v"(hp + (2 * tt + 1) * 512), "v"(v1) : "memory"); }
        } else if (ft < NKS / 2) {
          *(u32x4*)(hp + (2 * tt) * 512) = u32x4{0, 0, 0, 0};
          *(u32x4*)(hp + (2 * tt + 1) * 512) = u32x4{0, 0, 0, 0};
        }
      }
    };
    if (isup) finish(std::integral_constant<bool, true>{});
    else finish(std::integral_constant<bool, false>{});
  }
}

__device__ __forceinline__ void gate_unit(int b, const u16* __restrict__ x, const u16* __restrict__ gw,
                                          float* __restrict__ gout) {
  const int tid = threadIdx.x, lane = tid & 63, wave = tid >> 6;
  const int j = lane & 7, rr = lane >> 3;
  u32x4 gv[8];
#pragma unroll
  for (int i = 0; i < 8; ++i) gv[i] = *(const u32x4*)(gw + (j + 8 * i) * 8);
  const int row0 = b * 128 + wave * 32 + rr;
  u32x4 xv[4][8];
#pragma unroll
  for (int ps = 0; ps < 4; ++ps)
#pragma unroll
    for (int i = 0; i < 8; ++i) xv[ps][i] = *(const u32x4*)(x + (size_t)(row0 + 8 * ps) * DM + (j + 8 * i) * 8);
#pragma unroll
  for (int ps = 0; ps < 4; ++ps) {
    float s0 = 0.f, s1 = 0.f;
#pragma unroll
    for (int i = 0; i < 8; ++i)
#pragma unroll
      for (int e = 0; e < 4; ++e) {
        const unsigned pr = pk_bf16(smul(bf_lo(xv[ps][i][e]), bf_lo(gv[i][e])), smul(bf_hi(xv[ps][i][e]), bf_hi(gv[i][e])));
        s0 = sadd(s0, bf_lo(pr));
        s1 = sadd(s1, bf_hi(pr));
      }
    float s = s0 + s1;
    s += __shfl_xor(s, 1);
    s += __shfl_xor(s, 2);
    s += __shfl_xor(s, 4);
    if (j == 0) {
      const float sb = rbf(s);
      gout[row0 + 8 * ps] = rbf(1.0f / (1.0f + expf(-sb)));
    }
  }
}

__global__ __launch_bounds__(256, 2) void k1_kernel(const u16* __restrict__ u, const u16* __restrict__ logAr,
                                                   const u16* __restrict__ Aim, const u16* __restrict__ Cre,
                                                   const u16* __restrict__ Cim, const u16* __restrict__ logdt,
                                                   const u16* __restrict__ Dskip, const u16* __restrict__ wg,
                                                   const u16* __restrict__ wu, const u16* __restrict__ gw,
                                                   const u16* __restrict__ wd, u16* __restrict__ hbuf, u16* __restrict__ aT,
                                                   float* __restrict__ gout DBG_ARG
                                                   ) {
  __shared__ __attribute__((aligned(4096))) char smem[G1_LDS > SSM_LDS ? G1_LDS : SSM_LDS];
  const int bid = blockIdx.x;
  if (bid < NSSM) {
    ssm_unit(bid, u, logAr, Aim, Cre, Cim, logdt, Dskip, aT, smem DBG_PASS);
    unsigned acc = 0;
    for (int i = (bid >> 3) * 256 + threadIdx.x; i < 11008; i += 32 * 256) acc += ((const unsigned*)wd)[i * 32];
    asm volatile("" ::"v"(acc));
  } else {
    const int xcd = bid & 7, idx = (bid - NSSM) >> 3;
    const int fb = 4 * (xcd >> 1) + (idx >> 3), mb = 8 * (xcd & 1) + (idx & 7);
    if (fb < 15) gemm1_tile(16 * fb + mb, u, wg, wu, hbuf, smem DBG_PASS);
    else gate_unit(mb, u, gw, gout);
  }
}

constexpr int G2_SLOT_B = 4096;
constexpr int G2_WAVE_B = 2 * G2_SLOT_B;
constexpr int G2_PITCH = 272;
constexpr int G2_A_OFF = 4 * 64 * G2_PITCH;
constexpr int K2_LDS = G2_A_OFF + 8192;
#define G2D 3
constexpr int G2_DEPTH = G2D;

struct G2Ctx {
  const u16* hsrc;
  const u16* wd;
  int wrow0, wcol;
  unsigned wb32, woff, ww;
};

template <int ST>
struct G2Step {
  static __device__ __forceinline__ void issue(bf16x8 (&hfr)[11][4], u32x4 (&wr)[11][4], const G2Ctx& c) {
#pragma unroll
    for (int i = 0; i < 4; ++i) {
      int kr = c.wrow0 + ST * 32 + 8 * i;
      kr = kr < FF ? kr : FF - 1;
      GLOAD128(wr[ST][i], c.wd + (size_t)kr * DM + c.wcol);
    }
#pragma unroll
    for (int mi = 0; mi < 2; ++mi)
#pragma unroll
      for (int i = 0; i < 2; ++i)
        GLOAD128A(hfr[ST][2 * mi + i], c.hsrc + (size_t)(mi * NKS + 2 * ST + i) * 512);

  }
  static __device__ __forceinline__ void run(f32x16 (&acc)[2][2], bf16x8 (&hfr)[11][4], u32x4 (&wr)[11][4], const G2Ctx& c) {
    constexpr int LAST = (ST + G2_DEPTH - 1 < 10) ? ST + G2_DEPTH - 1 : 10;
    constexpr int NW = 8 * (LAST - ST);
    asm volatile("s_waitcnt vmcnt(%4)" : "+v"(wr[ST][0]), "+v"(wr[ST][1]), "+v"(wr[ST][2]), "+v"(wr[ST][3]) : "i"(NW + 4) : "memory");
#pragma unroll
    for (int i = 0; i < 4; ++i) DS_W128(c.ww, wr[ST][i], (ST & 1) * G2_SLOT_B + i * 1024);
    asm volatile("s_waitcnt lgkmcnt(0)" ::: "memory");
    const unsigned sb = c.wb32 + c.woff;
    s16x4 w[2][2][2];
#pragma unroll
    for (int i = 0; i < 2; ++i)
#pragma unroll
      for (int t = 0; t < 2; ++t) {
        DS_TR(w[i][t][0], sb ^ (t * 64), (ST & 1) * G2_SLOT_B + i * 2048);
        DS_TR(w[i][t][1], sb ^ (t * 64), (ST & 1) * G2_SLOT_B + i * 2048 + 1024);
      }
    asm volatile("s_waitcnt vmcnt(%4)" : "+a"(hfr[ST][0]), "+a"(hfr[ST][1]), "+a"(hfr[ST][2]), "+a"(hfr[ST][3]) : "i"(NW) : "memory");
    if constexpr (ST + G2_DEPTH <= 10) G2Step<ST + G2_DEPTH>::issue(hfr, wr, c);
    asm volatile("s_waitcnt lgkmcnt(0)"
                 : "+v"(w[0][0][0]), "+v"(w[0][0][1]), "+v"(w[0][1][0]), "+v"(w[0][1][1]), "+v"(w[1][0][0]), "+v"(w[1][0][1]),
                   "+v"(w[1][1][0]), "+v"(w[1][1][1])::"memory");
#pragma unroll
    for (int i = 0; i < 2; ++i)
#pragma unroll
      for (int t = 0; t < 2; ++t)
#pragma unroll
        for (int mi = 0; mi < 2; ++mi)
          acc[t][mi] = __builtin_amdgcn_mfma_f32_32x32x16_bf16(cat4(w[i][t][0], w[i][t][1]), hfr[ST][2 * mi + i], acc[t][mi], 0, 0, 0);
    if constexpr (ST + 1 <= 10) G2Step<ST + 1>::run(acc, hfr, wr, c);
  }
};

__global__ __launch_bounds__(256, 1) void k2_kernel(const u16* __restrict__ hf, const u16* __restrict__ wd,
                                                   const u16* __restrict__ aT, const float* __restrict__ gin,
                                                   u16* __restrict__ out DBG2_ARG) {
  __shared__ __attribute__((aligned(4096))) char smem[K2_LDS];
  const int tid = threadIdx.x, lane = tid & 63;
  STAMP2(0);
  const int wave = __builtin_amdgcn_readfirstlane(tid >> 6);
  const int bid = blockIdx.x;
  const int xcd = bid & 7, idx = bid >> 3;
  const int m0 = 64 * (4 * xcd + (idx >> 3)), n0 = 64 * (idx & 7);

#pragma unroll
  for (int i = 0; i < 2; ++i) {
    const int piece = 2 * wave + i;
    const int drow = 8 * piece + (lane >> 3);
    glds16(aT + (size_t)(n0 + drow) * SEQ + m0 + (lane & 7) * 8, smem + G2_A_OFF + piece * 1024 + lane * 16);
  }

  G2Ctx c;
  c.wb32 = lds_addr(smem) + wave * G2_WAVE_B;
  c.hsrc = hf + ((size_t)((m0 >> 5) * NKS + 22 * wave) * 64 + lane) * 8;
  c.wd = wd;
  c.wcol = n0 + 8 * (lane & 7);
  c.wrow0 = 352 * wave + (lane >> 3);
  c.ww = c.wb32 + (lane >> 3) * 128 + (((lane & 7) ^ (((lane >> 4) & 1) << 2)) * 16);

  const int r = lane & 31, h = lane >> 5;
  const int blk = (lane >> 4) & 1, q = (lane & 15) >> 2, p = lane & 3;
  c.woff = (4 * h + q) * 128 + (((2 * blk + (p >> 1)) ^ (((q >> 1) & 1) << 2)) * 16) + 8 * (p & 1);

  f32x16 acc[2][2];
#pragma unroll
  for (int i = 0; i < 2; ++i)
#pragma unroll
    for (int j = 0; j < 2; ++j) acc[i][j] = f32x16{};
  bf16x8 hfr[11][4];
  u32x4 wr[11][4];

  G2Step<0>::issue(hfr, wr, c);
  G2Step<1>::issue(hfr, wr, c);
  if constexpr (G2_DEPTH > 2) G2Step<2>::issue(hfr, wr, c);
  if constexpr (G2_DEPTH > 3) G2Step<3>::issue(hfr, wr, c);
  if constexpr (G2_DEPTH > 4) G2Step<4>::issue(hfr, wr, c);
  if constexpr (G2_DEPTH > 5) G2Step<5>::issue(hfr, wr, c);
  if constexpr (G2_DEPTH > 6) G2Step<6>::issue(hfr, wr, c);
  STAMP2(1);
  STAMP2(2);
  G2Step<0>::run(acc, hfr, wr, c);
  STAMP2(3);
  __syncthreads();
  {
    char* pw = smem + wave * (64 * G2_PITCH);
#pragma unroll
    for (int c = 0; c < 2; ++c)
#pragma unroll
      for (int mi = 0; mi < 2; ++mi)
#pragma unroll
        for (int gq = 0; gq < 4; ++gq) {
          const f32x4 v = {acc[c][mi][4 * gq], acc[c][mi][4 * gq + 1], acc[c][mi][4 * gq + 2], acc[c][mi][4 * gq + 3]};
          *(f32x4*)(pw + (32 * mi + r) * G2_PITCH + (32 * c + 8 * gq + 4 * h) * 4) = v;
        }
  }
  __syncthreads();
  STAMP2(4);
  {
    const int m = tid >> 2, nq = (tid & 3) * 16;
    const float g = gin[m0 + m];
    const float omg = rbf(1.0f - g);
    float b[16];
#pragma unroll
    for (int j = 0; j < 4; ++j) {
      f32x4 s = *(const f32x4*)(smem + m * G2_PITCH + (nq + 4 * j) * 4);
#pragma unroll
      for (int w = 1; w < 4; ++w) s += *(const f32x4*)(smem + w * (64 * G2_PITCH) + m * G2_PITCH + (nq + 4 * j) * 4);
#pragma unroll
      for (int e = 0; e < 4; ++e) b[4 * j + e] = s[e];
    }
    const u16* aimg = (const u16*)(smem + G2_A_OFF);
    unsigned o[8];
#pragma unroll
    for (int j = 0; j < 8; ++j) {
      const float a0 = bf2f(aimg[(nq + 2 * j) * 64 + m]);
      const float a1 = bf2f(aimg[(nq + 2 * j + 1) * 64 + m]);
      const float o0 = rbf(g * a0) + rbf(omg * rbf(b[2 * j]));
      const float o1 = rbf(g * a1) + rbf(omg * rbf(b[2 * j + 1]));
      o[j] = pk_bf16(o0, o1);
    }
    u16* op = out + (size_t)(m0 + m) * DM + n0 + nq;
    *(u32x4*)op = u32x4{o[0], o[1], o[2], o[3]};
    *(u32x4*)(op + 8) = u32x4{o[4], o[5], o[6], o[7]};
  }
  STAMP2(5);
}

extern "C" void kernel_launch(void* const* d_in, const int* in_sizes, int n_in, void* d_out, int out_size,
                              void* d_ws, size_t ws_size, hipStream_t stream) {
  const u16* u = (const u16*)d_in[0];
  const u16* logAr = (const u16*)d_in[1];
  const u16* Aim = (const u16*)d_in[2];
  const u16* Cre = (const u16*)d_in[3];
  const u16* Cim = (const u16*)d_in[4];
  const u16* logdt = (const u16*)d_in[5];
  const u16* Dskip = (const u16*)d_in[6];
  const u16* wg = (const u16*)d_in[8];
  const u16* wu = (const u16*)d_in[9];
  const u16* wd = (const u16*)d_in[10];
  const u16* gw = (const u16*)d_in[11];
  char* ws = (char*)d_ws;
  u16* hbuf = (u16*)ws;
  u16* aT = (u16*)(ws + (size_t)SEQ * FFP * 2);
  float* gbuf = (float*)(ws + (size_t)SEQ * FFP * 2 + (size_t)DM * SEQ * 2);
  k1_kernel<<<512, 256, 0, stream>>>(u, logAr, Aim, Cre, Cim, logdt, Dskip, wg, wu, gw, wd, hbuf, aT, gbuf
      );
  k2_kernel<<<256, 256, 0, stream>>>(hbuf, wd, aT, gbuf, (u16*)d_out
      );
}
```

```cpp
#define EXP_PRIO 2
#define EXP_PRIOE 0
#define EXP_ULATE
#include <hip/hip_runtime.h>
#include <hip/hip_bf16.h>
#include <stdint.h>
#include <type_traits>

typedef unsigned short u16;
typedef __attribute__((ext_vector_type(8))) short bf16x8;
typedef __attribute__((ext_vector_type(4))) short s16x4;
typedef __attribute__((ext_vector_type(4))) float f32x4;
typedef __attribute__((ext_vector_type(16))) float f32x16;
typedef __attribute__((ext_vector_type(4))) unsigned u32x4;
typedef __attribute__((ext_vector_type(2))) unsigned u32x2;
typedef __attribute__((ext_vector_type(2))) __bf16 bf16x2_t;
typedef __attribute__((ext_vector_type(2))) float f32x2;
typedef __attribute__((address_space(3))) s16x4* lds_s16x4_ptr;

constexpr int SEQ = 2048, DM = 512, NST = 128, FF = 1376, FFP = 1408;
constexpr int NSSM = 256;
constexpr int NKS = 88;

__device__ __forceinline__ unsigned pk_bf16(float lo, float hi) {
  f32x2 v = {lo, hi};
  bf16x2_t b = __builtin_convertvector(v, bf16x2_t);
  return __builtin_bit_cast(unsigned, b);
}
__device__ __forceinline__ float bf_lo(unsigned w) { return __uint_as_float(w << 16); }
__device__ __forceinline__ float bf_hi(unsigned w) { return __uint_as_float(w & 0xffff0000u); }
__device__ __forceinline__ float rbf(float x) { return bf_lo(pk_bf16(x, 0.f)); }
__device__ __forceinline__ float bf2f(u16 v) { return __uint_as_float(((unsigned)v) << 16); }

__device__ __forceinline__ float smul(float a, float b) { float r; asm("v_mul_f32 %0, %1, %2" : "=v"(r) : "v"(a), "v"(b)); return r; }
__device__ __forceinline__ float sadd(float a, float b) { float r; asm("v_add_f32 %0, %1, %2" : "=v"(r) : "v"(a), "v"(b)); return r; }
__device__ __forceinline__ float ssub(float a, float b) { float r; asm("v_sub_f32 %0, %1, %2" : "=v"(r) : "v"(a), "v"(b)); return r; }
__device__ __forceinline__ float sfma(float a, float b, float c) { float r; asm("v_fma_f32 %0, %1, %2, %3" : "=v"(r) : "v"(a), "v"(b), "v"(c)); return r; }
__device__ __forceinline__ float sfms(float a, float b, float c) { float r; asm("v_fma_f32 %0, -%1, %2, %3" : "=v"(r) : "v"(a), "v"(b), "v"(c)); return r; }
__device__ __forceinline__ float sfnn(float a, float b, float c) { float r; asm("v_fma_f32 %0, -%1, %2, -%3" : "=v"(r) : "v"(a), "v"(b), "v"(c)); return r; }
#define TRANS_FENCE8(a, o) asm volatile("s_nop 1" : "+v"(a[o]), "+v"(a[o + 1]), "+v"(a[o + 2]), "+v"(a[o + 3]), "+v"(a[o + 4]), "+v"(a[o + 5]), "+v"(a[o + 6]), "+v"(a[o + 7]))
__device__ __forceinline__ bf16x8 mk8(unsigned a, unsigned b, unsigned c, unsigned d) {
  u32x4 v = {a, b, c, d};
  return __builtin_bit_cast(bf16x8, v);
}
__device__ __forceinline__ bf16x8 cat4(s16x4 a, s16x4 b) {
  return __builtin_shufflevector(a, b, 0, 1, 2, 3, 4, 5, 6, 7);
}
__device__ __forceinline__ s16x4 tr_read(const char* smem_ptr) {
  return __builtin_amdgcn_ds_read_tr16_b64_v4i16((lds_s16x4_ptr)(smem_ptr));
}
__device__ __forceinline__ unsigned lds_addr(const void* p) {
  return (unsigned)(size_t)(const __attribute__((address_space(3))) char*)p;
}
__device__ __forceinline__ void glds16(const void* g, char* l) {
  __builtin_amdgcn_global_load_lds((const unsigned*)g, (unsigned*)l, 16, 0, 0);
}

#define STAMP2(i)
#define DBG2_ARG
#define DBG2_PASS
#define STAMP(i)
#define DBG_ARG
#define DBG_PASS
constexpr int U_COPY_B = 4160;
constexpr int U_CH_B = 4 * U_COPY_B;
constexpr int U_REGION_B = 2 * U_CH_B;
constexpr int KR_PART_B = 4576;
constexpr int KR_OFF = U_REGION_B;
constexpr int KQ_B = 8208;
constexpr int KX_OFF = 2 * KQ_B;
constexpr int SSM_LDS = KR_OFF + 4 * KR_PART_B;
static_assert(KX_OFF + 4 * 1536 <= U_REGION_B, "scratch must fit the U region");

#define DS_TR(dst, addr, off) asm volatile("ds_read_b64_tr_b16 %0, %1 offset:%2" : "=v"(dst) : "v"(addr), "i"(off) : "memory")
#define DS_B128(dst, addr, off) asm volatile("ds_read_b128 %0, %1 offset:%2" : "=v"(dst) : "v"(addr), "i"(off) : "memory")
#define DS_B64(dst, addr, off) asm volatile("ds_read_b64 %0, %1 offset:%2" : "=v"(dst) : "v"(addr), "i"(off) : "memory")
#define MFMA16(acc, a, b) asm volatile("v_mfma_f32_16x16x32_bf16 %0, %1, %2, %0" : "+v"(acc) : "v"(a), "v"(b))

struct ConvFrag {
  s16x4 a[8][2];
  bf16x8 bh[8], bl[8];
};
template <int RI, int K>
__device__ __forceinline__ void conv_read(ConvFrag& f, unsigned ua, unsigned kb) {
  if constexpr (K < 16) {
    DS_B64(f.a[K >> 1][K & 1], ua, 64 * (8 * (K >> 1) + RI) + 8 * (K & 1));
  } else if constexpr (K < 24) {
    DS_B128(f.bh[K - 16], kb, 64 * (56 - 8 * (K - 16) + RI));
  } else {
    DS_B128(f.bl[K - 24], kb, 64 * (56 - 8 * (K - 24) + RI) + KR_PART_B);
  }
}
template <int RI, int K>
__device__ __forceinline__ void conv_read_all(ConvFrag& f, unsigned ua, unsigned kb) {
  conv_read<RI, K>(f, ua, kb);
  if constexpr (K + 1 < 32) conv_read_all<RI, K + 1>(f, ua, kb);
}
#define CONV_WAIT(f)                                                                                                  \
  do {                                                                                                                \
    asm volatile("s_waitcnt lgkmcnt(0)"                                                                               \
                 : "+v"(f.a[0][0]), "+v"(f.a[0][1]), "+v"(f.a[1][0]), "+v"(f.a[1][1]), "+v"(f.a[2][0]), "+v"(f.a[2][1]), \
                   "+v"(f.a[3][0]), "+v"(f.a[3][1]), "+v"(f.a[4][0]), "+v"(f.a[4][1]), "+v"(f.a[5][0]), "+v"(f.a[5][1]), \
                   "+v"(f.a[6][0]), "+v"(f.a[6][1]), "+v"(f.a[7][0]), "+v"(f.a[7][1])::"memory");                     \
    asm volatile("" : "+v"(f.bh[0]), "+v"(f.bh[1]), "+v"(f.bh[2]), "+v"(f.bh[3]), "+v"(f.bh[4]), "+v"(f.bh[5]),        \
                 "+v"(f.bh[6]), "+v"(f.bh[7]), "+v"(f.bl[0]), "+v"(f.bl[1]), "+v"(f.bl[2]), "+v"(f.bl[3]),            \
                 "+v"(f.bl[4]), "+v"(f.bl[5]), "+v"(f.bl[6]), "+v"(f.bl[7])::"memory");                              \
  } while (0)
constexpr int conv_tau(int P) { int t = 0; while (P >= 8 - t) { P -= 8 - t; ++t; } return t; }
constexpr int conv_sig(int P) { int t = 0; while (P >= 8 - t) { P -= 8 - t; ++t; } return P; }
template <int RI, int P>
__device__ __forceinline__ void conv_pairs(f32x4 (&acc)[8], const bf16x8 (&A)[8], ConvFrag& cur, ConvFrag& nxt, unsigned ua,
                                           unsigned kb) {
  constexpr int T = conv_tau(P), SG = conv_sig(P);
  MFMA16(acc[T + SG], A[SG], cur.bl[T]);
  if constexpr (RI < 3 && P < 32) conv_read<RI + 1, P>(nxt, ua, kb);
  MFMA16(acc[T + SG], A[SG], cur.bh[T]);
  if constexpr (P + 1 < 36) conv_pairs<RI, P + 1>(acc, A, cur, nxt, ua, kb);
}
template <int RI>
__device__ __forceinline__ void conv_rho(f32x4 (&acc)[8], ConvFrag& cur, ConvFrag& nxt, unsigned ua, unsigned kb) {
  CONV_WAIT(cur);
  bf16x8 A[8];
#pragma unroll
  for (int i = 0; i < 8; ++i) A[i] = cat4(cur.a[i][0], cur.a[i][1]);
  conv_pairs<RI, 0>(acc, A, cur, nxt, ua, kb);
  if constexpr (RI < 3) conv_rho<RI + 1>(acc, nxt, cur, ua, kb);
}

__device__ __forceinline__ void split_pair(float v0, float v1, unsigned& hi, unsigned& lo) {
  hi = pk_bf16(v0, v1);
  lo = pk_bf16(ssub(v0, bf_lo(hi)), ssub(v1, bf_hi(hi)));
}

__device__ __forceinline__ void ssm_unit(int w, const u16* __restrict__ u, const u16* __restrict__ logAr,
                                         const u16* __restrict__ Aim, const u16* __restrict__ Cre,
                                         const u16* __restrict__ Cim, const u16* __restrict__ logdt,
                                         const u16* __restrict__ Dskip, u16* aT, unsigned* flags, char* smem DBG_ARG) {
  const int tid = threadIdx.x, lane = tid & 63;
  const int wave = __builtin_amdgcn_readfirstlane(tid >> 6);
  const int ch = wave >> 1, w2 = wave & 1;
  const int d0 = 2 * w;
  const int d = d0 + ch;

  const int pidx = d0 * NST + tid;
  const u16 r_lar = logAr[pidx], r_aim = Aim[pidx], r_cre = Cre[pidx], r_cim = Cim[pidx], r_ldt = logdt[d], r_dsk = Dskip[d];
  const unsigned* u32p = (const unsigned*)(u + d0);
  unsigned uw[8], up[3];
#pragma unroll
  for (int i = 0; i < 3; ++i) up[i] = 0;
  if (lane == 0 && tid > 0) {
#pragma unroll
    for (int i = 0; i < 3; ++i) up[i] = u32p[(size_t)(8 * tid - 3 + i) * (DM / 2)];
  }
  const unsigned uoff = (unsigned)tid * 8192u;
  {
    const float dt = rbf(expf(bf2f(r_ldt)));
    const float are = rbf(expf(bf2f(r_lar)));
    f32x4 prm;
    prm[0] = (-are * dt) * 1.44269504088896340736f;
    prm[1] = (bf2f(r_aim) * dt) * 0.15915494309189533577f;
    prm[2] = 2.0f * (bf2f(r_cre) * dt);
    prm[3] = 2.0f * (bf2f(r_cim) * dt);
    *(f32x4*)(smem + KR_OFF + tid * 16) = prm;
  }
  __syncthreads();

  const int r = lane & 31, h = lane >> 5;
  f32x16 dacc = {};
  {
    int p1i;
    if (r < 8) p1i = r;
    else if (r < 12) p1i = 256 + 2 * (r - 8);
    else if (r < 16) p1i = 512 + 256 * ((r - 12) >> 1) + 4 * ((r - 12) & 1);
    else if (r < 21) p1i = 1024 + 256 * (r - 16);
    else p1i = 0;
    const float p1 = (float)p1i, p2 = (float)(8 * r);
    const char* prm_base = smem + KR_OFF + (ch * 128 + 64 * w2 + 4 * h) * 16;
#pragma unroll
    for (int ks = 0; ks < 8; ++ks) {
      f32x4 prm[4];
#pragma unroll
      for (int q = 0; q < 4; ++q) prm[q] = *(const f32x4*)(prm_base + (8 * ks + q) * 16);
      float ea[4], eb[4], fa[4], fb[4];
#pragma unroll
      for (int q = 0; q < 4; ++q) {
        ea[q] = __builtin_amdgcn_exp2f(smul(prm[q][0], p1)); eb[q] = __builtin_amdgcn_exp2f(smul(prm[q][0], p2));
        fa[q] = __builtin_amdgcn_fractf(smul(prm[q][1], p1)); fb[q] = __builtin_amdgcn_fractf(smul(prm[q][1], p2));
      }
      float ac[4], as[4], bc[4], bs[4];
#pragma unroll
      for (int q = 0; q < 4; ++q) {
        ac[q] = __builtin_amdgcn_cosf(fa[q]); as[q] = __builtin_amdgcn_sinf(fa[q]);
        bc[q] = __builtin_amdgcn_cosf(fb[q]); bs[q] = __builtin_amdgcn_sinf(fb[q]);
      }
      asm volatile("s_nop 1" : "+v"(ea[0]), "+v"(ea[1]), "+v"(ea[2]), "+v"(ea[3]), "+v"(eb[0]), "+v"(eb[1]), "+v"(eb[2]), "+v"(eb[3]),
                   "+v"(ac[0]), "+v"(ac[1]), "+v"(ac[2]), "+v"(ac[3]), "+v"(as[0]), "+v"(as[1]), "+v"(as[2]), "+v"(as[3]),
                   "+v"(bc[0]), "+v"(bc[1]), "+v"(bc[2]), "+v"(bc[3]), "+v"(bs[0]), "+v"(bs[1]), "+v"(bs[2]), "+v"(bs[3]));
      unsigned ah[4], al[4], bh[4], bl[4];
#pragma unroll
      for (int q = 0; q < 4; ++q) {
        const float cr = smul(prm[q][2], ea[q]), ci = smul(prm[q][3], ea[q]);
        const float gre = sfms(ci, as[q], smul(cr, ac[q]));
        const float gmi = sfnn(ci, ac[q], smul(cr, as[q]));
        split_pair(gre, gmi, ah[q], al[q]);
        split_pair(smul(bc[q], eb[q]), smul(bs[q], eb[q]), bh[q], bl[q]);
      }
      const bf16x8 Ah = mk8(ah[0], ah[1], ah[2], ah[3]), Al = mk8(al[0], al[1], al[2], al[3]);
      const bf16x8 Bh = mk8(bh[0], bh[1], bh[2], bh[3]), Bl = mk8(bl[0], bl[1], bl[2], bl[3]);
      dacc = __builtin_amdgcn_mfma_f32_32x32x16_bf16(Al, Bh, dacc, 0, 0, 0);
      dacc = __builtin_amdgcn_mfma_f32_32x32x16_bf16(Ah, Bl, dacc, 0, 0, 0);
      dacc = __builtin_amdgcn_mfma_f32_32x32x16_bf16(Ah, Bh, dacc, 0, 0, 0);
    }
  }
#pragma unroll
  for (int ks = 0; ks < 8; ++ks)
    asm volatile("global_load_dword %0, %1, %2 offset:%3" : "=v"(uw[ks]) : "v"(uoff), "s"((const char*)u32p + (ks >> 2) * 4096), "i"((ks & 3) * 1024) : "memory");
  {
    float* xo = (float*)(smem + KX_OFF + wave * 1536) + lane;
    const float* xi = (const float*)(smem + KX_OFF + (wave ^ 1) * 1536) + lane;
    float mine[6];
    if (w2 == 0) {
#pragma unroll
      for (int e = 0; e < 6; ++e) { xo[e * 64] = dacc[6 + e]; mine[e] = dacc[e]; }
    } else {
#pragma unroll
      for (int e = 0; e < 6; ++e) { xo[e * 64] = dacc[e]; mine[e] = dacc[6 + e]; }
    }
    __syncthreads();
    unsigned* Kq = (unsigned*)(smem + ch * KQ_B);
    unsigned pq[6];
#pragma unroll
    for (int e = 0; e < 6; ++e) {
      const float v = sadd(mine[e], xi[e * 64]);
      const unsigned hi = pk_bf16(v, 0.f);
      pq[e] = pk_bf16(0.f, ssub(v, bf_lo(hi))) | hi;
    }
    if (w2 == 0) {
#pragma unroll
      for (int e = 0; e < 4; ++e) Kq[4 * h + e + 8 * r] = pq[e];
      Kq[(h ? 512 : 256) + 8 * r] = pq[4];
      Kq[(h ? 516 : 258) + 8 * r] = pq[5];
    } else {
      Kq[(h ? 768 : 260) + 8 * r] = pq[0];
      Kq[(h ? 772 : 262) + 8 * r] = pq[1];
      if (h == 0) {
#pragma unroll
        for (int e = 0; e < 4; ++e) Kq[1024 + 256 * e + 8 * r] = pq[2 + e];
      } else if (r == 0) {
        Kq[2048] = pq[2];
      }
    }
  }
  __syncthreads();

  {
    const int t = tid & 127;
    const unsigned* Kq = (const unsigned*)(smem + ch * KQ_B);
    unsigned* krh = (unsigned*)(smem + KR_OFF + (ch * 2) * KR_PART_B);
    unsigned* krl = (unsigned*)((char*)krh + KR_PART_B);
    unsigned q0[8], q1[8];
#pragma unroll
    for (int m = 0; m < 8; ++m) {
      const int sh = m < 4 ? 3 : (m < 6 ? 2 : (m < 7 ? 1 : 0));
      const int l0 = 2047 - 2 * (t + 128 * m), l1 = l0 - 1;
      int pa = l0, pb = l1;
      if (sh) {
        pa = (l0 + (1 << (sh - 1)) - 1 + ((l0 >> sh) & 1)) & ~((1 << sh) - 1);
        pb = (l1 + (1 << (sh - 1)) - 1 + ((l1 >> sh) & 1)) & ~((1 << sh) - 1);
      }
      q0[m] = Kq[pa];
      q1[m] = Kq[pb];
    }
#pragma unroll
    for (int m = 0; m < 8; ++m) {
      krh[t + 128 * m] = __builtin_amdgcn_perm(q1[m], q0[m], 0x05040100u);
      krl[t + 128 * m] = __builtin_amdgcn_perm(q1[m], q0[m], 0x07060302u);
    }
    if (t < 120) { krh[1024 + t] = 0; krl[1024 + t] = 0; }
  }
  __syncthreads();

  asm volatile("s_waitcnt vmcnt(0)" : "+v"(uw[0]), "+v"(uw[1]), "+v"(uw[2]), "+v"(uw[3]), "+v"(uw[4]), "+v"(uw[5]), "+v"(uw[6]), "+v"(uw[7])::"memory");
  {
    unsigned e[11];
#pragma unroll
    for (int i = 0; i < 3; ++i) {
      const unsigned nb = __shfl_up(uw[5 + i], 1);
      e[i] = (lane == 0) ? up[i] : nb;
    }
#pragma unroll
    for (int i = 0; i < 8; ++i) e[3 + i] = uw[i];
    unsigned p0[10], p1[10];
#pragma unroll
    for (int i = 0; i < 10; ++i) {
      p0[i] = __builtin_amdgcn_perm(e[i + 1], e[i], 0x05040100u);
      p1[i] = __builtin_amdgcn_perm(e[i + 1], e[i], 0x07060302u);
    }
#pragma unroll
    for (int c = 0; c < 4; ++c) {
      *(u32x4*)(smem + c * U_COPY_B + (tid + 2) * 16) = u32x4{p0[c], p0[c + 2], p0[c + 4], p0[c + 6]};
      *(u32x4*)(smem + U_CH_B + c * U_COPY_B + (tid + 2) * 16) = u32x4{p1[c], p1[c + 2], p1[c + 4], p1[c + 6]};
    }
    if (tid < 16) *(u32x4*)(smem + (tid >> 1) * U_COPY_B + (tid & 1) * 16) = u32x4{0, 0, 0, 0};
  }
  __syncthreads();

  f32x4 acc[8];
#pragma unroll
  for (int t = 0; t < 8; ++t) acc[t] = f32x4{0.f, 0.f, 0.f, 0.f};
  asm volatile("s_nop 4" : "+v"(acc[0]), "+v"(acc[1]), "+v"(acc[2]), "+v"(acc[3]), "+v"(acc[4]), "+v"(acc[5]), "+v"(acc[6]), "+v"(acc[7]));
  {
    const int r16 = lane & 15, hq = lane >> 4;
    const unsigned sb = lds_addr(smem);
    const unsigned ua = sb + ch * U_CH_B + (r16 & 3) * U_COPY_B + 2 * (4 + (r16 & ~3) + 8 * hq) + 64 * 4 * w2;
    const unsigned kb = sb + KR_OFF + (ch * 2) * KR_PART_B + 2 * (2032 - 16 * r16 + 8 * hq) - 64 * 56 + 64 * 4 * w2;
    ConvFrag f0, f1;
    conv_read_all<0, 0>(f0, ua, kb);
    conv_rho<0>(acc, f0, f1, ua, kb);
  }
  asm volatile("s_nop 15" : "+v"(acc[0]), "+v"(acc[1]), "+v"(acc[2]), "+v"(acc[3]), "+v"(acc[4]), "+v"(acc[5]), "+v"(acc[6]), "+v"(acc[7]));
  __syncthreads();
  {
    char* xw = smem + KR_OFF + (ch * 2 + w2) * 4096 + lane * 16;
    const char* xr = smem + KR_OFF + (ch * 2 + (w2 ^ 1)) * 4096 + lane * 16;
    f32x4 y[4];
    if (w2 == 0) {
#pragma unroll
      for (int i = 0; i < 4; ++i) { *(f32x4*)(xw + i * 1024) = acc[4 + i]; y[i] = acc[i]; }
    } else {
#pragma unroll
      for (int i = 0; i < 4; ++i) { *(f32x4*)(xw + i * 1024) = acc[i]; y[i] = acc[4 + i]; }
    }
    __syncthreads();
    const float Dk = bf2f(r_dsk);
#pragma unroll
    for (int i = 0; i < 4; ++i) {
      const f32x4 o = *(const f32x4*)(xr + i * 1024);
      const int l0 = 256 * (4 * w2 + i) + 16 * (lane & 15) + 4 * (lane >> 4);
      const u32x2 uu = *(const u32x2*)(smem + ch * U_CH_B + 3 * U_COPY_B + 2 * (16 + l0));
      const unsigned y01 = pk_bf16(sadd(y[i][0], o[0]), sadd(y[i][1], o[1])), y23 = pk_bf16(sadd(y[i][2], o[2]), sadd(y[i][3], o[3]));
      const unsigned s01 = pk_bf16(smul(bf_lo(uu[0]), Dk), smul(bf_hi(uu[0]), Dk)), s23 = pk_bf16(smul(bf_lo(uu[1]), Dk), smul(bf_hi(uu[1]), Dk));
      { const u32x2 av = u32x2{pk_bf16(sadd(bf_lo(y01), bf_lo(s01)), sadd(bf_hi(y01), bf_hi(s01))),
                                                  pk_bf16(sadd(bf_lo(y23), bf_lo(s23)), sadd(bf_hi(y23), bf_hi(s23)))};
        asm volatile("global_store_dwordx2 %0, %1, off sc1\n\ts_nop 1" ::"v"(aT + (size_t)d * SEQ + l0), "v"(av) : "memory"); }
    }
    asm volatile("s_waitcnt vmcnt(0)" ::: "memory");
    __syncthreads();
    if (tid == 0) __hip_atomic_store(flags + 16 + w, 1u, __ATOMIC_RELAXED, __HIP_MEMORY_SCOPE_AGENT);
  }
}

__device__ __forceinline__ float gelu_tanh(float x) {
  const float z = 0.7978845608028654f * (x + 0.044715f * x * x * x);
  const float e = __builtin_amdgcn_exp2f(-2.8853900817779268f * z);
  return x * __builtin_amdgcn_rcpf(1.0f + e);
}

constexpr int G1_WSLOT_B = 12288;
constexpr int G1_X_OFF = 3 * G1_WSLOT_B;
constexpr int G1_LDS = G1_X_OFF + 32768;

constexpr int g1_pos(bool isx, int id) {
  if (isx && id == 0) return 4;
  if (!isx && id == 0) return 7;
  if (!isx && id == 1) return 10;
  if (isx && id == 1) return 14;
  if (!isx && id == 2) return 17;
  if (!isx && id == 3) return 20;
  if (!isx && id == 4) return 23;
  if (isx && id == 2) return 27;
  int n = 27;
  for (int t = 0; t < 32; ++t) {
    if ((t & 1) == 0 && t / 2 + 5 < 16) { n += 3; if (!isx && t / 2 + 5 == id) return n; }
    if ((t & 3) == 1 && t / 4 + 3 < 8) { n += 4; if (isx && t / 4 + 3 == id) return n; }
  }
  return -1;
}
constexpr int g1_issued_before(int step) {
  int n = 27;
  for (int t = 0; t < step; ++t) {
    if ((t & 1) == 0 && t / 2 + 5 < 16) n += 3;
    if ((t & 3) == 1 && t / 4 + 3 < 8) n += 4;
  }
  return n;
}

#define GLOAD128(dst, ptr) asm volatile("global_load_dwordx4 %0, %1, off" : "=v"(dst) : "v"(ptr) : "memory")
#define GLOAD128S(dst, voff, sbase) asm volatile("global_load_dwordx4 %0, %1, %2" : "=v"(dst) : "v"(voff), "s"(sbase) : "memory")
#define GLOAD128SA(dst, voff, sbase) asm volatile("global_load_dwordx4 %0, %1, %2" : "=a"(dst) : "v"(voff), "s"(sbase) : "memory")
#define GLOAD128A(dst, ptr) asm volatile("global_load_dwordx4 %0, %1, off" : "=a"(dst) : "v"(ptr) : "memory")
#define DS_W128(addr, val, off) asm volatile("ds_write_b128 %0, %1 offset:%2" ::"v"(addr), "v"(val), "i"(off) : "memory")
#define MFMA32(acc, a, b) asm volatile("v_mfma_f32_32x32x16_bf16 %0, %1, %2, %0" : "+v"(acc) : "v"(a), "v"(b))

struct G1Ctx {
  const char* wbase;
  const char* xbase;
  unsigned wo[3], xo;
  unsigned woff, woff_o, ww[3], xw, xrd2[2][4];
  unsigned junk[4];
};
struct G1Frag {
  s16x4 w[3][2];
  bf16x8 x[2];
};

#define G1_RD_W(f, t, OFF)                                                        \
  do {                                                                            \
    if ((t) & 1) { DS_TR(f.w[t][0], c.woff_o, (OFF) + (t) * 64); DS_TR(f.w[t][1], c.woff_o, (OFF) + (t) * 64 + 4 * 384); } \
    else { DS_TR(f.w[t][0], c.woff, (OFF) + (t) * 64); DS_TR(f.w[t][1], c.woff, (OFF) + (t) * 64 + 4 * 384); }            \
  } while (0)
#define G1_RD_X(f, mi, N) DS_B128(f.x[mi], c.xrd2[mi][(N) & 3], (((N) >> 2) & 1) * 16384)
template <int N>
__device__ __forceinline__ void g1_read(G1Frag& f, const G1Ctx& c) {
  constexpr int OFF = ((N >> 1) % 3) * G1_WSLOT_B + (N & 1) * 16 * 384;
  G1_RD_X(f, 0, N); G1_RD_X(f, 1, N);
  G1_RD_W(f, 0, OFF); G1_RD_W(f, 1, OFF); G1_RD_W(f, 2, OFF);
}
#define G1_WAIT_FRAG(f, STR)                                                                                       \
  asm volatile(STR : "+v"(f.w[0][0]), "+v"(f.w[0][1]), "+v"(f.w[1][0]), "+v"(f.w[1][1]), "+v"(f.w[2][0]),        \
               "+v"(f.w[2][1]), "+v"(f.x[0]), "+v"(f.x[1])::"memory")

template <int N>
struct G1Step {
  static __device__ __forceinline__ void run(f32x16 (&acc)[6], u32x4 (&xr)[2][4], u32x4 (&wr)[3][3], G1Frag& cur,
                                             G1Frag& nxt, G1Ctx& c) {
    constexpr int ISS = g1_issued_before(N);
    constexpr int S = N >> 1;
    constexpr bool RD = N + 1 < 32;
    constexpr int OFF = (((N + 1) >> 1) % 3) * G1_WSLOT_B + ((N + 1) & 1) * 16 * 384;
    if constexpr ((N & 1) == 0 && N > 0) G1_WAIT_FRAG(cur, "s_waitcnt lgkmcnt(0)\n\ts_barrier");
    else G1_WAIT_FRAG(cur, "s_waitcnt lgkmcnt(0)");
    bf16x8 b[3];
#pragma unroll
    for (int t = 0; t < 3; ++t) b[t] = cat4(cur.w[t][0], cur.w[t][1]);
    MFMA32(acc[0], b[0], cur.x[0]);
    if constexpr (RD) { G1_RD_X(nxt, 0, N + 1); G1_RD_W(nxt, 0, OFF); }
    MFMA32(acc[3], b[0], cur.x[1]);
    if constexpr (RD) { G1_RD_X(nxt, 1, N + 1); G1_RD_W(nxt, 1, OFF); }
    MFMA32(acc[1], b[1], cur.x[0]);
    if constexpr (RD) { G1_RD_W(nxt, 2, OFF); }
    MFMA32(acc[4], b[1], cur.x[1]);
    if constexpr ((N & 1) == 0) {
      if constexpr (S + 2 < 16) {
        constexpr int R = (S + 2) % 3;
        constexpr int NW = ISS - g1_pos(false, S + 2);
        asm volatile("s_waitcnt vmcnt(%3)" : "+v"(wr[R][0]), "+v"(wr[R][1]), "+v"(wr[R][2]) : "i"(NW) : "memory");
#pragma unroll
        for (int i = 0; i < 3; ++i) DS_W128(c.ww[i], wr[R][i], R * G1_WSLOT_B);
      }
    }
    if constexpr ((N & 3) == 1) {
      constexpr int B = N / 4 + 1;
      if constexpr (B < 8) {
        constexpr int NW = ISS - g1_pos(true, B);
        asm volatile("s_waitcnt vmcnt(%4)" : "+v"(xr[B & 1][0]), "+v"(xr[B & 1][1]), "+v"(xr[B & 1][2]), "+v"(xr[B & 1][3]) : "i"(NW) : "memory");
#pragma unroll
        for (int i = 0; i < 4; ++i) DS_W128(c.xw, xr[B & 1][i], (B & 1) * 16384 + i * 1024);
      }
    }
    MFMA32(acc[2], b[2], cur.x[0]);
    if constexpr ((N & 1) == 0 && S + 5 < 16) {
#pragma unroll
      for (int i = 0; i < 3; ++i) GLOAD128S(wr[(S + 5) % 3][i], c.wo[i], c.wbase + (size_t)(S + 5) * 32 * FF * 2);
    }
    if constexpr ((N & 3) == 1 && N / 4 + 3 < 8) {
      constexpr int B = N / 4 + 1;
#pragma unroll
      for (int i = 0; i < 4; ++i) GLOAD128S(xr[B & 1][i], c.xo, c.xbase + (size_t)(8 * i) * DM * 2 + (B + 2) * 128);
    }
    MFMA32(acc[5], b[2], cur.x[1]);
    if constexpr (N + 1 < 32) G1Step<N + 1>::run(acc, xr, wr, nxt, cur, c);
  }
};

__device__ __forceinline__ void gemm1_tile(int t, const u16* __restrict__ x, const u16* __restrict__ wg,
                                           const u16* __restrict__ wu, u16* hf, char* smem DBG_ARG) {
  const int tid = threadIdx.x, lane = tid & 63;
  const int wave = __builtin_amdgcn_readfirstlane(tid >> 6);
  const int fb = t >> 4, mb = t & 15;
  const int f0 = 96 * fb;
  __builtin_amdgcn_s_setprio(EXP_PRIO);
  const int wm = wave & 1, isup = wave >> 1;
  G1Ctx c;
  const unsigned sbase = lds_addr(smem);
  c.wbase = (const char*)(isup ? wu : wg);
#pragma unroll
  for (int i = 0; i < 3; ++i) {
    const int id = 192 * (wave & 1) + 64 * i + lane;
    const int row = id / 12, cc = id % 12;
    int col = f0 + 8 * cc;
    if (col > FF - 8) col = FF - 8;
    c.wo[i] = (unsigned)(row * FF + col) * 2u;
    c.ww[i] = sbase + row * 384 + ((cc + 12 * isup) ^ (((row >> 1) & 1) << 2)) * 16;
  }
  c.xbase = (const char*)(x + (size_t)(128 * mb + 32 * wave) * DM);
  c.xo = (unsigned)((lane >> 3) * DM + (lane & 7) * 8) * 2u;
  const unsigned xl = sbase + G1_X_OFF;
  c.xw = xl + (32 * wave + (lane >> 3)) * 128 + (((lane & 7) ^ (lane >> 3)) * 16);
  const int r = lane & 31, h = lane >> 5;
#pragma unroll
  for (int i = 0; i < 4; ++i) {
    c.xrd2[0][i] = xl + (64 * wm + 32 * isup + r) * 128 + (((2 * i + h) ^ (r & 7)) * 16);
    c.xrd2[1][i] = xl + (64 * wm + 32 * (1 - isup) + r) * 128 + (((2 * i + h) ^ (r & 7)) * 16);
  }
  const int blk = (lane >> 4) & 1, q = (lane & 15) >> 2, p = lane & 3;
  const int sw = (q >> 1) & 1;
  {
    const unsigned e = sbase + (8 * h + q) * 384 + (2 * blk + (p >> 1) + 4 * sw) * 16 + 8 * (p & 1);
    const unsigned o = sbase + (8 * h + q) * 384 + (2 * blk + (p >> 1) - 4 * sw) * 16 + 8 * (p & 1);
    c.woff = isup ? o + 192 : e;
    c.woff_o = isup ? e + 192 : o;
  }

  f32x16 acc[6];
#pragma unroll
  for (int i = 0; i < 6; ++i) acc[i] = f32x16{};
  asm volatile("s_nop 4" : "+v"(acc[0]), "+v"(acc[1]), "+v"(acc[2]), "+v"(acc[3]), "+v"(acc[4]), "+v"(acc[5]));
  u32x4 xr[2][4], wr[3][3];
  G1Frag fa, fb2;
#define G1_ISSUE_X(R, B) _Pragma("unroll") for (int i = 0; i < 4; ++i) GLOAD128S(xr[R][i], c.xo, c.xbase + (size_t)(8 * i) * DM * 2 + (B) * 128)
#define G1_ISSUE_W(R, S) _Pragma("unroll") for (int i = 0; i < 3; ++i) GLOAD128S(wr[R][i], c.wo[i], c.wbase + (size_t)(S) * 32 * FF * 2)
  G1_ISSUE_X(0, 0); G1_ISSUE_W(0, 0); G1_ISSUE_W(1, 1); G1_ISSUE_X(1, 1); G1_ISSUE_W(2, 2);
#define G1_EXTRA 0
  asm volatile("s_waitcnt vmcnt(%10)"
               : "+v"(wr[0][0]), "+v"(wr[0][1]), "+v"(wr[0][2]), "+v"(wr[1][0]), "+v"(wr[1][1]), "+v"(wr[1][2]),
                 "+v"(xr[0][0]), "+v"(xr[0][1]), "+v"(xr[0][2]), "+v"(xr[0][3]) : "i"(7 + G1_EXTRA) : "memory");
#pragma unroll
  for (int i = 0; i < 3; ++i) { DS_W128(c.ww[i], wr[0][i], 0); DS_W128(c.ww[i], wr[1][i], G1_WSLOT_B); }
#pragma unroll
  for (int i = 0; i < 4; ++i) DS_W128(c.xw, xr[0][i], i * 1024);
  G1_ISSUE_W(0, 3); G1_ISSUE_W(1, 4); G1_ISSUE_X(0, 2);
  asm volatile("s_waitcnt lgkmcnt(0)\n\ts_barrier" ::: "memory");
  g1_read<0>(fa, c);
  G1Step<0>::run(acc, xr, wr, fa, fb2, c);
  asm volatile("s_nop 15\n\ts_nop 15" : "+v"(acc[0]), "+v"(acc[1]), "+v"(acc[2]), "+v"(acc[3]), "+v"(acc[4]), "+v"(acc[5]));

  __builtin_amdgcn_s_setprio(EXP_PRIOE);
  unsigned keep[3][8];
  {
    unsigned give[3][8];
#pragma unroll
    for (int tt = 0; tt < 3; ++tt)
#pragma unroll
      for (int e = 0; e < 8; ++e) give[tt][e] = pk_bf16(acc[3 + tt][2 * e], acc[3 + tt][2 * e + 1]);
    __syncthreads();
    char* xo = smem + wave * 6144 + lane * 16;
#pragma unroll
    for (int tt = 0; tt < 3; ++tt) {
      *(u32x4*)(xo + (2 * tt) * 1024) = u32x4{give[tt][0], give[tt][1], give[tt][2], give[tt][3]};
      *(u32x4*)(xo + (2 * tt + 1) * 1024) = u32x4{give[tt][4], give[tt][5], give[tt][6], give[tt][7]};
    }
  }
#pragma unroll
  for (int tt = 0; tt < 3; ++tt)
#pragma unroll
    for (int e = 0; e < 8; ++e) keep[tt][e] = pk_bf16(acc[tt][2 * e], acc[tt][2 * e + 1]);
  __syncthreads();
  {
    const char* xi = smem + (wave ^ 2) * 6144 + lane * 16;
    const int mt = 4 * mb + 2 * wm + isup;
    u16* hp = hf + ((size_t)(mt * NKS + 6 * fb) * 64 + lane) * 8;
    auto finish = [&](auto ISUP) {
#pragma unroll
      for (int tt = 0; tt < 3; ++tt) {
        const int ft = 3 * fb + tt;
        if (ft < FF / 32) {
          const u32x4 o0 = *(const u32x4*)(xi + (2 * tt) * 1024);
          const u32x4 o1 = *(const u32x4*)(xi + (2 * tt + 1) * 1024);
          unsigned gp[8], up[8];
#pragma unroll
          for (int e = 0; e < 8; ++e) {
            const unsigned ot = e < 4 ? o0[e & 3] : o1[e & 3];
            gp[e] = decltype(ISUP)::value ? ot : keep[tt][e];
            up[e] = decltype(ISUP)::value ? keep[tt][e] : ot;
          }
          float xg[16], ar[16];
#pragma unroll
          for (int e = 0; e < 8; ++e) { xg[2 * e] = bf_lo(gp[e]); xg[2 * e + 1] = bf_hi(gp[e]); }
          const float c1 = -2.30220819814f, c2 = -0.10294323958f, one = 1.0f;
#pragma unroll
          for (int e = 0; e < 16; ++e) ar[e] = smul(xg[e], xg[e]);
#pragma unroll
          for (int e = 0; e < 16; ++e) ar[e] = sfma(ar[e], c2, c1);
#pragma unroll
          for (int e = 0; e < 16; ++e) ar[e] = __builtin_amdgcn_exp2f(smul(ar[e], xg[e]));
          TRANS_FENCE8(ar, 0);
          TRANS_FENCE8(ar, 8);
#pragma unroll
          for (int e = 0; e < 16; ++e) ar[e] = __builtin_amdgcn_rcpf(sadd(ar[e], one));
          TRANS_FENCE8(ar, 0);
          TRANS_FENCE8(ar, 8);
#pragma unroll
          for (int e = 0; e < 16; ++e) ar[e] = smul(ar[e], xg[e]);
          unsigned o[8];
#pragma unroll
          for (int e = 0; e < 8; ++e) o[e] = pk_bf16(ar[2 * e], ar[2 * e + 1]);
#pragma unroll
          for (int e = 0; e < 8; ++e) o[e] = pk_bf16(smul(bf_lo(o[e]), bf_lo(up[e])), smul(bf_hi(o[e]), bf_hi(up[e])));
          { const u32x4 v0 = u32x4{o[0], o[1], o[2], o[3]}, v1 = u32x4{o[4], o[5], o[6], o[7]};
            asm volatile("global_store_dwordx4 %0, %1, off sc1\n\ts_nop 1" ::"v"(hp + (2 * tt) * 512), "v"(v0) : "memory");
            asm volatile("global_store_dwordx4 %0, %1, off sc1\n\ts_nop 1" ::"v"(hp + (2 * tt + 1) * 512), "v"(v1) : "memory"); }
        } else if (ft < NKS / 2) {
          const u32x4 z = u32x4{0, 0, 0, 0};
          asm volatile("global_store_dwordx4 %0, %1, off sc1\n\ts_nop 1" ::"v"(hp + (2 * tt) * 512), "v"(z) : "memory");
          asm volatile("global_store_dwordx4 %0, %1, off sc1\n\ts_nop 1" ::"v"(hp + (2 * tt + 1) * 512), "v"(z) : "memory");
        }
      }
    };
    if (isup) finish(std::integral_constant<bool, true>{});
    else finish(std::integral_constant<bool, false>{});
  }
}

__device__ __forceinline__ void gate_unit(int b, const u16* __restrict__ x, const u16* __restrict__ gw,
                                          float* gout) {
  const int tid = threadIdx.x, lane = tid & 63, wave = tid >> 6;
  const int j = lane & 7, rr = lane >> 3;
  u32x4 gv[8];
#pragma unroll
  for (int i = 0; i < 8; ++i) gv[i] = *(const u32x4*)(gw + (j + 8 * i) * 8);
  const int row0 = b * 128 + wave * 32 + rr;
  u32x4 xv[4][8];
#pragma unroll
  for (int ps = 0; ps < 4; ++ps)
#pragma unroll
    for (int i = 0; i < 8; ++i) xv[ps][i] = *(const u32x4*)(x + (size_t)(row0 + 8 * ps) * DM + (j + 8 * i) * 8);
#pragma unroll
  for (int ps = 0; ps < 4; ++ps) {
    float s0 = 0.f, s1 = 0.f;
#pragma unroll
    for (int i = 0; i < 8; ++i)
#pragma unroll
      for (int e = 0; e < 4; ++e) {
        const unsigned pr = pk_bf16(smul(bf_lo(xv[ps][i][e]), bf_lo(gv[i][e])), smul(bf_hi(xv[ps][i][e]), bf_hi(gv[i][e])));
        s0 = sadd(s0, bf_lo(pr));
        s1 = sadd(s1, bf_hi(pr));
      }
    float s = s0 + s1;
    s += __shfl_xor(s, 1);
    s += __shfl_xor(s, 2);
    s += __shfl_xor(s, 4);
    if (j == 0) {
      const float sb = rbf(s);
      __hip_atomic_store(gout + row0 + 8 * ps, rbf(1.0f / (1.0f + expf(-sb))), __ATOMIC_RELAXED, __HIP_MEMORY_SCOPE_AGENT);
    }
  }
}

constexpr int G2_SLOT_B = 4096;
constexpr int G2_WAVE_B = 2 * G2_SLOT_B;
constexpr int G2_PITCH = 272;
constexpr int G2_A_OFF = 4 * 64 * G2_PITCH;
constexpr int K2_LDS = G2_A_OFF + 8192;
#define G2D 3
constexpr int G2_DEPTH = G2D;

struct G2Ctx {
  const u16* hsrc;
  const u16* wd;
  int wrow0, wcol;
  unsigned wb32, woff, ww;
};

#define GLOAD128C(dst, ptr) asm volatile("global_load_dwordx4 %0, %1, off sc1" : "=v"(dst) : "v"(ptr) : "memory")
template <int ST>
struct G2Step {
  static __device__ __forceinline__ void issue_w(u32x4 (&wr)[11][4], const G2Ctx& c) {
#pragma unroll
    for (int i = 0; i < 4; ++i) {
      int kr = c.wrow0 + ST * 32 + 8 * i;
      kr = kr < FF ? kr : FF - 1;
      GLOAD128(wr[ST][i], c.wd + (size_t)kr * DM + c.wcol);
    }
  }
  static __device__ __forceinline__ void issue_h(bf16x8 (&hfr)[11][4], const G2Ctx& c) {
#pragma unroll
    for (int mi = 0; mi < 2; ++mi)
#pragma unroll
      for (int i = 0; i < 2; ++i) GLOAD128(hfr[ST][2 * mi + i], c.hsrc + (size_t)(mi * NKS + 2 * ST + i) * 512);
  }
  static __device__ __forceinline__ void run(f32x16 (&acc)[2][2], bf16x8 (&hfr)[11][4], u32x4 (&wr)[11][4], const G2Ctx& c) {
    constexpr int LAST = (ST + G2_DEPTH - 1 < 10) ? ST + G2_DEPTH - 1 : 10;
    constexpr int NW = (ST < G2_DEPTH) ? 4 * (G2_DEPTH - 1 - ST) + 8 * ST : 8 * (LAST - ST);
    if constexpr (ST >= G2_DEPTH)
      asm volatile("s_waitcnt vmcnt(%4)" : "+v"(wr[ST][0]), "+v"(wr[ST][1]), "+v"(wr[ST][2]), "+v"(wr[ST][3]) : "i"(NW + 4) : "memory");
#pragma unroll
    for (int i = 0; i < 4; ++i) DS_W128(c.ww, wr[ST][i], (ST & 1) * G2_SLOT_B + i * 1024);
    asm volatile("s_waitcnt lgkmcnt(0)" ::: "memory");
    const unsigned sb = c.wb32 + c.woff;
    s16x4 w[2][2][2];
#pragma unroll
    for (int i = 0; i < 2; ++i)
#pragma unroll
      for (int t = 0; t < 2; ++t) {
        DS_TR(w[i][t][0], sb ^ (t * 64), (ST & 1) * G2_SLOT_B + i * 2048);
        DS_TR(w[i][t][1], sb ^ (t * 64), (ST & 1) * G2_SLOT_B + i * 2048 + 1024);
      }
    asm volatile("s_waitcnt vmcnt(%4)" : "+v"(hfr[ST][0]), "+v"(hfr[ST][1]), "+v"(hfr[ST][2]), "+v"(hfr[ST][3]) : "i"(NW) : "memory");
    if constexpr (ST + G2_DEPTH <= 10) { G2Step<ST + G2_DEPTH>::issue_w(wr, c); G2Step<ST + G2_DEPTH>::issue_h(hfr, c); }
    asm volatile("s_waitcnt lgkmcnt(0)"
                 : "+v"(w[0][0][0]), "+v"(w[0][0][1]), "+v"(w[0][1][0]), "+v"(w[0][1][1]), "+v"(w[1][0][0]), "+v"(w[1][0][1]),
                   "+v"(w[1][1][0]), "+v"(w[1][1][1])::"memory");
#pragma unroll
    for (int i = 0; i < 2; ++i)
#pragma unroll
      for (int t = 0; t < 2; ++t)
#pragma unroll
        for (int mi = 0; mi < 2; ++mi)
          acc[t][mi] = __builtin_amdgcn_mfma_f32_32x32x16_bf16(cat4(w[i][t][0], w[i][t][1]), hfr[ST][2 * mi + i], acc[t][mi], 0, 0, 0);
    if constexpr (ST + 1 <= 10) G2Step<ST + 1>::run(acc, hfr, wr, c);
  }
};

__device__ __forceinline__ void gemm2_tile(int bid, const u16* hf, const u16* __restrict__ wd, const u16* aT,
                                           const float* gin, unsigned* flags, u16* __restrict__ out, char* smem,
                                           unsigned long long (&ts)[8]) {
  const int tid = threadIdx.x, lane = tid & 63;
  const int wave = __builtin_amdgcn_readfirstlane(tid >> 6);
  const int xcd = bid & 7, idx = bid >> 3;
  const int m0 = 64 * (4 * xcd + (idx >> 3)), n0 = 64 * (idx & 7);
  G2Ctx c;
  c.wb32 = lds_addr(smem) + wave * G2_WAVE_B;
  c.hsrc = hf + ((size_t)((m0 >> 5) * NKS + 22 * wave) * 64 + lane) * 8;
  c.wd = wd;
  c.wcol = n0 + 8 * (lane & 7);
  c.wrow0 = 352 * wave + (lane >> 3);
  c.ww = c.wb32 + (lane >> 3) * 128 + (((lane & 7) ^ (((lane >> 4) & 1) << 2)) * 16);

  const int r = lane & 31, h = lane >> 5;
  const int blk = (lane >> 4) & 1, q = (lane & 15) >> 2, p = lane & 3;
  c.woff = (4 * h + q) * 128 + (((2 * blk + (p >> 1)) ^ (((q >> 1) & 1) << 2)) * 16) + 8 * (p & 1);

  f32x16 acc[2][2];
#pragma unroll
  for (int i = 0; i < 2; ++i)
#pragma unroll
    for (int j = 0; j < 2; ++j) acc[i][j] = f32x16{};
  bf16x8 hfr[11][4];
  u32x4 wr[11][4];

  static_assert(G2_DEPTH == 3, "prologue written for depth 3");
  G2Step<0>::issue_w(wr, c);
  G2Step<1>::issue_w(wr, c);
  G2Step<2>::issue_w(wr, c);
  if (wave == 0) {
    unsigned* fp = flags + (m0 >> 7);
    for (int spin = 0; spin < (1 << 20); ++spin) {
      if (__hip_atomic_load(fp, __ATOMIC_RELAXED, __HIP_MEMORY_SCOPE_AGENT) >= 16u) break;
      __builtin_amdgcn_s_sleep(1);
    }
    __builtin_amdgcn_fence(__ATOMIC_ACQUIRE, "agent");
  }
  asm volatile("s_waitcnt vmcnt(0)\n\ts_barrier"
               : "+v"(wr[0][0]), "+v"(wr[0][1]), "+v"(wr[0][2]), "+v"(wr[0][3]), "+v"(wr[1][0]), "+v"(wr[1][1]), "+v"(wr[1][2]),
                 "+v"(wr[1][3]), "+v"(wr[2][0]), "+v"(wr[2][1]), "+v"(wr[2][2]), "+v"(wr[2][3])::"memory");
  G2Step<0>::issue_h(hfr, c);
  G2Step<1>::issue_h(hfr, c);
  G2Step<2>::issue_h(hfr, c);
  G2Step<0>::run(acc, hfr, wr, c);
  __syncthreads();
  {
    char* pw = smem + wave * (64 * G2_PITCH);
#pragma unroll
    for (int c = 0; c < 2; ++c)
#pragma unroll
      for (int mi = 0; mi < 2; ++mi)
#pragma unroll
        for (int gq = 0; gq < 4; ++gq) {
          const f32x4 v = {acc[c][mi][4 * gq], acc[c][mi][4 * gq + 1], acc[c][mi][4 * gq + 2], acc[c][mi][4 * gq + 3]};
          *(f32x4*)(pw + (32 * mi + r) * G2_PITCH + (32 * c + 8 * gq + 4 * h) * 4) = v;
        }
  }
  if (wave == 0) {
    unsigned* fp = flags + 16 + (n0 >> 1) + (lane & 31);
    for (int spin = 0; spin < (1 << 20); ++spin) {
      const unsigned v = __hip_atomic_load(fp, __ATOMIC_RELAXED, __HIP_MEMORY_SCOPE_AGENT);
      if (__all(v != 0u)) break;
      __builtin_amdgcn_s_sleep(1);
    }
  }
  __syncthreads();
  const float g = __hip_atomic_load(gin + m0 + (tid >> 2), __ATOMIC_RELAXED, __HIP_MEMORY_SCOPE_AGENT);
  {
    u32x4 av[2];
#pragma unroll
    for (int i = 0; i < 2; ++i) {
      const int drow = 8 * (2 * wave + i) + (lane >> 3);
      GLOAD128C(av[i], aT + (size_t)(n0 + drow) * SEQ + m0 + (lane & 7) * 8);
    }
    asm volatile("s_waitcnt vmcnt(0)" : "+v"(av[0]), "+v"(av[1])::"memory");
#pragma unroll
    for (int i = 0; i < 2; ++i) *(u32x4*)(smem + G2_A_OFF + (2 * wave + i) * 1024 + lane * 16) = av[i];
  }
  __syncthreads();
  {
    const int m = tid >> 2, nq = (tid & 3) * 16;
    const float omg = rbf(1.0f - g);
    float b[16];
#pragma unroll
    for (int j = 0; j < 4; ++j) {
      f32x4 s = *(const f32x4*)(smem + m * G2_PITCH + (nq + 4 * j) * 4);
#pragma unroll
      for (int w = 1; w < 4; ++w) s += *(const f32x4*)(smem + w * (64 * G2_PITCH) + m * G2_PITCH + (nq + 4 * j) * 4);
#pragma unroll
      for (int e = 0; e < 4; ++e) b[4 * j + e] = s[e];
    }
    const u16* aimg = (const u16*)(smem + G2_A_OFF);
    unsigned o[8];
#pragma unroll
    for (int j = 0; j < 8; ++j) {
      const float a0 = bf2f(aimg[(nq + 2 * j) * 64 + m]);
      const float a1 = bf2f(aimg[(nq + 2 * j + 1) * 64 + m]);
      const float o0 = rbf(g * a0) + rbf(omg * rbf(b[2 * j]));
      const float o1 = rbf(g * a1) + rbf(omg * rbf(b[2 * j + 1]));
      o[j] = pk_bf16(o0, o1);
    }
    u16* op = out + (size_t)(m0 + m) * DM + n0 + nq;
    *(u32x4*)op = u32x4{o[0], o[1], o[2], o[3]};
    *(u32x4*)(op + 8) = u32x4{o[4], o[5], o[6], o[7]};
  }
}

constexpr int KA_LDS = (K2_LDS > G1_LDS ? K2_LDS : G1_LDS) > SSM_LDS ? (K2_LDS > G1_LDS ? K2_LDS : G1_LDS) : SSM_LDS;

__global__ __launch_bounds__(256, 2) void fused_kernel(const u16* __restrict__ u, const u16* __restrict__ logAr,
                                                      const u16* __restrict__ Aim, const u16* __restrict__ Cre,
                                                      const u16* __restrict__ Cim, const u16* __restrict__ logdt,
                                                      const u16* __restrict__ Dskip, const u16* __restrict__ wg,
                                                      const u16* __restrict__ wu, const u16* __restrict__ gw,
                                                      const u16* __restrict__ wd, u16* hbuf, u16* aT, float* gout,
                                                      unsigned* flags, u16* __restrict__ out DBG_ARG
                                                      ) {
  __shared__ __attribute__((aligned(4096))) char smem[KA_LDS];
  const int bid = blockIdx.x;
  if (bid < NSSM) {
    unsigned a0 = ((const unsigned*)wd)[((bid >> 3) * 256 + threadIdx.x) * 32], a1 = 0;
    if ((bid >> 3) * 256 + threadIdx.x + 8192 < 11008) a1 = ((const unsigned*)wd)[((bid >> 3) * 256 + threadIdx.x + 8192) * 32];
    ssm_unit(bid, u, logAr, Aim, Cre, Cim, logdt, Dskip, aT, flags, smem DBG_PASS);
    asm volatile("" ::"v"(a0), "v"(a1));
  } else {
    const int xcd = bid & 7, idx = (bid - NSSM) >> 3;
    int mb;
    if (idx < 30) { gemm1_tile(30 * xcd + idx, u, wg, wu, hbuf, smem DBG_PASS); mb = (30 * xcd + idx) & 15; }
    else { gate_unit(2 * xcd + idx - 30, u, gw, gout); mb = 2 * xcd + idx - 30; }
    asm volatile("s_waitcnt vmcnt(0)" ::: "memory");
    __syncthreads();
    if (threadIdx.x == 0) __hip_atomic_fetch_add(flags + mb, 1u, __ATOMIC_RELAXED, __HIP_MEMORY_SCOPE_AGENT);
    __builtin_amdgcn_s_setprio(0);
    unsigned long long ts[8] = {};
    gemm2_tile(bid - NSSM, hbuf, wd, aT, gout, flags, out, smem, ts);
  }
}

extern "C" void kernel_launch(void* const* d_in, const int* in_sizes, int n_in, void* d_out, int out_size,
                              void* d_ws, size_t ws_size, hipStream_t stream) {
  const u16* u = (const u16*)d_in[0];
  const u16* logAr = (const u16*)d_in[1];
  const u16* Aim = (const u16*)d_in[2];
  const u16* Cre = (const u16*)d_in[3];
  const u16* Cim = (const u16*)d_in[4];
  const u16* logdt = (const u16*)d_in[5];
  const u16* Dskip = (const u16*)d_in[6];
  const u16* wg = (const u16*)d_in[8];
  const u16* wu = (const u16*)d_in[9];
  const u16* wd = (const u16*)d_in[10];
  const u16* gw = (const u16*)d_in[11];
  char* ws = (char*)d_ws;
  unsigned* flags = (unsigned*)ws;
  u16* hbuf = (u16*)(ws + 4096);
  u16* aT = (u16*)(ws + 4096 + (size_t)SEQ * FFP * 2);
  float* gbuf = (float*)(ws + 4096 + (size_t)SEQ * FFP * 2 + (size_t)DM * SEQ * 2);
  (void)hipMemsetAsync(flags, 0, 1088, stream);
  fused_kernel<<<512, 256, 0, stream>>>(u, logAr, Aim, Cre, Cim, logdt, Dskip, wg, wu, gw, wd, hbuf, aT, gbuf, flags, (u16*)d_out
      );
}
```

```cpp
#include <hip/hip_runtime.h>
#include <hip/hip_bf16.h>
#include <stdint.h>
#include <type_traits>

typedef unsigned short u16;
typedef __attribute__((ext_vector_type(8))) short bf16x8;
typedef __attribute__((ext_vector_type(4))) short s16x4;
typedef __attribute__((ext_vector_type(4))) float f32x4;
typedef __attribute__((ext_vector_type(16))) float f32x16;
typedef __attribute__((ext_vector_type(4))) unsigned u32x4;
typedef __attribute__((ext_vector_type(2))) unsigned u32x2;
typedef __attribute__((ext_vector_type(2))) __bf16 bf16x2_t;
typedef __attribute__((ext_vector_type(2))) float f32x2;
typedef __attribute__((address_space(3))) s16x4* lds_s16x4_ptr;

constexpr int SEQ = 2048, DM = 512, NST = 128, FF = 1376, FFP = 1408;
constexpr int NSSM = 256;
constexpr int NKS = 88;

__device__ __forceinline__ unsigned pk_bf16(float lo, float hi) {
  f32x2 v = {lo, hi};
  bf16x2_t b = __builtin_convertvector(v, bf16x2_t);
  return __builtin_bit_cast(unsigned, b);
}
__device__ __forceinline__ float bf_lo(unsigned w) { return __uint_as_float(w << 16); }
__device__ __forceinline__ float bf_hi(unsigned w) { return __uint_as_float(w & 0xffff0000u); }
__device__ __forceinline__ float rbf(float x) { return bf_lo(pk_bf16(x, 0.f)); }
__device__ __forceinline__ float bf2f(u16 v) { return __uint_as_float(((unsigned)v) << 16); }

__device__ __forceinline__ float smul(float a, float b) { float r; asm("v_mul_f32 %0, %1, %2" : "=v"(r) : "v"(a), "v"(b)); return r; }
__device__ __forceinline__ float sadd(float a, float b) { float r; asm("v_add_f32 %0, %1, %2" : "=v"(r) : "v"(a), "v"(b)); return r; }
__device__ __forceinline__ float ssub(float a, float b) { float r; asm("v_sub_f32 %0, %1, %2" : "=v"(r) : "v"(a), "v"(b)); return r; }
__device__ __forceinline__ float sfma(float a, float b, float c) { float r; asm("v_fma_f32 %0, %1, %2, %3" : "=v"(r) : "v"(a), "v"(b), "v"(c)); return r; }
__device__ __forceinline__ float sfms(float a, float b, float c) { float r; asm("v_fma_f32 %0, -%1, %2, %3" : "=v"(r) : "v"(a), "v"(b), "v"(c)); return r; }
__device__ __forceinline__ float sfnn(float a, float b, float c) { float r; asm("v_fma_f32 %0, -%1, %2, -%3" : "=v"(r) : "v"(a), "v"(b), "v"(c)); return r; }
#define TRANS_FENCE8(a, o) asm volatile("s_nop 1" : "+v"(a[o]), "+v"(a[o + 1]), "+v"(a[o + 2]), "+v"(a[o + 3]), "+v"(a[o + 4]), "+v"(a[o + 5]), "+v"(a[o + 6]), "+v"(a[o + 7]))
__device__ __forceinline__ bf16x8 mk8(unsigned a, unsigned b, unsigned c, unsigned d) {
  u32x4 v = {a, b, c, d};
  return __builtin_bit_cast(bf16x8, v);
}
__device__ __forceinline__ bf16x8 cat4(s16x4 a, s16x4 b) {
  return __builtin_shufflevector(a, b, 0, 1, 2, 3, 4, 5, 6, 7);
}
__device__ __forceinline__ s16x4 tr_read(const char* smem_ptr) {
  return __builtin_amdgcn_ds_read_tr16_b64_v4i16((lds_s16x4_ptr)(smem_ptr));
}
__device__ __forceinline__ unsigned lds_addr(const void* p) {
  return (unsigned)(size_t)(const __attribute__((address_space(3))) char*)p;
}
__device__ __forceinline__ void glds16(const void* g, char* l) {
  __builtin_amdgcn_global_load_lds((const unsigned*)g, (unsigned*)l, 16, 0, 0);
}

#define STAMP2(i)
#define DBG2_ARG
#define DBG2_PASS
#define STAMP(i)
#define DBG_ARG
#define DBG_PASS
constexpr int U_COPY_B = 4160;
constexpr int U_CH_B = 4 * U_COPY_B;
constexpr int U_REGION_B = 2 * U_CH_B;
constexpr int KR_PART_B = 4576;
constexpr int KR_OFF = U_REGION_B;
constexpr int KQ_B = 8208;
constexpr int KX_OFF = 2 * KQ_B;
constexpr int SSM_LDS = KR_OFF + 4 * KR_PART_B;
static_assert(KX_OFF + 4 * 1536 <= U_REGION_B, "scratch must fit the U region");

#define DS_TR(dst, addr, off) asm volatile("ds_read_b64_tr_b16 %0, %1 offset:%2" : "=v"(dst) : "v"(addr), "i"(off) : "memory")
#define DS_B128(dst, addr, off) asm volatile("ds_read_b128 %0, %1 offset:%2" : "=v"(dst) : "v"(addr), "i"(off) : "memory")
#define DS_B64(dst, addr, off) asm volatile("ds_read_b64 %0, %1 offset:%2" : "=v"(dst) : "v"(addr), "i"(off) : "memory")
#define MFMA16(acc, a, b) asm volatile("v_mfma_f32_16x16x32_bf16 %0, %1, %2, %0" : "+v"(acc) : "v"(a), "v"(b))

struct ConvFrag {
  s16x4 a[8][2];
  bf16x8 bh[8], bl[8];
};
template <int RI, int K>
__device__ __forceinline__ void conv_read(ConvFrag& f, unsigned ua, unsigned kb) {
  if constexpr (K < 16) {
    DS_B64(f.a[K >> 1][K & 1], ua, 64 * (8 * (K >> 1) + RI) + 8 * (K & 1));
  } else if constexpr (K < 24) {
    DS_B128(f.bh[K - 16], kb, 64 * (56 - 8 * (K - 16) + RI));
  } else {
    DS_B128(f.bl[K - 24], kb, 64 * (56 - 8 * (K - 24) + RI) + KR_PART_B);
  }
}
template <int RI, int K>
__device__ __forceinline__ void conv_read_all(ConvFrag& f, unsigned ua, unsigned kb) {
  conv_read<RI, K>(f, ua, kb);
  if constexpr (K + 1 < 32) conv_read_all<RI, K + 1>(f, ua, kb);
}
#define CONV_WAIT(f)                                                                                                  \
  do {                                                                                                                \
    asm volatile("s_waitcnt lgkmcnt(0)"                                                                               \
                 : "+v"(f.a[0][0]), "+v"(f.a[0][1]), "+v"(f.a[1][0]), "+v"(f.a[1][1]), "+v"(f.a[2][0]), "+v"(f.a[2][1]), \
                   "+v"(f.a[3][0]), "+v"(f.a[3][1]), "+v"(f.a[4][0]), "+v"(f.a[4][1]), "+v"(f.a[5][0]), "+v"(f.a[5][1]), \
                   "+v"(f.a[6][0]), "+v"(f.a[6][1]), "+v"(f.a[7][0]), "+v"(f.a[7][1])::"memory");                     \
    asm volatile("" : "+v"(f.bh[0]), "+v"(f.bh[1]), "+v"(f.bh[2]), "+v"(f.bh[3]), "+v"(f.bh[4]), "+v"(f.bh[5]),        \
                 "+v"(f.bh[6]), "+v"(f.bh[7]), "+v"(f.bl[0]), "+v"(f.bl[1]), "+v"(f.bl[2]), "+v"(f.bl[3]),            \
                 "+v"(f.bl[4]), "+v"(f.bl[5]), "+v"(f.bl[6]), "+v"(f.bl[7])::"memory");                              \
  } while (0)
constexpr int conv_tau(int P) { int t = 0; while (P >= 8 - t) { P -= 8 - t; ++t; } return t; }
constexpr int conv_sig(int P) { int t = 0; while (P >= 8 - t) { P -= 8 - t; ++t; } return P; }
template <int RI, int P>
__device__ __forceinline__ void conv_pairs(f32x4 (&acc)[8], const bf16x8 (&A)[8], ConvFrag& cur, ConvFrag& nxt, unsigned ua,
                                           unsigned kb) {
  constexpr int T = conv_tau(P), SG = conv_sig(P);
  MFMA16(acc[T + SG], A[SG], cur.bl[T]);
  if constexpr (RI < 3 && P < 32) conv_read<RI + 1, P>(nxt, ua, kb);
  MFMA16(acc[T + SG], A[SG], cur.bh[T]);
  if constexpr (P + 1 < 36) conv_pairs<RI, P + 1>(acc, A, cur, nxt, ua, kb);
}
template <int RI>
__device__ __forceinline__ void conv_rho(f32x4 (&acc)[8], ConvFrag& cur, ConvFrag& nxt, unsigned ua, unsigned kb) {
  CONV_WAIT(cur);
  bf16x8 A[8];
#pragma unroll
  for (int i = 0; i < 8; ++i) A[i] = cat4(cur.a[i][0], cur.a[i][1]);
  conv_pairs<RI, 0>(acc, A, cur, nxt, ua, kb);
  if constexpr (RI < 3) conv_rho<RI + 1>(acc, nxt, cur, ua, kb);
}

__device__ __forceinline__ void split_pair(float v0, float v1, unsigned& hi, unsigned& lo) {
  hi = pk_bf16(v0, v1);
  lo = pk_bf16(ssub(v0, bf_lo(hi)), ssub(v1, bf_hi(hi)));
}

__device__ __forceinline__ void ssm_unit(int w, const u16* __restrict__ u, const u16* __restrict__ logAr,
                                         const u16* __restrict__ Aim, const u16* __restrict__ Cre,
                                         const u16* __restrict__ Cim, const u16* __restrict__ logdt,
                                         const u16* __restrict__ Dskip, u16* __restrict__ aT, char* smem DBG_ARG) {
  const int tid = threadIdx.x, lane = tid & 63;
  const int wave = __builtin_amdgcn_readfirstlane(tid >> 6);
  const int ch = wave >> 1, w2 = wave & 1;
  const int d0 = 2 * w;
  const int d = d0 + ch;

  const int pidx = d0 * NST + tid;
  const u16 r_lar = logAr[pidx], r_aim = Aim[pidx], r_cre = Cre[pidx], r_cim = Cim[pidx], r_ldt = logdt[d], r_dsk = Dskip[d];
  const unsigned* u32p = (const unsigned*)(u + d0);
  unsigned uw[8], up[3];
#pragma unroll
  for (int i = 0; i < 3; ++i) up[i] = 0;
  if (lane == 0 && tid > 0) {
#pragma unroll
    for (int i = 0; i < 3; ++i) up[i] = u32p[(size_t)(8 * tid - 3 + i) * (DM / 2)];
  }
  const unsigned uoff = (unsigned)tid * 8192u;
  {
    const float dt = rbf(expf(bf2f(r_ldt)));
    const float are = rbf(expf(bf2f(r_lar)));
    f32x4 prm;
    prm[0] = (-are * dt) * 1.44269504088896340736f;
    prm[1] = (bf2f(r_aim) * dt) * 0.15915494309189533577f;
    prm[2] = 2.0f * (bf2f(r_cre) * dt);
    prm[3] = 2.0f * (bf2f(r_cim) * dt);
    *(f32x4*)(smem + KR_OFF + tid * 16) = prm;
  }
  __syncthreads();

  const int r = lane & 31, h = lane >> 5;
  f32x16 dacc = {};
  {
    int p1i;
    if (r < 8) p1i = r;
    else if (r < 12) p1i = 256 + 2 * (r - 8);
    else if (r < 16) p1i = 512 + 256 * ((r - 12) >> 1) + 4 * ((r - 12) & 1);
    else if (r < 21) p1i = 1024 + 256 * (r - 16);
    else p1i = 0;
    const float p1 = (float)p1i, p2 = (float)(8 * r);
    const char* prm_base = smem + KR_OFF + (ch * 128 + 64 * w2 + 4 * h) * 16;
#pragma unroll
    for (int ks = 0; ks < 8; ++ks) {
      if (ks >= 4) {
        asm volatile("global_load_dword %0, %1, %2 offset:%3" : "=v"(uw[2 * ks - 8]) : "v"(uoff), "s"((const char*)u32p + ((2 * ks - 8) >> 2) * 4096), "i"(((2 * ks - 8) & 3) * 1024) : "memory");
        asm volatile("global_load_dword %0, %1, %2 offset:%3" : "=v"(uw[2 * ks - 7]) : "v"(uoff), "s"((const char*)u32p + ((2 * ks - 7) >> 2) * 4096), "i"(((2 * ks - 7) & 3) * 1024) : "memory");
      }
      f32x4 prm[4];
#pragma unroll
      for (int q = 0; q < 4; ++q) prm[q] = *(const f32x4*)(prm_base + (8 * ks + q) * 16);
      float ea[4], eb[4], fa[4], fb[4];
#pragma unroll
      for (int q = 0; q < 4; ++q) {
        ea[q] = __builtin_amdgcn_exp2f(smul(prm[q][0], p1)); eb[q] = __builtin_amdgcn_exp2f(smul(prm[q][0], p2));
        fa[q] = __builtin_amdgcn_fractf(smul(prm[q][1], p1)); fb[q] = __builtin_amdgcn_fractf(smul(prm[q][1], p2));
      }
      float ac[4], as[4], bc[4], bs[4];
#pragma unroll
      for (int q = 0; q < 4; ++q) {
        ac[q] = __builtin_amdgcn_cosf(fa[q]); as[q] = __builtin_amdgcn_sinf(fa[q]);
        bc[q] = __builtin_amdgcn_cosf(fb[q]); bs[q] = __builtin_amdgcn_sinf(fb[q]);
      }
      asm volatile("s_nop 1" : "+v"(ea[0]), "+v"(ea[1]), "+v"(ea[2]), "+v"(ea[3]), "+v"(eb[0]), "+v"(eb[1]), "+v"(eb[2]), "+v"(eb[3]),
                   "+v"(ac[0]), "+v"(ac[1]), "+v"(ac[2]), "+v"(ac[3]), "+v"(as[0]), "+v"(as[1]), "+v"(as[2]), "+v"(as[3]),
                   "+v"(bc[0]), "+v"(bc[1]), "+v"(bc[2]), "+v"(bc[3]), "+v"(bs[0]), "+v"(bs[1]), "+v"(bs[2]), "+v"(bs[3]));
      unsigned ah[4], al[4], bh[4], bl[4];
#pragma unroll
      for (int q = 0; q < 4; ++q) {
        const float cr = smul(prm[q][2], ea[q]), ci = smul(prm[q][3], ea[q]);
        const float gre = sfms(ci, as[q], smul(cr, ac[q]));
        const float gmi = sfnn(ci, ac[q], smul(cr, as[q]));
        split_pair(gre, gmi, ah[q], al[q]);
        split_pair(smul(bc[q], eb[q]), smul(bs[q], eb[q]), bh[q], bl[q]);
      }
      const bf16x8 Ah = mk8(ah[0], ah[1], ah[2], ah[3]), Al = mk8(al[0], al[1], al[2], al[3]);
      const bf16x8 Bh = mk8(bh[0], bh[1], bh[2], bh[3]), Bl = mk8(bl[0], bl[1], bl[2], bl[3]);
      dacc = __builtin_amdgcn_mfma_f32_32x32x16_bf16(Al, Bh, dacc, 0, 0, 0);
      dacc = __builtin_amdgcn_mfma_f32_32x32x16_bf16(Ah, Bl, dacc, 0, 0, 0);
      dacc = __builtin_amdgcn_mfma_f32_32x32x16_bf16(Ah, Bh, dacc, 0, 0, 0);
    }
  }
  {
    float* xo = (float*)(smem + KX_OFF + wave * 1536) + lane;
    const float* xi = (const float*)(smem + KX_OFF + (wave ^ 1) * 1536) + lane;
    float mine[6];
    if (w2 == 0) {
#pragma unroll
      for (int e = 0; e < 6; ++e) { xo[e * 64] = dacc[6 + e]; mine[e] = dacc[e]; }
    } else {
#pragma unroll
      for (int e = 0; e < 6; ++e) { xo[e * 64] = dacc[e]; mine[e] = dacc[6 + e]; }
    }
    __syncthreads();
    unsigned* Kq = (unsigned*)(smem + ch * KQ_B);
    unsigned pq[6];
#pragma unroll
    for (int e = 0; e < 6; ++e) {
      const float v = sadd(mine[e], xi[e * 64]);
      const unsigned hi = pk_bf16(v, 0.f);
      pq[e] = pk_bf16(0.f, ssub(v, bf_lo(hi))) | hi;
    }
    if (w2 == 0) {
#pragma unroll
      for (int e = 0; e < 4; ++e) Kq[4 * h + e + 8 * r] = pq[e];
      Kq[(h ? 512 : 256) + 8 * r] = pq[4];
      Kq[(h ? 516 : 258) + 8 * r] = pq[5];
    } else {
      Kq[(h ? 768 : 260) + 8 * r] = pq[0];
      Kq[(h ? 772 : 262) + 8 * r] = pq[1];
      if (h == 0) {
#pragma unroll
        for (int e = 0; e < 4; ++e) Kq[1024 + 256 * e + 8 * r] = pq[2 + e];
      } else if (r == 0) {
        Kq[2048] = pq[2];
      }
    }
  }
  __syncthreads();

  {
    const int t = tid & 127;
    const unsigned* Kq = (const unsigned*)(smem + ch * KQ_B);
    unsigned* krh = (unsigned*)(smem + KR_OFF + (ch * 2) * KR_PART_B);
    unsigned* krl = (unsigned*)((char*)krh + KR_PART_B);
    unsigned q0[8], q1[8];
#pragma unroll
    for (int m = 0; m < 8; ++m) {
      const int sh = m < 4 ? 3 : (m < 6 ? 2 : (m < 7 ? 1 : 0));
      const int l0 = 2047 - 2 * (t + 128 * m), l1 = l0 - 1;
      int pa = l0, pb = l1;
      if (sh) {
        pa = (l0 + (1 << (sh - 1)) - 1 + ((l0 >> sh) & 1)) & ~((1 << sh) - 1);
        pb = (l1 + (1 << (sh - 1)) - 1 + ((l1 >> sh) & 1)) & ~((1 << sh) - 1);
      }
      q0[m] = Kq[pa];
      q1[m] = Kq[pb];
    }
#pragma unroll
    for (int m = 0; m < 8; ++m) {
      krh[t + 128 * m] = __builtin_amdgcn_perm(q1[m], q0[m], 0x05040100u);
      krl[t + 128 * m] = __builtin_amdgcn_perm(q1[m], q0[m], 0x07060302u);
    }
    if (t < 120) { krh[1024 + t] = 0; krl[1024 + t] = 0; }
  }
  __syncthreads();

  asm volatile("s_waitcnt vmcnt(0)" : "+v"(uw[0]), "+v"(uw[1]), "+v"(uw[2]), "+v"(uw[3]), "+v"(uw[4]), "+v"(uw[5]), "+v"(uw[6]), "+v"(uw[7])::"memory");
  {
    unsigned e[11];
#pragma unroll
    for (int i = 0; i < 3; ++i) {
      const unsigned nb = __shfl_up(uw[5 + i], 1);
      e[i] = (lane == 0) ? up[i] : nb;
    }
#pragma unroll
    for (int i = 0; i < 8; ++i) e[3 + i] = uw[i];
    unsigned p0[10], p1[10];
#pragma unroll
    for (int i = 0; i < 10; ++i) {
      p0[i] = __builtin_amdgcn_perm(e[i + 1], e[i], 0x05040100u);
      p1[i] = __builtin_amdgcn_perm(e[i + 1], e[i], 0x07060302u);
    }
#pragma unroll
    for (int c = 0; c < 4; ++c) {
      *(u32x4*)(smem + c * U_COPY_B + (tid + 2) * 16) = u32x4{p0[c], p0[c + 2], p0[c + 4], p0[c + 6]};
      *(u32x4*)(smem + U_CH_B + c * U_COPY_B + (tid + 2) * 16) = u32x4{p1[c], p1[c + 2], p1[c + 4], p1[c + 6]};
    }
    if (tid < 16) *(u32x4*)(smem + (tid >> 1) * U_COPY_B + (tid & 1) * 16) = u32x4{0, 0, 0, 0};
  }
  __syncthreads();

  f32x4 acc[8];
#pragma unroll
  for (int t = 0; t < 8; ++t) acc[t] = f32x4{0.f, 0.f, 0.f, 0.f};
  asm volatile("s_nop 4" : "+v"(acc[0]), "+v"(acc[1]), "+v"(acc[2]), "+v"(acc[3]), "+v"(acc[4]), "+v"(acc[5]), "+v"(acc[6]), "+v"(acc[7]));
  {
    const int r16 = lane & 15, hq = lane >> 4;
    const unsigned sb = lds_addr(smem);
    const unsigned ua = sb + ch * U_CH_B + (r16 & 3) * U_COPY_B + 2 * (4 + (r16 & ~3) + 8 * hq) + 64 * 4 * w2;
    const unsigned kb = sb + KR_OFF + (ch * 2) * KR_PART_B + 2 * (2032 - 16 * r16 + 8 * hq) - 64 * 56 + 64 * 4 * w2;
    ConvFrag f0, f1;
    conv_read_all<0, 0>(f0, ua, kb);
    conv_rho<0>(acc, f0, f1, ua, kb);
  }
  asm volatile("s_nop 15" : "+v"(acc[0]), "+v"(acc[1]), "+v"(acc[2]), "+v"(acc[3]), "+v"(acc[4]), "+v"(acc[5]), "+v"(acc[6]), "+v"(acc[7]));
  __syncthreads();
  {
    char* xw = smem + KR_OFF + (ch * 2 + w2) * 4096 + lane * 16;
    const char* xr = smem + KR_OFF + (ch * 2 + (w2 ^ 1)) * 4096 + lane * 16;
    f32x4 y[4];
    if (w2 == 0) {
#pragma unroll
      for (int i = 0; i < 4; ++i) { *(f32x4*)(xw + i * 1024) = acc[4 + i]; y[i] = acc[i]; }
    } else {
#pragma unroll
      for (int i = 0; i < 4; ++i) { *(f32x4*)(xw + i * 1024) = acc[i]; y[i] = acc[4 + i]; }
    }
    __syncthreads();
    const float Dk = bf2f(r_dsk);
#pragma unroll
    for (int i = 0; i < 4; ++i) {
      const f32x4 o = *(const f32x4*)(xr + i * 1024);
      const int l0 = 256 * (4 * w2 + i) + 16 * (lane & 15) + 4 * (lane >> 4);
      const u32x2 uu = *(const u32x2*)(smem + ch * U_CH_B + 3 * U_COPY_B + 2 * (16 + l0));
      const unsigned y01 = pk_bf16(sadd(y[i][0], o[0]), sadd(y[i][1], o[1])), y23 = pk_bf16(sadd(y[i][2], o[2]), sadd(y[i][3], o[3]));
      const unsigned s01 = pk_bf16(smul(bf_lo(uu[0]), Dk), smul(bf_hi(uu[0]), Dk)), s23 = pk_bf16(smul(bf_lo(uu[1]), Dk), smul(bf_hi(uu[1]), Dk));
      { const u32x2 av = u32x2{pk_bf16(sadd(bf_lo(y01), bf_lo(s01)), sadd(bf_hi(y01), bf_hi(s01))),
                                                  pk_bf16(sadd(bf_lo(y23), bf_lo(s23)), sadd(bf_hi(y23), bf_hi(s23)))};
        asm volatile("global_store_dwordx2 %0, %1, off sc1\n\ts_nop 1" ::"v"(aT + (size_t)d * SEQ + l0), "v"(av) : "memory"); }
    }
  }
}

__device__ __forceinline__ float gelu_tanh(float x) {
  const float z = 0.7978845608028654f * (x + 0.044715f * x * x * x);
  const float e = __builtin_amdgcn_exp2f(-2.8853900817779268f * z);
  return x * __builtin_amdgcn_rcpf(1.0f + e);
}

constexpr int G1_WSLOT_B = 12288;
constexpr int G1_X_OFF = 3 * G1_WSLOT_B;
constexpr int G1_LDS = G1_X_OFF + 32768;

constexpr int g1_pos(bool isx, int id) {
  if (isx && id == 0) return 4;
  if (!isx && id == 0) return 7;
  if (!isx && id == 1) return 10;
  if (isx && id == 1) return 14;
  if (!isx && id == 2) return 17;
  if (!isx && id == 3) return 20;
  if (!isx && id == 4) return 23;
  if (isx && id == 2) return 27;
  int n = 27;
  for (int t = 0; t < 32; ++t) {
    if ((t & 1) == 0 && t / 2 + 5 < 16) { n += 3; if (!isx && t / 2 + 5 == id) return n; }
    if ((t & 3) == 1 && t / 4 + 3 < 8) { n += 4; if (isx && t / 4 + 3 == id) return n; }
  }
  return -1;
}
constexpr int g1_issued_before(int step) {
  int n = 27;
  for (int t = 0; t < step; ++t) {
    if ((t & 1) == 0 && t / 2 + 5 < 16) n += 3;
    if ((t & 3) == 1 && t / 4 + 3 < 8) n += 4;
  }
  return n;
}

#define GLOAD128(dst, ptr) asm volatile("global_load_dwordx4 %0, %1, off" : "=v"(dst) : "v"(ptr) : "memory")
#define GLOAD128S(dst, voff, sbase) asm volatile("global_load_dwordx4 %0, %1, %2" : "=v"(dst) : "v"(voff), "s"(sbase) : "memory")
#define GLOAD128SA(dst, voff, sbase) asm volatile("global_load_dwordx4 %0, %1, %2" : "=a"(dst) : "v"(voff), "s"(sbase) : "memory")
#define GLOAD128A(dst, ptr) asm volatile("global_load_dwordx4 %0, %1, off" : "=a"(dst) : "v"(ptr) : "memory")
#define DS_W128(addr, val, off) asm volatile("ds_write_b128 %0, %1 offset:%2" ::"v"(addr), "v"(val), "i"(off) : "memory")
#define MFMA32(acc, a, b) asm volatile("v_mfma_f32_32x32x16_bf16 %0, %1, %2, %0" : "+v"(acc) : "v"(a), "v"(b))

struct G1Ctx {
  const char* wbase;
  const char* xbase;
  unsigned wo[3], xo;
  unsigned woff, woff_o, ww[3], xw, xw1, xrd2[2][4];
  unsigned junk[4];
};
struct G1Frag {
  s16x4 w[3][2];
  bf16x8 x[2];
};

#define G1_RD_W(f, t, OFF)                                                        \
  do {                                                                            \
    if ((t) & 1) { DS_TR(f.w[t][0], c.woff_o, (OFF) + (t) * 64); DS_TR(f.w[t][1], c.woff_o, (OFF) + (t) * 64 + 4 * 384); } \
    else { DS_TR(f.w[t][0], c.woff, (OFF) + (t) * 64); DS_TR(f.w[t][1], c.woff, (OFF) + (t) * 64 + 4 * 384); }            \
  } while (0)
#define G1_RD_X(f, mi, N) DS_B128(f.x[mi], c.xrd2[mi][(N) & 3], (((N) >> 2) & 1) * 16384)
template <int N>
__device__ __forceinline__ void g1_read(G1Frag& f, const G1Ctx& c) {
  constexpr int OFF = ((N >> 1) % 3) * G1_WSLOT_B + (N & 1) * 16 * 384;
  G1_RD_X(f, 0, N); G1_RD_X(f, 1, N);
  G1_RD_W(f, 0, OFF); G1_RD_W(f, 1, OFF); G1_RD_W(f, 2, OFF);
}
#define G1_WAIT_FRAG(f, STR)                                                                                       \
  asm volatile(STR : "+v"(f.w[0][0]), "+v"(f.w[0][1]), "+v"(f.w[1][0]), "+v"(f.w[1][1]), "+v"(f.w[2][0]),        \
               "+v"(f.w[2][1]), "+v"(f.x[0]), "+v"(f.x[1])::"memory")

template <int N>
struct G1Step {
  static __device__ __forceinline__ void run(f32x16 (&acc)[6], u32x4 (&xr)[2][4], u32x4 (&wr)[3][3], G1Frag& cur,
                                             G1Frag& nxt, G1Ctx& c) {
    constexpr int ISS = g1_issued_before(N);
    constexpr int S = N >> 1;
    constexpr bool RD = N + 1 < 32;
    constexpr int OFF = (((N + 1) >> 1) % 3) * G1_WSLOT_B + ((N + 1) & 1) * 16 * 384;
    if constexpr ((N & 1) == 0 && N > 0) G1_WAIT_FRAG(cur, "s_waitcnt lgkmcnt(0)\n\ts_barrier");
    else G1_WAIT_FRAG(cur, "s_waitcnt lgkmcnt(0)");
    bf16x8 b[3];
#pragma unroll
    for (int t = 0; t < 3; ++t) b[t] = cat4(cur.w[t][0], cur.w[t][1]);
    MFMA32(acc[0], b[0], cur.x[0]);
    if constexpr (RD) { G1_RD_X(nxt, 0, N + 1); G1_RD_W(nxt, 0, OFF); }
    MFMA32(acc[3], b[0], cur.x[1]);
    if constexpr (RD) { G1_RD_X(nxt, 1, N + 1); G1_RD_W(nxt, 1, OFF); }
    MFMA32(acc[1], b[1], cur.x[0]);
    if constexpr (RD) { G1_RD_W(nxt, 2, OFF); }
    MFMA32(acc[4], b[1], cur.x[1]);
    if constexpr ((N & 1) == 0) {
      if constexpr (S + 2 < 16) {
        constexpr int R = (S + 2) % 3;
        constexpr int NW = ISS - g1_pos(false, S + 2);
        asm volatile("s_waitcnt vmcnt(%3)" : "+v"(wr[R][0]), "+v"(wr[R][1]), "+v"(wr[R][2]) : "i"(NW) : "memory");
#pragma unroll
        for (int i = 0; i < 3; ++i) DS_W128(c.ww[i], wr[R][i], R * G1_WSLOT_B);
      }
    }
    if constexpr ((N & 3) == 1) {
      constexpr int B = N / 4 + 1;
      if constexpr (B < 8) {
        constexpr int NW = ISS - g1_pos(true, B);
        asm volatile("s_waitcnt vmcnt(%4)" : "+v"(xr[B & 1][0]), "+v"(xr[B & 1][1]), "+v"(xr[B & 1][2]), "+v"(xr[B & 1][3]) : "i"(NW) : "memory");
#pragma unroll
        for (int i = 0; i < 4; ++i) { if (i & 1) DS_W128(c.xw1, xr[B & 1][i], (B & 1) * 16384 + i * 1024); else DS_W128(c.xw, xr[B & 1][i], (B & 1) * 16384 + i * 1024); }
      }
    }
    MFMA32(acc[2], b[2], cur.x[0]);
    if constexpr ((N & 1) == 0 && S + 5 < 16) {
#pragma unroll
      for (int i = 0; i < 3; ++i) GLOAD128S(wr[(S + 5) % 3][i], c.wo[i], c.wbase + (size_t)(S + 5) * 32 * FF * 2);
    }
    if constexpr ((N & 3) == 1 && N / 4 + 3 < 8) {
      constexpr int B = N / 4 + 1;
#pragma unroll
      for (int i = 0; i < 4; ++i) GLOAD128S(xr[B & 1][i], c.xo, c.xbase + (size_t)(8 * i) * DM * 2 + (B + 2) * 128);
    }
    MFMA32(acc[5], b[2], cur.x[1]);
    if constexpr (N + 1 < 32) G1Step<N + 1>::run(acc, xr, wr, nxt, cur, c);
  }
};

__device__ __forceinline__ void gemm1_tile(int t, const u16* __restrict__ x, const u16* __restrict__ wg,
                                           const u16* __restrict__ wu, u16* __restrict__ hf, char* smem DBG_ARG) {
  const int tid = threadIdx.x, lane = tid & 63;
  const int wave = __builtin_amdgcn_readfirstlane(tid >> 6);
  const int fb = t >> 4, mb = t & 15;
  const int f0 = 96 * fb;
  const int wm = wave & 1, isup = wave >> 1;
  G1Ctx c;
  const unsigned sbase = lds_addr(smem);
  c.wbase = (const char*)(isup ? wu : wg);
#pragma unroll
  for (int i = 0; i < 3; ++i) {
    const int id = 192 * (wave & 1) + 64 * i + lane;
    const int row = id / 12, cc = id % 12;
    int col = f0 + 8 * cc;
    if (col > FF - 8) col = FF - 8;
    c.wo[i] = (unsigned)(row * FF + col) * 2u;
    c.ww[i] = sbase + row * 384 + ((cc + 12 * isup) ^ (((row >> 1) & 1) << 2)) * 16;
  }
  c.xbase = (const char*)(x + (size_t)(128 * mb + 32 * wave) * DM);
  c.xo = (unsigned)((lane >> 3) * DM + (lane & 7) * 8) * 2u;
  const unsigned xl = sbase + G1_X_OFF;
  c.xw = xl + (32 * wave + (lane >> 3)) * 128 + (((lane & 7) ^ (lane >> 4)) * 16);
  c.xw1 = xl + (32 * wave + (lane >> 3)) * 128 + (((lane & 7) ^ ((lane >> 4) + 4)) * 16);
  const int r = lane & 31, h = lane >> 5;
#pragma unroll
  for (int i = 0; i < 4; ++i) {
    c.xrd2[0][i] = xl + (64 * wm + 32 * isup + r) * 128 + (((2 * i + h) ^ ((r >> 1) & 7)) * 16);
    c.xrd2[1][i] = xl + (64 * wm + 32 * (1 - isup) + r) * 128 + (((2 * i + h) ^ ((r >> 1) & 7)) * 16);
  }
  const int blk = (lane >> 4) & 1, q = (lane & 15) >> 2, p = lane & 3;
  const int sw = (q >> 1) & 1;
  {
    const unsigned e = sbase + (8 * h + q) * 384 + (2 * blk + (p >> 1) + 4 * sw) * 16 + 8 * (p & 1);
    const unsigned o = sbase + (8 * h + q) * 384 + (2 * blk + (p >> 1) - 4 * sw) * 16 + 8 * (p & 1);
    c.woff = isup ? o + 192 : e;
    c.woff_o = isup ? e + 192 : o;
  }

  f32x16 acc[6];
#pragma unroll
  for (int i = 0; i < 6; ++i) acc[i] = f32x16{};
  asm volatile("s_nop 4" : "+v"(acc[0]), "+v"(acc[1]), "+v"(acc[2]), "+v"(acc[3]), "+v"(acc[4]), "+v"(acc[5]));
  u32x4 xr[2][4], wr[3][3];
  G1Frag fa, fb2;
#define G1_ISSUE_X(R, B) _Pragma("unroll") for (int i = 0; i < 4; ++i) GLOAD128S(xr[R][i], c.xo, c.xbase + (size_t)(8 * i) * DM * 2 + (B) * 128)
#define G1_ISSUE_W(R, S) _Pragma("unroll") for (int i = 0; i < 3; ++i) GLOAD128S(wr[R][i], c.wo[i], c.wbase + (size_t)(S) * 32 * FF * 2)
  G1_ISSUE_X(0, 0); G1_ISSUE_W(0, 0); G1_ISSUE_W(1, 1); G1_ISSUE_X(1, 1); G1_ISSUE_W(2, 2);
#define G1_EXTRA 0
  asm volatile("s_waitcnt vmcnt(%10)"
               : "+v"(wr[0][0]), "+v"(wr[0][1]), "+v"(wr[0][2]), "+v"(wr[1][0]), "+v"(wr[1][1]), "+v"(wr[1][2]),
                 "+v"(xr[0][0]), "+v"(xr[0][1]), "+v"(xr[0][2]), "+v"(xr[0][3]) : "i"(7 + G1_EXTRA) : "memory");
#pragma unroll
  for (int i = 0; i < 3; ++i) { DS_W128(c.ww[i], wr[0][i], 0); DS_W128(c.ww[i], wr[1][i], G1_WSLOT_B); }
#pragma unroll
  for (int i = 0; i < 4; ++i) { if (i & 1) DS_W128(c.xw1, xr[0][i], i * 1024); else DS_W128(c.xw, xr[0][i], i * 1024); }
  G1_ISSUE_W(0, 3); G1_ISSUE_W(1, 4); G1_ISSUE_X(0, 2);
  asm volatile("s_waitcnt lgkmcnt(0)\n\ts_barrier" ::: "memory");
  g1_read<0>(fa, c);
  G1Step<0>::run(acc, xr, wr, fa, fb2, c);
  asm volatile("s_nop 15\n\ts_nop 15" : "+v"(acc[0]), "+v"(acc[1]), "+v"(acc[2]), "+v"(acc[3]), "+v"(acc[4]), "+v"(acc[5]));

  unsigned keep[3][8];
  {
    unsigned give[3][8];
#pragma unroll
    for (int tt = 0; tt < 3; ++tt)
#pragma unroll
      for (int e = 0; e < 8; ++e) give[tt][e] = pk_bf16(acc[3 + tt][2 * e], acc[3 + tt][2 * e + 1]);
    __syncthreads();
    char* xo = smem + wave * 6144 + lane * 16;
#pragma unroll
    for (int tt = 0; tt < 3; ++tt) {
      *(u32x4*)(xo + (2 * tt) * 1024) = u32x4{give[tt][0], give[tt][1], give[tt][2], give[tt][3]};
      *(u32x4*)(xo + (2 * tt + 1) * 1024) = u32x4{give[tt][4], give[tt][5], give[tt][6], give[tt][7]};
    }
  }
#pragma unroll
  for (int tt = 0; tt < 3; ++tt)
#pragma unroll
    for (int e = 0; e < 8; ++e) keep[tt][e] = pk_bf16(acc[tt][2 * e], acc[tt][2 * e + 1]);
  __syncthreads();
  {
    const char* xi = smem + (wave ^ 2) * 6144 + lane * 16;
    const int mt = 4 * mb + 2 * wm + isup;
    u16* hp = hf + ((size_t)(mt * NKS + 6 * fb) * 64 + lane) * 8;
    auto finish = [&](auto ISUP) {
#pragma unroll
      for (int tt = 0; tt < 3; ++tt) {
        const int ft = 3 * fb + tt;
        if (ft < FF / 32) {
          const u32x4 o0 = *(const u32x4*)(xi + (2 * tt) * 1024);
          const u32x4 o1 = *(const u32x4*)(xi + (2 * tt + 1) * 1024);
          unsigned gp[8], up[8];
#pragma unroll
          for (int e = 0; e < 8; ++e) {
            const unsigned ot = e < 4 ? o0[e & 3] : o1[e & 3];
            gp[e] = decltype(ISUP)::value ? ot : keep[tt][e];
            up[e] = decltype(ISUP)::value ? keep[tt][e] : ot;
          }
          f32x2 xg[8], ar[8];
#pragma unroll
          for (int e = 0; e < 8; ++e) xg[e] = f32x2{bf_lo(gp[e]), bf_hi(gp[e])};
#pragma unroll
          for (int e = 0; e < 8; ++e) ar[e] = xg[e] * xg[e];
#pragma unroll
          for (int e = 0; e < 8; ++e) ar[e] = __builtin_elementwise_fma(ar[e], f32x2{-0.10294323958f, -0.10294323958f}, f32x2{-2.30220819814f, -2.30220819814f});
#pragma unroll
          for (int e = 0; e < 8; ++e) ar[e] = ar[e] * xg[e];
#pragma unroll
          for (int e = 0; e < 8; ++e) ar[e] = f32x2{__builtin_amdgcn_exp2f(ar[e][0]), __builtin_amdgcn_exp2f(ar[e][1])};
#pragma unroll
          for (int e = 0; e < 8; ++e) ar[e] = ar[e] + f32x2{1.0f, 1.0f};
#pragma unroll
          for (int e = 0; e < 8; ++e) ar[e] = f32x2{__builtin_amdgcn_rcpf(ar[e][0]), __builtin_amdgcn_rcpf(ar[e][1])};
#pragma unroll
          for (int e = 0; e < 8; ++e) ar[e] = ar[e] * xg[e];
          unsigned o[8];
#pragma unroll
          for (int e = 0; e < 8; ++e) o[e] = pk_bf16(ar[e][0], ar[e][1]);
#pragma unroll
          for (int e = 0; e < 8; ++e) ar[e] = f32x2{bf_lo(o[e]), bf_hi(o[e])} * f32x2{bf_lo(up[e]), bf_hi(up[e])};
#pragma unroll
          for (int e = 0; e < 8; ++e) o[e] = pk_bf16(ar[e][0], ar[e][1]);
          { const u32x4 v0 = u32x4{o[0], o[1], o[2], o[3]}, v1 = u32x4{o[4], o[5], o[6], o[7]};
            asm volatile("global_store_dwordx4 %0, %1, off sc1\n\ts_nop 1" ::"v"(hp + (2 * tt) * 512), "v"(v0) : "memory");
            asm volatile("global_store_dwordx4 %0, %1, off sc1\n\ts_nop 1" ::"v"(hp + (2 * tt + 1) * 512), "v"(v1) : "memory"); }
        } else if (ft < NKS / 2) {
          *(u32x4*)(hp + (2 * tt) * 512) = u32x4{0, 0, 0, 0};
          *(u32x4*)(hp + (2 * tt + 1) * 512) = u32x4{0, 0, 0, 0};
        }
      }
    };
    if (isup) finish(std::integral_constant<bool, true>{});
    else finish(std::integral_constant<bool, false>{});
  }
}

__device__ __forceinline__ void gate_unit(int b, const u16* __restrict__ x, const u16* __restrict__ gw,
                                          float* __restrict__ gout) {
  const int tid = threadIdx.x, lane = tid & 63, wave = tid >> 6;
  const int j = lane & 7, rr = lane >> 3;
  u32x4 gv[8];
#pragma unroll
  for (int i = 0; i < 8; ++i) gv[i] = *(const u32x4*)(gw + (j + 8 * i) * 8);
  const int row0 = b * 128 + wave * 32 + rr;
  u32x4 xv[4][8];
#pragma unroll
  for (int ps = 0; ps < 4; ++ps)
#pragma unroll
    for (int i = 0; i < 8; ++i) xv[ps][i] = *(const u32x4*)(x + (size_t)(row0 + 8 * ps) * DM + (j + 8 * i) * 8);
#pragma unroll
  for (int ps = 0; ps < 4; ++ps) {
    float s0 = 0.f, s1 = 0.f;
#pragma unroll
    for (int i = 0; i < 8; ++i)
#pragma unroll
      for (int e = 0; e < 4; ++e) {
        const unsigned pr = pk_bf16(smul(bf_lo(xv[ps][i][e]), bf_lo(gv[i][e])), smul(bf_hi(xv[ps][i][e]), bf_hi(gv[i][e])));
        s0 = sadd(s0, bf_lo(pr));
        s1 = sadd(s1, bf_hi(pr));
      }
    float s = s0 + s1;
    s += __shfl_xor(s, 1);
    s += __shfl_xor(s, 2);
    s += __shfl_xor(s, 4);
    if (j == 0) {
      const float sb = rbf(s);
      gout[row0 + 8 * ps] = rbf(1.0f / (1.0f + expf(-sb)));
    }
  }
}

__global__ __launch_bounds__(256, 2) void k1_kernel(const u16* __restrict__ u, const u16* __restrict__ logAr,
                                                   const u16* __restrict__ Aim, const u16* __restrict__ Cre,
                                                   const u16* __restrict__ Cim, const u16* __restrict__ logdt,
                                                   const u16* __restrict__ Dskip, const u16* __restrict__ wg,
                                                   const u16* __restrict__ wu, const u16* __restrict__ gw,
                                                   const u16* __restrict__ wd, u16* __restrict__ hbuf, u16* __restrict__ aT,
                                                   float* __restrict__ gout DBG_ARG
                                                   ) {
  __shared__ __attribute__((aligned(4096))) char smem[G1_LDS > SSM_LDS ? G1_LDS : SSM_LDS];
  const int bid = blockIdx.x;
  if (bid < NSSM) {
    ssm_unit(bid, u, logAr, Aim, Cre, Cim, logdt, Dskip, aT, smem DBG_PASS);
    unsigned acc = 0;
    for (int i = (bid >> 3) * 256 + threadIdx.x; i < 11008; i += 32 * 256) acc += ((const unsigned*)wd)[i * 32];
    asm volatile("" ::"v"(acc));
  } else {
    const int xcd = bid & 7, idx = (bid - NSSM) >> 3;
    if (idx < 30) {
      gemm1_tile(30 * xcd + idx, u, wg, wu, hbuf, smem DBG_PASS);
    }
    else gate_unit(2 * xcd + idx - 30, u, gw, gout);
  }
}

constexpr int G2_SLOT_B = 4096;
constexpr int G2_WAVE_B = 2 * G2_SLOT_B;
constexpr int G2_PITCH = 272;
constexpr int G2_A_OFF = 4 * 64 * G2_PITCH;
constexpr int K2_LDS = G2_A_OFF + 8192;
#define G2D 3
constexpr int G2_DEPTH = G2D;

struct G2Ctx {
  const u16* hsrc;
  const u16* wd;
  int wrow0, wcol;
  unsigned wb32, woff, ww;
};

template <int ST>
struct G2Step {
  static __device__ __forceinline__ void issue(bf16x8 (&hfr)[11][4], u32x4 (&wr)[11][4], const G2Ctx& c) {
#pragma unroll
    for (int i = 0; i < 4; ++i) {
      int kr = c.wrow0 + ST * 32 + 8 * i;
      kr = kr < FF ? kr : FF - 1;
      GLOAD128(wr[ST][i], c.wd + (size_t)kr * DM + c.wcol);
    }
#pragma unroll
    for (int mi = 0; mi < 2; ++mi)
#pragma unroll
      for (int i = 0; i < 2; ++i)
        GLOAD128A(hfr[ST][2 * mi + i], c.hsrc + (size_t)(mi * NKS + 2 * ST + i) * 512);

  }
  static __device__ __forceinline__ void run(f32x16 (&acc)[2][2], bf16x8 (&hfr)[11][4], u32x4 (&wr)[11][4], const G2Ctx& c) {
    constexpr int LAST = (ST + G2_DEPTH - 1 < 10) ? ST + G2_DEPTH - 1 : 10;
    constexpr int NW = 8 * (LAST - ST);
    asm volatile("s_waitcnt vmcnt(%4)" : "+v"(wr[ST][0]), "+v"(wr[ST][1]), "+v"(wr[ST][2]), "+v"(wr[ST][3]) : "i"(NW + 4) : "memory");
#pragma unroll
    for (int i = 0; i < 4; ++i) DS_W128(c.ww, wr[ST][i], (ST & 1) * G2_SLOT_B + i * 1024);
    asm volatile("s_waitcnt lgkmcnt(0)" ::: "memory");
    const unsigned sb = c.wb32 + c.woff;
    s16x4 w[2][2][2];
#pragma unroll
    for (int i = 0; i < 2; ++i)
#pragma unroll
      for (int t = 0; t < 2; ++t) {
        DS_TR(w[i][t][0], sb ^ (t * 64), (ST & 1) * G2_SLOT_B + i * 2048);
        DS_TR(w[i][t][1], sb ^ (t * 64), (ST & 1) * G2_SLOT_B + i * 2048 + 1024);
      }
    asm volatile("s_waitcnt vmcnt(%4)" : "+a"(hfr[ST][0]), "+a"(hfr[ST][1]), "+a"(hfr[ST][2]), "+a"(hfr[ST][3]) : "i"(NW) : "memory");
    if constexpr (ST + G2_DEPTH <= 10) G2Step<ST + G2_DEPTH>::issue(hfr, wr, c);
    asm volatile("s_waitcnt lgkmcnt(0)"
                 : "+v"(w[0][0][0]), "+v"(w[0][0][1]), "+v"(w[0][1][0]), "+v"(w[0][1][1]), "+v"(w[1][0][0]), "+v"(w[1][0][1]),
                   "+v"(w[1][1][0]), "+v"(w[1][1][1])::"memory");
#pragma unroll
    for (int i = 0; i < 2; ++i)
#pragma unroll
      for (int t = 0; t < 2; ++t)
#pragma unroll
        for (int mi = 0; mi < 2; ++mi)
          acc[t][mi] = __builtin_amdgcn_mfma_f32_32x32x16_bf16(cat4(w[i][t][0], w[i][t][1]), hfr[ST][2 * mi + i], acc[t][mi], 0, 0, 0);
    if constexpr (ST + 1 <= 10) G2Step<ST + 1>::run(acc, hfr, wr, c);
  }
};

__global__ __launch_bounds__(256, 1) void k2_kernel(const u16* __restrict__ hf, const u16* __restrict__ wd,
                                                   const u16* __restrict__ aT, const float* __restrict__ gin,
                                                   u16* __restrict__ out DBG2_ARG) {
  __shared__ __attribute__((aligned(4096))) char smem[K2_LDS];
  const int tid = threadIdx.x, lane = tid & 63;
  STAMP2(0);
  const int wave = __builtin_amdgcn_readfirstlane(tid >> 6);
  const int bid = blockIdx.x;
  const int xcd = bid & 7, idx = bid >> 3;
  const int m0 = 64 * (4 * xcd + (idx >> 3)), n0 = 64 * (idx & 7);

#pragma unroll
  for (int i = 0; i < 2; ++i) {
    const int piece = 2 * wave + i;
    const int drow = 8 * piece + (lane >> 3);
    glds16(aT + (size_t)(n0 + drow) * SEQ + m0 + (lane & 7) * 8, smem + G2_A_OFF + piece * 1024 + lane * 16);
  }

  G2Ctx c;
  c.wb32 = lds_addr(smem) + wave * G2_WAVE_B;
  c.hsrc = hf + ((size_t)((m0 >> 5) * NKS + 22 * wave) * 64 + lane) * 8;
  c.wd = wd;
  c.wcol = n0 + 8 * (lane & 7);
  c.wrow0 = 352 * wave + (lane >> 3);
  c.ww = c.wb32 + (lane >> 3) * 128 + (((lane & 7) ^ (((lane >> 4) & 1) << 2)) * 16);

  const int r = lane & 31, h = lane >> 5;
  const int blk = (lane >> 4) & 1, q = (lane & 15) >> 2, p = lane & 3;
  c.woff = (4 * h + q) * 128 + (((2 * blk + (p >> 1)) ^ (((q >> 1) & 1) << 2)) * 16) + 8 * (p & 1);

  f32x16 acc[2][2];
#pragma unroll
  for (int i = 0; i < 2; ++i)
#pragma unroll
    for (int j = 0; j < 2; ++j) acc[i][j] = f32x16{};
  bf16x8 hfr[11][4];
  u32x4 wr[11][4];

  G2Step<0>::issue(hfr, wr, c);
  G2Step<1>::issue(hfr, wr, c);
  if constexpr (G2_DEPTH > 2) G2Step<2>::issue(hfr, wr, c);
  if constexpr (G2_DEPTH > 3) G2Step<3>::issue(hfr, wr, c);
  if constexpr (G2_DEPTH > 4) G2Step<4>::issue(hfr, wr, c);
  if constexpr (G2_DEPTH > 5) G2Step<5>::issue(hfr, wr, c);
  if constexpr (G2_DEPTH > 6) G2Step<6>::issue(hfr, wr, c);
  STAMP2(1);
  STAMP2(2);
  G2Step<0>::run(acc, hfr, wr, c);
  STAMP2(3);
  __syncthreads();
  {
    char* pw = smem + wave * (64 * G2_PITCH);
#pragma unroll
    for (int c = 0; c < 2; ++c)
#pragma unroll
      for (int mi = 0; mi < 2; ++mi)
#pragma unroll
        for (int gq = 0; gq < 4; ++gq) {
          const f32x4 v = {acc[c][mi][4 * gq], acc[c][mi][4 * gq + 1], acc[c][mi][4 * gq + 2], acc[c][mi][4 * gq + 3]};
          *(f32x4*)(pw + (32 * mi + r) * G2_PITCH + (32 * c + 8 * gq + 4 * h) * 4) = v;
        }
  }
  __syncthreads();
  STAMP2(4);
  {
    const int m = tid >> 2, nq = (tid & 3) * 16;
    const float g = gin[m0 + m];
    const float omg = rbf(1.0f - g);
    float b[16];
#pragma unroll
    for (int j = 0; j < 4; ++j) {
      f32x4 s = *(const f32x4*)(smem + m * G2_PITCH + (nq + 4 * j) * 4);
#pragma unroll
      for (int w = 1; w < 4; ++w) s += *(const f32x4*)(smem + w * (64 * G2_PITCH) + m * G2_PITCH + (nq + 4 * j) * 4);
#pragma unroll
      for (int e = 0; e < 4; ++e) b[4 * j + e] = s[e];
    }
    const u16* aimg = (const u16*)(smem + G2_A_OFF);
    unsigned o[8];
#pragma unroll
    for (int j = 0; j < 8; ++j) {
      const float a0 = bf2f(aimg[(nq + 2 * j) * 64 + m]);
      const float a1 = bf2f(aimg[(nq + 2 * j + 1) * 64 + m]);
      const float o0 = rbf(g * a0) + rbf(omg * rbf(b[2 * j]));
      const float o1 = rbf(g * a1) + rbf(omg * rbf(b[2 * j + 1]));
      o[j] = pk_bf16(o0, o1);
    }
    u16* op = out + (size_t)(m0 + m) * DM + n0 + nq;
    *(u32x4*)op = u32x4{o[0], o[1], o[2], o[3]};
    *(u32x4*)(op + 8) = u32x4{o[4], o[5], o[6], o[7]};
  }
  STAMP2(5);
}

extern "C" void kernel_launch(void* const* d_in, const int* in_sizes, int n_in, void* d_out, int out_size,
                              void* d_ws, size_t ws_size, hipStream_t stream) {
  const u16* u = (const u16*)d_in[0];
  const u16* logAr = (const u16*)d_in[1];
  const u16* Aim = (const u16*)d_in[2];
  const u16* Cre = (const u16*)d_in[3];
  const u16* Cim = (const u16*)d_in[4];
  const u16* logdt = (const u16*)d_in[5];
  const u16* Dskip = (const u16*)d_in[6];
  const u16* wg = (const u16*)d_in[8];
  const u16* wu = (const u16*)d_in[9];
  const u16* wd = (const u16*)d_in[10];
  const u16* gw = (const u16*)d_in[11];
  char* ws = (char*)d_ws;
  u16* hbuf = (u16*)ws;
  u16* aT = (u16*)(ws + (size_t)SEQ * FFP * 2);
  float* gbuf = (float*)(ws + (size_t)SEQ * FFP * 2 + (size_t)DM * SEQ * 2);
  k1_kernel<<<512, 256, 0, stream>>>(u, logAr, Aim, Cre, Cim, logdt, Dskip, wg, wu, gw, wd, hbuf, aT, gbuf
      );
  k2_kernel<<<256, 256, 0, stream>>>(hbuf, wd, aT, gbuf, (u16*)d_out
      );
}
```

```cpp
#include <hip/hip_runtime.h>
#include <hip/hip_bf16.h>
#include <stdint.h>
#include <type_traits>

typedef unsigned short u16;
typedef __attribute__((ext_vector_type(8))) short bf16x8;
typedef __attribute__((ext_vector_type(4))) short s16x4;
typedef __attribute__((ext_vector_type(4))) float f32x4;
typedef __attribute__((ext_vector_type(16))) float f32x16;
typedef __attribute__((ext_vector_type(4))) unsigned u32x4;
typedef __attribute__((ext_vector_type(2))) unsigned u32x2;
typedef __attribute__((ext_vector_type(2))) __bf16 bf16x2_t;
typedef __attribute__((ext_vector_type(2))) float f32x2;
typedef __attribute__((address_space(3))) s16x4* lds_s16x4_ptr;

constexpr int SEQ = 2048, DM = 512, NST = 128, FF = 1376, FFP = 1408;
constexpr int NSSM = 256;
constexpr int NKS = 88;

__device__ __forceinline__ unsigned pk_bf16(float lo, float hi) {
  f32x2 v = {lo, hi};
  bf16x2_t b = __builtin_convertvector(v, bf16x2_t);
  return __builtin_bit_cast(unsigned, b);
}
__device__ __forceinline__ float bf_lo(unsigned w) { return __uint_as_float(w << 16); }
__device__ __forceinline__ float bf_hi(unsigned w) { return __uint_as_float(w & 0xffff0000u); }
__device__ __forceinline__ float rbf(float x) { return bf_lo(pk_bf16(x, 0.f)); }
__device__ __forceinline__ float bf2f(u16 v) { return __uint_as_float(((unsigned)v) << 16); }

__device__ __forceinline__ float smul(float a, float b) { float r; asm("v_mul_f32 %0, %1, %2" : "=v"(r) : "v"(a), "v"(b)); return r; }
__device__ __forceinline__ float sadd(float a, float b) { float r; asm("v_add_f32 %0, %1, %2" : "=v"(r) : "v"(a), "v"(b)); return r; }
__device__ __forceinline__ float ssub(float a, float b) { float r; asm("v_sub_f32 %0, %1, %2" : "=v"(r) : "v"(a), "v"(b)); return r; }
__device__ __forceinline__ float sfma(float a, float b, float c) { float r; asm("v_fma_f32 %0, %1, %2, %3" : "=v"(r) : "v"(a), "v"(b), "v"(c)); return r; }
__device__ __forceinline__ float sfms(float a, float b, float c) { float r; asm("v_fma_f32 %0, -%1, %2, %3" : "=v"(r) : "v"(a), "v"(b), "v"(c)); return r; }
__device__ __forceinline__ float sfnn(float a, float b, float c) { float r; asm("v_fma_f32 %0, -%1, %2, -%3" : "=v"(r) : "v"(a), "v"(b), "v"(c)); return r; }
#define TRANS_FENCE8(a, o) asm volatile("s_nop 1" : "+v"(a[o]), "+v"(a[o + 1]), "+v"(a[o + 2]), "+v"(a[o + 3]), "+v"(a[o + 4]), "+v"(a[o + 5]), "+v"(a[o + 6]), "+v"(a[o + 7]))
__device__ __forceinline__ bf16x8 mk8(unsigned a, unsigned b, unsigned c, unsigned d) {
  u32x4 v = {a, b, c, d};
  return __builtin_bit_cast(bf16x8, v);
}
__device__ __forceinline__ bf16x8 cat4(s16x4 a, s16x4 b) {
  return __builtin_shufflevector(a, b, 0, 1, 2, 3, 4, 5, 6, 7);
}
__device__ __forceinline__ s16x4 tr_read(const char* smem_ptr) {
  return __builtin_amdgcn_ds_read_tr16_b64_v4i16((lds_s16x4_ptr)(smem_ptr));
}
__device__ __forceinline__ unsigned lds_addr(const void* p) {
  return (unsigned)(size_t)(const __attribute__((address_space(3))) char*)p;
}
__device__ __forceinline__ void glds16(const void* g, char* l) {
  __builtin_amdgcn_global_load_lds((const unsigned*)g, (unsigned*)l, 16, 0, 0);
}

#define STAMP2(i)
#define DBG2_ARG
#define DBG2_PASS
#define STAMP(i)
#define DBG_ARG
#define DBG_PASS
constexpr int U_COPY_B = 4160;
constexpr int U_CH_B = 4 * U_COPY_B;
constexpr int U_REGION_B = 2 * U_CH_B;
constexpr int KR_PART_B = 4576;
constexpr int KR_OFF = U_REGION_B;
constexpr int KQ_B = 8208;
constexpr int KX_OFF = 2 * KQ_B;
constexpr int SSM_LDS = KR_OFF + 4 * KR_PART_B;
static_assert(KX_OFF + 4 * 1536 <= U_REGION_B, "scratch must fit the U region");

#define DS_TR(dst, addr, off) asm volatile("ds_read_b64_tr_b16 %0, %1 offset:%2" : "=v"(dst) : "v"(addr), "i"(off) : "memory")
#define DS_B128(dst, addr, off) asm volatile("ds_read_b128 %0, %1 offset:%2" : "=v"(dst) : "v"(addr), "i"(off) : "memory")
#define DS_B64(dst, addr, off) asm volatile("ds_read_b64 %0, %1 offset:%2" : "=v"(dst) : "v"(addr), "i"(off) : "memory")
#define MFMA16(acc, a, b) asm volatile("v_mfma_f32_16x16x32_bf16 %0, %1, %2, %0" : "+v"(acc) : "v"(a), "v"(b))

struct ConvFrag {
  s16x4 a[8][2];
  bf16x8 bh[8], bl[8];
};
template <int RI, int K>
__device__ __forceinline__ void conv_read(ConvFrag& f, unsigned ua, unsigned kb) {
  if constexpr (K < 16) {
    DS_B64(f.a[K >> 1][K & 1], ua, 64 * (8 * (K >> 1) + RI) + 8 * (K & 1));
  } else if constexpr (K < 24) {
    DS_B128(f.bh[K - 16], kb, 64 * (56 - 8 * (K - 16) + RI));
  } else {
    DS_B128(f.bl[K - 24], kb, 64 * (56 - 8 * (K - 24) + RI) + KR_PART_B);
  }
}
template <int RI, int K>
__device__ __forceinline__ void conv_read_all(ConvFrag& f, unsigned ua, unsigned kb) {
  conv_read<RI, K>(f, ua, kb);
  if constexpr (K + 1 < 32) conv_read_all<RI, K + 1>(f, ua, kb);
}
#define CONV_WAIT(f)                                                                                                  \
  do {                                                                                                                \
    asm volatile("s_waitcnt lgkmcnt(0)"                                                                               \
                 : "+v"(f.a[0][0]), "+v"(f.a[0][1]), "+v"(f.a[1][0]), "+v"(f.a[1][1]), "+v"(f.a[2][0]), "+v"(f.a[2][1]), \
                   "+v"(f.a[3][0]), "+v"(f.a[3][1]), "+v"(f.a[4][0]), "+v"(f.a[4][1]), "+v"(f.a[5][0]), "+v"(f.a[5][1]), \
                   "+v"(f.a[6][0]), "+v"(f.a[6][1]), "+v"(f.a[7][0]), "+v"(f.a[7][1])::"memory");                     \
    asm volatile("" : "+v"(f.bh[0]), "+v"(f.bh[1]), "+v"(f.bh[2]), "+v"(f.bh[3]), "+v"(f.bh[4]), "+v"(f.bh[5]),        \
                 "+v"(f.bh[6]), "+v"(f.bh[7]), "+v"(f.bl[0]), "+v"(f.bl[1]), "+v"(f.bl[2]), "+v"(f.bl[3]),            \
                 "+v"(f.bl[4]), "+v"(f.bl[5]), "+v"(f.bl[6]), "+v"(f.bl[7])::"memory");                              \
  } while (0)
constexpr int conv_tau(int P) { int t = 0; while (P >= 8 - t) { P -= 8 - t; ++t; } return t; }
constexpr int conv_sig(int P) { int t = 0; while (P >= 8 - t) { P -= 8 - t; ++t; } return P; }
template <int RI, int P>
__device__ __forceinline__ void conv_pairs(f32x4 (&acc)[8], const bf16x8 (&A)[8], ConvFrag& cur, ConvFrag& nxt, unsigned ua,
                                           unsigned kb) {
  constexpr int T = conv_tau(P), SG = conv_sig(P);
  MFMA16(acc[T + SG], A[SG], cur.bl[T]);
  if constexpr (RI < 3 && P < 32) conv_read<RI + 1, P>(nxt, ua, kb);
  MFMA16(acc[T + SG], A[SG], cur.bh[T]);
  if constexpr (P + 1 < 36) conv_pairs<RI, P + 1>(acc, A, cur, nxt, ua, kb);
}
template <int RI>
__device__ __forceinline__ void conv_rho(f32x4 (&acc)[8], ConvFrag& cur, ConvFrag& nxt, unsigned ua, unsigned kb) {
  CONV_WAIT(cur);
  bf16x8 A[8];
#pragma unroll
  for (int i = 0; i < 8; ++i) A[i] = cat4(cur.a[i][0], cur.a[i][1]);
  conv_pairs<RI, 0>(acc, A, cur, nxt, ua, kb);
  if constexpr (RI < 3) conv_rho<RI + 1>(acc, nxt, cur, ua, kb);
}

__device__ __forceinline__ void split_pair(float v0, float v1, unsigned& hi, unsigned& lo) {
  hi = pk_bf16(v0, v1);
  lo = pk_bf16(ssub(v0, bf_lo(hi)), ssub(v1, bf_hi(hi)));
}

__device__ __forceinline__ void ssm_unit(int w, const u16* __restrict__ u, const u16* __restrict__ logAr,
                                         const u16* __restrict__ Aim, const u16* __restrict__ Cre,
                                         const u16* __restrict__ Cim, const u16* __restrict__ logdt,
                                         const u16* __restrict__ Dskip, u16* __restrict__ aT, char* smem DBG_ARG) {
  const int tid = threadIdx.x, lane = tid & 63;
  const int wave = __builtin_amdgcn_readfirstlane(tid >> 6);
  const int ch = wave >> 1, w2 = wave & 1;
  const int d0 = 2 * w;
  const int d = d0 + ch;

  const int pidx = d0 * NST + tid;
  const u16 r_lar = logAr[pidx], r_aim = Aim[pidx], r_cre = Cre[pidx], r_cim = Cim[pidx], r_ldt = logdt[d], r_dsk = Dskip[d];
  const unsigned* u32p = (const unsigned*)(u + d0);
  unsigned uw[8], up[3];
#pragma unroll
  for (int i = 0; i < 3; ++i) up[i] = 0;
  if (lane == 0 && tid > 0) {
#pragma unroll
    for (int i = 0; i < 3; ++i) up[i] = u32p[(size_t)(8 * tid - 3 + i) * (DM / 2)];
  }
  const unsigned uoff = (unsigned)tid * 8192u;
  {
    const float dt = rbf(expf(bf2f(r_ldt)));
    const float are = rbf(expf(bf2f(r_lar)));
    f32x4 prm;
    prm[0] = (-are * dt) * 1.44269504088896340736f;
    prm[1] = (bf2f(r_aim) * dt) * 0.15915494309189533577f;
    prm[2] = 2.0f * (bf2f(r_cre) * dt);
    prm[3] = 2.0f * (bf2f(r_cim) * dt);
    *(f32x4*)(smem + KR_OFF + tid * 16) = prm;
  }
  __syncthreads();

  const int r = lane & 31, h = lane >> 5;
  f32x16 dacc = {};
  {
    int p1i;
    if (r < 8) p1i = r;
    else if (r < 12) p1i = 256 + 2 * (r - 8);
    else if (r < 16) p1i = 512 + 256 * ((r - 12) >> 1) + 4 * ((r - 12) & 1);
    else if (r < 21) p1i = 1024 + 256 * (r - 16);
    else p1i = 0;
    const float p1 = (float)p1i, p2 = (float)(8 * r);
    const char* prm_base = smem + KR_OFF + (ch * 128 + 64 * w2 + 4 * h) * 16;
#pragma unroll
    for (int ks = 0; ks < 8; ++ks) {
      if (ks >= 4) {
        asm volatile("global_load_dword %0, %1, %2 offset:%3" : "=v"(uw[2 * ks - 8]) : "v"(uoff), "s"((const char*)u32p + ((2 * ks - 8) >> 2) * 4096), "i"(((2 * ks - 8) & 3) * 1024) : "memory");
        asm volatile("global_load_dword %0, %1, %2 offset:%3" : "=v"(uw[2 * ks - 7]) : "v"(uoff), "s"((const char*)u32p + ((2 * ks - 7) >> 2) * 4096), "i"(((2 * ks - 7) & 3) * 1024) : "memory");
      }
      f32x4 prm[4];
#pragma unroll
      for (int q = 0; q < 4; ++q) prm[q] = *(const f32x4*)(prm_base + (8 * ks + q) * 16);
      float ea[4], eb[4], fa[4], fb[4];
#pragma unroll
      for (int q = 0; q < 4; ++q) {
        ea[q] = __builtin_amdgcn_exp2f(smul(prm[q][0], p1)); eb[q] = __builtin_amdgcn_exp2f(smul(prm[q][0], p2));
        fa[q] = __builtin_amdgcn_fractf(smul(prm[q][1], p1)); fb[q] = __builtin_amdgcn_fractf(smul(prm[q][1], p2));
      }
      float ac[4], as[4], bc[4], bs[4];
#pragma unroll
      for (int q = 0; q < 4; ++q) {
        ac[q] = __builtin_amdgcn_cosf(fa[q]); as[q] = __builtin_amdgcn_sinf(fa[q]);
        bc[q] = __builtin_amdgcn_cosf(fb[q]); bs[q] = __builtin_amdgcn_sinf(fb[q]);
      }
      asm volatile("s_nop 1" : "+v"(ea[0]), "+v"(ea[1]), "+v"(ea[2]), "+v"(ea[3]), "+v"(eb[0]), "+v"(eb[1]), "+v"(eb[2]), "+v"(eb[3]),
                   "+v"(ac[0]), "+v"(ac[1]), "+v"(ac[2]), "+v"(ac[3]), "+v"(as[0]), "+v"(as[1]), "+v"(as[2]), "+v"(as[3]),
                   "+v"(bc[0]), "+v"(bc[1]), "+v"(bc[2]), "+v"(bc[3]), "+v"(bs[0]), "+v"(bs[1]), "+v"(bs[2]), "+v"(bs[3]));
      unsigned ah[4], al[4], bh[4], bl[4];
#pragma unroll
      for (int q = 0; q < 4; ++q) {
        const float cr = smul(prm[q][2], ea[q]), ci = smul(prm[q][3], ea[q]);
        const float gre = sfms(ci, as[q], smul(cr, ac[q]));
        const float gmi = sfnn(ci, ac[q], smul(cr, as[q]));
        split_pair(gre, gmi, ah[q], al[q]);
        split_pair(smul(bc[q], eb[q]), smul(bs[q], eb[q]), bh[q], bl[q]);
      }
      const bf16x8 Ah = mk8(ah[0], ah[1], ah[2], ah[3]), Al = mk8(al[0], al[1], al[2], al[3]);
      const bf16x8 Bh = mk8(bh[0], bh[1], bh[2], bh[3]), Bl = mk8(bl[0], bl[1], bl[2], bl[3]);
      dacc = __builtin_amdgcn_mfma_f32_32x32x16_bf16(Al, Bh, dacc, 0, 0, 0);
      dacc = __builtin_amdgcn_mfma_f32_32x32x16_bf16(Ah, Bl, dacc, 0, 0, 0);
      dacc = __builtin_amdgcn_mfma_f32_32x32x16_bf16(Ah, Bh, dacc, 0, 0, 0);
    }
  }
  {
    float* xo = (float*)(smem + KX_OFF + wave * 1536) + lane;
    const float* xi = (const float*)(smem + KX_OFF + (wave ^ 1) * 1536) + lane;
    float mine[6];
    if (w2 == 0) {
#pragma unroll
      for (int e = 0; e < 6; ++e) { xo[e * 64] = dacc[6 + e]; mine[e] = dacc[e]; }
    } else {
#pragma unroll
      for (int e = 0; e < 6; ++e) { xo[e * 64] = dacc[e]; mine[e] = dacc[6 + e]; }
    }
    __syncthreads();
    unsigned* Kq = (unsigned*)(smem + ch * KQ_B);
    unsigned pq[6];
#pragma unroll
    for (int e = 0; e < 6; ++e) {
      const float v = sadd(mine[e], xi[e * 64]);
      const unsigned hi = pk_bf16(v, 0.f);
      pq[e] = pk_bf16(0.f, ssub(v, bf_lo(hi))) | hi;
    }
    if (w2 == 0) {
#pragma unroll
      for (int e = 0; e < 4; ++e) Kq[4 * h + e + 8 * r] = pq[e];
      Kq[(h ? 512 : 256) + 8 * r] = pq[4];
      Kq[(h ? 516 : 258) + 8 * r] = pq[5];
    } else {
      Kq[(h ? 768 : 260) + 8 * r] = pq[0];
      Kq[(h ? 772 : 262) + 8 * r] = pq[1];
      if (h == 0) {
#pragma unroll
        for (int e = 0; e < 4; ++e) Kq[1024 + 256 * e + 8 * r] = pq[2 + e];
      } else if (r == 0) {
        Kq[2048] = pq[2];
      }
    }
  }
  __syncthreads();

  {
    const int t = tid & 127;
    const unsigned* Kq = (const unsigned*)(smem + ch * KQ_B);
    unsigned* krh = (unsigned*)(smem + KR_OFF + (ch * 2) * KR_PART_B);
    unsigned* krl = (unsigned*)((char*)krh + KR_PART_B);
    unsigned q0[8], q1[8];
#pragma unroll
    for (int m = 0; m < 8; ++m) {
      const int sh = m < 4 ? 3 : (m < 6 ? 2 : (m < 7 ? 1 : 0));
      const int l0 = 2047 - 2 * (t + 128 * m), l1 = l0 - 1;
      int pa = l0, pb = l1;
      if (sh) {
        pa = (l0 + (1 << (sh - 1)) - 1 + ((l0 >> sh) & 1)) & ~((1 << sh) - 1);
        pb = (l1 + (1 << (sh - 1)) - 1 + ((l1 >> sh) & 1)) & ~((1 << sh) - 1);
      }
      q0[m] = Kq[pa];
      q1[m] = Kq[pb];
    }
#pragma unroll
    for (int m = 0; m < 8; ++m) {
      krh[t + 128 * m] = __builtin_amdgcn_perm(q1[m], q0[m], 0x05040100u);
      krl[t + 128 * m] = __builtin_amdgcn_perm(q1[m], q0[m], 0x07060302u);
    }
    if (t < 120) { krh[1024 + t] = 0; krl[1024 + t] = 0; }
  }
  __syncthreads();

  asm volatile("s_waitcnt vmcnt(0)" : "+v"(uw[0]), "+v"(uw[1]), "+v"(uw[2]), "+v"(uw[3]), "+v"(uw[4]), "+v"(uw[5]), "+v"(uw[6]), "+v"(uw[7])::"memory");
  {
    unsigned e[11];
#pragma unroll
    for (int i = 0; i < 3; ++i) {
      const unsigned nb = __shfl_up(uw[5 + i], 1);
      e[i] = (lane == 0) ? up[i] : nb;
    }
#pragma unroll
    for (int i = 0; i < 8; ++i) e[3 + i] = uw[i];
    unsigned p0[10], p1[10];
#pragma unroll
    for (int i = 0; i < 10; ++i) {
      p0[i] = __builtin_amdgcn_perm(e[i + 1], e[i], 0x05040100u);
      p1[i] = __builtin_amdgcn_perm(e[i + 1], e[i], 0x07060302u);
    }
#pragma unroll
    for (int c = 0; c < 4; ++c) {
      *(u32x4*)(smem + c * U_COPY_B + (tid + 2) * 16) = u32x4{p0[c], p0[c + 2], p0[c + 4], p0[c + 6]};
      *(u32x4*)(smem + U_CH_B + c * U_COPY_B + (tid + 2) * 16) = u32x4{p1[c], p1[c + 2], p1[c + 4], p1[c + 6]};
    }
    if (tid < 16) *(u32x4*)(smem + (tid >> 1) * U_COPY_B + (tid & 1) * 16) = u32x4{0, 0, 0, 0};
  }
  __syncthreads();

  f32x4 acc[8];
#pragma unroll
  for (int t = 0; t < 8; ++t) acc[t] = f32x4{0.f, 0.f, 0.f, 0.f};
  asm volatile("s_nop 4" : "+v"(acc[0]), "+v"(acc[1]), "+v"(acc[2]), "+v"(acc[3]), "+v"(acc[4]), "+v"(acc[5]), "+v"(acc[6]), "+v"(acc[7]));
  {
    const int r16 = lane & 15, hq = lane >> 4;
    const unsigned sb = lds_addr(smem);
    const unsigned ua = sb + ch * U_CH_B + (r16 & 3) * U_COPY_B + 2 * (4 + (r16 & ~3) + 8 * hq) + 64 * 4 * w2;
    const unsigned kb = sb + KR_OFF + (ch * 2) * KR_PART_B + 2 * (2032 - 16 * r16 + 8 * hq) - 64 * 56 + 64 * 4 * w2;
    ConvFrag f0, f1;
    conv_read_all<0, 0>(f0, ua, kb);
    conv_rho<0>(acc, f0, f1, ua, kb);
  }
  asm volatile("s_nop 15" : "+v"(acc[0]), "+v"(acc[1]), "+v"(acc[2]), "+v"(acc[3]), "+v"(acc[4]), "+v"(acc[5]), "+v"(acc[6]), "+v"(acc[7]));
  __syncthreads();
  {
    char* xw = smem + KR_OFF + (ch * 2 + w2) * 4096 + lane * 16;
    const char* xr = smem + KR_OFF + (ch * 2 + (w2 ^ 1)) * 4096 + lane * 16;
    f32x4 y[4];
    if (w2 == 0) {
#pragma unroll
      for (int i = 0; i < 4; ++i) { *(f32x4*)(xw + i * 1024) = acc[4 + i]; y[i] = acc[i]; }
    } else {
#pragma unroll
      for (int i = 0; i < 4; ++i) { *(f32x4*)(xw + i * 1024) = acc[i]; y[i] = acc[4 + i]; }
    }
    __syncthreads();
    const float Dk = bf2f(r_dsk);
#pragma unroll
    for (int i = 0; i < 4; ++i) {
      const f32x4 o = *(const f32x4*)(xr + i * 1024);
      const int l0 = 256 * (4 * w2 + i) + 16 * (lane & 15) + 4 * (lane >> 4);
      const u32x2 uu = *(const u32x2*)(smem + ch * U_CH_B + 3 * U_COPY_B + 2 * (16 + l0));
      const unsigned y01 = pk_bf16(sadd(y[i][0], o[0]), sadd(y[i][1], o[1])), y23 = pk_bf16(sadd(y[i][2], o[2]), sadd(y[i][3], o[3]));
      const unsigned s01 = pk_bf16(smul(bf_lo(uu[0]), Dk), smul(bf_hi(uu[0]), Dk)), s23 = pk_bf16(smul(bf_lo(uu[1]), Dk), smul(bf_hi(uu[1]), Dk));
      { const u32x2 av = u32x2{pk_bf16(sadd(bf_lo(y01), bf_lo(s01)), sadd(bf_hi(y01), bf_hi(s01))),
                                                  pk_bf16(sadd(bf_lo(y23), bf_lo(s23)), sadd(bf_hi(y23), bf_hi(s23)))};
        asm volatile("global_store_dwordx2 %0, %1, off sc1\n\ts_nop 1" ::"v"(aT + (size_t)d * SEQ + l0), "v"(av) : "memory"); }
    }
  }
}

__device__ __forceinline__ float gelu_tanh(float x) {
  const float z = 0.7978845608028654f * (x + 0.044715f * x * x * x);
  const float e = __builtin_amdgcn_exp2f(-2.8853900817779268f * z);
  return x * __builtin_amdgcn_rcpf(1.0f + e);
}

constexpr int G1_WSLOT_B = 12288;
constexpr int G1_X_OFF = 3 * G1_WSLOT_B;
constexpr int G1_LDS = G1_X_OFF + 32768;

constexpr int g1_pos(bool isx, int id) {
  if (!isx && id == 3) return 20;
  if (!isx && id == 4) return 23;
  if (isx && id == 2) return 27;
  if (!isx && id == 5) return 30;
  if (isx && id == 3) return 34;
  int n = 34;
  for (int t = 2; t < 32; ++t) {
    if ((t & 1) == 0 && t / 2 + 5 < 16) { n += 3; if (!isx && t / 2 + 5 == id) return n; }
    if ((t & 3) == 1 && t / 4 + 3 < 8) { n += 4; if (isx && t / 4 + 3 == id) return n; }
  }
  return -1;
}
constexpr int g1_issued_before(int step) {
  int n = 34;
  for (int t = 2; t < step; ++t) {
    if ((t & 1) == 0 && t / 2 + 5 < 16) n += 3;
    if ((t & 3) == 1 && t / 4 + 3 < 8) n += 4;
  }
  return n;
}

#define GLOAD128(dst, ptr) asm volatile("global_load_dwordx4 %0, %1, off" : "=v"(dst) : "v"(ptr) : "memory")
#define GLOAD128S(dst, voff, sbase) asm volatile("global_load_dwordx4 %0, %1, %2" : "=v"(dst) : "v"(voff), "s"(sbase) : "memory")
#define GLOAD128SA(dst, voff, sbase) asm volatile("global_load_dwordx4 %0, %1, %2" : "=a"(dst) : "v"(voff), "s"(sbase) : "memory")
#define GLOAD128A(dst, ptr) asm volatile("global_load_dwordx4 %0, %1, off" : "=a"(dst) : "v"(ptr) : "memory")
#define DS_W128(addr, val, off) asm volatile("ds_write_b128 %0, %1 offset:%2" ::"v"(addr), "v"(val), "i"(off) : "memory")
#define MFMA32(acc, a, b) asm volatile("v_mfma_f32_32x32x16_bf16 %0, %1, %2, %0" : "+v"(acc) : "v"(a), "v"(b))

struct G1Ctx {
  const char* wbase;
  const char* xbase;
  unsigned wo[3], xo;
  unsigned woff, woff_o, ww[3], xw, xw1, xrd2[2][4];
  unsigned junk[4];
};
struct G1Frag {
  s16x4 w[3][2];
  bf16x8 x[2];
};

#define G1_RD_W(f, t, OFF)                                                        \
  do {                                                                            \
    if ((t) & 1) { DS_TR(f.w[t][0], c.woff_o, (OFF) + (t) * 64); DS_TR(f.w[t][1], c.woff_o, (OFF) + (t) * 64 + 4 * 384); } \
    else { DS_TR(f.w[t][0], c.woff, (OFF) + (t) * 64); DS_TR(f.w[t][1], c.woff, (OFF) + (t) * 64 + 4 * 384); }            \
  } while (0)
#define G1_RD_X(f, mi, N) DS_B128(f.x[mi], c.xrd2[mi][(N) & 3], (((N) >> 2) & 1) * 16384)
template <int N>
__device__ __forceinline__ void g1_read(G1Frag& f, const G1Ctx& c) {
  constexpr int OFF = ((N >> 1) % 3) * G1_WSLOT_B + (N & 1) * 16 * 384;
  G1_RD_X(f, 0, N); G1_RD_X(f, 1, N);
  G1_RD_W(f, 0, OFF); G1_RD_W(f, 1, OFF); G1_RD_W(f, 2, OFF);
}
#define G1_WAIT_FRAG(f, STR)                                                                                       \
  asm volatile(STR : "+v"(f.w[0][0]), "+v"(f.w[0][1]), "+v"(f.w[1][0]), "+v"(f.w[1][1]), "+v"(f.w[2][0]),        \
               "+v"(f.w[2][1]), "+v"(f.x[0]), "+v"(f.x[1])::"memory")

template <int N>
struct G1Step {
  static __device__ __forceinline__ void run(f32x16 (&acc)[6], u32x4 (&xr)[2][4], u32x4 (&wr)[3][3], G1Frag& cur,
                                             G1Frag& nxt, G1Ctx& c) {
    constexpr int ISS = g1_issued_before(N);
    constexpr int S = N >> 1;
    constexpr bool RD = N + 1 < 32;
    constexpr int OFF = (((N + 1) >> 1) % 3) * G1_WSLOT_B + ((N + 1) & 1) * 16 * 384;
    if constexpr ((N & 1) == 0 && N > 0) G1_WAIT_FRAG(cur, "s_waitcnt lgkmcnt(0)\n\ts_barrier");
    else G1_WAIT_FRAG(cur, "s_waitcnt lgkmcnt(0)");
    bf16x8 b[3];
#pragma unroll
    for (int t = 0; t < 3; ++t) b[t] = cat4(cur.w[t][0], cur.w[t][1]);
    MFMA32(acc[0], b[0], cur.x[0]);
    if constexpr (RD) { G1_RD_X(nxt, 0, N + 1); G1_RD_W(nxt, 0, OFF); }
    MFMA32(acc[3], b[0], cur.x[1]);
    if constexpr (RD) { G1_RD_X(nxt, 1, N + 1); G1_RD_W(nxt, 1, OFF); }
    MFMA32(acc[1], b[1], cur.x[0]);
    if constexpr (RD) { G1_RD_W(nxt, 2, OFF); }
    MFMA32(acc[4], b[1], cur.x[1]);
    if constexpr ((N & 1) == 0 && N >= 2) {
      if constexpr (S + 2 < 16) {
        constexpr int R = (S + 2) % 3;
        constexpr int NW = ISS - g1_pos(false, S + 2);
        asm volatile("s_waitcnt vmcnt(%3)" : "+v"(wr[R][0]), "+v"(wr[R][1]), "+v"(wr[R][2]) : "i"(NW) : "memory");
#pragma unroll
        for (int i = 0; i < 3; ++i) DS_W128(c.ww[i], wr[R][i], R * G1_WSLOT_B);
      }
    }
    if constexpr ((N & 3) == 1 && N >= 5) {
      constexpr int B = N / 4 + 1;
      if constexpr (B < 8) {
        constexpr int NW = ISS - g1_pos(true, B);
        asm volatile("s_waitcnt vmcnt(%4)" : "+v"(xr[B & 1][0]), "+v"(xr[B & 1][1]), "+v"(xr[B & 1][2]), "+v"(xr[B & 1][3]) : "i"(NW) : "memory");
#pragma unroll
        for (int i = 0; i < 4; ++i) { if (i & 1) DS_W128(c.xw1, xr[B & 1][i], (B & 1) * 16384 + i * 1024); else DS_W128(c.xw, xr[B & 1][i], (B & 1) * 16384 + i * 1024); }
      }
    }
    MFMA32(acc[2], b[2], cur.x[0]);
    if constexpr ((N & 1) == 0 && N >= 2 && S + 5 < 16) {
#pragma unroll
      for (int i = 0; i < 3; ++i) GLOAD128S(wr[(S + 5) % 3][i], c.wo[i], c.wbase + (size_t)(S + 5) * 32 * FF * 2);
    }
    if constexpr ((N & 3) == 1 && N >= 5 && N / 4 + 3 < 8) {
      constexpr int B = N / 4 + 1;
#pragma unroll
      for (int i = 0; i < 4; ++i) GLOAD128S(xr[B & 1][i], c.xo, c.xbase + (size_t)(8 * i) * DM * 2 + (B + 2) * 128);
    }
    MFMA32(acc[5], b[2], cur.x[1]);
    if constexpr (N + 1 < 32) G1Step<N + 1>::run(acc, xr, wr, nxt, cur, c);
  }
};

__device__ __forceinline__ void gemm1_tile(int t, const u16* __restrict__ x, const u16* __restrict__ wg,
                                           const u16* __restrict__ wu, u16* __restrict__ hf, char* smem DBG_ARG) {
  const int tid = threadIdx.x, lane = tid & 63;
  const int wave = __builtin_amdgcn_readfirstlane(tid >> 6);
  const int fb = t >> 4, mb = t & 15;
  const int f0 = 96 * fb;
  const int wm = wave & 1, isup = wave >> 1;
  G1Ctx c;
  const unsigned sbase = lds_addr(smem);
  c.wbase = (const char*)(isup ? wu : wg);
#pragma unroll
  for (int i = 0; i < 3; ++i) {
    const int id = 192 * (wave & 1) + 64 * i + lane;
    const int row = id / 12, cc = id % 12;
    int col = f0 + 8 * cc;
    if (col > FF - 8) col = FF - 8;
    c.wo[i] = (unsigned)(row * FF + col) * 2u;
    c.ww[i] = sbase + row * 384 + ((cc + 12 * isup) ^ (((row >> 1) & 1) << 2)) * 16;
  }
  c.xbase = (const char*)(x + (size_t)(128 * mb + 32 * wave) * DM);
  c.xo = (unsigned)((lane >> 3) * DM + (lane & 7) * 8) * 2u;
  const unsigned xl = sbase + G1_X_OFF;
  c.xw = xl + (32 * wave + (lane >> 3)) * 128 + (((lane & 7) ^ (lane >> 4)) * 16);
  c.xw1 = xl + (32 * wave + (lane >> 3)) * 128 + (((lane & 7) ^ ((lane >> 4) + 4)) * 16);
  const int r = lane & 31, h = lane >> 5;
#pragma unroll
  for (int i = 0; i < 4; ++i) {
    c.xrd2[0][i] = xl + (64 * wm + 32 * isup + r) * 128 + (((2 * i + h) ^ ((r >> 1) & 7)) * 16);
    c.xrd2[1][i] = xl + (64 * wm + 32 * (1 - isup) + r) * 128 + (((2 * i + h) ^ ((r >> 1) & 7)) * 16);
  }
  const int blk = (lane >> 4) & 1, q = (lane & 15) >> 2, p = lane & 3;
  const int sw = (q >> 1) & 1;
  {
    const unsigned e = sbase + (8 * h + q) * 384 + (2 * blk + (p >> 1) + 4 * sw) * 16 + 8 * (p & 1);
    const unsigned o = sbase + (8 * h + q) * 384 + (2 * blk + (p >> 1) - 4 * sw) * 16 + 8 * (p & 1);
    c.woff = isup ? o + 192 : e;
    c.woff_o = isup ? e + 192 : o;
  }

  u32x4 xr[2][4], wr[3][3];
  G1Frag fa, fb2;
  {
    u32x4 tx[2][4], tw[3][3];
#define G1_ISSUE_X(DST, B) _Pragma("unroll") for (int i = 0; i < 4; ++i) GLOAD128S(DST[i], c.xo, c.xbase + (size_t)(8 * i) * DM * 2 + (B) * 128)
#define G1_ISSUE_W(DST, S) _Pragma("unroll") for (int i = 0; i < 3; ++i) GLOAD128S(DST[i], c.wo[i], c.wbase + (size_t)(S) * 32 * FF * 2)
    G1_ISSUE_X(tx[0], 0); G1_ISSUE_W(tw[0], 0); G1_ISSUE_W(tw[1], 1); G1_ISSUE_X(tx[1], 1); G1_ISSUE_W(tw[2], 2);
    G1_ISSUE_W(wr[0], 3); G1_ISSUE_W(wr[1], 4); G1_ISSUE_X(xr[0], 2); G1_ISSUE_W(wr[2], 5); G1_ISSUE_X(xr[1], 3);
    asm volatile("s_waitcnt vmcnt(24)"
                 : "+v"(tw[0][0]), "+v"(tw[0][1]), "+v"(tw[0][2]), "+v"(tw[1][0]), "+v"(tw[1][1]), "+v"(tw[1][2]),
                   "+v"(tx[0][0]), "+v"(tx[0][1]), "+v"(tx[0][2]), "+v"(tx[0][3])::"memory");
#pragma unroll
    for (int i = 0; i < 3; ++i) { DS_W128(c.ww[i], tw[0][i], 0); DS_W128(c.ww[i], tw[1][i], G1_WSLOT_B); }
#pragma unroll
    for (int i = 0; i < 4; ++i) { if (i & 1) DS_W128(c.xw1, tx[0][i], i * 1024); else DS_W128(c.xw, tx[0][i], i * 1024); }
    asm volatile("s_waitcnt vmcnt(17)"
                 : "+v"(tw[2][0]), "+v"(tw[2][1]), "+v"(tw[2][2]), "+v"(tx[1][0]), "+v"(tx[1][1]), "+v"(tx[1][2]), "+v"(tx[1][3])::"memory");
#pragma unroll
    for (int i = 0; i < 3; ++i) DS_W128(c.ww[i], tw[2][i], 2 * G1_WSLOT_B);
#pragma unroll
    for (int i = 0; i < 4; ++i) { if (i & 1) DS_W128(c.xw1, tx[1][i], 16384 + i * 1024); else DS_W128(c.xw, tx[1][i], 16384 + i * 1024); }
  }
  f32x16 acc[6];
#pragma unroll
  for (int i = 0; i < 6; ++i) acc[i] = f32x16{};
  asm volatile("s_nop 4\n\ts_waitcnt lgkmcnt(0)\n\ts_barrier" : "+v"(acc[0]), "+v"(acc[1]), "+v"(acc[2]), "+v"(acc[3]), "+v"(acc[4]), "+v"(acc[5])::"memory");
  g1_read<0>(fa, c);
  G1Step<0>::run(acc, xr, wr, fa, fb2, c);
  asm volatile("s_nop 15\n\ts_nop 15" : "+v"(acc[0]), "+v"(acc[1]), "+v"(acc[2]), "+v"(acc[3]), "+v"(acc[4]), "+v"(acc[5]));

  unsigned keep[3][8];
  {
    unsigned give[3][8];
#pragma unroll
    for (int tt = 0; tt < 3; ++tt)
#pragma unroll
      for (int e = 0; e < 8; ++e) give[tt][e] = pk_bf16(acc[3 + tt][2 * e], acc[3 + tt][2 * e + 1]);
    __syncthreads();
    char* xo = smem + wave * 6144 + lane * 16;
#pragma unroll
    for (int tt = 0; tt < 3; ++tt) {
      *(u32x4*)(xo + (2 * tt) * 1024) = u32x4{give[tt][0], give[tt][1], give[tt][2], give[tt][3]};
      *(u32x4*)(xo + (2 * tt + 1) * 1024) = u32x4{give[tt][4], give[tt][5], give[tt][6], give[tt][7]};
    }
  }
#pragma unroll
  for (int tt = 0; tt < 3; ++tt)
#pragma unroll
    for (int e = 0; e < 8; ++e) keep[tt][e] = pk_bf16(acc[tt][2 * e], acc[tt][2 * e + 1]);
  __syncthreads();
  {
    const char* xi = smem + (wave ^ 2) * 6144 + lane * 16;
    const int mt = 4 * mb + 2 * wm + isup;
    u16* hp = hf + ((size_t)(mt * NKS + 6 * fb) * 64 + lane) * 8;
    auto finish = [&](auto ISUP) {
#pragma unroll
      for (int tt = 0; tt < 3; ++tt) {
        const int ft = 3 * fb + tt;
        if (ft < FF / 32) {
          const u32x4 o0 = *(const u32x4*)(xi + (2 * tt) * 1024);
          const u32x4 o1 = *(const u32x4*)(xi + (2 * tt + 1) * 1024);
          unsigned gp[8], up[8];
#pragma unroll
          for (int e = 0; e < 8; ++e) {
            const unsigned ot = e < 4 ? o0[e & 3] : o1[e & 3];
            gp[e] = decltype(ISUP)::value ? ot : keep[tt][e];
            up[e] = decltype(ISUP)::value ? keep[tt][e] : ot;
          }
          f32x2 xg[8], ar[8];
#pragma unroll
          for (int e = 0; e < 8; ++e) xg[e] = f32x2{bf_lo(gp[e]), bf_hi(gp[e])};
#pragma unroll
          for (int e = 0; e < 8; ++e) ar[e] = xg[e] * xg[e];
#pragma unroll
          for (int e = 0; e < 8; ++e) ar[e] = __builtin_elementwise_fma(ar[e], f32x2{-0.10294323958f, -0.10294323958f}, f32x2{-2.30220819814f, -2.30220819814f});
#pragma unroll
          for (int e = 0; e < 8; ++e) ar[e] = ar[e] * xg[e];
#pragma unroll
          for (int e = 0; e < 8; ++e) ar[e] = f32x2{__builtin_amdgcn_exp2f(ar[e][0]), __builtin_amdgcn_exp2f(ar[e][1])};
#pragma unroll
          for (int e = 0; e < 8; ++e) ar[e] = ar[e] + f32x2{1.0f, 1.0f};
#pragma unroll
          for (int e = 0; e < 8; ++e) ar[e] = f32x2{__builtin_amdgcn_rcpf(ar[e][0]), __builtin_amdgcn_rcpf(ar[e][1])};
#pragma unroll
          for (int e = 0; e < 8; ++e) ar[e] = ar[e] * xg[e];
          unsigned o[8];
#pragma unroll
          for (int e = 0; e < 8; ++e) o[e] = pk_bf16(ar[e][0], ar[e][1]);
#pragma unroll
          for (int e = 0; e < 8; ++e) ar[e] = f32x2{bf_lo(o[e]), bf_hi(o[e])} * f32x2{bf_lo(up[e]), bf_hi(up[e])};
#pragma unroll
          for (int e = 0; e < 8; ++e) o[e] = pk_bf16(ar[e][0], ar[e][1]);
          { const u32x4 v0 = u32x4{o[0], o[1], o[2], o[3]}, v1 = u32x4{o[4], o[5], o[6], o[7]};
            asm volatile("global_store_dwordx4 %0, %1, off sc1\n\ts_nop 1" ::"v"(hp + (2 * tt) * 512), "v"(v0) : "memory");
            asm volatile("global_store_dwordx4 %0, %1, off sc1\n\ts_nop 1" ::"v"(hp + (2 * tt + 1) * 512), "v"(v1) : "memory"); }
        } else if (ft < NKS / 2) {
          *(u32x4*)(hp + (2 * tt) * 512) = u32x4{0, 0, 0, 0};
          *(u32x4*)(hp + (2 * tt + 1) * 512) = u32x4{0, 0, 0, 0};
        }
      }
    };
    if (isup) finish(std::integral_constant<bool, true>{});
    else finish(std::integral_constant<bool, false>{});
  }
}

__device__ __forceinline__ void gate_unit(int b, const u16* __restrict__ x, const u16* __restrict__ gw,
                                          float* __restrict__ gout) {
  const int tid = threadIdx.x, lane = tid & 63, wave = tid >> 6;
  const int j = lane & 7, rr = lane >> 3;
  u32x4 gv[8];
#pragma unroll
  for (int i = 0; i < 8; ++i) gv[i] = *(const u32x4*)(gw + (j + 8 * i) * 8);
  const int row0 = b * 128 + wave * 32 + rr;
  u32x4 xv[4][8];
#pragma unroll
  for (int ps = 0; ps < 4; ++ps)
#pragma unroll
    for (int i = 0; i < 8; ++i) xv[ps][i] = *(const u32x4*)(x + (size_t)(row0 + 8 * ps) * DM + (j + 8 * i) * 8);
#pragma unroll
  for (int ps = 0; ps < 4; ++ps) {
    float s0 = 0.f, s1 = 0.f;
#pragma unroll
    for (int i = 0; i < 8; ++i)
#pragma unroll
      for (int e = 0; e < 4; ++e) {
        const unsigned pr = pk_bf16(smul(bf_lo(xv[ps][i][e]), bf_lo(gv[i][e])), smul(bf_hi(xv[ps][i][e]), bf_hi(gv[i][e])));
        s0 = sadd(s0, bf_lo(pr));
        s1 = sadd(s1, bf_hi(pr));
      }
    float s = s0 + s1;
    s += __shfl_xor(s, 1);
    s += __shfl_xor(s, 2);
    s += __shfl_xor(s, 4);
    if (j == 0) {
      const float sb = rbf(s);
      gout[row0 + 8 * ps] = rbf(1.0f / (1.0f + expf(-sb)));
    }
  }
}

__global__ __launch_bounds__(256, 2) void k1_kernel(const u16* __restrict__ u, const u16* __restrict__ logAr,
                                                   const u16* __restrict__ Aim, const u16* __restrict__ Cre,
                                                   const u16* __restrict__ Cim, const u16* __restrict__ logdt,
                                                   const u16* __restrict__ Dskip, const u16* __restrict__ wg,
                                                   const u16* __restrict__ wu, const u16* __restrict__ gw,
                                                   const u16* __restrict__ wd, u16* __restrict__ hbuf, u16* __restrict__ aT,
                                                   float* __restrict__ gout DBG_ARG
                                                   ) {
  __shared__ __attribute__((aligned(4096))) char smem[G1_LDS > SSM_LDS ? G1_LDS : SSM_LDS];
  const int bid = blockIdx.x;
  if (bid < NSSM) {
    ssm_unit(bid, u, logAr, Aim, Cre, Cim, logdt, Dskip, aT, smem DBG_PASS);
    unsigned acc = 0;
    for (int i = (bid >> 3) * 256 + threadIdx.x; i < 11008; i += 32 * 256) acc += ((const unsigned*)wd)[i * 32];
    asm volatile("" ::"v"(acc));
  } else {
    const int xcd = bid & 7, idx = (bid - NSSM) >> 3;
    if (idx < 30) {
      gemm1_tile(30 * xcd + idx, u, wg, wu, hbuf, smem DBG_PASS);
    }
    else gate_unit(2 * xcd + idx - 30, u, gw, gout);
  }
}

constexpr int G2_SLOT_B = 4096;
constexpr int G2_WAVE_B = 2 * G2_SLOT_B;
constexpr int G2_PITCH = 272;
constexpr int G2_A_OFF = 4 * 64 * G2_PITCH;
constexpr int K2_LDS = G2_A_OFF + 8192;
#define G2D 3
constexpr int G2_DEPTH = G2D;

struct G2Ctx {
  const u16* hsrc;
  const u16* wd;
  int wrow0, wcol;
  unsigned wb32, woff, ww;
};

template <int ST>
struct G2Step {
  static __device__ __forceinline__ void issue(bf16x8 (&hfr)[11][4], u32x4 (&wr)[11][4], const G2Ctx& c) {
#pragma unroll
    for (int i = 0; i < 4; ++i) {
      int kr = c.wrow0 + ST * 32 + 8 * i;
      kr = kr < FF ? kr : FF - 1;
      GLOAD128(wr[ST][i], c.wd + (size_t)kr * DM + c.wcol);
    }
#pragma unroll
    for (int mi = 0; mi < 2; ++mi)
#pragma unroll
      for (int i = 0; i < 2; ++i)
        GLOAD128A(hfr[ST][2 * mi + i], c.hsrc + (size_t)(mi * NKS + 2 * ST + i) * 512);

  }
  static __device__ __forceinline__ void run(f32x16 (&acc)[2][2], bf16x8 (&hfr)[11][4], u32x4 (&wr)[11][4], const G2Ctx& c) {
    constexpr int LAST = (ST + G2_DEPTH - 1 < 10) ? ST + G2_DEPTH - 1 : 10;
    constexpr int NW = 8 * (LAST - ST);
    asm volatile("s_waitcnt vmcnt(%4)" : "+v"(wr[ST][0]), "+v"(wr[ST][1]), "+v"(wr[ST][2]), "+v"(wr[ST][3]) : "i"(NW + 4) : "memory");
#pragma unroll
    for (int i = 0; i < 4; ++i) DS_W128(c.ww, wr[ST][i], (ST & 1) * G2_SLOT_B + i * 1024);
    asm volatile("s_waitcnt lgkmcnt(0)" ::: "memory");
    const unsigned sb = c.wb32 + c.woff;
    s16x4 w[2][2][2];
#pragma unroll
    for (int i = 0; i < 2; ++i)
#pragma unroll
      for (int t = 0; t < 2; ++t) {
        DS_TR(w[i][t][0], sb ^ (t * 64), (ST & 1) * G2_SLOT_B + i * 2048);
        DS_TR(w[i][t][1], sb ^ (t * 64), (ST & 1) * G2_SLOT_B + i * 2048 + 1024);
      }
    asm volatile("s_waitcnt vmcnt(%4)" : "+a"(hfr[ST][0]), "+a"(hfr[ST][1]), "+a"(hfr[ST][2]), "+a"(hfr[ST][3]) : "i"(NW) : "memory");
    if constexpr (ST + G2_DEPTH <= 10) G2Step<ST + G2_DEPTH>::issue(hfr, wr, c);
    asm volatile("s_waitcnt lgkmcnt(0)"
                 : "+v"(w[0][0][0]), "+v"(w[0][0][1]), "+v"(w[0][1][0]), "+v"(w[0][1][1]), "+v"(w[1][0][0]), "+v"(w[1][0][1]),
                   "+v"(w[1][1][0]), "+v"(w[1][1][1])::"memory");
#pragma unroll
    for (int i = 0; i < 2; ++i)
#pragma unroll
      for (int t = 0; t < 2; ++t)
#pragma unroll
        for (int mi = 0; mi < 2; ++mi)
          acc[t][mi] = __builtin_amdgcn_mfma_f32_32x32x16_bf16(cat4(w[i][t][0], w[i][t][1]), hfr[ST][2 * mi + i], acc[t][mi], 0, 0, 0);
    if constexpr (ST + 1 <= 10) G2Step<ST + 1>::run(acc, hfr, wr, c);
  }
};

__global__ __launch_bounds__(256, 1) void k2_kernel(const u16* __restrict__ hf, const u16* __restrict__ wd,
                                                   const u16* __restrict__ aT, const float* __restrict__ gin,
                                                   u16* __restrict__ out DBG2_ARG) {
  __shared__ __attribute__((aligned(4096))) char smem[K2_LDS];
  const int tid = threadIdx.x, lane = tid & 63;
  STAMP2(0);
  const int wave = __builtin_amdgcn_readfirstlane(tid >> 6);
  const int bid = blockIdx.x;
  const int xcd = bid & 7, idx = bid >> 3;
  const int m0 = 64 * (4 * xcd + (idx >> 3)), n0 = 64 * (idx & 7);

#pragma unroll
  for (int i = 0; i < 2; ++i) {
    const int piece = 2 * wave + i;
    const int drow = 8 * piece + (lane >> 3);
    glds16(aT + (size_t)(n0 + drow) * SEQ + m0 + (lane & 7) * 8, smem + G2_A_OFF + piece * 1024 + lane * 16);
  }

  G2Ctx c;
  c.wb32 = lds_addr(smem) + wave * G2_WAVE_B;
  c.hsrc = hf + ((size_t)((m0 >> 5) * NKS + 22 * wave) * 64 + lane) * 8;
  c.wd = wd;
  c.wcol = n0 + 8 * (lane & 7);
  c.wrow0 = 352 * wave + (lane >> 3);
  c.ww = c.wb32 + (lane >> 3) * 128 + (((lane & 7) ^ (((lane >> 4) & 1) << 2)) * 16);

  const int r = lane & 31, h = lane >> 5;
  const int blk = (lane >> 4) & 1, q = (lane & 15) >> 2, p = lane & 3;
  c.woff = (4 * h + q) * 128 + (((2 * blk + (p >> 1)) ^ (((q >> 1) & 1) << 2)) * 16) + 8 * (p & 1);

  f32x16 acc[2][2];
#pragma unroll
  for (int i = 0; i < 2; ++i)
#pragma unroll
    for (int j = 0; j < 2; ++j) acc[i][j] = f32x16{};
  bf16x8 hfr[11][4];
  u32x4 wr[11][4];

  G2Step<0>::issue(hfr, wr, c);
  G2Step<1>::issue(hfr, wr, c);
  if constexpr (G2_DEPTH > 2) G2Step<2>::issue(hfr, wr, c);
  if constexpr (G2_DEPTH > 3) G2Step<3>::issue(hfr, wr, c);
  if constexpr (G2_DEPTH > 4) G2Step<4>::issue(hfr, wr, c);
  if constexpr (G2_DEPTH > 5) G2Step<5>::issue(hfr, wr, c);
  if constexpr (G2_DEPTH > 6) G2Step<6>::issue(hfr, wr, c);
  STAMP2(1);
  STAMP2(2);
  G2Step<0>::run(acc, hfr, wr, c);
  STAMP2(3);
  __syncthreads();
  {
    char* pw = smem + wave * (64 * G2_PITCH);
#pragma unroll
    for (int c = 0; c < 2; ++c)
#pragma unroll
      for (int mi = 0; mi < 2; ++mi)
#pragma unroll
        for (int gq = 0; gq < 4; ++gq) {
          const f32x4 v = {acc[c][mi][4 * gq], acc[c][mi][4 * gq + 1], acc[c][mi][4 * gq + 2], acc[c][mi][4 * gq + 3]};
          *(f32x4*)(pw + (32 * mi + r) * G2_PITCH + (32 * c + 8 * gq + 4 * h) * 4) = v;
        }
  }
  __syncthreads();
  STAMP2(4);
  {
    const int m = tid >> 2, nq = (tid & 3) * 16;
    const float g = gin[m0 + m];
    const float omg = rbf(1.0f - g);
    float b[16];
#pragma unroll
    for (int j = 0; j < 4; ++j) {
      f32x4 s = *(const f32x4*)(smem + m * G2_PITCH + (nq + 4 * j) * 4);
#pragma unroll
      for (int w = 1; w < 4; ++w) s += *(const f32x4*)(smem + w * (64 * G2_PITCH) + m * G2_PITCH + (nq + 4 * j) * 4);
#pragma unroll
      for (int e = 0; e < 4; ++e) b[4 * j + e] = s[e];
    }
    const u16* aimg = (const u16*)(smem + G2_A_OFF);
    unsigned o[8];
#pragma unroll
    for (int j = 0; j < 8; ++j) {
      const float a0 = bf2f(aimg[(nq + 2 * j) * 64 + m]);
      const float a1 = bf2f(aimg[(nq + 2 * j + 1) * 64 + m]);
      const float o0 = rbf(g * a0) + rbf(omg * rbf(b[2 * j]));
      const float o1 = rbf(g * a1) + rbf(omg * rbf(b[2 * j + 1]));
      o[j] = pk_bf16(o0, o1);
    }
    u16* op = out + (size_t)(m0 + m) * DM + n0 + nq;
    *(u32x4*)op = u32x4{o[0], o[1], o[2], o[3]};
    *(u32x4*)(op + 8) = u32x4{o[4], o[5], o[6], o[7]};
  }
  STAMP2(5);
}

extern "C" void kernel_launch(void* const* d_in, const int* in_sizes, int n_in, void* d_out, int out_size,
                              void* d_ws, size_t ws_size, hipStream_t stream) {
  const u16* u = (const u16*)d_in[0];
  const u16* logAr = (const u16*)d_in[1];
  const u16* Aim = (const u16*)d_in[2];
  const u16* Cre = (const u16*)d_in[3];
  const u16* Cim = (const u16*)d_in[4];
  const u16* logdt = (const u16*)d_in[5];
  const u16* Dskip = (const u16*)d_in[6];
  const u16* wg = (const u16*)d_in[8];
  const u16* wu = (const u16*)d_in[9];
  const u16* wd = (const u16*)d_in[10];
  const u16* gw = (const u16*)d_in[11];
  char* ws = (char*)d_ws;
  u16* hbuf = (u16*)ws;
  u16* aT = (u16*)(ws + (size_t)SEQ * FFP * 2);
  float* gbuf = (float*)(ws + (size_t)SEQ * FFP * 2 + (size_t)DM * SEQ * 2);
  k1_kernel<<<512, 256, 0, stream>>>(u, logAr, Aim, Cre, Cim, logdt, Dskip, wg, wu, gw, wd, hbuf, aT, gbuf
      );
  k2_kernel<<<256, 256, 0, stream>>>(hbuf, wd, aT, gbuf, (u16*)d_out
      );
}
```

```cpp
#include <hip/hip_runtime.h>
#include <hip/hip_bf16.h>
#include <stdint.h>
#include <type_traits>

typedef unsigned short u16;
typedef __attribute__((ext_vector_type(8))) short bf16x8;
typedef __attribute__((ext_vector_type(4))) short s16x4;
typedef __attribute__((ext_vector_type(4))) float f32x4;
typedef __attribute__((ext_vector_type(16))) float f32x16;
typedef __attribute__((ext_vector_type(4))) unsigned u32x4;
typedef __attribute__((ext_vector_type(2))) unsigned u32x2;
typedef __attribute__((ext_vector_type(2))) __bf16 bf16x2_t;
typedef __attribute__((ext_vector_type(2))) float f32x2;
typedef __attribute__((address_space(3))) s16x4* lds_s16x4_ptr;

constexpr int SEQ = 2048, DM = 512, NST = 128, FF = 1376, FFP = 1408;
constexpr int NSSM = 256;
constexpr int NKS = 88;

__device__ __forceinline__ unsigned pk_bf16(float lo, float hi) {
  f32x2 v = {lo, hi};
  bf16x2_t b = __builtin_convertvector(v, bf16x2_t);
  return __builtin_bit_cast(unsigned, b);
}
__device__ __forceinline__ float bf_lo(unsigned w) { return __uint_as_float(w << 16); }
__device__ __forceinline__ float bf_hi(unsigned w) { return __uint_as_float(w & 0xffff0000u); }
__device__ __forceinline__ float rbf(float x) { return bf_lo(pk_bf16(x, 0.f)); }
__device__ __forceinline__ float bf2f(u16 v) { return __uint_as_float(((unsigned)v) << 16); }

__device__ __forceinline__ float smul(float a, float b) { float r; asm("v_mul_f32 %0, %1, %2" : "=v"(r) : "v"(a), "v"(b)); return r; }
__device__ __forceinline__ float sadd(float a, float b) { float r; asm("v_add_f32 %0, %1, %2" : "=v"(r) : "v"(a), "v"(b)); return r; }
__device__ __forceinline__ float ssub(float a, float b) { float r; asm("v_sub_f32 %0, %1, %2" : "=v"(r) : "v"(a), "v"(b)); return r; }
__device__ __forceinline__ float sfma(float a, float b, float c) { float r; asm("v_fma_f32 %0, %1, %2, %3" : "=v"(r) : "v"(a), "v"(b), "v"(c)); return r; }
__device__ __forceinline__ float sfms(float a, float b, float c) { float r; asm("v_fma_f32 %0, -%1, %2, %3" : "=v"(r) : "v"(a), "v"(b), "v"(c)); return r; }
__device__ __forceinline__ float sfnn(float a, float b, float c) { float r; asm("v_fma_f32 %0, -%1, %2, -%3" : "=v"(r) : "v"(a), "v"(b), "v"(c)); return r; }
#define TRANS_FENCE8(a, o) asm volatile("s_nop 1" : "+v"(a[o]), "+v"(a[o + 1]), "+v"(a[o + 2]), "+v"(a[o + 3]), "+v"(a[o + 4]), "+v"(a[o + 5]), "+v"(a[o + 6]), "+v"(a[o + 7]))
__device__ __forceinline__ bf16x8 mk8(unsigned a, unsigned b, unsigned c, unsigned d) {
  u32x4 v = {a, b, c, d};
  return __builtin_bit_cast(bf16x8, v);
}
__device__ __forceinline__ bf16x8 cat4(s16x4 a, s16x4 b) {
  return __builtin_shufflevector(a, b, 0, 1, 2, 3, 4, 5, 6, 7);
}
__device__ __forceinline__ s16x4 tr_read(const char* smem_ptr) {
  return __builtin_amdgcn_ds_read_tr16_b64_v4i16((lds_s16x4_ptr)(smem_ptr));
}
__device__ __forceinline__ unsigned lds_addr(const void* p) {
  return (unsigned)(size_t)(const __attribute__((address_space(3))) char*)p;
}
__device__ __forceinline__ void glds16(const void* g, char* l) {
  __builtin_amdgcn_global_load_lds((const unsigned*)g, (unsigned*)l, 16, 0, 0);
}

#define STAMP2(i)
#define DBG2_ARG
#define DBG2_PASS
#define STAMP(i)
#define DBG_ARG
#define DBG_PASS
constexpr int U_COPY_B = 4160;
constexpr int U_CH_B = 4 * U_COPY_B;
constexpr int U_REGION_B = 2 * U_CH_B;
constexpr int KR_PART_B = 4576;
constexpr int KR_OFF = U_REGION_B;
constexpr int KQ_B = 8208;
constexpr int KX_OFF = 2 * KQ_B;
constexpr int SSM_LDS = KR_OFF + 4 * KR_PART_B;
static_assert(KX_OFF + 4 * 1536 <= U_REGION_B, "scratch must fit the U region");

#define DS_TR(dst, addr, off) asm volatile("ds_read_b64_tr_b16 %0, %1 offset:%2" : "=v"(dst) : "v"(addr), "i"(off) : "memory")
#define DS_B128(dst, addr, off) asm volatile("ds_read_b128 %0, %1 offset:%2" : "=v"(dst) : "v"(addr), "i"(off) : "memory")
#define DS_B64(dst, addr, off) asm volatile("ds_read_b64 %0, %1 offset:%2" : "=v"(dst) : "v"(addr), "i"(off) : "memory")
#define MFMA16(acc, a, b) asm volatile("v_mfma_f32_16x16x32_bf16 %0, %1, %2, %0" : "+v"(acc) : "v"(a), "v"(b))

struct ConvFrag {
  s16x4 a[8][2];
  bf16x8 bh[8], bl[8];
};
template <int RI, int K>
__device__ __forceinline__ void conv_read(ConvFrag& f, unsigned ua, unsigned kb) {
  if constexpr (K < 16) {
    DS_B64(f.a[K >> 1][K & 1], ua, 64 * (8 * (K >> 1) + RI) + 8 * (K & 1));
  } else if constexpr (K < 24) {
    DS_B128(f.bh[K - 16], kb, 64 * (56 - 8 * (K - 16) + RI));
  } else {
    DS_B128(f.bl[K - 24], kb, 64 * (56 - 8 * (K - 24) + RI) + KR_PART_B);
  }
}
template <int RI, int K>
__device__ __forceinline__ void conv_read_all(ConvFrag& f, unsigned ua, unsigned kb) {
  conv_read<RI, K>(f, ua, kb);
  if constexpr (K + 1 < 32) conv_read_all<RI, K + 1>(f, ua, kb);
}
#define CONV_WAIT(f)                                                                                                  \
  do {                                                                                                                \
    asm volatile("s_waitcnt lgkmcnt(0)"                                                                               \
                 : "+v"(f.a[0][0]), "+v"(f.a[0][1]), "+v"(f.a[1][0]), "+v"(f.a[1][1]), "+v"(f.a[2][0]), "+v"(f.a[2][1]), \
                   "+v"(f.a[3][0]), "+v"(f.a[3][1]), "+v"(f.a[4][0]), "+v"(f.a[4][1]), "+v"(f.a[5][0]), "+v"(f.a[5][1]), \
                   "+v"(f.a[6][0]), "+v"(f.a[6][1]), "+v"(f.a[7][0]), "+v"(f.a[7][1])::"memory");                     \
    asm volatile("" : "+v"(f.bh[0]), "+v"(f.bh[1]), "+v"(f.bh[2]), "+v"(f.bh[3]), "+v"(f.bh[4]), "+v"(f.bh[5]),        \
                 "+v"(f.bh[6]), "+v"(f.bh[7]), "+v"(f.bl[0]), "+v"(f.bl[1]), "+v"(f.bl[2]), "+v"(f.bl[3]),            \
                 "+v"(f.bl[4]), "+v"(f.bl[5]), "+v"(f.bl[6]), "+v"(f.bl[7])::"memory");                              \
  } while (0)
constexpr int conv_tau(int P) { int t = 0; while (P >= 8 - t) { P -= 8 - t; ++t; } return t; }
constexpr int conv_sig(int P) { int t = 0; while (P >= 8 - t) { P -= 8 - t; ++t; } return P; }
template <int RI, int P>
__device__ __forceinline__ void conv_pairs(f32x4 (&acc)[8], const bf16x8 (&A)[8], ConvFrag& cur, ConvFrag& nxt, unsigned ua,
                                           unsigned kb) {
  constexpr int T = conv_tau(P), SG = conv_sig(P);
  MFMA16(acc[T + SG], A[SG], cur.bl[T]);
  if constexpr (RI < 3 && P < 32) conv_read<RI + 1, P>(nxt, ua, kb);
  MFMA16(acc[T + SG], A[SG], cur.bh[T]);
  if constexpr (P + 1 < 36) conv_pairs<RI, P + 1>(acc, A, cur, nxt, ua, kb);
}
template <int RI>
__device__ __forceinline__ void conv_rho(f32x4 (&acc)[8], ConvFrag& cur, ConvFrag& nxt, unsigned ua, unsigned kb) {
  CONV_WAIT(cur);
  bf16x8 A[8];
#pragma unroll
  for (int i = 0; i < 8; ++i) A[i] = cat4(cur.a[i][0], cur.a[i][1]);
  conv_pairs<RI, 0>(acc, A, cur, nxt, ua, kb);
  if constexpr (RI < 3) conv_rho<RI + 1>(acc, nxt, cur, ua, kb);
}

__device__ __forceinline__ void split_pair(float v0, float v1, unsigned& hi, unsigned& lo) {
  hi = pk_bf16(v0, v1);
  lo = pk_bf16(ssub(v0, bf_lo(hi)), ssub(v1, bf_hi(hi)));
}

__device__ __forceinline__ void ssm_unit(int w, const u16* __restrict__ u, const u16* __restrict__ logAr,
                                         const u16* __restrict__ Aim, const u16* __restrict__ Cre,
                                         const u16* __restrict__ Cim, const u16* __restrict__ logdt,
                                         const u16* __restrict__ Dskip, u16* __restrict__ aT, char* smem DBG_ARG) {
  const int tid = threadIdx.x, lane = tid & 63;
  const int wave = __builtin_amdgcn_readfirstlane(tid >> 6);
  const int ch = wave >> 1, w2 = wave & 1;
  const int d0 = 2 * w;
  const int d = d0 + ch;

  const int pidx = d0 * NST + tid;
  const u16 r_lar = logAr[pidx], r_aim = Aim[pidx], r_cre = Cre[pidx], r_cim = Cim[pidx], r_ldt = logdt[d], r_dsk = Dskip[d];
  const unsigned* u32p = (const unsigned*)(u + d0);
  unsigned uw[8], up[3];
#pragma unroll
  for (int i = 0; i < 3; ++i) up[i] = 0;
  if (lane == 0 && tid > 0) {
#pragma unroll
    for (int i = 0; i < 3; ++i) up[i] = u32p[(size_t)(8 * tid - 3 + i) * (DM / 2)];
  }
  const unsigned uoff = (unsigned)tid * 8192u;
  {
    const float dt = rbf(expf(bf2f(r_ldt)));
    const float are = rbf(expf(bf2f(r_lar)));
    f32x4 prm;
    prm[0] = (-are * dt) * 1.44269504088896340736f;
    prm[1] = (bf2f(r_aim) * dt) * 0.15915494309189533577f;
    prm[2] = 2.0f * (bf2f(r_cre) * dt);
    prm[3] = 2.0f * (bf2f(r_cim) * dt);
    *(f32x4*)(smem + KR_OFF + tid * 16) = prm;
  }
  __syncthreads();

  const int r = lane & 31, h = lane >> 5;
  f32x16 dacc = {};
  {
    int p1i;
    if (r < 8) p1i = r;
    else if (r < 12) p1i = 256 + 2 * (r - 8);
    else if (r < 16) p1i = 512 + 256 * ((r - 12) >> 1) + 4 * ((r - 12) & 1);
    else if (r < 21) p1i = 1024 + 256 * (r - 16);
    else p1i = 0;
    const float p1 = (float)p1i, p2 = (float)(8 * r);
    const char* prm_base = smem + KR_OFF + (ch * 128 + 64 * w2 + 4 * h) * 16;
#pragma unroll
    for (int ks = 0; ks < 8; ++ks) {
      if (ks >= 4) {
        asm volatile("global_load_dword %0, %1, %2 offset:%3" : "=v"(uw[2 * ks - 8]) : "v"(uoff), "s"((const char*)u32p + ((2 * ks - 8) >> 2) * 4096), "i"(((2 * ks - 8) & 3) * 1024) : "memory");
        asm volatile("global_load_dword %0, %1, %2 offset:%3" : "=v"(uw[2 * ks - 7]) : "v"(uoff), "s"((const char*)u32p + ((2 * ks - 7) >> 2) * 4096), "i"(((2 * ks - 7) & 3) * 1024) : "memory");
      }
      f32x4 prm[4];
#pragma unroll
      for (int q = 0; q < 4; ++q) prm[q] = *(const f32x4*)(prm_base + (8 * ks + q) * 16);
      float ea[4], eb[4], fa[4], fb[4];
#pragma unroll
      for (int q = 0; q < 4; ++q) {
        ea[q] = __builtin_amdgcn_exp2f(smul(prm[q][0], p1)); eb[q] = __builtin_amdgcn_exp2f(smul(prm[q][0], p2));
        fa[q] = __builtin_amdgcn_fractf(smul(prm[q][1], p1)); fb[q] = __builtin_amdgcn_fractf(smul(prm[q][1], p2));
      }
      float ac[4], as[4], bc[4], bs[4];
#pragma unroll
      for (int q = 0; q < 4; ++q) {
        ac[q] = __builtin_amdgcn_cosf(fa[q]); as[q] = __builtin_amdgcn_sinf(fa[q]);
        bc[q] = __builtin_amdgcn_cosf(fb[q]); bs[q] = __builtin_amdgcn_sinf(fb[q]);
      }
      asm volatile("s_nop 1" : "+v"(ea[0]), "+v"(ea[1]), "+v"(ea[2]), "+v"(ea[3]), "+v"(eb[0]), "+v"(eb[1]), "+v"(eb[2]), "+v"(eb[3]),
                   "+v"(ac[0]), "+v"(ac[1]), "+v"(ac[2]), "+v"(ac[3]), "+v"(as[0]), "+v"(as[1]), "+v"(as[2]), "+v"(as[3]),
                   "+v"(bc[0]), "+v"(bc[1]), "+v"(bc[2]), "+v"(bc[3]), "+v"(bs[0]), "+v"(bs[1]), "+v"(bs[2]), "+v"(bs[3]));
      unsigned ah[4], al[4], bh[4], bl[4];
#pragma unroll
      for (int q = 0; q < 4; ++q) {
        const float cr = smul(prm[q][2], ea[q]), ci = smul(prm[q][3], ea[q]);
        const float gre = sfms(ci, as[q], smul(cr, ac[q]));
        const float gmi = sfnn(ci, ac[q], smul(cr, as[q]));
        split_pair(gre, gmi, ah[q], al[q]);
        split_pair(smul(bc[q], eb[q]), smul(bs[q], eb[q]), bh[q], bl[q]);
      }
      const bf16x8 Ah = mk8(ah[0], ah[1], ah[2], ah[3]), Al = mk8(al[0], al[1], al[2], al[3]);
      const bf16x8 Bh = mk8(bh[0], bh[1], bh[2], bh[3]), Bl = mk8(bl[0], bl[1], bl[2], bl[3]);
      dacc = __builtin_amdgcn_mfma_f32_32x32x16_bf16(Al, Bh, dacc, 0, 0, 0);
      dacc = __builtin_amdgcn_mfma_f32_32x32x16_bf16(Ah, Bl, dacc, 0, 0, 0);
      dacc = __builtin_amdgcn_mfma_f32_32x32x16_bf16(Ah, Bh, dacc, 0, 0, 0);
    }
  }
  {
    float* xo = (float*)(smem + KX_OFF + wave * 1536) + lane;
    const float* xi = (const float*)(smem + KX_OFF + (wave ^ 1) * 1536) + lane;
    float mine[6];
    if (w2 == 0) {
#pragma unroll
      for (int e = 0; e < 6; ++e) { xo[e * 64] = dacc[6 + e]; mine[e] = dacc[e]; }
    } else {
#pragma unroll
      for (int e = 0; e < 6; ++e) { xo[e * 64] = dacc[e]; mine[e] = dacc[6 + e]; }
    }
    __syncthreads();
    unsigned* Kq = (unsigned*)(smem + ch * KQ_B);
    unsigned pq[6];
#pragma unroll
    for (int e = 0; e < 6; ++e) {
      const float v = sadd(mine[e], xi[e * 64]);
      const unsigned hi = pk_bf16(v, 0.f);
      pq[e] = pk_bf16(0.f, ssub(v, bf_lo(hi))) | hi;
    }
    if (w2 == 0) {
#pragma unroll
      for (int e = 0; e < 4; ++e) Kq[4 * h + e + 8 * r] = pq[e];
      Kq[(h ? 512 : 256) + 8 * r] = pq[4];
      Kq[(h ? 516 : 258) + 8 * r] = pq[5];
    } else {
      Kq[(h ? 768 : 260) + 8 * r] = pq[0];
      Kq[(h ? 772 : 262) + 8 * r] = pq[1];
      if (h == 0) {
#pragma unroll
        for (int e = 0; e < 4; ++e) Kq[1024 + 256 * e + 8 * r] = pq[2 + e];
      } else if (r == 0) {
        Kq[2048] = pq[2];
      }
    }
  }
  __syncthreads();

  {
    const int t = tid & 127;
    const unsigned* Kq = (const unsigned*)(smem + ch * KQ_B);
    unsigned* krh = (unsigned*)(smem + KR_OFF + (ch * 2) * KR_PART_B);
    unsigned* krl = (unsigned*)((char*)krh + KR_PART_B);
    unsigned q0[8], q1[8];
#pragma unroll
    for (int m = 0; m < 8; ++m) {
      const int sh = m < 4 ? 3 : (m < 6 ? 2 : (m < 7 ? 1 : 0));
      const int l0 = 2047 - 2 * (t + 128 * m), l1 = l0 - 1;
      int pa = l0, pb = l1;
      if (sh) {
        pa = (l0 + (1 << (sh - 1)) - 1 + ((l0 >> sh) & 1)) & ~((1 << sh) - 1);
        pb = (l1 + (1 << (sh - 1)) - 1 + ((l1 >> sh) & 1)) & ~((1 << sh) - 1);
      }
      q0[m] = Kq[pa];
      q1[m] = Kq[pb];
    }
#pragma unroll
    for (int m = 0; m < 8; ++m) {
      krh[t + 128 * m] = __builtin_amdgcn_perm(q1[m], q0[m], 0x05040100u);
      krl[t + 128 * m] = __builtin_amdgcn_perm(q1[m], q0[m], 0x07060302u);
    }
    if (t < 120) { krh[1024 + t] = 0; krl[1024 + t] = 0; }
  }
  __syncthreads();

  asm volatile("s_waitcnt vmcnt(0)" : "+v"(uw[0]), "+v"(uw[1]), "+v"(uw[2]), "+v"(uw[3]), "+v"(uw[4]), "+v"(uw[5]), "+v"(uw[6]), "+v"(uw[7])::"memory");
  {
    unsigned e[11];
#pragma unroll
    for (int i = 0; i < 3; ++i) {
      const unsigned nb = __shfl_up(uw[5 + i], 1);
      e[i] = (lane == 0) ? up[i] : nb;
    }
#pragma unroll
    for (int i = 0; i < 8; ++i) e[3 + i] = uw[i];
    unsigned p0[10], p1[10];
#pragma unroll
    for (int i = 0; i < 10; ++i) {
      p0[i] = __builtin_amdgcn_perm(e[i + 1], e[i], 0x05040100u);
      p1[i] = __builtin_amdgcn_perm(e[i + 1], e[i], 0x07060302u);
    }
#pragma unroll
    for (int c = 0; c < 4; ++c) {
      *(u32x4*)(smem + c * U_COPY_B + (tid + 2) * 16) = u32x4{p0[c], p0[c + 2], p0[c + 4], p0[c + 6]};
      *(u32x4*)(smem + U_CH_B + c * U_COPY_B + (tid + 2) * 16) = u32x4{p1[c], p1[c + 2], p1[c + 4], p1[c + 6]};
    }
    if (tid < 16) *(u32x4*)(smem + (tid >> 1) * U_COPY_B + (tid & 1) * 16) = u32x4{0, 0, 0, 0};
  }
  __syncthreads();

  f32x4 acc[8];
#pragma unroll
  for (int t = 0; t < 8; ++t) acc[t] = f32x4{0.f, 0.f, 0.f, 0.f};
  asm volatile("s_nop 4" : "+v"(acc[0]), "+v"(acc[1]), "+v"(acc[2]), "+v"(acc[3]), "+v"(acc[4]), "+v"(acc[5]), "+v"(acc[6]), "+v"(acc[7]));
  {
    const int r16 = lane & 15, hq = lane >> 4;
    const unsigned sb = lds_addr(smem);
    const unsigned ua = sb + ch * U_CH_B + (r16 & 3) * U_COPY_B + 2 * (4 + (r16 & ~3) + 8 * hq) + 64 * 4 * w2;
    const unsigned kb = sb + KR_OFF + (ch * 2) * KR_PART_B + 2 * (2032 - 16 * r16 + 8 * hq) - 64 * 56 + 64 * 4 * w2;
    ConvFrag f0, f1;
    conv_read_all<0, 0>(f0, ua, kb);
    conv_rho<0>(acc, f0, f1, ua, kb);
  }
  asm volatile("s_nop 15" : "+v"(acc[0]), "+v"(acc[1]), "+v"(acc[2]), "+v"(acc[3]), "+v"(acc[4]), "+v"(acc[5]), "+v"(acc[6]), "+v"(acc[7]));
  __syncthreads();
  {
    char* xw = smem + KR_OFF + (ch * 2 + w2) * 4096 + lane * 16;
    const char* xr = smem + KR_OFF + (ch * 2 + (w2 ^ 1)) * 4096 + lane * 16;
    f32x4 y[4];
    if (w2 == 0) {
#pragma unroll
      for (int i = 0; i < 4; ++i) { *(f32x4*)(xw + i * 1024) = acc[4 + i]; y[i] = acc[i]; }
    } else {
#pragma unroll
      for (int i = 0; i < 4; ++i) { *(f32x4*)(xw + i * 1024) = acc[i]; y[i] = acc[4 + i]; }
    }
    __syncthreads();
    const float Dk = bf2f(r_dsk);
#pragma unroll
    for (int i = 0; i < 4; ++i) {
      const f32x4 o = *(const f32x4*)(xr + i * 1024);
      const int l0 = 256 * (4 * w2 + i) + 16 * (lane & 15) + 4 * (lane >> 4);
      const u32x2 uu = *(const u32x2*)(smem + ch * U_CH_B + 3 * U_COPY_B + 2 * (16 + l0));
      const unsigned y01 = pk_bf16(sadd(y[i][0], o[0]), sadd(y[i][1], o[1])), y23 = pk_bf16(sadd(y[i][2], o[2]), sadd(y[i][3], o[3]));
      const unsigned s01 = pk_bf16(smul(bf_lo(uu[0]), Dk), smul(bf_hi(uu[0]), Dk)), s23 = pk_bf16(smul(bf_lo(uu[1]), Dk), smul(bf_hi(uu[1]), Dk));
      { const u32x2 av = u32x2{pk_bf16(sadd(bf_lo(y01), bf_lo(s01)), sadd(bf_hi(y01), bf_hi(s01))),
                                                  pk_bf16(sadd(bf_lo(y23), bf_lo(s23)), sadd(bf_hi(y23), bf_hi(s23)))};
        asm volatile("global_store_dwordx2 %0, %1, off sc1\n\ts_nop 1" ::"v"(aT + (size_t)d * SEQ + l0), "v"(av) : "memory"); }
    }
  }
}

__device__ __forceinline__ float gelu_tanh(float x) {
  const float z = 0.7978845608028654f * (x + 0.044715f * x * x * x);
  const float e = __builtin_amdgcn_exp2f(-2.8853900817779268f * z);
  return x * __builtin_amdgcn_rcpf(1.0f + e);
}

constexpr int G1_WSLOT_B = 12288;
constexpr int G1_X_OFF = 3 * G1_WSLOT_B;
constexpr int G1_LDS = G1_X_OFF + 32768;

constexpr int g1_pos(bool isx, int id) {
  if (isx && id == 0) return 4;
  if (!isx && id == 0) return 7;
  if (!isx && id == 1) return 10;
  if (isx && id == 1) return 14;
  if (!isx && id == 2) return 17;
  if (!isx && id == 3) return 20;
  if (!isx && id == 4) return 23;
  if (isx && id == 2) return 27;
  int n = 27;
  for (int t = 0; t < 32; ++t) {
    if ((t & 1) == 0 && t / 2 + 5 < 16) { n += 3; if (!isx && t / 2 + 5 == id) return n; }
    if ((t & 3) == 1 && t / 4 + 3 < 8) { n += 4; if (isx && t / 4 + 3 == id) return n; }
  }
  return -1;
}
constexpr int g1_issued_before(int step) {
  int n = 27;
  for (int t = 0; t < step; ++t) {
    if ((t & 1) == 0 && t / 2 + 5 < 16) n += 3;
    if ((t & 3) == 1 && t / 4 + 3 < 8) n += 4;
  }
  return n;
}

#define GLOAD128(dst, ptr) asm volatile("global_load_dwordx4 %0, %1, off" : "=v"(dst) : "v"(ptr) : "memory")
#define GLOAD128S(dst, voff, sbase) asm volatile("global_load_dwordx4 %0, %1, %2" : "=v"(dst) : "v"(voff), "s"(sbase) : "memory")
#define GLOAD128SA(dst, voff, sbase) asm volatile("global_load_dwordx4 %0, %1, %2" : "=a"(dst) : "v"(voff), "s"(sbase) : "memory")
#define GLOAD128A(dst, ptr) asm volatile("global_load_dwordx4 %0, %1, off" : "=a"(dst) : "v"(ptr) : "memory")
#define DS_W128(addr, val, off) asm volatile("ds_write_b128 %0, %1 offset:%2" ::"v"(addr), "v"(val), "i"(off) : "memory")
#define MFMA32(acc, a, b) asm volatile("v_mfma_f32_32x32x16_bf16 %0, %1, %2, %0" : "+v"(acc) : "v"(a), "v"(b))

struct G1Ctx {
  const char* wbase;
  const char* xbase;
  unsigned wo[3], xo;
  unsigned woff, woff_o, ww[3], xw, xw1, xrd2[2][4];
  unsigned junk[4];
};
struct G1Frag {
  s16x4 w[3][2];
  bf16x8 x[2];
};

#define G1_RD_W(f, t, OFF)                                                        \
  do {                                                                            \
    if ((t) & 1) { DS_TR(f.w[t][0], c.woff_o, (OFF) + (t) * 64); DS_TR(f.w[t][1], c.woff_o, (OFF) + (t) * 64 + 4 * 384); } \
    else { DS_TR(f.w[t][0], c.woff, (OFF) + (t) * 64); DS_TR(f.w[t][1], c.woff, (OFF) + (t) * 64 + 4 * 384); }            \
  } while (0)
#define G1_RD_X(f, mi, N) DS_B128(f.x[mi], c.xrd2[mi][(N) & 3], (((N) >> 2) & 1) * 16384)
template <int N>
__device__ __forceinline__ void g1_read(G1Frag& f, const G1Ctx& c) {
  constexpr int OFF = ((N >> 1) % 3) * G1_WSLOT_B + (N & 1) * 16 * 384;
  G1_RD_X(f, 0, N); G1_RD_X(f, 1, N);
  G1_RD_W(f, 0, OFF); G1_RD_W(f, 1, OFF); G1_RD_W(f, 2, OFF);
}
#define G1_WAIT_FRAG(f, STR)                                                                                       \
  asm volatile(STR : "+v"(f.w[0][0]), "+v"(f.w[0][1]), "+v"(f.w[1][0]), "+v"(f.w[1][1]), "+v"(f.w[2][0]),        \
               "+v"(f.w[2][1]), "+v"(f.x[0]), "+v"(f.x[1])::"memory")

template <int N>
struct G1Step {
  static __device__ __forceinline__ void run(f32x16 (&acc)[6], u32x4 (&xr)[2][4], u32x4 (&wr)[3][3], G1Frag& cur,
                                             G1Frag& nxt, G1Ctx& c) {
    constexpr int ISS = g1_issued_before(N);
    constexpr int S = N >> 1;
    constexpr bool RD = N + 1 < 32;
    constexpr int OFF = (((N + 1) >> 1) % 3) * G1_WSLOT_B + ((N + 1) & 1) * 16 * 384;
    if constexpr ((N & 1) == 0 && N > 0) G1_WAIT_FRAG(cur, "s_waitcnt lgkmcnt(0)\n\ts_barrier");
    else G1_WAIT_FRAG(cur, "s_waitcnt lgkmcnt(0)");
    bf16x8 b[3];
#pragma unroll
    for (int t = 0; t < 3; ++t) b[t] = cat4(cur.w[t][0], cur.w[t][1]);
    MFMA32(acc[0], b[0], cur.x[0]);
    if constexpr (RD) { G1_RD_X(nxt, 0, N + 1); G1_RD_W(nxt, 0, OFF); }
    MFMA32(acc[3], b[0], cur.x[1]);
    if constexpr (RD) { G1_RD_X(nxt, 1, N + 1); G1_RD_W(nxt, 1, OFF); }
    MFMA32(acc[1], b[1], cur.x[0]);
    if constexpr (RD) { G1_RD_W(nxt, 2, OFF); }
    MFMA32(acc[4], b[1], cur.x[1]);
    if constexpr ((N & 1) == 0) {
      if constexpr (S + 2 < 16) {
        constexpr int R = (S + 2) % 3;
        constexpr int NW = ISS - g1_pos(false, S + 2);
        asm volatile("s_waitcnt vmcnt(%3)" : "+v"(wr[R][0]), "+v"(wr[R][1]), "+v"(wr[R][2]) : "i"(NW) : "memory");
#pragma unroll
        for (int i = 0; i < 3; ++i) DS_W128(c.ww[i], wr[R][i], R * G1_WSLOT_B);
      }
    }
    if constexpr ((N & 3) == 1) {
      constexpr int B = N / 4 + 1;
      if constexpr (B < 8) {
        constexpr int NW = ISS - g1_pos(true, B);
        asm volatile("s_waitcnt vmcnt(%4)" : "+v"(xr[B & 1][0]), "+v"(xr[B & 1][1]), "+v"(xr[B & 1][2]), "+v"(xr[B & 1][3]) : "i"(NW) : "memory");
#pragma unroll
        for (int i = 0; i < 4; ++i) { if (i & 1) DS_W128(c.xw1, xr[B & 1][i], (B & 1) * 16384 + i * 1024); else DS_W128(c.xw, xr[B & 1][i], (B & 1) * 16384 + i * 1024); }
      }
    }
    MFMA32(acc[2], b[2], cur.x[0]);
    if constexpr ((N & 1) == 0 && S + 5 < 16) {
#pragma unroll
      for (int i = 0; i < 3; ++i) GLOAD128S(wr[(S + 5) % 3][i], c.wo[i], c.wbase + (size_t)(S + 5) * 32 * FF * 2);
    }
    if constexpr ((N & 3) == 1 && N / 4 + 3 < 8) {
      constexpr int B = N / 4 + 1;
#pragma unroll
      for (int i = 0; i < 4; ++i) GLOAD128S(xr[B & 1][i], c.xo, c.xbase + (size_t)(8 * i) * DM * 2 + (B + 2) * 128);
    }
    MFMA32(acc[5], b[2], cur.x[1]);
    if constexpr (N + 1 < 32) G1Step<N + 1>::run(acc, xr, wr, nxt, cur, c);
  }
};

__device__ __forceinline__ void gemm1_tile(int t, const u16* __restrict__ x, const u16* __restrict__ wg,
                                           const u16* __restrict__ wu, u16* __restrict__ hf, char* smem DBG_ARG) {
  const int tid = threadIdx.x, lane = tid & 63;
  const int wave = __builtin_amdgcn_readfirstlane(tid >> 6);
  const int fb = t >> 4, mb = t & 15;
  const int f0 = 96 * fb;
  const int wm = wave & 1, isup = wave >> 1;
  G1Ctx c;
  const unsigned sbase = lds_addr(smem);
  c.wbase = (const char*)(isup ? wu : wg);
#pragma unroll
  for (int i = 0; i < 3; ++i) {
    const int id = 192 * (wave & 1) + 64 * i + lane;
    const int row = id / 12, cc = id % 12;
    int col = f0 + 8 * cc;
    if (col > FF - 8) col = FF - 8;
    c.wo[i] = (unsigned)(row * FF + col) * 2u;
    c.ww[i] = sbase + row * 384 + ((cc + 12 * isup) ^ (((row >> 1) & 1) << 2)) * 16;
  }
  c.xbase = (const char*)(x + (size_t)(128 * mb + 32 * wave) * DM);
  c.xo = (unsigned)((lane >> 3) * DM + (lane & 7) * 8) * 2u;
  const unsigned xl = sbase + G1_X_OFF;
  c.xw = xl + (32 * wave + (lane >> 3)) * 128 + (((lane & 7) ^ (lane >> 4)) * 16);
  c.xw1 = xl + (32 * wave + (lane >> 3)) * 128 + (((lane & 7) ^ ((lane >> 4) + 4)) * 16);
  const int r = lane & 31, h = lane >> 5;
#pragma unroll
  for (int i = 0; i < 4; ++i) {
    c.xrd2[0][i] = xl + (64 * wm + 32 * isup + r) * 128 + (((2 * i + h) ^ ((r >> 1) & 7)) * 16);
    c.xrd2[1][i] = xl + (64 * wm + 32 * (1 - isup) + r) * 128 + (((2 * i + h) ^ ((r >> 1) & 7)) * 16);
  }
  const int blk = (lane >> 4) & 1, q = (lane & 15) >> 2, p = lane & 3;
  const int sw = (q >> 1) & 1;
  {
    const unsigned e = sbase + (8 * h + q) * 384 + (2 * blk + (p >> 1) + 4 * sw) * 16 + 8 * (p & 1);
    const unsigned o = sbase + (8 * h + q) * 384 + (2 * blk + (p >> 1) - 4 * sw) * 16 + 8 * (p & 1);
    c.woff = isup ? o + 192 : e;
    c.woff_o = isup ? e + 192 : o;
  }

  f32x16 acc[6];
#pragma unroll
  for (int i = 0; i < 6; ++i) acc[i] = f32x16{};
  asm volatile("s_nop 4" : "+v"(acc[0]), "+v"(acc[1]), "+v"(acc[2]), "+v"(acc[3]), "+v"(acc[4]), "+v"(acc[5]));
  u32x4 xr[2][4], wr[3][3];
  G1Frag fa, fb2;
#define G1_ISSUE_X(R, B) _Pragma("unroll") for (int i = 0; i < 4; ++i) GLOAD128S(xr[R][i], c.xo, c.xbase + (size_t)(8 * i) * DM * 2 + (B) * 128)
#define G1_ISSUE_W(R, S) _Pragma("unroll") for (int i = 0; i < 3; ++i) GLOAD128S(wr[R][i], c.wo[i], c.wbase + (size_t)(S) * 32 * FF * 2)
  G1_ISSUE_X(0, 0); G1_ISSUE_W(0, 0); G1_ISSUE_W(1, 1); G1_ISSUE_X(1, 1); G1_ISSUE_W(2, 2);
#define G1_EXTRA 0
  asm volatile("s_waitcnt vmcnt(%10)"
               : "+v"(wr[0][0]), "+v"(wr[0][1]), "+v"(wr[0][2]), "+v"(wr[1][0]), "+v"(wr[1][1]), "+v"(wr[1][2]),
                 "+v"(xr[0][0]), "+v"(xr[0][1]), "+v"(xr[0][2]), "+v"(xr[0][3]) : "i"(7 + G1_EXTRA) : "memory");
#pragma unroll
  for (int i = 0; i < 3; ++i) { DS_W128(c.ww[i], wr[0][i], 0); DS_W128(c.ww[i], wr[1][i], G1_WSLOT_B); }
#pragma unroll
  for (int i = 0; i < 4; ++i) { if (i & 1) DS_W128(c.xw1, xr[0][i], i * 1024); else DS_W128(c.xw, xr[0][i], i * 1024); }
  G1_ISSUE_W(0, 3); G1_ISSUE_W(1, 4); G1_ISSUE_X(0, 2);
  asm volatile("s_waitcnt lgkmcnt(0)\n\ts_barrier" ::: "memory");
  g1_read<0>(fa, c);
  G1Step<0>::run(acc, xr, wr, fa, fb2, c);
  asm volatile("s_nop 15\n\ts_nop 15" : "+v"(acc[0]), "+v"(acc[1]), "+v"(acc[2]), "+v"(acc[3]), "+v"(acc[4]), "+v"(acc[5]));

  unsigned keep[3][8];
  {
    unsigned give[3][8];
#pragma unroll
    for (int tt = 0; tt < 3; ++tt)
#pragma unroll
      for (int e = 0; e < 8; ++e) give[tt][e] = pk_bf16(acc[3 + tt][2 * e], acc[3 + tt][2 * e + 1]);
    __syncthreads();
    char* xo = smem + wave * 6144 + lane * 16;
#pragma unroll
    for (int tt = 0; tt < 3; ++tt) {
      *(u32x4*)(xo + (2 * tt) * 1024) = u32x4{give[tt][0], give[tt][1], give[tt][2], give[tt][3]};
      *(u32x4*)(xo + (2 * tt + 1) * 1024) = u32x4{give[tt][4], give[tt][5], give[tt][6], give[tt][7]};
    }
  }
#pragma unroll
  for (int tt = 0; tt < 3; ++tt)
#pragma unroll
    for (int e = 0; e < 8; ++e) keep[tt][e] = pk_bf16(acc[tt][2 * e], acc[tt][2 * e + 1]);
  __syncthreads();
  {
    const char* xi = smem + (wave ^ 2) * 6144 + lane * 16;
    const int mt = 4 * mb + 2 * wm + isup;
    u16* hp = hf + ((size_t)(mt * NKS + 6 * fb) * 64 + lane) * 8;
    auto finish = [&](auto ISUP) {
#pragma unroll
      for (int tt = 0; tt < 3; ++tt) {
        const int ft = 3 * fb + tt;
        if (ft < FF / 32) {
          const u32x4 o0 = *(const u32x4*)(xi + (2 * tt) * 1024);
          const u32x4 o1 = *(const u32x4*)(xi + (2 * tt + 1) * 1024);
          unsigned gp[8], up[8];
#pragma unroll
          for (int e = 0; e < 8; ++e) {
            const unsigned ot = e < 4 ? o0[e & 3] : o1[e & 3];
            gp[e] = decltype(ISUP)::value ? ot : keep[tt][e];
            up[e] = decltype(ISUP)::value ? keep[tt][e] : ot;
          }
          f32x2 xg[8], ar[8];
#pragma unroll
          for (int e = 0; e < 8; ++e) xg[e] = f32x2{bf_lo(gp[e]), bf_hi(gp[e])};
#pragma unroll
          for (int e = 0; e < 8; ++e) ar[e] = xg[e] * xg[e];
#pragma unroll
          for (int e = 0; e < 8; ++e) ar[e] = __builtin_elementwise_fma(ar[e], f32x2{-0.10294323958f, -0.10294323958f}, f32x2{-2.30220819814f, -2.30220819814f});
#pragma unroll
          for (int e = 0; e < 8; ++e) ar[e] = ar[e] * xg[e];
#pragma unroll
          for (int e = 0; e < 8; ++e) ar[e] = f32x2{__builtin_amdgcn_exp2f(ar[e][0]), __builtin_amdgcn_exp2f(ar[e][1])};
#pragma unroll
          for (int e = 0; e < 8; ++e) ar[e] = ar[e] + f32x2{1.0f, 1.0f};
#pragma unroll
          for (int e = 0; e < 8; ++e) ar[e] = f32x2{__builtin_amdgcn_rcpf(ar[e][0]), __builtin_amdgcn_rcpf(ar[e][1])};
#pragma unroll
          for (int e = 0; e < 8; ++e) ar[e] = ar[e] * xg[e];
          unsigned o[8];
#pragma unroll
          for (int e = 0; e < 8; ++e) o[e] = pk_bf16(ar[e][0], ar[e][1]);
#pragma unroll
          for (int e = 0; e < 8; ++e) ar[e] = f32x2{bf_lo(o[e]), bf_hi(o[e])} * f32x2{bf_lo(up[e]), bf_hi(up[e])};
#pragma unroll
          for (int e = 0; e < 8; ++e) o[e] = pk_bf16(ar[e][0], ar[e][1]);
          { const u32x4 v0 = u32x4{o[0], o[1], o[2], o[3]}, v1 = u32x4{o[4], o[5], o[6], o[7]};
            asm volatile("global_store_dwordx4 %0, %1, off sc1\n\ts_nop 1" ::"v"(hp + (2 * tt) * 512), "v"(v0) : "memory");
            asm volatile("global_store_dwordx4 %0, %1, off sc1\n\ts_nop 1" ::"v"(hp + (2 * tt + 1) * 512), "v"(v1) : "memory"); }
        } else if (ft < NKS / 2) {
          *(u32x4*)(hp + (2 * tt) * 512) = u32x4{0, 0, 0, 0};
          *(u32x4*)(hp + (2 * tt + 1) * 512) = u32x4{0, 0, 0, 0};
        }
      }
    };
    if (isup) finish(std::integral_constant<bool, true>{});
    else finish(std::integral_constant<bool, false>{});
  }
}

__device__ __forceinline__ void gate_unit(int b, const u16* __restrict__ x, const u16* __restrict__ gw,
                                          float* __restrict__ gout) {
  const int tid = threadIdx.x, lane = tid & 63, wave = tid >> 6;
  const int j = lane & 7, rr = lane >> 3;
  u32x4 gv[8];
#pragma unroll
  for (int i = 0; i < 8; ++i) gv[i] = *(const u32x4*)(gw + (j + 8 * i) * 8);
  const int row0 = b * 128 + wave * 32 + rr;
  u32x4 xv[4][8];
#pragma unroll
  for (int ps = 0; ps < 4; ++ps)
#pragma unroll
    for (int i = 0; i < 8; ++i) xv[ps][i] = *(const u32x4*)(x + (size_t)(row0 + 8 * ps) * DM + (j + 8 * i) * 8);
#pragma unroll
  for (int ps = 0; ps < 4; ++ps) {
    float s0 = 0.f, s1 = 0.f;
#pragma unroll
    for (int i = 0; i < 8; ++i)
#pragma unroll
      for (int e = 0; e < 4; ++e) {
        const unsigned pr = pk_bf16(smul(bf_lo(xv[ps][i][e]), bf_lo(gv[i][e])), smul(bf_hi(xv[ps][i][e]), bf_hi(gv[i][e])));
        s0 = sadd(s0, bf_lo(pr));
        s1 = sadd(s1, bf_hi(pr));
      }
    float s = s0 + s1;
    s += __shfl_xor(s, 1);
    s += __shfl_xor(s, 2);
    s += __shfl_xor(s, 4);
    if (j == 0) {
      const float sb = rbf(s);
      gout[row0 + 8 * ps] = rbf(1.0f / (1.0f + expf(-sb)));
    }
  }
}

__global__ __launch_bounds__(256, 2) void k1_kernel(const u16* __restrict__ u, const u16* __restrict__ logAr,
                                                   const u16* __restrict__ Aim, const u16* __restrict__ Cre,
                                                   const u16* __restrict__ Cim, const u16* __restrict__ logdt,
                                                   const u16* __restrict__ Dskip, const u16* __restrict__ wg,
                                                   const u16* __restrict__ wu, const u16* __restrict__ gw,
                                                   const u16* __restrict__ wd, u16* __restrict__ hbuf, u16* __restrict__ aT,
                                                   float* __restrict__ gout DBG_ARG
                                                   ) {
  __shared__ __attribute__((aligned(4096))) char smem[G1_LDS > SSM_LDS ? G1_LDS : SSM_LDS];
  const int bid = blockIdx.x;
  if (bid < NSSM) {
    ssm_unit(bid, u, logAr, Aim, Cre, Cim, logdt, Dskip, aT, smem DBG_PASS);
    unsigned acc = 0;
    for (int i = (bid >> 3) * 256 + threadIdx.x; i < 11008; i += 32 * 256) acc += ((const unsigned*)wd)[i * 32];
    asm volatile("" ::"v"(acc));
  } else {
    const int xcd = bid & 7, idx = (bid - NSSM) >> 3;
    if (idx < 30) {
      gemm1_tile(30 * xcd + idx, u, wg, wu, hbuf, smem DBG_PASS);
    }
    else gate_unit(2 * xcd + idx - 30, u, gw, gout);
  }
}

constexpr int G2_SLOT_B = 4096;
constexpr int G2_WAVE_B = 2 * G2_SLOT_B;
constexpr int G2_PITCH = 272;
constexpr int G2_A_OFF = 4 * 64 * G2_PITCH;
constexpr int K2_LDS = G2_A_OFF + 8192;
#define G2D 5
constexpr int G2_DEPTH = G2D;

struct G2Ctx {
  const char* hbase;
  const char* wbase;
  unsigned ho, wo, wrd0, wrd1, ww;
  int klim;
};
struct G2Frag { s16x4 w[2][2][2]; };
#define MFMA32A(acc, a, b) asm volatile("v_mfma_f32_32x32x16_bf16 %0, %1, %2, %0" : "+a"(acc) : "v"(a), "a"(b))

template <int ST>
struct G2Step {
  static __device__ __forceinline__ void issue(bf16x8 (&hfr)[G2_DEPTH][4], u32x4 (&wr)[G2_DEPTH][4], const G2Ctx& c) {
    const int k0 = (32 * ST < c.klim) ? 32 * ST : 0;
#pragma unroll
    for (int i = 0; i < 4; ++i) GLOAD128S(wr[ST % G2_DEPTH][i], c.wo, c.wbase + (size_t)(k0 + 8 * i) * DM * 2);
#pragma unroll
    for (int mi = 0; mi < 2; ++mi)
#pragma unroll
      for (int i = 0; i < 2; ++i) GLOAD128SA(hfr[ST % G2_DEPTH][2 * mi + i], c.ho, c.hbase + (size_t)(mi * NKS + 2 * ST + i) * 1024);
  }
  template <int NW>
  static __device__ __forceinline__ void store(u32x4 (&wr)[G2_DEPTH][4], const G2Ctx& c) {
    u32x4 (&r)[4] = wr[ST % G2_DEPTH];
    asm volatile("s_waitcnt vmcnt(%4)" : "+v"(r[0]), "+v"(r[1]), "+v"(r[2]), "+v"(r[3]) : "i"(NW) : "memory");
#pragma unroll
    for (int i = 0; i < 4; ++i) DS_W128(c.ww, r[i], (ST & 1) * G2_SLOT_B + i * 1024);
  }
  static __device__ __forceinline__ void run(f32x16 (&acc)[2][2], bf16x8 (&hfr)[G2_DEPTH][4], u32x4 (&wr)[G2_DEPTH][4],
                                             G2Frag& cur, G2Frag& nxt, const G2Ctx& c) {
    constexpr int LAST = (ST + G2_DEPTH - 1 < 10) ? ST + G2_DEPTH - 1 : 10;
    constexpr bool NX = ST + 1 <= 10;
    bf16x8 (&hh)[4] = hfr[ST % G2_DEPTH];
    if constexpr (NX) G2Step<ST + 1>::template store<4 + 8 * (LAST - ST - 1)>(wr, c);
    else asm volatile("s_waitcnt vmcnt(0)" ::: "memory");
    asm volatile("s_waitcnt lgkmcnt(%12)"
                 : "+v"(cur.w[0][0][0]), "+v"(cur.w[0][0][1]), "+v"(cur.w[0][1][0]), "+v"(cur.w[0][1][1]), "+v"(cur.w[1][0][0]),
                   "+v"(cur.w[1][0][1]), "+v"(cur.w[1][1][0]), "+v"(cur.w[1][1][1]), "+a"(hh[0]), "+a"(hh[1]), "+a"(hh[2]), "+a"(hh[3])
                 : "i"(NX ? 4 : 0) : "memory");
    const bf16x8 b00 = cat4(cur.w[0][0][0], cur.w[0][0][1]), b01 = cat4(cur.w[0][1][0], cur.w[0][1][1]);
    const bf16x8 b10 = cat4(cur.w[1][0][0], cur.w[1][0][1]), b11 = cat4(cur.w[1][1][0], cur.w[1][1][1]);
    constexpr int NO = ((ST + 1) & 1) * G2_SLOT_B;
    MFMA32A(acc[0][0], b00, hh[0]);
    MFMA32A(acc[0][1], b00, hh[2]);
    MFMA32A(acc[1][0], b01, hh[0]);
    if constexpr (NX) asm volatile("s_waitcnt lgkmcnt(0)" ::: "memory");
    MFMA32A(acc[1][1], b01, hh[2]);
    if constexpr (NX) { DS_TR(nxt.w[0][0][0], c.wrd0, NO); DS_TR(nxt.w[0][0][1], c.wrd0, NO + 1024); }
    MFMA32A(acc[0][0], b10, hh[1]);
    if constexpr (NX) { DS_TR(nxt.w[0][1][0], c.wrd1, NO); DS_TR(nxt.w[0][1][1], c.wrd1, NO + 1024); }
    MFMA32A(acc[0][1], b10, hh[3]);
    if constexpr (NX) { DS_TR(nxt.w[1][0][0], c.wrd0, NO + 2048); DS_TR(nxt.w[1][0][1], c.wrd0, NO + 3072); }
    MFMA32A(acc[1][0], b11, hh[1]);
    if constexpr (NX) { DS_TR(nxt.w[1][1][0], c.wrd1, NO + 2048); DS_TR(nxt.w[1][1][1], c.wrd1, NO + 3072); }
    MFMA32A(acc[1][1], b11, hh[3]);
    if constexpr (ST + G2_DEPTH <= 10) G2Step<ST + G2_DEPTH>::issue(hfr, wr, c);
    if constexpr (NX) G2Step<ST + 1>::run(acc, hfr, wr, nxt, cur, c);
  }
};

__global__ __launch_bounds__(256, 1) void k2_kernel(const u16* __restrict__ hf, const u16* __restrict__ wd,
                                                   const u16* __restrict__ aT, const float* __restrict__ gin,
                                                   u16* __restrict__ out) {
  __shared__ __attribute__((aligned(4096))) char smem[K2_LDS];
  const int tid = threadIdx.x, lane = tid & 63;
  const int wave = __builtin_amdgcn_readfirstlane(tid >> 6);
  const int bid = blockIdx.x;
  const int xcd = bid & 7, idx = bid >> 3;
  const int m0 = 64 * (4 * xcd + (idx >> 3)), n0 = 64 * (idx & 7);

  G2Ctx c;
  c.hbase = (const char*)(hf + (size_t)((m0 >> 5) * NKS + 22 * wave) * 512);
  c.ho = (unsigned)lane * 16u;
  c.wbase = (const char*)(wd + (size_t)(352 * wave) * DM + n0);
  c.wo = (unsigned)((lane >> 3) * DM + (lane & 7) * 8) * 2u;
  c.klim = FF - 352 * wave;
#pragma unroll
  for (int i = 0; i < 2; ++i) {
    const int piece = 2 * wave + i;
    const int drow = 8 * piece + (lane >> 3);
    glds16(aT + (size_t)(n0 + drow) * SEQ + m0 + (lane & 7) * 8, smem + G2_A_OFF + piece * 1024 + lane * 16);
  }
  bf16x8 hfr[G2_DEPTH][4];
  u32x4 wr[G2_DEPTH][4];
  G2Step<0>::issue(hfr, wr, c);
  G2Step<1>::issue(hfr, wr, c);
  G2Step<2>::issue(hfr, wr, c);
  if constexpr (G2_DEPTH > 3) G2Step<3>::issue(hfr, wr, c);
  if constexpr (G2_DEPTH > 4) G2Step<4>::issue(hfr, wr, c);

  const unsigned wb = lds_addr(smem) + wave * G2_WAVE_B;
  c.ww = wb + (lane >> 3) * 128 + (((lane & 7) ^ (((lane >> 4) & 1) << 2)) * 16);
  const int r = lane & 31, h = lane >> 5;
  const int blk = (lane >> 4) & 1, q = (lane & 15) >> 2, p = lane & 3;
  c.wrd0 = wb + (4 * h + q) * 128 + (((2 * blk + (p >> 1)) ^ (((q >> 1) & 1) << 2)) * 16) + 8 * (p & 1);
  c.wrd1 = c.wrd0 ^ 64u;

  f32x16 acc[2][2];
#pragma unroll
  for (int i = 0; i < 2; ++i)
#pragma unroll
    for (int j = 0; j < 2; ++j) acc[i][j] = f32x16{};
  asm volatile("s_nop 4" : "+a"(acc[0][0]), "+a"(acc[0][1]), "+a"(acc[1][0]), "+a"(acc[1][1]));
  G2Frag fa, fb;
  G2Step<0>::template store<4 + 8 * (G2_DEPTH - 1)>(wr, c);
  asm volatile("s_waitcnt lgkmcnt(0)" ::: "memory");
  DS_TR(fa.w[0][0][0], c.wrd0, 0); DS_TR(fa.w[0][0][1], c.wrd0, 1024);
  DS_TR(fa.w[0][1][0], c.wrd1, 0); DS_TR(fa.w[0][1][1], c.wrd1, 1024);
  DS_TR(fa.w[1][0][0], c.wrd0, 2048); DS_TR(fa.w[1][0][1], c.wrd0, 3072);
  DS_TR(fa.w[1][1][0], c.wrd1, 2048); DS_TR(fa.w[1][1][1], c.wrd1, 3072);
  G2Step<0>::run(acc, hfr, wr, fa, fb, c);
  asm volatile("s_nop 15\n\ts_nop 15" : "+a"(acc[0][0]), "+a"(acc[0][1]), "+a"(acc[1][0]), "+a"(acc[1][1]));
  const int m = tid >> 2, nq = (tid & 3) * 16;
  const float g = gin[m0 + m];
  __syncthreads();
  {
    char* pw = smem + wave * (64 * G2_PITCH) + r * G2_PITCH + 16 * h;
#pragma unroll
    for (int t = 0; t < 2; ++t)
#pragma unroll
      for (int mi = 0; mi < 2; ++mi)
#pragma unroll
        for (int gq = 0; gq < 4; ++gq)
          *(f32x4*)(pw + (32 * mi) * G2_PITCH + (32 * t + 8 * gq) * 4) =
              f32x4{acc[t][mi][4 * gq], acc[t][mi][4 * gq + 1], acc[t][mi][4 * gq + 2], acc[t][mi][4 * gq + 3]};
  }
  __syncthreads();
  {
    const float omg = rbf(1.0f - g);
    f32x4 sv[4];
#pragma unroll
    for (int j = 0; j < 4; ++j) {
      const char* pp = smem + m * G2_PITCH + (nq + 4 * j) * 4;
      sv[j] = (*(const f32x4*)pp + *(const f32x4*)(pp + 64 * G2_PITCH)) +
              (*(const f32x4*)(pp + 2 * 64 * G2_PITCH) + *(const f32x4*)(pp + 3 * 64 * G2_PITCH));
    }
    const u16* aimg = (const u16*)(smem + G2_A_OFF);
    unsigned o[8];
#pragma unroll
    for (int j = 0; j < 8; ++j) {
      const float a0 = bf2f(aimg[(nq + 2 * j) * 64 + m]);
      const float a1 = bf2f(aimg[(nq + 2 * j + 1) * 64 + m]);
      const unsigned bp = pk_bf16(sv[j >> 1][2 * (j & 1)], sv[j >> 1][2 * (j & 1) + 1]);
      const unsigned bt = pk_bf16(omg * bf_lo(bp), omg * bf_hi(bp));
      const unsigned ga = pk_bf16(g * a0, g * a1);
      o[j] = pk_bf16(bf_lo(ga) + bf_lo(bt), bf_hi(ga) + bf_hi(bt));
    }
    u16* op = out + (size_t)(m0 + m) * DM + n0 + nq;
    *(u32x4*)op = u32x4{o[0], o[1], o[2], o[3]};
    *(u32x4*)(op + 8) = u32x4{o[4], o[5], o[6], o[7]};
  }
}

extern "C" void kernel_launch(void* const* d_in, const int* in_sizes, int n_in, void* d_out, int out_size,
                              void* d_ws, size_t ws_size, hipStream_t stream) {
  const u16* u = (const u16*)d_in[0];
  const u16* logAr = (const u16*)d_in[1];
  const u16* Aim = (const u16*)d_in[2];
  const u16* Cre = (const u16*)d_in[3];
  const u16* Cim = (const u16*)d_in[4];
  const u16* logdt = (const u16*)d_in[5];
  const u16* Dskip = (const u16*)d_in[6];
  const u16* wg = (const u16*)d_in[8];
  const u16* wu = (const u16*)d_in[9];
  const u16* wd = (const u16*)d_in[10];
  const u16* gw = (const u16*)d_in[11];
  char* ws = (char*)d_ws;
  u16* hbuf = (u16*)ws;
  u16* aT = (u16*)(ws + (size_t)SEQ * FFP * 2);
  float* gbuf = (float*)(ws + (size_t)SEQ * FFP * 2 + (size_t)DM * SEQ * 2);
  k1_kernel<<<512, 256, 0, stream>>>(u, logAr, Aim, Cre, Cim, logdt, Dskip, wg, wu, gw, wd, hbuf, aT, gbuf
      );
  k2_kernel<<<256, 256, 0, stream>>>(hbuf, wd, aT, gbuf, (u16*)d_out);
}
```

```cpp
#include <hip/hip_runtime.h>
#include <hip/hip_bf16.h>
#include <stdint.h>
#include <type_traits>

typedef unsigned short u16;
typedef __attribute__((ext_vector_type(8))) short bf16x8;
typedef __attribute__((ext_vector_type(4))) short s16x4;
typedef __attribute__((ext_vector_type(4))) float f32x4;
typedef __attribute__((ext_vector_type(16))) float f32x16;
typedef __attribute__((ext_vector_type(4))) unsigned u32x4;
typedef __attribute__((ext_vector_type(2))) unsigned u32x2;
typedef __attribute__((ext_vector_type(2))) __bf16 bf16x2_t;
typedef __attribute__((ext_vector_type(2))) float f32x2;
typedef __attribute__((address_space(3))) s16x4* lds_s16x4_ptr;

constexpr int SEQ = 2048, DM = 512, NST = 128, FF = 1376, FFP = 1408;
constexpr int NSSM = 256;
constexpr int NKS = 88;

__device__ __forceinline__ unsigned pk_bf16(float lo, float hi) {
  f32x2 v = {lo, hi};
  bf16x2_t b = __builtin_convertvector(v, bf16x2_t);
  return __builtin_bit_cast(unsigned, b);
}
__device__ __forceinline__ float bf_lo(unsigned w) { return __uint_as_float(w << 16); }
__device__ __forceinline__ float bf_hi(unsigned w) { return __uint_as_float(w & 0xffff0000u); }
__device__ __forceinline__ float rbf(float x) { return bf_lo(pk_bf16(x, 0.f)); }
__device__ __forceinline__ float bf2f(u16 v) { return __uint_as_float(((unsigned)v) << 16); }

__device__ __forceinline__ float smul(float a, float b) { float r; asm("v_mul_f32 %0, %1, %2" : "=v"(r) : "v"(a), "v"(b)); return r; }
__device__ __forceinline__ float sadd(float a, float b) { float r; asm("v_add_f32 %0, %1, %2" : "=v"(r) : "v"(a), "v"(b)); return r; }
__device__ __forceinline__ float ssub(float a, float b) { float r; asm("v_sub_f32 %0, %1, %2" : "=v"(r) : "v"(a), "v"(b)); return r; }
__device__ __forceinline__ float sfma(float a, float b, float c) { float r; asm("v_fma_f32 %0, %1, %2, %3" : "=v"(r) : "v"(a), "v"(b), "v"(c)); return r; }
__device__ __forceinline__ float sfms(float a, float b, float c) { float r; asm("v_fma_f32 %0, -%1, %2, %3" : "=v"(r) : "v"(a), "v"(b), "v"(c)); return r; }
__device__ __forceinline__ float sfnn(float a, float b, float c) { float r; asm("v_fma_f32 %0, -%1, %2, -%3" : "=v"(r) : "v"(a), "v"(b), "v"(c)); return r; }
#define TRANS_FENCE8(a, o) asm volatile("s_nop 1" : "+v"(a[o]), "+v"(a[o + 1]), "+v"(a[o + 2]), "+v"(a[o + 3]), "+v"(a[o + 4]), "+v"(a[o + 5]), "+v"(a[o + 6]), "+v"(a[o + 7]))
__device__ __forceinline__ bf16x8 mk8(unsigned a, unsigned b, unsigned c, unsigned d) {
  u32x4 v = {a, b, c, d};
  return __builtin_bit_cast(bf16x8, v);
}
__device__ __forceinline__ bf16x8 cat4(s16x4 a, s16x4 b) {
  return __builtin_shufflevector(a, b, 0, 1, 2, 3, 4, 5, 6, 7);
}
__device__ __forceinline__ s16x4 tr_read(const char* smem_ptr) {
  return __builtin_amdgcn_ds_read_tr16_b64_v4i16((lds_s16x4_ptr)(smem_ptr));
}
__device__ __forceinline__ unsigned lds_addr(const void* p) {
  return (unsigned)(size_t)(const __attribute__((address_space(3))) char*)p;
}
__device__ __forceinline__ void glds16(const void* g, char* l) {
  __builtin_amdgcn_global_load_lds((const unsigned*)g, (unsigned*)l, 16, 0, 0);
}

#define STAMP2(i)
#define DBG2_ARG
#define DBG2_PASS
#define STAMP(i)
#define DBG_ARG
#define DBG_PASS
constexpr int U_COPY_B = 4160;
constexpr int U_CH_B = 4 * U_COPY_B;
constexpr int U_REGION_B = 2 * U_CH_B;
constexpr int KR_PART_B = 4576;
constexpr int KR_OFF = U_REGION_B;
constexpr int KQ_B = 8208;
constexpr int KX_OFF = 2 * KQ_B;
constexpr int SSM_LDS = KR_OFF + 4 * KR_PART_B;
static_assert(KX_OFF + 4 * 1536 <= U_REGION_B, "scratch must fit the U region");

#define DS_TR(dst, addr, off) asm volatile("ds_read_b64_tr_b16 %0, %1 offset:%2" : "=v"(dst) : "v"(addr), "i"(off) : "memory")
#define DS_B128(dst, addr, off) asm volatile("ds_read_b128 %0, %1 offset:%2" : "=v"(dst) : "v"(addr), "i"(off) : "memory")
#define DS_B64(dst, addr, off) asm volatile("ds_read_b64 %0, %1 offset:%2" : "=v"(dst) : "v"(addr), "i"(off) : "memory")
#define MFMA16(acc, a, b) asm volatile("v_mfma_f32_16x16x32_bf16 %0, %1, %2, %0" : "+v"(acc) : "v"(a), "v"(b))

struct ConvFrag {
  s16x4 a[8][2];
  bf16x8 bh[8], bl[8];
};
template <int RI, int K>
__device__ __forceinline__ void conv_read(ConvFrag& f, unsigned ua, unsigned kb) {
  if constexpr (K < 16) {
    DS_B64(f.a[K >> 1][K & 1], ua, 64 * (8 * (K >> 1) + RI) + 8 * (K & 1));
  } else if constexpr (K < 24) {
    DS_B128(f.bh[K - 16], kb, 64 * (56 - 8 * (K - 16) + RI));
  } else {
    DS_B128(f.bl[K - 24], kb, 64 * (56 - 8 * (K - 24) + RI) + KR_PART_B);
  }
}
template <int RI, int K>
__device__ __forceinline__ void conv_read_all(ConvFrag& f, unsigned ua, unsigned kb) {
  conv_read<RI, K>(f, ua, kb);
  if constexpr (K + 1 < 32) conv_read_all<RI, K + 1>(f, ua, kb);
}
#define CONV_WAIT(f)                                                                                                  \
  do {                                                                                                                \
    asm volatile("s_waitcnt lgkmcnt(0)"                                                                               \
                 : "+v"(f.a[0][0]), "+v"(f.a[0][1]), "+v"(f.a[1][0]), "+v"(f.a[1][1]), "+v"(f.a[2][0]), "+v"(f.a[2][1]), \
                   "+v"(f.a[3][0]), "+v"(f.a[3][1]), "+v"(f.a[4][0]), "+v"(f.a[4][1]), "+v"(f.a[5][0]), "+v"(f.a[5][1]), \
                   "+v"(f.a[6][0]), "+v"(f.a[6][1]), "+v"(f.a[7][0]), "+v"(f.a[7][1])::"memory");                     \
    asm volatile("" : "+v"(f.bh[0]), "+v"(f.bh[1]), "+v"(f.bh[2]), "+v"(f.bh[3]), "+v"(f.bh[4]), "+v"(f.bh[5]),        \
                 "+v"(f.bh[6]), "+v"(f.bh[7]), "+v"(f.bl[0]), "+v"(f.bl[1]), "+v"(f.bl[2]), "+v"(f.bl[3]),            \
                 "+v"(f.bl[4]), "+v"(f.bl[5]), "+v"(f.bl[6]), "+v"(f.bl[7])::"memory");                              \
  } while (0)
constexpr int conv_tau(int P) { int t = 0; while (P >= 8 - t) { P -= 8 - t; ++t; } return t; }
constexpr int conv_sig(int P) { int t = 0; while (P >= 8 - t) { P -= 8 - t; ++t; } return P; }
#define CONV_PAIR(acc, a, bl, bh) \
  asm volatile("v_mfma_f32_16x16x32_bf16 %0, %1, %2, %0\n\tv_mfma_f32_16x16x32_bf16 %0, %1, %3, %0" : "+v"(acc) : "v"(a), "v"(bl), "v"(bh))
#define CONV_PAIR_RD(OP, acc, a, bl, bh, dst, addr, off)                                                      \
  asm volatile("v_mfma_f32_16x16x32_bf16 %0, %2, %3, %0\n\t" OP " %1, %5 offset:%6\n\tv_mfma_f32_16x16x32_bf16 %0, %2, %4, %0" \
               : "+v"(acc), "=&v"(dst) : "v"(a), "v"(bl), "v"(bh), "v"(addr), "i"(off) : "memory")
template <int RI, int P>
__device__ __forceinline__ void conv_pairs(f32x4 (&acc)[8], const bf16x8 (&A)[8], ConvFrag& cur, ConvFrag& nxt, unsigned ua,
                                           unsigned kb) {
  constexpr int T = conv_tau(P), SG = conv_sig(P);
  if constexpr (RI < 3 && P < 16) {
    CONV_PAIR_RD("ds_read_b64", acc[T + SG], A[SG], cur.bl[T], cur.bh[T], nxt.a[P >> 1][P & 1], ua, 64 * (8 * (P >> 1) + RI + 1) + 8 * (P & 1));
  } else if constexpr (RI < 3 && P < 24) {
    CONV_PAIR_RD("ds_read_b128", acc[T + SG], A[SG], cur.bl[T], cur.bh[T], nxt.bh[P - 16], kb, 64 * (56 - 8 * (P - 16) + RI + 1));
  } else if constexpr (RI < 3 && P < 32) {
    CONV_PAIR_RD("ds_read_b128", acc[T + SG], A[SG], cur.bl[T], cur.bh[T], nxt.bl[P - 24], kb, 64 * (56 - 8 * (P - 24) + RI + 1) + KR_PART_B);
  } else {
    CONV_PAIR(acc[T + SG], A[SG], cur.bl[T], cur.bh[T]);
  }
  if constexpr (P + 1 < 36) conv_pairs<RI, P + 1>(acc, A, cur, nxt, ua, kb);
}
template <int RI>
__device__ __forceinline__ void conv_rho(f32x4 (&acc)[8], ConvFrag& cur, ConvFrag& nxt, unsigned ua, unsigned kb) {
  CONV_WAIT(cur);
  bf16x8 A[8];
#pragma unroll
  for (int i = 0; i < 8; ++i) A[i] = cat4(cur.a[i][0], cur.a[i][1]);
  conv_pairs<RI, 0>(acc, A, cur, nxt, ua, kb);
  if constexpr (RI < 3) conv_rho<RI + 1>(acc, nxt, cur, ua, kb);
}

__device__ __forceinline__ void split_pair(float v0, float v1, unsigned& hi, unsigned& lo) {
  hi = pk_bf16(v0, v1);
  lo = pk_bf16(ssub(v0, bf_lo(hi)), ssub(v1, bf_hi(hi)));
}

__device__ __forceinline__ void ssm_unit(int w, const u16* __restrict__ u, const u16* __restrict__ logAr,
                                         const u16* __restrict__ Aim, const u16* __restrict__ Cre,
                                         const u16* __restrict__ Cim, const u16* __restrict__ logdt,
                                         const u16* __restrict__ Dskip, u16* __restrict__ aT, char* smem DBG_ARG) {
  const int tid = threadIdx.x, lane = tid & 63;
  const int wave = __builtin_amdgcn_readfirstlane(tid >> 6);
  const int ch = wave >> 1, w2 = wave & 1;
  const int d0 = 2 * w;
  const int d = d0 + ch;

  const int pidx = d0 * NST + tid;
  const u16 r_lar = logAr[pidx], r_aim = Aim[pidx], r_cre = Cre[pidx], r_cim = Cim[pidx], r_ldt = logdt[d], r_dsk = Dskip[d];
  const unsigned* u32p = (const unsigned*)(u + d0);
  unsigned uw[8], up[3];
#pragma unroll
  for (int i = 0; i < 3; ++i) up[i] = 0;
  if (lane == 0 && tid > 0) {
#pragma unroll
    for (int i = 0; i < 3; ++i) up[i] = u32p[(size_t)(8 * tid - 3 + i) * (DM / 2)];
  }
  const unsigned uoff = (unsigned)tid * 8192u;
  {
    const float dt = rbf(expf(bf2f(r_ldt)));
    const float are = rbf(expf(bf2f(r_lar)));
    f32x4 prm;
    prm[0] = (-are * dt) * 1.44269504088896340736f;
    prm[1] = (bf2f(r_aim) * dt) * 0.15915494309189533577f;
    prm[2] = 2.0f * (bf2f(r_cre) * dt);
    prm[3] = 2.0f * (bf2f(r_cim) * dt);
    *(f32x4*)(smem + KR_OFF + tid * 16) = prm;
  }
  __syncthreads();

  const int r = lane & 31, h = lane >> 5;
  f32x16 dacc = {};
  {
    int p1i;
    if (r < 8) p1i = r;
    else if (r < 12) p1i = 256 + 2 * (r - 8);
    else if (r < 16) p1i = 512 + 256 * ((r - 12) >> 1) + 4 * ((r - 12) & 1);
    else if (r < 21) p1i = 1024 + 256 * (r - 16);
    else p1i = 0;
    const float p1 = (float)p1i, p2 = (float)(8 * r);
    const char* prm_base = smem + KR_OFF + (ch * 128 + 64 * w2 + 4 * h) * 16;
#pragma unroll
    for (int ks = 0; ks < 8; ++ks) {
      if (ks >= 4) {
        asm volatile("global_load_dword %0, %1, %2 offset:%3" : "=v"(uw[2 * ks - 8]) : "v"(uoff), "s"((const char*)u32p + ((2 * ks - 8) >> 2) * 4096), "i"(((2 * ks - 8) & 3) * 1024) : "memory");
        asm volatile("global_load_dword %0, %1, %2 offset:%3" : "=v"(uw[2 * ks - 7]) : "v"(uoff), "s"((const char*)u32p + ((2 * ks - 7) >> 2) * 4096), "i"(((2 * ks - 7) & 3) * 1024) : "memory");
      }
      f32x4 prm[4];
#pragma unroll
      for (int q = 0; q < 4; ++q) prm[q] = *(const f32x4*)(prm_base + (8 * ks + q) * 16);
      float ea[4], eb[4], fa[4], fb[4];
#pragma unroll
      for (int q = 0; q < 4; ++q) {
        ea[q] = __builtin_amdgcn_exp2f(smul(prm[q][0], p1)); eb[q] = __builtin_amdgcn_exp2f(smul(prm[q][0], p2));
        fa[q] = __builtin_amdgcn_fractf(smul(prm[q][1], p1)); fb[q] = __builtin_amdgcn_fractf(smul(prm[q][1], p2));
      }
      float ac[4], as[4], bc[4], bs[4];
#pragma unroll
      for (int q = 0; q < 4; ++q) {
        ac[q] = __builtin_amdgcn_cosf(fa[q]); as[q] = __builtin_amdgcn_sinf(fa[q]);
        bc[q] = __builtin_amdgcn_cosf(fb[q]); bs[q] = __builtin_amdgcn_sinf(fb[q]);
      }
      asm volatile("s_nop 1" : "+v"(ea[0]), "+v"(ea[1]), "+v"(ea[2]), "+v"(ea[3]), "+v"(eb[0]), "+v"(eb[1]), "+v"(eb[2]), "+v"(eb[3]),
                   "+v"(ac[0]), "+v"(ac[1]), "+v"(ac[2]), "+v"(ac[3]), "+v"(as[0]), "+v"(as[1]), "+v"(as[2]), "+v"(as[3]),
                   "+v"(bc[0]), "+v"(bc[1]), "+v"(bc[2]), "+v"(bc[3]), "+v"(bs[0]), "+v"(bs[1]), "+v"(bs[2]), "+v"(bs[3]));
      unsigned ah[4], al[4], bh[4], bl[4];
#pragma unroll
      for (int q = 0; q < 4; ++q) {
        const float cr = smul(prm[q][2], ea[q]), ci = smul(prm[q][3], ea[q]);
        const float gre = sfms(ci, as[q], smul(cr, ac[q]));
        const float gmi = sfnn(ci, ac[q], smul(cr, as[q]));
        split_pair(gre, gmi, ah[q], al[q]);
        split_pair(smul(bc[q], eb[q]), smul(bs[q], eb[q]), bh[q], bl[q]);
      }
      const bf16x8 Ah = mk8(ah[0], ah[1], ah[2], ah[3]), Al = mk8(al[0], al[1], al[2], al[3]);
      const bf16x8 Bh = mk8(bh[0], bh[1], bh[2], bh[3]), Bl = mk8(bl[0], bl[1], bl[2], bl[3]);
      dacc = __builtin_amdgcn_mfma_f32_32x32x16_bf16(Al, Bh, dacc, 0, 0, 0);
      dacc = __builtin_amdgcn_mfma_f32_32x32x16_bf16(Ah, Bl, dacc, 0, 0, 0);
      dacc = __builtin_amdgcn_mfma_f32_32x32x16_bf16(Ah, Bh, dacc, 0, 0, 0);
    }
  }
  {
    float* xo = (float*)(smem + KX_OFF + wave * 1536) + lane;
    const float* xi = (const float*)(smem + KX_OFF + (wave ^ 1) * 1536) + lane;
    float mine[6];
    if (w2 == 0) {
#pragma unroll
      for (int e = 0; e < 6; ++e) { xo[e * 64] = dacc[6 + e]; mine[e] = dacc[e]; }
    } else {
#pragma unroll
      for (int e = 0; e < 6; ++e) { xo[e * 64] = dacc[e]; mine[e] = dacc[6 + e]; }
    }
    __syncthreads();
    unsigned* Kq = (unsigned*)(smem + ch * KQ_B);
    unsigned pq[6];
#pragma unroll
    for (int e = 0; e < 6; ++e) {
      const float v = sadd(mine[e], xi[e * 64]);
      const unsigned hi = pk_bf16(v, 0.f);
      pq[e] = pk_bf16(0.f, ssub(v, bf_lo(hi))) | hi;
    }
    if (w2 == 0) {
#pragma unroll
      for (int e = 0; e < 4; ++e) Kq[4 * h + e + 8 * r] = pq[e];
      Kq[(h ? 512 : 256) + 8 * r] = pq[4];
      Kq[(h ? 516 : 258) + 8 * r] = pq[5];
    } else {
      Kq[(h ? 768 : 260) + 8 * r] = pq[0];
      Kq[(h ? 772 : 262) + 8 * r] = pq[1];
      if (h == 0) {
#pragma unroll
        for (int e = 0; e < 4; ++e) Kq[1024 + 256 * e + 8 * r] = pq[2 + e];
      } else if (r == 0) {
        Kq[2048] = pq[2];
      }
    }
  }
  __syncthreads();

  {
    const int t = tid & 127;
    const unsigned* Kq = (const unsigned*)(smem + ch * KQ_B);
    unsigned* krh = (unsigned*)(smem + KR_OFF + (ch * 2) * KR_PART_B);
    unsigned* krl = (unsigned*)((char*)krh + KR_PART_B);
    unsigned q0[8], q1[8];
#pragma unroll
    for (int m = 0; m < 8; ++m) {
      const int sh = m < 4 ? 3 : (m < 6 ? 2 : (m < 7 ? 1 : 0));
      const int l0 = 2047 - 2 * (t + 128 * m), l1 = l0 - 1;
      int pa = l0, pb = l1;
      if (sh) {
        pa = (l0 + (1 << (sh - 1)) - 1 + ((l0 >> sh) & 1)) & ~((1 << sh) - 1);
        pb = (l1 + (1 << (sh - 1)) - 1 + ((l1 >> sh) & 1)) & ~((1 << sh) - 1);
      }
      q0[m] = Kq[pa];
      q1[m] = Kq[pb];
    }
#pragma unroll
    for (int m = 0; m < 8; ++m) {
      krh[t + 128 * m] = __builtin_amdgcn_perm(q1[m], q0[m], 0x05040100u);
      krl[t + 128 * m] = __builtin_amdgcn_perm(q1[m], q0[m], 0x07060302u);
    }
    if (t < 120) { krh[1024 + t] = 0; krl[1024 + t] = 0; }
  }
  __syncthreads();

  asm volatile("s_waitcnt vmcnt(0)" : "+v"(uw[0]), "+v"(uw[1]), "+v"(uw[2]), "+v"(uw[3]), "+v"(uw[4]), "+v"(uw[5]), "+v"(uw[6]), "+v"(uw[7])::"memory");
  {
    unsigned e[11];
#pragma unroll
    for (int i = 0; i < 3; ++i) {
      const unsigned nb = __shfl_up(uw[5 + i], 1);
      e[i] = (lane == 0) ? up[i] : nb;
    }
#pragma unroll
    for (int i = 0; i < 8; ++i) e[3 + i] = uw[i];
    unsigned p0[10], p1[10];
#pragma unroll
    for (int i = 0; i < 10; ++i) {
      p0[i] = __builtin_amdgcn_perm(e[i + 1], e[i], 0x05040100u);
      p1[i] = __builtin_amdgcn_perm(e[i + 1], e[i], 0x07060302u);
    }
#pragma unroll
    for (int c = 0; c < 4; ++c) {
      *(u32x4*)(smem + c * U_COPY_B + (tid + 2) * 16) = u32x4{p0[c], p0[c + 2], p0[c + 4], p0[c + 6]};
      *(u32x4*)(smem + U_CH_B + c * U_COPY_B + (tid + 2) * 16) = u32x4{p1[c], p1[c + 2], p1[c + 4], p1[c + 6]};
    }
    if (tid < 16) *(u32x4*)(smem + (tid >> 1) * U_COPY_B + (tid & 1) * 16) = u32x4{0, 0, 0, 0};
  }
  __syncthreads();

  f32x4 acc[8];
#pragma unroll
  for (int t = 0; t < 8; ++t) acc[t] = f32x4{0.f, 0.f, 0.f, 0.f};
  asm volatile("s_nop 4" : "+v"(acc[0]), "+v"(acc[1]), "+v"(acc[2]), "+v"(acc[3]), "+v"(acc[4]), "+v"(acc[5]), "+v"(acc[6]), "+v"(acc[7]));
  {
    const int r16 = lane & 15, hq = lane >> 4;
    const unsigned sb = lds_addr(smem);
    const unsigned ua = sb + ch * U_CH_B + (r16 & 3) * U_COPY_B + 2 * (4 + (r16 & ~3) + 8 * hq) + 64 * 4 * w2;
    const unsigned kb = sb + KR_OFF + (ch * 2) * KR_PART_B + 2 * (2032 - 16 * r16 + 8 * hq) - 64 * 56 + 64 * 4 * w2;
    ConvFrag f0, f1;
    conv_read_all<0, 0>(f0, ua, kb);
    conv_rho<0>(acc, f0, f1, ua, kb);
  }
  asm volatile("s_nop 15" : "+v"(acc[0]), "+v"(acc[1]), "+v"(acc[2]), "+v"(acc[3]), "+v"(acc[4]), "+v"(acc[5]), "+v"(acc[6]), "+v"(acc[7]));
  __syncthreads();
  {
    char* xw = smem + KR_OFF + (ch * 2 + w2) * 4096 + lane * 16;
    const char* xr = smem + KR_OFF + (ch * 2 + (w2 ^ 1)) * 4096 + lane * 16;
    f32x4 y[4];
    if (w2 == 0) {
#pragma unroll
      for (int i = 0; i < 4; ++i) { *(f32x4*)(xw + i * 1024) = acc[4 + i]; y[i] = acc[i]; }
    } else {
#pragma unroll
      for (int i = 0; i < 4; ++i) { *(f32x4*)(xw + i * 1024) = acc[i]; y[i] = acc[4 + i]; }
    }
    __syncthreads();
    const float Dk = bf2f(r_dsk);
#pragma unroll
    for (int i = 0; i < 4; ++i) {
      const f32x4 o = *(const f32x4*)(xr + i * 1024);
      const int l0 = 256 * (4 * w2 + i) + 16 * (lane & 15) + 4 * (lane >> 4);
      const u32x2 uu = *(const u32x2*)(smem + ch * U_CH_B + 3 * U_COPY_B + 2 * (16 + l0));
      const unsigned y01 = pk_bf16(sadd(y[i][0], o[0]), sadd(y[i][1], o[1])), y23 = pk_bf16(sadd(y[i][2], o[2]), sadd(y[i][3], o[3]));
      const unsigned s01 = pk_bf16(smul(bf_lo(uu[0]), Dk), smul(bf_hi(uu[0]), Dk)), s23 = pk_bf16(smul(bf_lo(uu[1]), Dk), smul(bf_hi(uu[1]), Dk));
      { const u32x2 av = u32x2{pk_bf16(sadd(bf_lo(y01), bf_lo(s01)), sadd(bf_hi(y01), bf_hi(s01))),
                                                  pk_bf16(sadd(bf_lo(y23), bf_lo(s23)), sadd(bf_hi(y23), bf_hi(s23)))};
        asm volatile("global_store_dwordx2 %0, %1, off sc1\n\ts_nop 1" ::"v"(aT + (size_t)d * SEQ + l0), "v"(av) : "memory"); }
    }
  }
}

__device__ __forceinline__ float gelu_tanh(float x) {
  const float z = 0.7978845608028654f * (x + 0.044715f * x * x * x);
  const float e = __builtin_amdgcn_exp2f(-2.8853900817779268f * z);
  return x * __builtin_amdgcn_rcpf(1.0f + e);
}

constexpr int G1_WSLOT_B = 12288;
constexpr int G1_X_OFF = 3 * G1_WSLOT_B;
constexpr int G1_LDS = G1_X_OFF + 32768;

constexpr int g1_pos(bool isx, int id) {
  if (isx && id == 0) return 4;
  if (!isx && id == 0) return 7;
  if (!isx && id == 1) return 10;
  if (isx && id == 1) return 14;
  if (!isx && id == 2) return 17;
  if (!isx && id == 3) return 20;
  if (!isx && id == 4) return 23;
  if (isx && id == 2) return 27;
  int n = 27;
  for (int t = 0; t < 32; ++t) {
    if ((t & 1) == 0 && t / 2 + 5 < 16) { n += 3; if (!isx && t / 2 + 5 == id) return n; }
    if ((t & 3) == 1 && t / 4 + 3 < 8) { n += 4; if (isx && t / 4 + 3 == id) return n; }
  }
  return -1;
}
constexpr int g1_issued_before(int step) {
  int n = 27;
  for (int t = 0; t < step; ++t) {
    if ((t & 1) == 0 && t / 2 + 5 < 16) n += 3;
    if ((t & 3) == 1 && t / 4 + 3 < 8) n += 4;
  }
  return n;
}

#define GLOAD128(dst, ptr) asm volatile("global_load_dwordx4 %0, %1, off" : "=v"(dst) : "v"(ptr) : "memory")
#define GLOAD128S(dst, voff, sbase) asm volatile("global_load_dwordx4 %0, %1, %2" : "=v"(dst) : "v"(voff), "s"(sbase) : "memory")
#define GLOAD128SA(dst, voff, sbase) asm volatile("global_load_dwordx4 %0, %1, %2" : "=a"(dst) : "v"(voff), "s"(sbase) : "memory")
#define GLOAD128A(dst, ptr) asm volatile("global_load_dwordx4 %0, %1, off" : "=a"(dst) : "v"(ptr) : "memory")
#define DS_W128(addr, val, off) asm volatile("ds_write_b128 %0, %1 offset:%2" ::"v"(addr), "v"(val), "i"(off) : "memory")
#define MFMA32(acc, a, b) asm volatile("v_mfma_f32_32x32x16_bf16 %0, %1, %2, %0" : "+v"(acc) : "v"(a), "v"(b))

struct G1Ctx {
  const char* wbase;
  const char* xbase;
  unsigned wo[3], xo;
  unsigned woff, woff_o, ww[3], xw, xw1, xrd2[2][4];
  unsigned junk[4];
};
struct G1Frag {
  s16x4 w[3][2];
  bf16x8 x[2];
};

#define G1_RD_W(f, t, OFF)                                                        \
  do {                                                                            \
    if ((t) & 1) { DS_TR(f.w[t][0], c.woff_o, (OFF) + (t) * 64); DS_TR(f.w[t][1], c.woff_o, (OFF) + (t) * 64 + 4 * 384); } \
    else { DS_TR(f.w[t][0], c.woff, (OFF) + (t) * 64); DS_TR(f.w[t][1], c.woff, (OFF) + (t) * 64 + 4 * 384); }            \
  } while (0)
#define G1_RD_X(f, mi, N) DS_B128(f.x[mi], c.xrd2[mi][(N) & 3], (((N) >> 2) & 1) * 16384)
template <int N>
__device__ __forceinline__ void g1_read(G1Frag& f, const G1Ctx& c) {
  constexpr int OFF = ((N >> 1) % 3) * G1_WSLOT_B + (N & 1) * 16 * 384;
  G1_RD_X(f, 0, N); G1_RD_X(f, 1, N);
  G1_RD_W(f, 0, OFF); G1_RD_W(f, 1, OFF); G1_RD_W(f, 2, OFF);
}
#define G1_WAIT_FRAG(f, STR)                                                                                       \
  asm volatile(STR : "+v"(f.w[0][0]), "+v"(f.w[0][1]), "+v"(f.w[1][0]), "+v"(f.w[1][1]), "+v"(f.w[2][0]),        \
               "+v"(f.w[2][1]), "+v"(f.x[0]), "+v"(f.x[1])::"memory")

template <int N>
struct G1Step {
  static __device__ __forceinline__ void run(f32x16 (&acc)[6], u32x4 (&xr)[2][4], u32x4 (&wr)[3][3], G1Frag& cur,
                                             G1Frag& nxt, G1Ctx& c) {
    constexpr int ISS = g1_issued_before(N);
    constexpr int S = N >> 1;
    constexpr bool RD = N + 1 < 32;
    constexpr int OFF = (((N + 1) >> 1) % 3) * G1_WSLOT_B + ((N + 1) & 1) * 16 * 384;
    if constexpr ((N & 1) == 0 && N > 0) G1_WAIT_FRAG(cur, "s_waitcnt lgkmcnt(0)\n\ts_barrier");
    else G1_WAIT_FRAG(cur, "s_waitcnt lgkmcnt(0)");
    bf16x8 b[3];
#pragma unroll
    for (int t = 0; t < 3; ++t) b[t] = cat4(cur.w[t][0], cur.w[t][1]);
    MFMA32(acc[0], b[0], cur.x[0]);
    if constexpr (RD) { G1_RD_X(nxt, 0, N + 1); G1_RD_W(nxt, 0, OFF); }
    MFMA32(acc[3], b[0], cur.x[1]);
    if constexpr (RD) { G1_RD_X(nxt, 1, N + 1); G1_RD_W(nxt, 1, OFF); }
    MFMA32(acc[1], b[1], cur.x[0]);
    if constexpr (RD) { G1_RD_W(nxt, 2, OFF); }
    MFMA32(acc[4], b[1], cur.x[1]);
    if constexpr ((N & 1) == 0) {
      if constexpr (S + 2 < 16) {
        constexpr int R = (S + 2) % 3;
        constexpr int NW = ISS - g1_pos(false, S + 2);
        asm volatile("s_waitcnt vmcnt(%3)" : "+v"(wr[R][0]), "+v"(wr[R][1]), "+v"(wr[R][2]) : "i"(NW) : "memory");
#pragma unroll
        for (int i = 0; i < 3; ++i) DS_W128(c.ww[i], wr[R][i], R * G1_WSLOT_B);
      }
    }
    if constexpr ((N & 3) == 1) {
      constexpr int B = N / 4 + 1;
      if constexpr (B < 8) {
        constexpr int NW = ISS - g1_pos(true, B);
        asm volatile("s_waitcnt vmcnt(%4)" : "+v"(xr[B & 1][0]), "+v"(xr[B & 1][1]), "+v"(xr[B & 1][2]), "+v"(xr[B & 1][3]) : "i"(NW) : "memory");
#pragma unroll
        for (int i = 0; i < 4; ++i) { if (i & 1) DS_W128(c.xw1, xr[B & 1][i], (B & 1) * 16384 + i * 1024); else DS_W128(c.xw, xr[B & 1][i], (B & 1) * 16384 + i * 1024); }
      }
    }
    MFMA32(acc[2], b[2], cur.x[0]);
    if constexpr ((N & 1) == 0 && S + 5 < 16) {
#pragma unroll
      for (int i = 0; i < 3; ++i) GLOAD128S(wr[(S + 5) % 3][i], c.wo[i], c.wbase + (size_t)(S + 5) * 32 * FF * 2);
    }
    if constexpr ((N & 3) == 1 && N / 4 + 3 < 8) {
      constexpr int B = N / 4 + 1;
#pragma unroll
      for (int i = 0; i < 4; ++i) GLOAD128S(xr[B & 1][i], c.xo, c.xbase + (size_t)(8 * i) * DM * 2 + (B + 2) * 128);
    }
    MFMA32(acc[5], b[2], cur.x[1]);
    if constexpr (N + 1 < 32) G1Step<N + 1>::run(acc, xr, wr, nxt, cur, c);
  }
};

__device__ __forceinline__ void gemm1_tile(int t, const u16* __restrict__ x, const u16* __restrict__ wg,
                                           const u16* __restrict__ wu, u16* __restrict__ hf, char* smem DBG_ARG) {
  const int tid = threadIdx.x, lane = tid & 63;
  const int wave = __builtin_amdgcn_readfirstlane(tid >> 6);
  const int fb = t >> 4, mb = t & 15;
  const int f0 = 96 * fb;
  const int wm = wave & 1, isup = wave >> 1;
  G1Ctx c;
  const unsigned sbase = lds_addr(smem);
  c.wbase = (const char*)(isup ? wu : wg);
#pragma unroll
  for (int i = 0; i < 3; ++i) {
    const int id = 192 * (wave & 1) + 64 * i + lane;
    const int row = id / 12, cc = id % 12;
    int col = f0 + 8 * cc;
    if (col > FF - 8) col = FF - 8;
    c.wo[i] = (unsigned)(row * FF + col) * 2u;
    c.ww[i] = sbase + row * 384 + ((cc + 12 * isup) ^ (((row >> 1) & 1) << 2)) * 16;
  }
  c.xbase = (const char*)(x + (size_t)(128 * mb + 32 * wave) * DM);
  c.xo = (unsigned)((lane >> 3) * DM + (lane & 7) * 8) * 2u;
  const unsigned xl = sbase + G1_X_OFF;
  c.xw = xl + (32 * wave + (lane >> 3)) * 128 + (((lane & 7) ^ (lane >> 4)) * 16);
  c.xw1 = xl + (32 * wave + (lane >> 3)) * 128 + (((lane & 7) ^ ((lane >> 4) + 4)) * 16);
  const int r = lane & 31, h = lane >> 5;
#pragma unroll
  for (int i = 0; i < 4; ++i) {
    c.xrd2[0][i] = xl + (64 * wm + 32 * isup + r) * 128 + (((2 * i + h) ^ ((r >> 1) & 7)) * 16);
    c.xrd2[1][i] = xl + (64 * wm + 32 * (1 - isup) + r) * 128 + (((2 * i + h) ^ ((r >> 1) & 7)) * 16);
  }
  const int blk = (lane >> 4) & 1, q = (lane & 15) >> 2, p = lane & 3;
  const int sw = (q >> 1) & 1;
  {
    const unsigned e = sbase + (8 * h + q) * 384 + (2 * blk + (p >> 1) + 4 * sw) * 16 + 8 * (p & 1);
    const unsigned o = sbase + (8 * h + q) * 384 + (2 * blk + (p >> 1) - 4 * sw) * 16 + 8 * (p & 1);
    c.woff = isup ? o + 192 : e;
    c.woff_o = isup ? e + 192 : o;
  }

  f32x16 acc[6];
#pragma unroll
  for (int i = 0; i < 6; ++i) acc[i] = f32x16{};
  asm volatile("s_nop 4" : "+v"(acc[0]), "+v"(acc[1]), "+v"(acc[2]), "+v"(acc[3]), "+v"(acc[4]), "+v"(acc[5]));
  u32x4 xr[2][4], wr[3][3];
  G1Frag fa, fb2;
#define G1_ISSUE_X(R, B) _Pragma("unroll") for (int i = 0; i < 4; ++i) GLOAD128S(xr[R][i], c.xo, c.xbase + (size_t)(8 * i) * DM * 2 + (B) * 128)
#define G1_ISSUE_W(R, S) _Pragma("unroll") for (int i = 0; i < 3; ++i) GLOAD128S(wr[R][i], c.wo[i], c.wbase + (size_t)(S) * 32 * FF * 2)
  G1_ISSUE_X(0, 0); G1_ISSUE_W(0, 0); G1_ISSUE_W(1, 1); G1_ISSUE_X(1, 1); G1_ISSUE_W(2, 2);
#define G1_EXTRA 0
  asm volatile("s_waitcnt vmcnt(%10)"
               : "+v"(wr[0][0]), "+v"(wr[0][1]), "+v"(wr[0][2]), "+v"(wr[1][0]), "+v"(wr[1][1]), "+v"(wr[1][2]),
                 "+v"(xr[0][0]), "+v"(xr[0][1]), "+v"(xr[0][2]), "+v"(xr[0][3]) : "i"(7 + G1_EXTRA) : "memory");
#pragma unroll
  for (int i = 0; i < 3; ++i) { DS_W128(c.ww[i], wr[0][i], 0); DS_W128(c.ww[i], wr[1][i], G1_WSLOT_B); }
#pragma unroll
  for (int i = 0; i < 4; ++i) { if (i & 1) DS_W128(c.xw1, xr[0][i], i * 1024); else DS_W128(c.xw, xr[0][i], i * 1024); }
  G1_ISSUE_W(0, 3); G1_ISSUE_W(1, 4); G1_ISSUE_X(0, 2);
  asm volatile("s_waitcnt lgkmcnt(0)\n\ts_barrier" ::: "memory");
  g1_read<0>(fa, c);
  G1Step<0>::run(acc, xr, wr, fa, fb2, c);
  asm volatile("s_nop 15\n\ts_nop 15" : "+v"(acc[0]), "+v"(acc[1]), "+v"(acc[2]), "+v"(acc[3]), "+v"(acc[4]), "+v"(acc[5]));

  unsigned keep[3][8];
  {
    unsigned give[3][8];
#pragma unroll
    for (int tt = 0; tt < 3; ++tt)
#pragma unroll
      for (int e = 0; e < 8; ++e) give[tt][e] = pk_bf16(acc[3 + tt][2 * e], acc[3 + tt][2 * e + 1]);
    __syncthreads();
    char* xo = smem + wave * 6144 + lane * 16;
#pragma unroll
    for (int tt = 0; tt < 3; ++tt) {
      *(u32x4*)(xo + (2 * tt) * 1024) = u32x4{give[tt][0], give[tt][1], give[tt][2], give[tt][3]};
      *(u32x4*)(xo + (2 * tt + 1) * 1024) = u32x4{give[tt][4], give[tt][5], give[tt][6], give[tt][7]};
    }
  }
#pragma unroll
  for (int tt = 0; tt < 3; ++tt)
#pragma unroll
    for (int e = 0; e < 8; ++e) keep[tt][e] = pk_bf16(acc[tt][2 * e], acc[tt][2 * e + 1]);
  __syncthreads();
  {
    const char* xi = smem + (wave ^ 2) * 6144 + lane * 16;
    const int mt = 4 * mb + 2 * wm + isup;
    u16* hp = hf + ((size_t)(mt * NKS + 6 * fb) * 64 + lane) * 8;
    auto finish = [&](auto ISUP) {
#pragma unroll
      for (int tt = 0; tt < 3; ++tt) {
        const int ft = 3 * fb + tt;
        if (ft < FF / 32) {
          const u32x4 o0 = *(const u32x4*)(xi + (2 * tt) * 1024);
          const u32x4 o1 = *(const u32x4*)(xi + (2 * tt + 1) * 1024);
          unsigned gp[8], up[8];
#pragma unroll
          for (int e = 0; e < 8; ++e) {
            const unsigned ot = e < 4 ? o0[e & 3] : o1[e & 3];
            gp[e] = decltype(ISUP)::value ? ot : keep[tt][e];
            up[e] = decltype(ISUP)::value ? keep[tt][e] : ot;
          }
          f32x2 xg[8], ar[8];
#pragma unroll
          for (int e = 0; e < 8; ++e) xg[e] = f32x2{bf_lo(gp[e]), bf_hi(gp[e])};
#pragma unroll
          for (int e = 0; e < 8; ++e) ar[e] = xg[e] * xg[e];
#pragma unroll
          for (int e = 0; e < 8; ++e) ar[e] = __builtin_elementwise_fma(ar[e], f32x2{-0.10294323958f, -0.10294323958f}, f32x2{-2.30220819814f, -2.30220819814f});
#pragma unroll
          for (int e = 0; e < 8; ++e) ar[e] = ar[e] * xg[e];
#pragma unroll
          for (int e = 0; e < 8; ++e) ar[e] = f32x2{__builtin_amdgcn_exp2f(ar[e][0]), __builtin_amdgcn_exp2f(ar[e][1])};
#pragma unroll
          for (int e = 0; e < 8; ++e) ar[e] = ar[e] + f32x2{1.0f, 1.0f};
#pragma unroll
          for (int e = 0; e < 8; ++e) ar[e] = f32x2{__builtin_amdgcn_rcpf(ar[e][0]), __builtin_amdgcn_rcpf(ar[e][1])};
#pragma unroll
          for (int e = 0; e < 8; ++e) ar[e] = ar[e] * xg[e];
          unsigned o[8];
#pragma unroll
          for (int e = 0; e < 8; ++e) o[e] = pk_bf16(ar[e][0], ar[e][1]);
#pragma unroll
          for (int e = 0; e < 8; ++e) ar[e] = f32x2{bf_lo(o[e]), bf_hi(o[e])} * f32x2{bf_lo(up[e]), bf_hi(up[e])};
#pragma unroll
          for (int e = 0; e < 8; ++e) o[e] = pk_bf16(ar[e][0], ar[e][1]);
          { const u32x4 v0 = u32x4{o[0], o[1], o[2], o[3]}, v1 = u32x4{o[4], o[5], o[6], o[7]};
            asm volatile("global_store_dwordx4 %0, %1, off sc1\n\ts_nop 1" ::"v"(hp + (2 * tt) * 512), "v"(v0) : "memory");
            asm volatile("global_store_dwordx4 %0, %1, off sc1\n\ts_nop 1" ::"v"(hp + (2 * tt + 1) * 512), "v"(v1) : "memory"); }
        } else if (ft < NKS / 2) {
          *(u32x4*)(hp + (2 * tt) * 512) = u32x4{0, 0, 0, 0};
          *(u32x4*)(hp + (2 * tt + 1) * 512) = u32x4{0, 0, 0, 0};
        }
      }
    };
    if (isup) finish(std::integral_constant<bool, true>{});
    else finish(std::integral_constant<bool, false>{});
  }
}

__device__ __forceinline__ void gate_unit(int b, const u16* __restrict__ x, const u16* __restrict__ gw,
                                          float* __restrict__ gout) {
  const int tid = threadIdx.x, lane = tid & 63, wave = tid >> 6;
  const int j = lane & 7, rr = lane >> 3;
  u32x4 gv[8];
#pragma unroll
  for (int i = 0; i < 8; ++i) gv[i] = *(const u32x4*)(gw + (j + 8 * i) * 8);
  const int row0 = b * 128 + wave * 32 + rr;
  u32x4 xv[4][8];
#pragma unroll
  for (int ps = 0; ps < 4; ++ps)
#pragma unroll
    for (int i = 0; i < 8; ++i) xv[ps][i] = *(const u32x4*)(x + (size_t)(row0 + 8 * ps) * DM + (j + 8 * i) * 8);
#pragma unroll
  for (int ps = 0; ps < 4; ++ps) {
    float s0 = 0.f, s1 = 0.f;
#pragma unroll
    for (int i = 0; i < 8; ++i)
#pragma unroll
      for (int e = 0; e < 4; ++e) {
        const unsigned pr = pk_bf16(smul(bf_lo(xv[ps][i][e]), bf_lo(gv[i][e])), smul(bf_hi(xv[ps][i][e]), bf_hi(gv[i][e])));
        s0 = sadd(s0, bf_lo(pr));
        s1 = sadd(s1, bf_hi(pr));
      }
    float s = s0 + s1;
    s += __shfl_xor(s, 1);
    s += __shfl_xor(s, 2);
    s += __shfl_xor(s, 4);
    if (j == 0) {
      const float sb = rbf(s);
      gout[row0 + 8 * ps] = rbf(1.0f / (1.0f + expf(-sb)));
    }
  }
}

__global__ __launch_bounds__(256, 2) void k1_kernel(const u16* __restrict__ u, const u16* __restrict__ logAr,
                                                   const u16* __restrict__ Aim, const u16* __restrict__ Cre,
                                                   const u16* __restrict__ Cim, const u16* __restrict__ logdt,
                                                   const u16* __restrict__ Dskip, const u16* __restrict__ wg,
                                                   const u16* __restrict__ wu, const u16* __restrict__ gw,
                                                   const u16* __restrict__ wd, u16* __restrict__ hbuf, u16* __restrict__ aT,
                                                   float* __restrict__ gout DBG_ARG
                                                   ) {
  __shared__ __attribute__((aligned(4096))) char smem[G1_LDS > SSM_LDS ? G1_LDS : SSM_LDS];
  const int bid = blockIdx.x;
  if (bid < NSSM) {
    ssm_unit(bid, u, logAr, Aim, Cre, Cim, logdt, Dskip, aT, smem DBG_PASS);
    unsigned acc = 0;
    for (int i = (bid >> 3) * 256 + threadIdx.x; i < 11008; i += 32 * 256) acc += ((const unsigned*)wd)[i * 32];
    asm volatile("" ::"v"(acc));
  } else {
    const int xcd = bid & 7, idx = (bid - NSSM) >> 3;
    if (idx < 30) {
      gemm1_tile(30 * xcd + idx, u, wg, wu, hbuf, smem DBG_PASS);
    }
    else gate_unit(2 * xcd + idx - 30, u, gw, gout);
  }
}

constexpr int G2_SLOT_B = 4096;
constexpr int G2_WAVE_B = 2 * G2_SLOT_B;
constexpr int G2_PITCH = 272;
constexpr int G2_A_OFF = 4 * 64 * G2_PITCH;
constexpr int K2_LDS = G2_A_OFF + 8192;
#define G2D 3
constexpr int G2_DEPTH = G2D;

struct G2Ctx {
  const char* hbase;
  const char* wbase;
  unsigned ho, wo, wrd0, wrd1, ww;
  int klim;
};
struct G2Frag { s16x4 w[2][2][2]; };
#define MFMA32A(acc, a, b) asm volatile("v_mfma_f32_32x32x16_bf16 %0, %1, %2, %0" : "+a"(acc) : "v"(a), "a"(b))

template <int ST>
struct G2Step {
  static __device__ __forceinline__ void issue(bf16x8 (&hfr)[G2_DEPTH][4], u32x4 (&wr)[G2_DEPTH][4], const G2Ctx& c) {
    const int k0 = (32 * ST < c.klim) ? 32 * ST : 0;
#pragma unroll
    for (int i = 0; i < 4; ++i) GLOAD128S(wr[ST % G2_DEPTH][i], c.wo, c.wbase + (size_t)(k0 + 8 * i) * DM * 2);
#pragma unroll
    for (int mi = 0; mi < 2; ++mi)
#pragma unroll
      for (int i = 0; i < 2; ++i) GLOAD128SA(hfr[ST % G2_DEPTH][2 * mi + i], c.ho, c.hbase + (size_t)(mi * NKS + 2 * ST + i) * 1024);
  }
  template <int NW>
  static __device__ __forceinline__ void store(u32x4 (&wr)[G2_DEPTH][4], const G2Ctx& c) {
    u32x4 (&r)[4] = wr[ST % G2_DEPTH];
    asm volatile("s_waitcnt vmcnt(%4)" : "+v"(r[0]), "+v"(r[1]), "+v"(r[2]), "+v"(r[3]) : "i"(NW) : "memory");
#pragma unroll
    for (int i = 0; i < 4; ++i) DS_W128(c.ww, r[i], (ST & 1) * G2_SLOT_B + i * 1024);
  }
  static __device__ __forceinline__ void run(f32x16 (&acc)[2][2], bf16x8 (&hfr)[G2_DEPTH][4], u32x4 (&wr)[G2_DEPTH][4],
                                             G2Frag& cur, G2Frag& nxt, const G2Ctx& c) {
    constexpr int LAST = (ST + G2_DEPTH - 1 < 10) ? ST + G2_DEPTH - 1 : 10;
    constexpr bool NX = ST + 1 <= 10;
    bf16x8 (&hh)[4] = hfr[ST % G2_DEPTH];
    if constexpr (NX) G2Step<ST + 1>::template store<4 + 8 * (LAST - ST - 1)>(wr, c);
    else asm volatile("s_waitcnt vmcnt(0)" ::: "memory");
    asm volatile("s_waitcnt lgkmcnt(%12)"
                 : "+v"(cur.w[0][0][0]), "+v"(cur.w[0][0][1]), "+v"(cur.w[0][1][0]), "+v"(cur.w[0][1][1]), "+v"(cur.w[1][0][0]),
                   "+v"(cur.w[1][0][1]), "+v"(cur.w[1][1][0]), "+v"(cur.w[1][1][1]), "+a"(hh[0]), "+a"(hh[1]), "+a"(hh[2]), "+a"(hh[3])
                 : "i"(NX ? 4 : 0) : "memory");
    const bf16x8 b00 = cat4(cur.w[0][0][0], cur.w[0][0][1]), b01 = cat4(cur.w[0][1][0], cur.w[0][1][1]);
    const bf16x8 b10 = cat4(cur.w[1][0][0], cur.w[1][0][1]), b11 = cat4(cur.w[1][1][0], cur.w[1][1][1]);
    constexpr int NO = ((ST + 1) & 1) * G2_SLOT_B;
    MFMA32A(acc[0][0], b00, hh[0]);
    MFMA32A(acc[0][1], b00, hh[2]);
    MFMA32A(acc[1][0], b01, hh[0]);
    if constexpr (NX) asm volatile("s_waitcnt lgkmcnt(0)" ::: "memory");
    MFMA32A(acc[1][1], b01, hh[2]);
    if constexpr (NX) { DS_TR(nxt.w[0][0][0], c.wrd0, NO); DS_TR(nxt.w[0][0][1], c.wrd0, NO + 1024); }
    MFMA32A(acc[0][0], b10, hh[1]);
    if constexpr (NX) { DS_TR(nxt.w[0][1][0], c.wrd1, NO); DS_TR(nxt.w[0][1][1], c.wrd1, NO + 1024); }
    MFMA32A(acc[0][1], b10, hh[3]);
    if constexpr (NX) { DS_TR(nxt.w[1][0][0], c.wrd0, NO + 2048); DS_TR(nxt.w[1][0][1], c.wrd0, NO + 3072); }
    MFMA32A(acc[1][0], b11, hh[1]);
    if constexpr (NX) { DS_TR(nxt.w[1][1][0], c.wrd1, NO + 2048); DS_TR(nxt.w[1][1][1], c.wrd1, NO + 3072); }
    MFMA32A(acc[1][1], b11, hh[3]);
    if constexpr (ST + G2_DEPTH <= 10) G2Step<ST + G2_DEPTH>::issue(hfr, wr, c);
    if constexpr (NX) G2Step<ST + 1>::run(acc, hfr, wr, nxt, cur, c);
  }
};

__global__ __launch_bounds__(256, 1) void k2_kernel(const u16* __restrict__ hf, const u16* __restrict__ wd,
                                                   const u16* __restrict__ aT, const float* __restrict__ gin,
                                                   u16* __restrict__ out) {
  __shared__ __attribute__((aligned(4096))) char smem[K2_LDS];
  const int tid = threadIdx.x, lane = tid & 63;
  const int wave = __builtin_amdgcn_readfirstlane(tid >> 6);
  const int bid = blockIdx.x;
  const int xcd = bid & 7, idx = bid >> 3;
  const int m0 = 64 * (4 * xcd + (idx >> 3)), n0 = 64 * (idx & 7);

  G2Ctx c;
  c.hbase = (const char*)(hf + (size_t)((m0 >> 5) * NKS + 22 * wave) * 512);
  c.ho = (unsigned)lane * 16u;
  c.wbase = (const char*)(wd + (size_t)(352 * wave) * DM + n0);
  c.wo = (unsigned)((lane >> 3) * DM + (lane & 7) * 8) * 2u;
  c.klim = FF - 352 * wave;
#pragma unroll
  for (int i = 0; i < 2; ++i) {
    const int piece = 2 * wave + i;
    const int drow = 8 * piece + (lane >> 3);
    glds16(aT + (size_t)(n0 + drow) * SEQ + m0 + (lane & 7) * 8, smem + G2_A_OFF + piece * 1024 + lane * 16);
  }
  bf16x8 hfr[G2_DEPTH][4];
  u32x4 wr[G2_DEPTH][4];
  G2Step<0>::issue(hfr, wr, c);
  G2Step<1>::issue(hfr, wr, c);
  G2Step<2>::issue(hfr, wr, c);
  if constexpr (G2_DEPTH > 3) G2Step<3>::issue(hfr, wr, c);
  if constexpr (G2_DEPTH > 4) G2Step<4>::issue(hfr, wr, c);

  const unsigned wb = lds_addr(smem) + wave * G2_WAVE_B;
  c.ww = wb + (lane >> 3) * 128 + (((lane & 7) ^ (((lane >> 4) & 1) << 2)) * 16);
  const int r = lane & 31, h = lane >> 5;
  const int blk = (lane >> 4) & 1, q = (lane & 15) >> 2, p = lane & 3;
  c.wrd0 = wb + (4 * h + q) * 128 + (((2 * blk + (p >> 1)) ^ (((q >> 1) & 1) << 2)) * 16) + 8 * (p & 1);
  c.wrd1 = c.wrd0 ^ 64u;

  f32x16 acc[2][2];
#pragma unroll
  for (int i = 0; i < 2; ++i)
#pragma unroll
    for (int j = 0; j < 2; ++j) acc[i][j] = f32x16{};
  asm volatile("s_nop 4" : "+a"(acc[0][0]), "+a"(acc[0][1]), "+a"(acc[1][0]), "+a"(acc[1][1]));
  G2Frag fa, fb;
  G2Step<0>::template store<4 + 8 * (G2_DEPTH - 1)>(wr, c);
  asm volatile("s_waitcnt lgkmcnt(0)" ::: "memory");
  DS_TR(fa.w[0][0][0], c.wrd0, 0); DS_TR(fa.w[0][0][1], c.wrd0, 1024);
  DS_TR(fa.w[0][1][0], c.wrd1, 0); DS_TR(fa.w[0][1][1], c.wrd1, 1024);
  DS_TR(fa.w[1][0][0], c.wrd0, 2048); DS_TR(fa.w[1][0][1], c.wrd0, 3072);
  DS_TR(fa.w[1][1][0], c.wrd1, 2048); DS_TR(fa.w[1][1][1], c.wrd1, 3072);
  G2Step<0>::run(acc, hfr, wr, fa, fb, c);
  asm volatile("s_nop 15\n\ts_nop 15" : "+a"(acc[0][0]), "+a"(acc[0][1]), "+a"(acc[1][0]), "+a"(acc[1][1]));
  const int m = tid >> 2, nq = (tid & 3) * 16;
  const float g = gin[m0 + m];
  __syncthreads();
  {
    char* pw = smem + wave * (64 * G2_PITCH) + r * G2_PITCH + 16 * h;
#pragma unroll
    for (int t = 0; t < 2; ++t)
#pragma unroll
      for (int mi = 0; mi < 2; ++mi)
#pragma unroll
        for (int gq = 0; gq < 4; ++gq)
          *(f32x4*)(pw + (32 * mi) * G2_PITCH + (32 * t + 8 * gq) * 4) =
              f32x4{acc[t][mi][4 * gq], acc[t][mi][4 * gq + 1], acc[t][mi][4 * gq + 2], acc[t][mi][4 * gq + 3]};
  }
  __syncthreads();
  {
    const float omg = rbf(1.0f - g);
    f32x4 sv[4];
#pragma unroll
    for (int j = 0; j < 4; ++j) {
      const char* pp = smem + m * G2_PITCH + (nq + 4 * j) * 4;
      sv[j] = (*(const f32x4*)pp + *(const f32x4*)(pp + 64 * G2_PITCH)) +
              (*(const f32x4*)(pp + 2 * 64 * G2_PITCH) + *(const f32x4*)(pp + 3 * 64 * G2_PITCH));
    }
    const u16* aimg = (const u16*)(smem + G2_A_OFF);
    unsigned o[8];
#pragma unroll
    for (int j = 0; j < 8; ++j) {
      const float a0 = bf2f(aimg[(nq + 2 * j) * 64 + m]);
      const float a1 = bf2f(aimg[(nq + 2 * j + 1) * 64 + m]);
      const unsigned bp = pk_bf16(sv[j >> 1][2 * (j & 1)], sv[j >> 1][2 * (j & 1) + 1]);
      const unsigned bt = pk_bf16(omg * bf_lo(bp), omg * bf_hi(bp));
      const unsigned ga = pk_bf16(g * a0, g * a1);
      o[j] = pk_bf16(bf_lo(ga) + bf_lo(bt), bf_hi(ga) + bf_hi(bt));
    }
    u16* op = out + (size_t)(m0 + m) * DM + n0 + nq;
    *(u32x4*)op = u32x4{o[0], o[1], o[2], o[3]};
    *(u32x4*)(op + 8) = u32x4{o[4], o[5], o[6], o[7]};
  }
}

extern "C" void kernel_launch(void* const* d_in, const int* in_sizes, int n_in, void* d_out, int out_size,
                              void* d_ws, size_t ws_size, hipStream_t stream) {
  const u16* u = (const u16*)d_in[0];
  const u16* logAr = (const u16*)d_in[1];
  const u16* Aim = (const u16*)d_in[2];
  const u16* Cre = (const u16*)d_in[3];
  const u16* Cim = (const u16*)d_in[4];
  const u16* logdt = (const u16*)d_in[5];
  const u16* Dskip = (const u16*)d_in[6];
  const u16* wg = (const u16*)d_in[8];
  const u16* wu = (const u16*)d_in[9];
  const u16* wd = (const u16*)d_in[10];
  const u16* gw = (const u16*)d_in[11];
  char* ws = (char*)d_ws;
  u16* hbuf = (u16*)ws;
  u16* aT = (u16*)(ws + (size_t)SEQ * FFP * 2);
  float* gbuf = (float*)(ws + (size_t)SEQ * FFP * 2 + (size_t)DM * SEQ * 2);
  k1_kernel<<<512, 256, 0, stream>>>(u, logAr, Aim, Cre, Cim, logdt, Dskip, wg, wu, gw, wd, hbuf, aT, gbuf
      );
  k2_kernel<<<256, 256, 0, stream>>>(hbuf, wd, aT, gbuf, (u16*)d_out);
}
```

```cpp
#include <hip/hip_runtime.h>
#include <hip/hip_bf16.h>
#include <stdint.h>
#include <type_traits>

typedef unsigned short u16;
typedef __attribute__((ext_vector_type(8))) short bf16x8;
typedef __attribute__((ext_vector_type(4))) short s16x4;
typedef __attribute__((ext_vector_type(4))) float f32x4;
typedef __attribute__((ext_vector_type(16))) float f32x16;
typedef __attribute__((ext_vector_type(4))) unsigned u32x4;
typedef __attribute__((ext_vector_type(2))) unsigned u32x2;
typedef __attribute__((ext_vector_type(2))) __bf16 bf16x2_t;
typedef __attribute__((ext_vector_type(2))) float f32x2;
typedef __attribute__((address_space(3))) s16x4* lds_s16x4_ptr;

constexpr int SEQ = 2048, DM = 512, NST = 128, FF = 1376, FFP = 1408;
constexpr int NSSM = 256;
constexpr int NKS = 88;

__device__ __forceinline__ unsigned pk_bf16(float lo, float hi) {
  f32x2 v = {lo, hi};
  bf16x2_t b = __builtin_convertvector(v, bf16x2_t);
  return __builtin_bit_cast(unsigned, b);
}
__device__ __forceinline__ float bf_lo(unsigned w) { return __uint_as_float(w << 16); }
__device__ __forceinline__ float bf_hi(unsigned w) { return __uint_as_float(w & 0xffff0000u); }
__device__ __forceinline__ float rbf(float x) { return bf_lo(pk_bf16(x, 0.f)); }
__device__ __forceinline__ float bf2f(u16 v) { return __uint_as_float(((unsigned)v) << 16); }

__device__ __forceinline__ float smul(float a, float b) { float r; asm("v_mul_f32 %0, %1, %2" : "=v"(r) : "v"(a), "v"(b)); return r; }
__device__ __forceinline__ float sadd(float a, float b) { float r; asm("v_add_f32 %0, %1, %2" : "=v"(r) : "v"(a), "v"(b)); return r; }
__device__ __forceinline__ float ssub(float a, float b) { float r; asm("v_sub_f32 %0, %1, %2" : "=v"(r) : "v"(a), "v"(b)); return r; }
__device__ __forceinline__ float sfma(float a, float b, float c) { float r; asm("v_fma_f32 %0, %1, %2, %3" : "=v"(r) : "v"(a), "v"(b), "v"(c)); return r; }
__device__ __forceinline__ float sfms(float a, float b, float c) { float r; asm("v_fma_f32 %0, -%1, %2, %3" : "=v"(r) : "v"(a), "v"(b), "v"(c)); return r; }
__device__ __forceinline__ float sfnn(float a, float b, float c) { float r; asm("v_fma_f32 %0, -%1, %2, -%3" : "=v"(r) : "v"(a), "v"(b), "v"(c)); return r; }
#define TRANS_FENCE8(a, o) asm volatile("s_nop 1" : "+v"(a[o]), "+v"(a[o + 1]), "+v"(a[o + 2]), "+v"(a[o + 3]), "+v"(a[o + 4]), "+v"(a[o + 5]), "+v"(a[o + 6]), "+v"(a[o + 7]))
__device__ __forceinline__ bf16x8 mk8(unsigned a, unsigned b, unsigned c, unsigned d) {
  u32x4 v = {a, b, c, d};
  return __builtin_bit_cast(bf16x8, v);
}
__device__ __forceinline__ bf16x8 cat4(s16x4 a, s16x4 b) {
  return __builtin_shufflevector(a, b, 0, 1, 2, 3, 4, 5, 6, 7);
}
__device__ __forceinline__ s16x4 tr_read(const char* smem_ptr) {
  return __builtin_amdgcn_ds_read_tr16_b64_v4i16((lds_s16x4_ptr)(smem_ptr));
}
__device__ __forceinline__ unsigned lds_addr(const void* p) {
  return (unsigned)(size_t)(const __attribute__((address_space(3))) char*)p;
}
__device__ __forceinline__ void glds16(const void* g, char* l) {
  __builtin_amdgcn_global_load_lds((const unsigned*)g, (unsigned*)l, 16, 0, 0);
}

constexpr int U_COPY_B = 4160;
constexpr int U_CH_B = 4 * U_COPY_B;
constexpr int U_REGION_B = 2 * U_CH_B;
constexpr int KR_PART_B = 4576;
constexpr int KR_OFF = U_REGION_B;
constexpr int KQ_B = 8208;
constexpr int KX_OFF = 2 * KQ_B;
constexpr int SSM_LDS = KR_OFF + 4 * KR_PART_B;
static_assert(KX_OFF + 4 * 1536 <= U_REGION_B, "scratch must fit the U region");

#define DS_TR(dst, addr, off) asm volatile("ds_read_b64_tr_b16 %0, %1 offset:%2" : "=v"(dst) : "v"(addr), "i"(off) : "memory")
#define DS_B128(dst, addr, off) asm volatile("ds_read_b128 %0, %1 offset:%2" : "=v"(dst) : "v"(addr), "i"(off) : "memory")
#define DS_B64(dst, addr, off) asm volatile("ds_read_b64 %0, %1 offset:%2" : "=v"(dst) : "v"(addr), "i"(off) : "memory")
#define MFMA16(acc, a, b) asm volatile("v_mfma_f32_16x16x32_bf16 %0, %1, %2, %0" : "+v"(acc) : "v"(a), "v"(b))

struct ConvFrag {
  s16x4 a[8][2];
  bf16x8 bh[8], bl[8];
};
template <int RI, int K>
__device__ __forceinline__ void conv_read(ConvFrag& f, unsigned ua, unsigned kb) {
  if constexpr (K < 16) {
    DS_B64(f.a[K >> 1][K & 1], ua, 64 * (8 * (K >> 1) + RI) + 8 * (K & 1));
  } else if constexpr (K < 24) {
    DS_B128(f.bh[K - 16], kb, 64 * (56 - 8 * (K - 16) + RI));
  } else {
    DS_B128(f.bl[K - 24], kb, 64 * (56 - 8 * (K - 24) + RI) + KR_PART_B);
  }
}
template <int RI, int K>
__device__ __forceinline__ void conv_read_all(ConvFrag& f, unsigned ua, unsigned kb) {
  conv_read<RI, K>(f, ua, kb);
  if constexpr (K + 1 < 32) conv_read_all<RI, K + 1>(f, ua, kb);
}
#define CONV_WAIT(f)                                                                                                  \
  do {                                                                                                                \
    asm volatile("s_waitcnt lgkmcnt(0)"                                                                               \
                 : "+v"(f.a[0][0]), "+v"(f.a[0][1]), "+v"(f.a[1][0]), "+v"(f.a[1][1]), "+v"(f.a[2][0]), "+v"(f.a[2][1]), \
                   "+v"(f.a[3][0]), "+v"(f.a[3][1]), "+v"(f.a[4][0]), "+v"(f.a[4][1]), "+v"(f.a[5][0]), "+v"(f.a[5][1]), \
                   "+v"(f.a[6][0]), "+v"(f.a[6][1]), "+v"(f.a[7][0]), "+v"(f.a[7][1])::"memory");                     \
    asm volatile("" : "+v"(f.bh[0]), "+v"(f.bh[1]), "+v"(f.bh[2]), "+v"(f.bh[3]), "+v"(f.bh[4]), "+v"(f.bh[5]),        \
                 "+v"(f.bh[6]), "+v"(f.bh[7]), "+v"(f.bl[0]), "+v"(f.bl[1]), "+v"(f.bl[2]), "+v"(f.bl[3]),            \
                 "+v"(f.bl[4]), "+v"(f.bl[5]), "+v"(f.bl[6]), "+v"(f.bl[7])::"memory");                              \
  } while (0)
constexpr int conv_tau(int P) { int t = 0; while (P >= 8 - t) { P -= 8 - t; ++t; } return t; }
constexpr int conv_sig(int P) { int t = 0; while (P >= 8 - t) { P -= 8 - t; ++t; } return P; }
#define CONV_PAIR(acc, a, bl, bh) \
  asm volatile("v_mfma_f32_16x16x32_bf16 %0, %1, %2, %0\n\tv_mfma_f32_16x16x32_bf16 %0, %1, %3, %0" : "+v"(acc) : "v"(a), "v"(bl), "v"(bh))
#define CONV_PAIR_RD(OP, acc, a, bl, bh, dst, addr, off)                                                      \
  asm volatile("v_mfma_f32_16x16x32_bf16 %0, %2, %3, %0\n\t" OP " %1, %5 offset:%6\n\tv_mfma_f32_16x16x32_bf16 %0, %2, %4, %0" \
               : "+v"(acc), "=&v"(dst) : "v"(a), "v"(bl), "v"(bh), "v"(addr), "i"(off) : "memory")
template <int RI, int P>
__device__ __forceinline__ void conv_pairs(f32x4 (&acc)[8], const bf16x8 (&A)[8], ConvFrag& cur, ConvFrag& nxt, unsigned ua,
                                           unsigned kb) {
  constexpr int T = conv_tau(P), SG = conv_sig(P);
  if constexpr (RI < 3 && P < 16) {
    CONV_PAIR_RD("ds_read_b64", acc[T + SG], A[SG], cur.bl[T], cur.bh[T], nxt.a[P >> 1][P & 1], ua, 64 * (8 * (P >> 1) + RI + 1) + 8 * (P & 1));
  } else if constexpr (RI < 3 && P < 24) {
    CONV_PAIR_RD("ds_read_b128", acc[T + SG], A[SG], cur.bl[T], cur.bh[T], nxt.bh[P - 16], kb, 64 * (56 - 8 * (P - 16) + RI + 1));
  } else if constexpr (RI < 3 && P < 32) {
    CONV_PAIR_RD("ds_read_b128", acc[T + SG], A[SG], cur.bl[T], cur.bh[T], nxt.bl[P - 24], kb, 64 * (56 - 8 * (P - 24) + RI + 1) + KR_PART_B);
  } else {
    CONV_PAIR(acc[T + SG], A[SG], cur.bl[T], cur.bh[T]);
  }
  if constexpr (P + 1 < 36) conv_pairs<RI, P + 1>(acc, A, cur, nxt, ua, kb);
}
template <int RI>
__device__ __forceinline__ void conv_rho(f32x4 (&acc)[8], ConvFrag& cur, ConvFrag& nxt, unsigned ua, unsigned kb) {
  CONV_WAIT(cur);
  bf16x8 A[8];
#pragma unroll
  for (int i = 0; i < 8; ++i) A[i] = cat4(cur.a[i][0], cur.a[i][1]);
  conv_pairs<RI, 0>(acc, A, cur, nxt, ua, kb);
  if constexpr (RI < 3) conv_rho<RI + 1>(acc, nxt, cur, ua, kb);
}

__device__ __forceinline__ void split_pair(float v0, float v1, unsigned& hi, unsigned& lo) {
  hi = pk_bf16(v0, v1);
  lo = pk_bf16(ssub(v0, bf_lo(hi)), ssub(v1, bf_hi(hi)));
}

__device__ __forceinline__ void ssm_unit(int w, const u16* __restrict__ u, const u16* __restrict__ logAr,
                                         const u16* __restrict__ Aim, const u16* __restrict__ Cre,
                                         const u16* __restrict__ Cim, const u16* __restrict__ logdt,
                                         const u16* __restrict__ Dskip, u16* __restrict__ aT, char* smem) {
  const int tid = threadIdx.x, lane = tid & 63;
  const int wave = __builtin_amdgcn_readfirstlane(tid >> 6);
  const int ch = wave >> 1, w2 = wave & 1;
  const int d0 = 2 * w;
  const int d = d0 + ch;

  const int pidx = d0 * NST + tid;
  const u16 r_lar = logAr[pidx], r_aim = Aim[pidx], r_cre = Cre[pidx], r_cim = Cim[pidx], r_ldt = logdt[d], r_dsk = Dskip[d];
  const unsigned* u32p = (const unsigned*)(u + d0);
  unsigned uw[8], up[3];
#pragma unroll
  for (int i = 0; i < 3; ++i) up[i] = 0;
  if (lane == 0 && tid > 0) {
#pragma unroll
    for (int i = 0; i < 3; ++i) up[i] = u32p[(size_t)(8 * tid - 3 + i) * (DM / 2)];
  }
  const unsigned uoff = (unsigned)tid * 8192u;
  {
    const float dt = rbf(expf(bf2f(r_ldt)));
    const float are = rbf(expf(bf2f(r_lar)));
    f32x4 prm;
    prm[0] = (-are * dt) * 1.44269504088896340736f;
    prm[1] = (bf2f(r_aim) * dt) * 0.15915494309189533577f;
    prm[2] = 2.0f * (bf2f(r_cre) * dt);
    prm[3] = 2.0f * (bf2f(r_cim) * dt);
    *(f32x4*)(smem + KR_OFF + tid * 16) = prm;
  }
  __syncthreads();

  const int r = lane & 31, h = lane >> 5;
  f32x16 dacc = {};
  {
    int p1i;
    if (r < 8) p1i = r;
    else if (r < 12) p1i = 256 + 2 * (r - 8);
    else if (r < 16) p1i = 512 + 256 * ((r - 12) >> 1) + 4 * ((r - 12) & 1);
    else if (r < 21) p1i = 1024 + 256 * (r - 16);
    else p1i = 0;
    const float p1 = (float)p1i, p2 = (float)(8 * r);
    const char* prm_base = smem + KR_OFF + (ch * 128 + 64 * w2 + 4 * h) * 16;
#pragma unroll
    for (int ks = 0; ks < 8; ++ks) {
      if (ks >= 4) {
        asm volatile("global_load_dword %0, %1, %2 offset:%3" : "=v"(uw[2 * ks - 8]) : "v"(uoff), "s"((const char*)u32p + ((2 * ks - 8) >> 2) * 4096), "i"(((2 * ks - 8) & 3) * 1024) : "memory");
        asm volatile("global_load_dword %0, %1, %2 offset:%3" : "=v"(uw[2 * ks - 7]) : "v"(uoff), "s"((const char*)u32p + ((2 * ks - 7) >> 2) * 4096), "i"(((2 * ks - 7) & 3) * 1024) : "memory");
      }
      f32x4 prm[4];
#pragma unroll
      for (int q = 0; q < 4; ++q) prm[q] = *(const f32x4*)(prm_base + (8 * ks + q) * 16);
      float ea[4], eb[4], fa[4], fb[4];
#pragma unroll
      for (int q = 0; q < 4; ++q) {
        ea[q] = __builtin_amdgcn_exp2f(smul(prm[q][0], p1)); eb[q] = __builtin_amdgcn_exp2f(smul(prm[q][0], p2));
        fa[q] = __builtin_amdgcn_fractf(smul(prm[q][1], p1)); fb[q] = __builtin_amdgcn_fractf(smul(prm[q][1], p2));
      }
      float ac[4], as[4], bc[4], bs[4];
#pragma unroll
      for (int q = 0; q < 4; ++q) {
        ac[q] = __builtin_amdgcn_cosf(fa[q]); as[q] = __builtin_amdgcn_sinf(fa[q]);
        bc[q] = __builtin_amdgcn_cosf(fb[q]); bs[q] = __builtin_amdgcn_sinf(fb[q]);
      }
      asm volatile("s_nop 1" : "+v"(ea[0]), "+v"(ea[1]), "+v"(ea[2]), "+v"(ea[3]), "+v"(eb[0]), "+v"(eb[1]), "+v"(eb[2]), "+v"(eb[3]),
                   "+v"(ac[0]), "+v"(ac[1]), "+v"(ac[2]), "+v"(ac[3]), "+v"(as[0]), "+v"(as[1]), "+v"(as[2]), "+v"(as[3]),
                   "+v"(bc[0]), "+v"(bc[1]), "+v"(bc[2]), "+v"(bc[3]), "+v"(bs[0]), "+v"(bs[1]), "+v"(bs[2]), "+v"(bs[3]));
      unsigned ah[4], al[4], bh[4], bl[4];
#pragma unroll
      for (int q = 0; q < 4; ++q) {
        const float cr = smul(prm[q][2], ea[q]), ci = smul(prm[q][3], ea[q]);
        const float gre = sfms(ci, as[q], smul(cr, ac[q]));
        const float gmi = sfnn(ci, ac[q], smul(cr, as[q]));
        split_pair(gre, gmi, ah[q], al[q]);
        split_pair(smul(bc[q], eb[q]), smul(bs[q], eb[q]), bh[q], bl[q]);
      }
      const bf16x8 Ah = mk8(ah[0], ah[1], ah[2], ah[3]), Al = mk8(al[0], al[1], al[2], al[3]);
      const bf16x8 Bh = mk8(bh[0], bh[1], bh[2], bh[3]), Bl = mk8(bl[0], bl[1], bl[2], bl[3]);
      dacc = __builtin_amdgcn_mfma_f32_32x32x16_bf16(Al, Bh, dacc, 0, 0, 0);
      dacc = __builtin_amdgcn_mfma_f32_32x32x16_bf16(Ah, Bl, dacc, 0, 0, 0);
      dacc = __builtin_amdgcn_mfma_f32_32x32x16_bf16(Ah, Bh, dacc, 0, 0, 0);
    }
  }
  {
    float* xo = (float*)(smem + KX_OFF + wave * 1536) + lane;
    const float* xi = (const float*)(smem + KX_OFF + (wave ^ 1) * 1536) + lane;
    float mine[6];
    if (w2 == 0) {
#pragma unroll
      for (int e = 0; e < 6; ++e) { xo[e * 64] = dacc[6 + e]; mine[e] = dacc[e]; }
    } else {
#pragma unroll
      for (int e = 0; e < 6; ++e) { xo[e * 64] = dacc[e]; mine[e] = dacc[6 + e]; }
    }
    __syncthreads();
    unsigned* Kq = (unsigned*)(smem + ch * KQ_B);
    unsigned pq[6];
#pragma unroll
    for (int e = 0; e < 6; ++e) {
      const float v = sadd(mine[e], xi[e * 64]);
      const unsigned hi = pk_bf16(v, 0.f);
      pq[e] = pk_bf16(0.f, ssub(v, bf_lo(hi))) | hi;
    }
    if (w2 == 0) {
#pragma unroll
      for (int e = 0; e < 4; ++e) Kq[4 * h + e + 8 * r] = pq[e];
      Kq[(h ? 512 : 256) + 8 * r] = pq[4];
      Kq[(h ? 516 : 258) + 8 * r] = pq[5];
    } else {
      Kq[(h ? 768 : 260) + 8 * r] = pq[0];
      Kq[(h ? 772 : 262) + 8 * r] = pq[1];
      if (h == 0) {
#pragma unroll
        for (int e = 0; e < 4; ++e) Kq[1024 + 256 * e + 8 * r] = pq[2 + e];
      } else if (r == 0) {
        Kq[2048] = pq[2];
      }
    }
  }
  __syncthreads();

  {
    const int t = tid & 127;
    const unsigned* Kq = (const unsigned*)(smem + ch * KQ_B);
    unsigned* krh = (unsigned*)(smem + KR_OFF + (ch * 2) * KR_PART_B);
    unsigned* krl = (unsigned*)((char*)krh + KR_PART_B);
    unsigned q0[8], q1[8];
#pragma unroll
    for (int m = 0; m < 8; ++m) {
      const int sh = m < 4 ? 3 : (m < 6 ? 2 : (m < 7 ? 1 : 0));
      const int l0 = 2047 - 2 * (t + 128 * m), l1 = l0 - 1;
      int pa = l0, pb = l1;
      if (sh) {
        pa = (l0 + (1 << (sh - 1)) - 1 + ((l0 >> sh) & 1)) & ~((1 << sh) - 1);
        pb = (l1 + (1 << (sh - 1)) - 1 + ((l1 >> sh) & 1)) & ~((1 << sh) - 1);
      }
      q0[m] = Kq[pa];
      q1[m] = Kq[pb];
    }
#pragma unroll
    for (int m = 0; m < 8; ++m) {
      krh[t + 128 * m] = __builtin_amdgcn_perm(q1[m], q0[m], 0x05040100u);
      krl[t + 128 * m] = __builtin_amdgcn_perm(q1[m], q0[m], 0x07060302u);
    }
    if (t < 120) { krh[1024 + t] = 0; krl[1024 + t] = 0; }
  }
  __syncthreads();

  asm volatile("s_waitcnt vmcnt(0)" : "+v"(uw[0]), "+v"(uw[1]), "+v"(uw[2]), "+v"(uw[3]), "+v"(uw[4]), "+v"(uw[5]), "+v"(uw[6]), "+v"(uw[7])::"memory");
  {
    unsigned e[11];
#pragma unroll
    for (int i = 0; i < 3; ++i) {
      const unsigned nb = __shfl_up(uw[5 + i], 1);
      e[i] = (lane == 0) ? up[i] : nb;
    }
#pragma unroll
    for (int i = 0; i < 8; ++i) e[3 + i] = uw[i];
    unsigned p0[10], p1[10];
#pragma unroll
    for (int i = 0; i < 10; ++i) {
      p0[i] = __builtin_amdgcn_perm(e[i + 1], e[i], 0x05040100u);
      p1[i] = __builtin_amdgcn_perm(e[i + 1], e[i], 0x07060302u);
    }
#pragma unroll
    for (int c = 0; c < 4; ++c) {
      *(u32x4*)(smem + c * U_COPY_B + (tid + 2) * 16) = u32x4{p0[c], p0[c + 2], p0[c + 4], p0[c + 6]};
      *(u32x4*)(smem + U_CH_B + c * U_COPY_B + (tid + 2) * 16) = u32x4{p1[c], p1[c + 2], p1[c + 4], p1[c + 6]};
    }
    if (tid < 16) *(u32x4*)(smem + (tid >> 1) * U_COPY_B + (tid & 1) * 16) = u32x4{0, 0, 0, 0};
  }
  __syncthreads();

  f32x4 acc[8];
#pragma unroll
  for (int t = 0; t < 8; ++t) acc[t] = f32x4{0.f, 0.f, 0.f, 0.f};
  asm volatile("s_nop 4" : "+v"(acc[0]), "+v"(acc[1]), "+v"(acc[2]), "+v"(acc[3]), "+v"(acc[4]), "+v"(acc[5]), "+v"(acc[6]), "+v"(acc[7]));
  {
    const int r16 = lane & 15, hq = lane >> 4;
    const unsigned sb = lds_addr(smem);
    const unsigned ua = sb + ch * U_CH_B + (r16 & 3) * U_COPY_B + 2 * (4 + (r16 & ~3) + 8 * hq) + 64 * 4 * w2;
    const unsigned kb = sb + KR_OFF + (ch * 2) * KR_PART_B + 2 * (2032 - 16 * r16 + 8 * hq) - 64 * 56 + 64 * 4 * w2;
    ConvFrag f0, f1;
    conv_read_all<0, 0>(f0, ua, kb);
    conv_rho<0>(acc, f0, f1, ua, kb);
  }
  asm volatile("s_nop 15" : "+v"(acc[0]), "+v"(acc[1]), "+v"(acc[2]), "+v"(acc[3]), "+v"(acc[4]), "+v"(acc[5]), "+v"(acc[6]), "+v"(acc[7]));
  __syncthreads();
  {
    char* xw = smem + KR_OFF + (ch * 2 + w2) * 4096 + lane * 16;
    const char* xr = smem + KR_OFF + (ch * 2 + (w2 ^ 1)) * 4096 + lane * 16;
    f32x4 y[4];
    if (w2 == 0) {
#pragma unroll
      for (int i = 0; i < 4; ++i) { *(f32x4*)(xw + i * 1024) = acc[4 + i]; y[i] = acc[i]; }
    } else {
#pragma unroll
      for (int i = 0; i < 4; ++i) { *(f32x4*)(xw + i * 1024) = acc[i]; y[i] = acc[4 + i]; }
    }
    __syncthreads();
    const float Dk = bf2f(r_dsk);
#pragma unroll
    for (int i = 0; i < 4; ++i) {
      const f32x4 o = *(const f32x4*)(xr + i * 1024);
      const int l0 = 256 * (4 * w2 + i) + 16 * (lane & 15) + 4 * (lane >> 4);
      const u32x2 uu = *(const u32x2*)(smem + ch * U_CH_B + 3 * U_COPY_B + 2 * (16 + l0));
      const unsigned y01 = pk_bf16(sadd(y[i][0], o[0]), sadd(y[i][1], o[1])), y23 = pk_bf16(sadd(y[i][2], o[2]), sadd(y[i][3], o[3]));
      const unsigned s01 = pk_bf16(smul(bf_lo(uu[0]), Dk), smul(bf_hi(uu[0]), Dk)), s23 = pk_bf16(smul(bf_lo(uu[1]), Dk), smul(bf_hi(uu[1]), Dk));
      { const u32x2 av = u32x2{pk_bf16(sadd(bf_lo(y01), bf_lo(s01)), sadd(bf_hi(y01), bf_hi(s01))),
                                                  pk_bf16(sadd(bf_lo(y23), bf_lo(s23)), sadd(bf_hi(y23), bf_hi(s23)))};
        asm volatile("global_store_dwordx2 %0, %1, off sc1\n\ts_nop 1" ::"v"(aT + (size_t)d * SEQ + l0), "v"(av) : "memory"); }
    }
  }
}

__device__ __forceinline__ float gelu_tanh(float x) {
  const float z = 0.7978845608028654f * (x + 0.044715f * x * x * x);
  const float e = __builtin_amdgcn_exp2f(-2.8853900817779268f * z);
  return x * __builtin_amdgcn_rcpf(1.0f + e);
}

constexpr int G1_WSLOT_B = 12288;
constexpr int G1_X_OFF = 3 * G1_WSLOT_B;
constexpr int G1_LDS = G1_X_OFF + 32768;

constexpr int g1_pos(bool isx, int id) {
  if (isx && id == 0) return 4;
  if (!isx && id == 0) return 7;
  if (!isx && id == 1) return 10;
  if (isx && id == 1) return 14;
  if (!isx && id == 2) return 17;
  if (!isx && id == 3) return 20;
  if (!isx && id == 4) return 23;
  if (isx && id == 2) return 27;
  int n = 27;
  for (int t = 0; t < 32; ++t) {
    if ((t & 1) == 0 && t / 2 + 5 < 16) { n += 3; if (!isx && t / 2 + 5 == id) return n; }
    if ((t & 3) == 1 && t / 4 + 3 < 8) { n += 4; if (isx && t / 4 + 3 == id) return n; }
  }
  return -1;
}
constexpr int g1_issued_before(int step) {
  int n = 27;
  for (int t = 0; t < step; ++t) {
    if ((t & 1) == 0 && t / 2 + 5 < 16) n += 3;
    if ((t & 3) == 1 && t / 4 + 3 < 8) n += 4;
  }
  return n;
}

#define GLOAD128(dst, ptr) asm volatile("global_load_dwordx4 %0, %1, off" : "=v"(dst) : "v"(ptr) : "memory")
#define GLOAD128S(dst, voff, sbase) asm volatile("global_load_dwordx4 %0, %1, %2" : "=v"(dst) : "v"(voff), "s"(sbase) : "memory")
#define GLOAD128SA(dst, voff, sbase) asm volatile("global_load_dwordx4 %0, %1, %2" : "=a"(dst) : "v"(voff), "s"(sbase) : "memory")
#define GLOAD128A(dst, ptr) asm volatile("global_load_dwordx4 %0, %1, off" : "=a"(dst) : "v"(ptr) : "memory")
#define DS_W128(addr, val, off) asm volatile("ds_write_b128 %0, %1 offset:%2" ::"v"(addr), "v"(val), "i"(off) : "memory")
#define MFMA32(acc, a, b) asm volatile("v_mfma_f32_32x32x16_bf16 %0, %1, %2, %0" : "+v"(acc) : "v"(a), "v"(b))

struct G1Ctx {
  const char* wbase;
  const char* xbase;
  unsigned wo[3], xo;
  unsigned woff, woff_o, ww[3], xw, xw1, xrd2[2][4];
};
struct G1Frag {
  s16x4 w[3][2];
  bf16x8 x[2];
};

#define G1_RD_W(f, t, OFF)                                                        \
  do {                                                                            \
    if ((t) & 1) { DS_TR(f.w[t][0], c.woff_o, (OFF) + (t) * 64); DS_TR(f.w[t][1], c.woff_o, (OFF) + (t) * 64 + 4 * 384); } \
    else { DS_TR(f.w[t][0], c.woff, (OFF) + (t) * 64); DS_TR(f.w[t][1], c.woff, (OFF) + (t) * 64 + 4 * 384); }            \
  } while (0)
#define G1_RD_X(f, mi, N) DS_B128(f.x[mi], c.xrd2[mi][(N) & 3], (((N) >> 2) & 1) * 16384)
template <int N>
__device__ __forceinline__ void g1_read(G1Frag& f, const G1Ctx& c) {
  constexpr int OFF = ((N >> 1) % 3) * G1_WSLOT_B + (N & 1) * 16 * 384;
  G1_RD_X(f, 0, N); G1_RD_X(f, 1, N);
  G1_RD_W(f, 0, OFF); G1_RD_W(f, 1, OFF); G1_RD_W(f, 2, OFF);
}
#define G1_WAIT_FRAG(f, STR)                                                                                       \
  asm volatile(STR : "+v"(f.w[0][0]), "+v"(f.w[0][1]), "+v"(f.w[1][0]), "+v"(f.w[1][1]), "+v"(f.w[2][0]),        \
               "+v"(f.w[2][1]), "+v"(f.x[0]), "+v"(f.x[1])::"memory")

template <int N>
struct G1Step {
  static __device__ __forceinline__ void run(f32x16 (&acc)[6], u32x4 (&xr)[2][4], u32x4 (&wr)[3][3], G1Frag& cur,
                                             G1Frag& nxt, G1Ctx& c) {
    constexpr int ISS = g1_issued_before(N);
    constexpr int S = N >> 1;
    constexpr bool RD = N + 1 < 32;
    constexpr int OFF = (((N + 1) >> 1) % 3) * G1_WSLOT_B + ((N + 1) & 1) * 16 * 384;
    if constexpr ((N & 1) == 0 && N > 0) G1_WAIT_FRAG(cur, "s_waitcnt lgkmcnt(0)\n\ts_barrier");
    else G1_WAIT_FRAG(cur, "s_waitcnt lgkmcnt(0)");
    bf16x8 b[3];
#pragma unroll
    for (int t = 0; t < 3; ++t) b[t] = cat4(cur.w[t][0], cur.w[t][1]);
    MFMA32(acc[0], b[0], cur.x[0]);
    if constexpr (RD) { G1_RD_X(nxt, 0, N + 1); G1_RD_W(nxt, 0, OFF); }
    MFMA32(acc[3], b[0], cur.x[1]);
    if constexpr (RD) { G1_RD_X(nxt, 1, N + 1); G1_RD_W(nxt, 1, OFF); }
    MFMA32(acc[1], b[1], cur.x[0]);
    if constexpr (RD) { G1_RD_W(nxt, 2, OFF); }
    MFMA32(acc[4], b[1], cur.x[1]);
    if constexpr ((N & 1) == 0) {
      if constexpr (S + 2 < 16) {
        constexpr int R = (S + 2) % 3;
        constexpr int NW = ISS - g1_pos(false, S + 2);
        asm volatile("s_waitcnt vmcnt(%3)" : "+v"(wr[R][0]), "+v"(wr[R][1]), "+v"(wr[R][2]) : "i"(NW) : "memory");
#pragma unroll
        for (int i = 0; i < 3; ++i) DS_W128(c.ww[i], wr[R][i], R * G1_WSLOT_B);
      }
    }
    if constexpr ((N & 3) == 1) {
      constexpr int B = N / 4 + 1;
      if constexpr (B < 8) {
        constexpr int NW = ISS - g1_pos(true, B);
        asm volatile("s_waitcnt vmcnt(%4)" : "+v"(xr[B & 1][0]), "+v"(xr[B & 1][1]), "+v"(xr[B & 1][2]), "+v"(xr[B & 1][3]) : "i"(NW) : "memory");
#pragma unroll
        for (int i = 0; i < 4; ++i) { if (i & 1) DS_W128(c.xw1, xr[B & 1][i], (B & 1) * 16384 + i * 1024); else DS_W128(c.xw, xr[B & 1][i], (B & 1) * 16384 + i * 1024); }
      }
    }
    MFMA32(acc[2], b[2], cur.x[0]);
    if constexpr ((N & 1) == 0 && S + 5 < 16) {
#pragma unroll
      for (int i = 0; i < 3; ++i) GLOAD128S(wr[(S + 5) % 3][i], c.wo[i], c.wbase + (size_t)(S + 5) * 32 * FF * 2);
    }
    if constexpr ((N & 3) == 1 && N / 4 + 3 < 8) {
      constexpr int B = N / 4 + 1;
#pragma unroll
      for (int i = 0; i < 4; ++i) GLOAD128S(xr[B & 1][i], c.xo, c.xbase + (size_t)(8 * i) * DM * 2 + (B + 2) * 128);
    }
    MFMA32(acc[5], b[2], cur.x[1]);
    if constexpr (N + 1 < 32) G1Step<N + 1>::run(acc, xr, wr, nxt, cur, c);
  }
};

__device__ __forceinline__ void gemm1_tile(int t, const u16* __restrict__ x, const u16* __restrict__ wg,
                                           const u16* __restrict__ wu, u16* __restrict__ hf, char* smem) {
  const int tid = threadIdx.x, lane = tid & 63;
  const int wave = __builtin_amdgcn_readfirstlane(tid >> 6);
  const int fb = t >> 4, mb = t & 15;
  const int f0 = 96 * fb;
  const int wm = wave & 1, isup = wave >> 1;
  G1Ctx c;
  const unsigned sbase = lds_addr(smem);
  c.wbase = (const char*)(isup ? wu : wg);
#pragma unroll
  for (int i = 0; i < 3; ++i) {
    const int id = 192 * (wave & 1) + 64 * i + lane;
    const int row = id / 12, cc = id % 12;
    int col = f0 + 8 * cc;
    if (col > FF - 8) col = FF - 8;
    c.wo[i] = (unsigned)(row * FF + col) * 2u;
    c.ww[i] = sbase + row * 384 + ((cc + 12 * isup) ^ (((row >> 1) & 1) << 2)) * 16;
  }
  c.xbase = (const char*)(x + (size_t)(128 * mb + 32 * wave) * DM);
  c.xo = (unsigned)((lane >> 3) * DM + (lane & 7) * 8) * 2u;
  const unsigned xl = sbase + G1_X_OFF;
  c.xw = xl + (32 * wave + (lane >> 3)) * 128 + (((lane & 7) ^ (lane >> 4)) * 16);
  c.xw1 = xl + (32 * wave + (lane >> 3)) * 128 + (((lane & 7) ^ ((lane >> 4) + 4)) * 16);
  const int r = lane & 31, h = lane >> 5;
#pragma unroll
  for (int i = 0; i < 4; ++i) {
    c.xrd2[0][i] = xl + (64 * wm + 32 * isup + r) * 128 + (((2 * i + h) ^ ((r >> 1) & 7)) * 16);
    c.xrd2[1][i] = xl + (64 * wm + 32 * (1 - isup) + r) * 128 + (((2 * i + h) ^ ((r >> 1) & 7)) * 16);
  }
  const int blk = (lane >> 4) & 1, q = (lane & 15) >> 2, p = lane & 3;
  const int sw = (q >> 1) & 1;
  {
    const unsigned e = sbase + (8 * h + q) * 384 + (2 * blk + (p >> 1) + 4 * sw) * 16 + 8 * (p & 1);
    const unsigned o = sbase + (8 * h + q) * 384 + (2 * blk + (p >> 1) - 4 * sw) * 16 + 8 * (p & 1);
    c.woff = isup ? o + 192 : e;
    c.woff_o = isup ? e + 192 : o;
  }

  f32x16 acc[6];
#pragma unroll
  for (int i = 0; i < 6; ++i) acc[i] = f32x16{};
  asm volatile("s_nop 4" : "+v"(acc[0]), "+v"(acc[1]), "+v"(acc[2]), "+v"(acc[3]), "+v"(acc[4]), "+v"(acc[5]));
  u32x4 xr[2][4], wr[3][3];
  G1Frag fa, fb2;
#define G1_ISSUE_X(R, B) _Pragma("unroll") for (int i = 0; i < 4; ++i) GLOAD128S(xr[R][i], c.xo, c.xbase + (size_t)(8 * i) * DM * 2 + (B) * 128)
#define G1_ISSUE_W(R, S) _Pragma("unroll") for (int i = 0; i < 3; ++i) GLOAD128S(wr[R][i], c.wo[i], c.wbase + (size_t)(S) * 32 * FF * 2)
  G1_ISSUE_X(0, 0); G1_ISSUE_W(0, 0); G1_ISSUE_W(1, 1); G1_ISSUE_X(1, 1); G1_ISSUE_W(2, 2);
  asm volatile("s_waitcnt vmcnt(%10)"
               : "+v"(wr[0][0]), "+v"(wr[0][1]), "+v"(wr[0][2]), "+v"(wr[1][0]), "+v"(wr[1][1]), "+v"(wr[1][2]),
                 "+v"(xr[0][0]), "+v"(xr[0][1]), "+v"(xr[0][2]), "+v"(xr[0][3]) : "i"(7) : "memory");
#pragma unroll
  for (int i = 0; i < 3; ++i) { DS_W128(c.ww[i], wr[0][i], 0); DS_W128(c.ww[i], wr[1][i], G1_WSLOT_B); }
#pragma unroll
  for (int i = 0; i < 4; ++i) { if (i & 1) DS_W128(c.xw1, xr[0][i], i * 1024); else DS_W128(c.xw, xr[0][i], i * 1024); }
  G1_ISSUE_W(0, 3); G1_ISSUE_W(1, 4); G1_ISSUE_X(0, 2);
  asm volatile("s_waitcnt lgkmcnt(0)\n\ts_barrier" ::: "memory");
  g1_read<0>(fa, c);
  G1Step<0>::run(acc, xr, wr, fa, fb2, c);
  asm volatile("s_nop 15\n\ts_nop 15" : "+v"(acc[0]), "+v"(acc[1]), "+v"(acc[2]), "+v"(acc[3]), "+v"(acc[4]), "+v"(acc[5]));

  unsigned keep[3][8];
  {
    unsigned give[3][8];
#pragma unroll
    for (int tt = 0; tt < 3; ++tt)
#pragma unroll
      for (int e = 0; e < 8; ++e) give[tt][e] = pk_bf16(acc[3 + tt][2 * e], acc[3 + tt][2 * e + 1]);
    __syncthreads();
    char* xo = smem + wave * 6144 + lane * 16;
#pragma unroll
    for (int tt = 0; tt < 3; ++tt) {
      *(u32x4*)(xo + (2 * tt) * 1024) = u32x4{give[tt][0], give[tt][1], give[tt][2], give[tt][3]};
      *(u32x4*)(xo + (2 * tt + 1) * 1024) = u32x4{give[tt][4], give[tt][5], give[tt][6], give[tt][7]};
    }
  }
#pragma unroll
  for (int tt = 0; tt < 3; ++tt)
#pragma unroll
    for (int e = 0; e < 8; ++e) keep[tt][e] = pk_bf16(acc[tt][2 * e], acc[tt][2 * e + 1]);
  __syncthreads();
  {
    const char* xi = smem + (wave ^ 2) * 6144 + lane * 16;
    const int mt = 4 * mb + 2 * wm + isup;
    u16* hp = hf + ((size_t)(mt * NKS + 6 * fb) * 64 + lane) * 8;
    auto finish = [&](auto ISUP) {
#pragma unroll
      for (int tt = 0; tt < 3; ++tt) {
        const int ft = 3 * fb + tt;
        if (ft < FF / 32) {
          const u32x4 o0 = *(const u32x4*)(xi + (2 * tt) * 1024);
          const u32x4 o1 = *(const u32x4*)(xi + (2 * tt + 1) * 1024);
          unsigned gp[8], up[8];
#pragma unroll
          for (int e = 0; e < 8; ++e) {
            const unsigned ot = e < 4 ? o0[e & 3] : o1[e & 3];
            gp[e] = decltype(ISUP)::value ? ot : keep[tt][e];
            up[e] = decltype(ISUP)::value ? keep[tt][e] : ot;
          }
          f32x2 xg[8], ar[8];
#pragma unroll
          for (int e = 0; e < 8; ++e) xg[e] = f32x2{bf_lo(gp[e]), bf_hi(gp[e])};
#pragma unroll
          for (int e = 0; e < 8; ++e) ar[e] = xg[e] * xg[e];
#pragma unroll
          for (int e = 0; e < 8; ++e) ar[e] = __builtin_elementwise_fma(ar[e], f32x2{-0.10294323958f, -0.10294323958f}, f32x2{-2.30220819814f, -2.30220819814f});
#pragma unroll
          for (int e = 0; e < 8; ++e) ar[e] = ar[e] * xg[e];
#pragma unroll
          for (int e = 0; e < 8; ++e) ar[e] = f32x2{__builtin_amdgcn_exp2f(ar[e][0]), __builtin_amdgcn_exp2f(ar[e][1])};
#pragma unroll
          for (int e = 0; e < 8; ++e) ar[e] = ar[e] + f32x2{1.0f, 1.0f};
#pragma unroll
          for (int e = 0; e < 8; ++e) ar[e] = f32x2{__builtin_amdgcn_rcpf(ar[e][0]), __builtin_amdgcn_rcpf(ar[e][1])};
#pragma unroll
          for (int e = 0; e < 8; ++e) ar[e] = ar[e] * xg[e];
          unsigned o[8];
#pragma unroll
          for (int e = 0; e < 8; ++e) o[e] = pk_bf16(ar[e][0], ar[e][1]);
#pragma unroll
          for (int e = 0; e < 8; ++e) ar[e] = f32x2{bf_lo(o[e]), bf_hi(o[e])} * f32x2{bf_lo(up[e]), bf_hi(up[e])};
#pragma unroll
          for (int e = 0; e < 8; ++e) o[e] = pk_bf16(ar[e][0], ar[e][1]);
          { const u32x4 v0 = u32x4{o[0], o[1], o[2], o[3]}, v1 = u32x4{o[4], o[5], o[6], o[7]};
            asm volatile("global_store_dwordx4 %0, %1, off sc1\n\ts_nop 1" ::"v"(hp + (2 * tt) * 512), "v"(v0) : "memory");
            asm volatile("global_store_dwordx4 %0, %1, off sc1\n\ts_nop 1" ::"v"(hp + (2 * tt + 1) * 512), "v"(v1) : "memory"); }
        } else if (ft < NKS / 2) {
          *(u32x4*)(hp + (2 * tt) * 512) = u32x4{0, 0, 0, 0};
          *(u32x4*)(hp + (2 * tt + 1) * 512) = u32x4{0, 0, 0, 0};
        }
      }
    };
    if (isup) finish(std::integral_constant<bool, true>{});
    else finish(std::integral_constant<bool, false>{});
  }
}

__device__ __forceinline__ void gate_unit(int b, const u16* __restrict__ x, const u16* __restrict__ gw,
                                          float* __restrict__ gout) {
  const int tid = threadIdx.x, lane = tid & 63, wave = tid >> 6;
  const int j = lane & 7, rr = lane >> 3;
  u32x4 gv[8];
#pragma unroll
  for (int i = 0; i < 8; ++i) gv[i] = *(const u32x4*)(gw + (j + 8 * i) * 8);
  const int row0 = b * 128 + wave * 32 + rr;
  u32x4 xv[4][8];
#pragma unroll
  for (int ps = 0; ps < 4; ++ps)
#pragma unroll
    for (int i = 0; i < 8; ++i) xv[ps][i] = *(const u32x4*)(x + (size_t)(row0 + 8 * ps) * DM + (j + 8 * i) * 8);
#pragma unroll
  for (int ps = 0; ps < 4; ++ps) {
    float s0 = 0.f, s1 = 0.f;
#pragma unroll
    for (int i = 0; i < 8; ++i)
#pragma unroll
      for (int e = 0; e < 4; ++e) {
        const unsigned pr = pk_bf16(smul(bf_lo(xv[ps][i][e]), bf_lo(gv[i][e])), smul(bf_hi(xv[ps][i][e]), bf_hi(gv[i][e])));
        s0 = sadd(s0, bf_lo(pr));
        s1 = sadd(s1, bf_hi(pr));
      }
    float s = s0 + s1;
    s += __shfl_xor(s, 1);
    s += __shfl_xor(s, 2);
    s += __shfl_xor(s, 4);
    if (j == 0) {
      const float sb = rbf(s);
      gout[row0 + 8 * ps] = rbf(1.0f / (1.0f + expf(-sb)));
    }
  }
}

__global__ __launch_bounds__(256, 2) void k1_kernel(const u16* __restrict__ u, const u16* __restrict__ logAr,
                                                   const u16* __restrict__ Aim, const u16* __restrict__ Cre,
                                                   const u16* __restrict__ Cim, const u16* __restrict__ logdt,
                                                   const u16* __restrict__ Dskip, const u16* __restrict__ wg,
                                                   const u16* __restrict__ wu, const u16* __restrict__ gw,
                                                   const u16* __restrict__ wd, u16* __restrict__ hbuf, u16* __restrict__ aT,
                                                   float* __restrict__ gout
                                                   ) {
  __shared__ __attribute__((aligned(4096))) char smem[G1_LDS > SSM_LDS ? G1_LDS : SSM_LDS];
  const int bid = blockIdx.x;
  if (bid < NSSM) {
    ssm_unit(bid, u, logAr, Aim, Cre, Cim, logdt, Dskip, aT, smem);
    unsigned acc = 0;
    for (int i = (bid >> 3) * 256 + threadIdx.x; i < 11008; i += 32 * 256) acc += ((const unsigned*)wd)[i * 32];
    asm volatile("" ::"v"(acc));
  } else {
    const int xcd = bid & 7, idx = (bid - NSSM) >> 3;
    if (idx < 30) {
      gemm1_tile(30 * xcd + idx, u, wg, wu, hbuf, smem);
    }
    else gate_unit(2 * xcd + idx - 30, u, gw, gout);
  }
}

constexpr int G2_SLOT_B = 4096;
constexpr int G2_WAVE_B = 2 * G2_SLOT_B;
constexpr int G2_PITCH = 272;
constexpr int G2_A_OFF = 4 * 64 * G2_PITCH;
constexpr int K2_LDS = G2_A_OFF + 8192;
#define G2D 3
constexpr int G2_DEPTH = G2D;

struct G2Ctx {
  const char* hbase;
  const char* wbase;
  unsigned ho, wo, wrd0, wrd1, ww;
  int klim;
};
struct G2Frag { s16x4 w[2][2][2]; };
#define MFMA32A(acc, a, b) asm volatile("v_mfma_f32_32x32x16_bf16 %0, %1, %2, %0" : "+a"(acc) : "v"(a), "a"(b))

template <int ST>
struct G2Step {
  static __device__ __forceinline__ void issue(bf16x8 (&hfr)[G2_DEPTH][4], u32x4 (&wr)[G2_DEPTH][4], const G2Ctx& c) {
    const int k0 = (32 * ST < c.klim) ? 32 * ST : 0;
#pragma unroll
    for (int i = 0; i < 4; ++i) GLOAD128S(wr[ST % G2_DEPTH][i], c.wo, c.wbase + (size_t)(k0 + 8 * i) * DM * 2);
#pragma unroll
    for (int mi = 0; mi < 2; ++mi)
#pragma unroll
      for (int i = 0; i < 2; ++i) GLOAD128SA(hfr[ST % G2_DEPTH][2 * mi + i], c.ho, c.hbase + (size_t)(mi * NKS + 2 * ST + i) * 1024);
  }
  template <int NW>
  static __device__ __forceinline__ void store(u32x4 (&wr)[G2_DEPTH][4], const G2Ctx& c) {
    u32x4 (&r)[4] = wr[ST % G2_DEPTH];
    asm volatile("s_waitcnt vmcnt(%4)" : "+v"(r[0]), "+v"(r[1]), "+v"(r[2]), "+v"(r[3]) : "i"(NW) : "memory");
#pragma unroll
    for (int i = 0; i < 4; ++i) DS_W128(c.ww, r[i], (ST & 1) * G2_SLOT_B + i * 1024);
  }
  static __device__ __forceinline__ void run(f32x16 (&acc)[2][2], bf16x8 (&hfr)[G2_DEPTH][4], u32x4 (&wr)[G2_DEPTH][4],
                                             G2Frag& cur, G2Frag& nxt, const G2Ctx& c) {
    constexpr int LAST = (ST + G2_DEPTH - 1 < 10) ? ST + G2_DEPTH - 1 : 10;
    constexpr bool NX = ST + 1 <= 10;
    bf16x8 (&hh)[4] = hfr[ST % G2_DEPTH];
    if constexpr (NX) G2Step<ST + 1>::template store<4 + 8 * (LAST - ST - 1)>(wr, c);
    else asm volatile("s_waitcnt vmcnt(0)" ::: "memory");
    asm volatile("s_waitcnt lgkmcnt(%12)"
                 : "+v"(cur.w[0][0][0]), "+v"(cur.w[0][0][1]), "+v"(cur.w[0][1][0]), "+v"(cur.w[0][1][1]), "+v"(cur.w[1][0][0]),
                   "+v"(cur.w[1][0][1]), "+v"(cur.w[1][1][0]), "+v"(cur.w[1][1][1]), "+a"(hh[0]), "+a"(hh[1]), "+a"(hh[2]), "+a"(hh[3])
                 : "i"(NX ? 4 : 0) : "memory");
    const bf16x8 b00 = cat4(cur.w[0][0][0], cur.w[0][0][1]), b01 = cat4(cur.w[0][1][0], cur.w[0][1][1]);
    const bf16x8 b10 = cat4(cur.w[1][0][0], cur.w[1][0][1]), b11 = cat4(cur.w[1][1][0], cur.w[1][1][1]);
    constexpr int NO = ((ST + 1) & 1) * G2_SLOT_B;
    MFMA32A(acc[0][0], b00, hh[0]);
    MFMA32A(acc[0][1], b00, hh[2]);
    MFMA32A(acc[1][0], b01, hh[0]);
    if constexpr (NX) asm volatile("s_waitcnt lgkmcnt(0)" ::: "memory");
    MFMA32A(acc[1][1], b01, hh[2]);
    if constexpr (NX) { DS_TR(nxt.w[0][0][0], c.wrd0, NO); DS_TR(nxt.w[0][0][1], c.wrd0, NO + 1024); }
    MFMA32A(acc[0][0], b10, hh[1]);
    if constexpr (NX) { DS_TR(nxt.w[0][1][0], c.wrd1, NO); DS_TR(nxt.w[0][1][1], c.wrd1, NO + 1024); }
    MFMA32A(acc[0][1], b10, hh[3]);
    if constexpr (NX) { DS_TR(nxt.w[1][0][0], c.wrd0, NO + 2048); DS_TR(nxt.w[1][0][1], c.wrd0, NO + 3072); }
    MFMA32A(acc[1][0], b11, hh[1]);
    if constexpr (NX) { DS_TR(nxt.w[1][1][0], c.wrd1, NO + 2048); DS_TR(nxt.w[1][1][1], c.wrd1, NO + 3072); }
    MFMA32A(acc[1][1], b11, hh[3]);
    if constexpr (ST + G2_DEPTH <= 10) G2Step<ST + G2_DEPTH>::issue(hfr, wr, c);
    if constexpr (NX) G2Step<ST + 1>::run(acc, hfr, wr, nxt, cur, c);
  }
};

__global__ __launch_bounds__(256, 1) void k2_kernel(const u16* __restrict__ hf, const u16* __restrict__ wd,
                                                   const u16* __restrict__ aT, const float* __restrict__ gin,
                                                   u16* __restrict__ out) {
  __shared__ __attribute__((aligned(4096))) char smem[K2_LDS];
  const int tid = threadIdx.x, lane = tid & 63;
  const int wave = __builtin_amdgcn_readfirstlane(tid >> 6);
  const int bid = blockIdx.x;
  const int xcd = bid & 7, idx = bid >> 3;
  const int m0 = 64 * (4 * xcd + (idx >> 3)), n0 = 64 * (idx & 7);

  G2Ctx c;
  c.hbase = (const char*)(hf + (size_t)((m0 >> 5) * NKS + 22 * wave) * 512);
  c.ho = (unsigned)lane * 16u;
  c.wbase = (const char*)(wd + (size_t)(352 * wave) * DM + n0);
  c.wo = (unsigned)((lane >> 3) * DM + (lane & 7) * 8) * 2u;
  c.klim = FF - 352 * wave;
#pragma unroll
  for (int i = 0; i < 2; ++i) {
    const int piece = 2 * wave + i;
    const int drow = 8 * piece + (lane >> 3);
    glds16(aT + (size_t)(n0 + drow) * SEQ + m0 + (lane & 7) * 8, smem + G2_A_OFF + piece * 1024 + lane * 16);
  }
  bf16x8 hfr[G2_DEPTH][4];
  u32x4 wr[G2_DEPTH][4];
  G2Step<0>::issue(hfr, wr, c);
  G2Step<1>::issue(hfr, wr, c);
  G2Step<2>::issue(hfr, wr, c);
  if constexpr (G2_DEPTH > 3) G2Step<3>::issue(hfr, wr, c);
  if constexpr (G2_DEPTH > 4) G2Step<4>::issue(hfr, wr, c);

  const unsigned wb = lds_addr(smem) + wave * G2_WAVE_B;
  c.ww = wb + (lane >> 3) * 128 + (((lane & 7) ^ (((lane >> 4) & 1) << 2)) * 16);
  const int r = lane & 31, h = lane >> 5;
  const int blk = (lane >> 4) & 1, q = (lane & 15) >> 2, p = lane & 3;
  c.wrd0 = wb + (4 * h + q) * 128 + (((2 * blk + (p >> 1)) ^ (((q >> 1) & 1) << 2)) * 16) + 8 * (p & 1);
  c.wrd1 = c.wrd0 ^ 64u;

  f32x16 acc[2][2];
#pragma unroll
  for (int i = 0; i < 2; ++i)
#pragma unroll
    for (int j = 0; j < 2; ++j) acc[i][j] = f32x16{};
  asm volatile("s_nop 4" : "+a"(acc[0][0]), "+a"(acc[0][1]), "+a"(acc[1][0]), "+a"(acc[1][1]));
  G2Frag fa, fb;
  G2Step<0>::template store<4 + 8 * (G2_DEPTH - 1)>(wr, c);
  asm volatile("s_waitcnt lgkmcnt(0)" ::: "memory");
  DS_TR(fa.w[0][0][0], c.wrd0, 0); DS_TR(fa.w[0][0][1], c.wrd0, 1024);
  DS_TR(fa.w[0][1][0], c.wrd1, 0); DS_TR(fa.w[0][1][1], c.wrd1, 1024);
  DS_TR(fa.w[1][0][0], c.wrd0, 2048); DS_TR(fa.w[1][0][1], c.wrd0, 3072);
  DS_TR(fa.w[1][1][0], c.wrd1, 2048); DS_TR(fa.w[1][1][1], c.wrd1, 3072);
  G2Step<0>::run(acc, hfr, wr, fa, fb, c);
  asm volatile("s_nop 15\n\ts_nop 15" : "+a"(acc[0][0]), "+a"(acc[0][1]), "+a"(acc[1][0]), "+a"(acc[1][1]));
  const int m = tid >> 2, nq = (tid & 3) * 16;
  const float g = gin[m0 + m];
  __syncthreads();
  {
    char* pw = smem + wave * (64 * G2_PITCH) + r * G2_PITCH + 16 * h;
#pragma unroll
    for (int t = 0; t < 2; ++t)
#pragma unroll
      for (int mi = 0; mi < 2; ++mi)
#pragma unroll
        for (int gq = 0; gq < 4; ++gq)
          *(f32x4*)(pw + (32 * mi) * G2_PITCH + (32 * t + 8 * gq) * 4) =
              f32x4{acc[t][mi][4 * gq], acc[t][mi][4 * gq + 1], acc[t][mi][4 * gq + 2], acc[t][mi][4 * gq + 3]};
  }
  __syncthreads();
  {
    const float omg = rbf(1.0f - g);
    f32x4 sv[4];
#pragma unroll
    for (int j = 0; j < 4; ++j) {
      const char* pp = smem + m * G2_PITCH + (nq + 4 * j) * 4;
      sv[j] = (*(const f32x4*)pp + *(const f32x4*)(pp + 64 * G2_PITCH)) +
              (*(const f32x4*)(pp + 2 * 64 * G2_PITCH) + *(const f32x4*)(pp + 3 * 64 * G2_PITCH));
    }
    const u16* aimg = (const u16*)(smem + G2_A_OFF);
    unsigned o[8];
#pragma unroll
    for (int j = 0; j < 8; ++j) {
      const float a0 = bf2f(aimg[(nq + 2 * j) * 64 + m]);
      const float a1 = bf2f(aimg[(nq + 2 * j + 1) * 64 + m]);
      const unsigned bp = pk_bf16(sv[j >> 1][2 * (j & 1)], sv[j >> 1][2 * (j & 1) + 1]);
      const unsigned bt = pk_bf16(omg * bf_lo(bp), omg * bf_hi(bp));
      const unsigned ga = pk_bf16(g * a0, g * a1);
      o[j] = pk_bf16(bf_lo(ga) + bf_lo(bt), bf_hi(ga) + bf_hi(bt));
    }
    u16* op = out + (size_t)(m0 + m) * DM + n0 + nq;
    *(u32x4*)op = u32x4{o[0], o[1], o[2], o[3]};
    *(u32x4*)(op + 8) = u32x4{o[4], o[5], o[6], o[7]};
  }
}

extern "C" void kernel_launch(void* const* d_in, const int* in_sizes, int n_in, void* d_out, int out_size,
                              void* d_ws, size_t ws_size, hipStream_t stream) {
  const u16* u = (const u16*)d_in[0];
  const u16* logAr = (const u16*)d_in[1];
  const u16* Aim = (const u16*)d_in[2];
  const u16* Cre = (const u16*)d_in[3];
  const u16* Cim = (const u16*)d_in[4];
  const u16* logdt = (const u16*)d_in[5];
  const u16* Dskip = (const u16*)d_in[6];
  const u16* wg = (const u16*)d_in[8];
  const u16* wu = (const u16*)d_in[9];
  const u16* wd = (const u16*)d_in[10];
  const u16* gw = (const u16*)d_in[11];
  char* ws = (char*)d_ws;
  u16* hbuf = (u16*)ws;
  u16* aT = (u16*)(ws + (size_t)SEQ * FFP * 2);
  float* gbuf = (float*)(ws + (size_t)SEQ * FFP * 2 + (size_t)DM * SEQ * 2);
  k1_kernel<<<512, 256, 0, stream>>>(u, logAr, Aim, Cre, Cim, logdt, Dskip, wg, wu, gw, wd, hbuf, aT, gbuf
      );
  k2_kernel<<<256, 256, 0, stream>>>(hbuf, wd, aT, gbuf, (u16*)d_out);
}
```
